```python
import torch

def _softmax(x, axis=-1):
    return torch.softmax(x, dim=axis)

def _gelu(x):
    return torch.nn.functional.gelu(x, approximate='tanh')

def _silu(x):
    return torch.nn.functional.silu(x)

def _sigmoid(x):
    return torch.sigmoid(x)

def _layernorm(x, axis=-1, eps=1e-05):
    m = x.mean(dim=axis, keepdim=True)
    v = x.var(dim=axis, keepdim=True, unbiased=False)
    return (x - m) / torch.sqrt(v + eps)

def _scatter_add(tgt, idx, src, axis=0):
    return tgt.clone().index_add_(axis, idx, src)

def _segment_sum(src, seg, n):
    out = torch.zeros((n,) + src.shape[1:], dtype=src.dtype)
    return out.index_add_(0, seg, src)

def _pad(x, pw):
    flat = tuple((v for pair in reversed(pw) for v in pair))
    return torch.nn.functional.pad(x, flat)
kernel_name = 'interleave_layers__memory_aug_recurrent_mem_tr__glu_gated_geglu__prompt'

def setup_inputs():

    _w = lambda a: (lambda t: t.to(torch.bfloat16) if t.is_floating_point() else t)(torch.as_tensor(a))

    def _A_setup():
        B, S, Dm = (1, 2048, 576)
        H, Dh = (32, 128)
        M, seg = (8, 32)
        seg = min(seg, S)
        S = S // seg * seg
        return {'x': _w((lambda *s: torch.randn(*s, dtype=torch.bfloat16) * 0.05)(B, S, Dm)), 'mem0': _w((lambda *s: torch.randn(*s, dtype=torch.bfloat16) * 0.2)(B, M, Dm)), 'Wq': _w((lambda *s: torch.randn(*s, dtype=torch.bfloat16) * 1.6)(Dm, H, Dh) * Dm ** (-0.5)), 'Wk': _w((lambda *s: torch.randn(*s, dtype=torch.bfloat16) * 1.6)(Dm, H, Dh) * Dm ** (-0.5)), 'Wv': _w((lambda *s: torch.randn(*s, dtype=torch.bfloat16) * 0.2)(Dm, H, Dh) * Dm ** (-0.5)), 'Wo': _w((lambda *s: torch.randn(*s, dtype=torch.bfloat16) * 0.2)(H, Dh, Dm) * (H * Dh) ** (-0.5)), 'seg_len': seg}
    _A_in = _A_setup()

    def _B_setup():
        B, S, Dm, Df = (1, 2048, 576, 4608)
        return {'x': _w((lambda *s: torch.randn(*s, dtype=torch.bfloat16) * 0.05)(B, S, Dm)), 'w_gate': _w((lambda *s: torch.randn(*s, dtype=torch.bfloat16) * 0.4)(Dm, Df) * Dm ** (-0.5)), 'w_up': _w((lambda *s: torch.randn(*s, dtype=torch.bfloat16) * 0.4)(Dm, Df) * Dm ** (-0.5)), 'w_down': _w((lambda *s: torch.randn(*s, dtype=torch.bfloat16) * 0.4)(Df, Dm) * Df ** (-0.5))}
    _B_in = _B_setup()
    return {'A_x': _A_in['x'], 'A_mem0': _A_in['mem0'], 'A_Wq': _A_in['Wq'], 'A_Wk': _A_in['Wk'], 'A_Wv': _A_in['Wv'], 'A_Wo': _A_in['Wo'], 'A_seg_len': _A_in['seg_len'], 'B_x': _B_in['x'], 'B_w_gate': _B_in['w_gate'], 'B_w_up': _B_in['w_up'], 'B_w_down': _B_in['w_down']}

def reference(A_x, A_mem0, A_Wq, A_Wk, A_Wv, A_Wo, A_seg_len, B_x, B_w_gate, B_w_up, B_w_down):

    def _A_ref(x, mem0, Wq, Wk, Wv, Wo, seg_len):
        B, S, Dm = x.shape
        H, Dh = (Wq.shape[1], Wq.shape[2])
        M = mem0.shape[1]
        scale = Dh ** (-0.5)
        n_seg = S // seg_len
        L = M + seg_len
        idx = torch.arange(L)
        mask = (idx[:, None] < M) | (idx[:, None] >= idx[None, :])
        mem = mem0
        outs = []
        for s in range(n_seg):
            xs = x[:, s * seg_len:(s + 1) * seg_len, :].clone()
            h = torch.concatenate([mem, xs], axis=1)
            q = torch.einsum('btd,dhe->bhte', h, Wq)
            k = torch.einsum('btd,dhe->bhte', h, Wk)
            v = torch.einsum('btd,dhe->bhte', h, Wv)
            scores = torch.einsum('bhqe,bhke->bhqk', q, k) * scale
            scores = torch.where(mask[None, None], scores, -1000000000.0)
            p = _softmax(scores, axis=-1)
            o = torch.einsum('bhqk,bhke->bhqe', p, v)
            o = torch.einsum('bhte,hed->btd', o, Wo)
            mem = o[:, :M, :].clone()
            outs.append(o[:, M:, :])
        return torch.concatenate(outs, axis=1)

    def _B_ref(x, w_gate, w_up, w_down):
        g = torch.einsum('bsd,df->bsf', x, w_gate)
        u = torch.einsum('bsd,df->bsf', x, w_up)
        h = _gelu(g) * u
        return torch.einsum('bsf,fd->bsd', h, w_down)
    x = A_x
    x = x + _A_ref(_layernorm(x, axis=-1), A_mem0, A_Wq, A_Wk, A_Wv, A_Wo, A_seg_len)
    x = x + _B_ref(_layernorm(x, axis=-1), B_w_gate, B_w_up, B_w_down)
    x = x + _A_ref(_layernorm(x, axis=-1), A_mem0, A_Wq, A_Wk, A_Wv, A_Wo, A_seg_len)
    x = x + _B_ref(_layernorm(x, axis=-1), B_w_gate, B_w_up, B_w_down)
    return x
```

```cpp
#include <hip/hip_runtime.h>
#include <hip/hip_bf16.h>
#include <type_traits>
#include <cstdint>

#define LAS __attribute__((address_space(3)))
typedef unsigned v2u __attribute__((ext_vector_type(2)));
typedef unsigned v4u __attribute__((ext_vector_type(4)));
typedef unsigned v8u __attribute__((ext_vector_type(8)));
typedef float f32x4 __attribute__((ext_vector_type(4)));
typedef short v4s __attribute__((ext_vector_type(4)));
typedef __bf16 v2bf __attribute__((ext_vector_type(2)));
typedef __amdgpu_buffer_rsrc_t rsrc_t;

namespace {

constexpr int DM = 576, HD = 4096, DF = 4608, NSEG = 64;
constexpr int LDS_BYTES = 160 * 1024;
constexpr int AUX_SC1 = 16;
constexpr unsigned OOB = 0x80000000u;
constexpr unsigned SENT = 0xFFFFFFFFu;

constexpr int RING_OFF = 0;
constexpr int RING_SLOT = 36 * 16384;
constexpr int FLAG_OFF = 3 * RING_SLOT;
constexpr int ABORT_OFF = FLAG_OFF + 256 * 128;
constexpr int MEM_OFF = 2 << 20;
constexpr int INIT_BYTES = 4 << 20;
constexpr int XN_I = 36 * 1024, XRES_I = 72 * 1024, KF_I = 256 * 1024, KM_I = 128 * 1024, OF_I = 256 * 1024,
              PP_I = 144 * 1024, HF_I = 288 * 1024;
constexpr int L_XN = 0, L_XRES = L_XN + NSEG * XN_I, L_KF = L_XRES + NSEG * XRES_I, L_VF = L_KF + NSEG * KF_I,
              L_KM = L_VF + NSEG * KF_I, L_OF = L_KM + NSEG * KM_I, L_P1 = L_OF + NSEG * OF_I, L_XN1 = L_P1 + NSEG * PP_I,
              L_HF = L_XN1 + NSEG * XN_I, L_P2 = L_HF + NSEG * HF_I, L_SIZE = L_P2 + NSEG * PP_I;
constexpr int BULK_OFF = INIT_BYTES;
constexpr int DBG_OFF = BULK_OFF + 2 * L_SIZE;
constexpr int WS_USED = DBG_OFF + (4 << 20);
static_assert(WS_USED + (2 << 20) <= (256 << 20), "workspace");

constexpr int R_CH = 0, R_B = 32, R_D = 44, R_E = 56, R_QA = 60, R_KV = 76, R_C = 98, R_END = 127;

struct Params {
  const void *x, *mem0, *wq, *wk, *wv, *wo, *wg, *wu, *wd;
  void* out;
  unsigned char* ws;
};

template <int I, int N, class F> __device__ __forceinline__ void sfor(F&& f) {
  if constexpr (I < N) { f(std::integral_constant<int, I>{}); sfor<I + 1, N>(f); }
}

struct Ctx {
  rsrc_t r;
  v4u rs;
  LAS unsigned char* lds;
  int lane, w, layer, idx, lb;
  int myflag;
  int dead;
};

__device__ __forceinline__ v4u make_srd(const void* p, unsigned bytes) {
  const unsigned long long a = (unsigned long long)p;
  return v4u{(unsigned)a, (unsigned)(a >> 32) & 0xFFFFu, bytes, 0x00020000u};
}
__device__ __forceinline__ unsigned pk(float a, float b) {
  v2bf r; r[0] = (__bf16)a; r[1] = (__bf16)b; return __builtin_bit_cast(unsigned, r);
}
__device__ __forceinline__ v2u pk4(const f32x4& a) { return v2u{pk(a[0], a[1]), pk(a[2], a[3])}; }
__device__ __forceinline__ v4u cat(v2u a, v2u b) { return v4u{a.x, a.y, b.x, b.y}; }
__device__ __forceinline__ void bar() { asm volatile("s_waitcnt lgkmcnt(0)\n\ts_barrier" ::: "memory"); }
template <int N> __device__ __forceinline__ void vmwait() { asm volatile("s_waitcnt vmcnt(%0)" :: "i"(N) : "memory"); }
__device__ __forceinline__ v4u ld16(const Ctx& c, int voff, int soff) { return __builtin_amdgcn_raw_buffer_load_b128(c.r, voff, soff, AUX_SC1); }
__device__ __forceinline__ v2u ld8(const Ctx& c, int voff, int soff) { return __builtin_amdgcn_raw_buffer_load_b64(c.r, voff, soff, AUX_SC1); }
__device__ __forceinline__ void st16(const Ctx& c, v4u v, int voff, int soff) {
  asm volatile("buffer_store_dwordx4 %0, %1, %2, %3 offen sc1\n\ts_nop 1" :: "v"(v), "v"(voff), "s"(c.rs), "s"(soff) : "memory");
}
__device__ __forceinline__ void st8(const Ctx& c, v2u v, int voff, int soff) {
  asm volatile("buffer_store_dwordx2 %0, %1, %2, %3 offen sc1" :: "v"(v), "v"(voff), "s"(c.rs), "s"(soff) : "memory");
}
__device__ __forceinline__ void st4(const Ctx& c, unsigned v, int voff, int soff) {
  asm volatile("buffer_store_dword %0, %1, %2, %3 offen sc1" :: "v"(v), "v"(voff), "s"(c.rs), "s"(soff) : "memory");
}
template <int IMM, bool SC1> __device__ __forceinline__ void dma(v4u srd, int voff, int soff, int ldsa) {
  if constexpr (SC1)
    asm volatile("s_mov_b32 m0, %3\n\ts_nop 0\n\tbuffer_load_dwordx4 %0, %1, %2 offen offset:%4 sc1 lds" :: "v"(voff), "s"(srd), "s"(soff), "s"(ldsa), "i"(IMM) : "memory");
  else
    asm volatile("s_mov_b32 m0, %3\n\ts_nop 0\n\tbuffer_load_dwordx4 %0, %1, %2 offen offset:%4 lds" :: "v"(voff), "s"(srd), "s"(soff), "s"(ldsa), "i"(IMM) : "memory");
}
__device__ __forceinline__ void st4u(const Ctx& c, unsigned v, int soff) {
  asm volatile("buffer_store_dword %0, off, %1, %2 sc1" :: "v"(v), "s"(c.rs), "s"(soff) : "memory");
}
__device__ __forceinline__ void signal(const Ctx& c, int item) { st4u(c, (unsigned)item, c.myflag); }
__device__ __forceinline__ int opaque(int x) { asm volatile("" : "+v"(x)); return x; }
__device__ __forceinline__ int flag_at(int layer, int idx, int word) { return FLAG_OFF + (layer * 128 + idx) * 128 + word * 4; }
__device__ __forceinline__ bool give_up(Ctx& c, unsigned& spins) {
  ++spins;
  if ((spins & 255u) == 0u) {
    if (spins >= (1u << 17)) st4u(c, 1u, ABORT_OFF);
    const unsigned a = __builtin_amdgcn_raw_buffer_load_b32(c.r, 0, ABORT_OFF, AUX_SC1);
    if (__builtin_amdgcn_readfirstlane(a) == 1u || spins >= (1u << 17)) { c.dead = 1; return true; }
  }
  return false;
}
__device__ __forceinline__ void wait_flags(Ctx& c, int voff, int item) {
  if (c.dead) return;
  for (unsigned spins = 0;;) {
    asm volatile("" ::: "memory");
    const int f = (int)__builtin_amdgcn_raw_buffer_load_b32(c.r, voff, 0, AUX_SC1);
    if (__all(f >= item)) break;
    if (give_up(c, spins)) break;
    __builtin_amdgcn_s_sleep(1);
  }
  asm volatile("" ::: "memory");
}
__device__ __forceinline__ void flag_issue(const Ctx& c, int& f, int voff) {
  asm volatile("buffer_load_dword %0, %1, %2, 0 offen sc1" : "=v"(f) : "v"(voff), "s"(c.rs) : "memory");
}
__device__ __forceinline__ void flag_land(int& f) { asm volatile("s_waitcnt vmcnt(0)" : "+v"(f) :: "memory"); }

#define STAMP(i, k)
#define STAMPC(i, k)

template <bool AG, bool FIRST, bool WA> __device__ __forceinline__ void mmw(f32x4& c, const v4u& wgt, const v4u& x) {
  if constexpr (FIRST) {
    if constexpr (AG && WA) asm volatile("v_mfma_f32_16x16x32_bf16 %0, %1, %2, 0" : "=&v"(c) : "a"(wgt), "v"(x));
    else if constexpr (AG) asm volatile("v_mfma_f32_16x16x32_bf16 %0, %2, %1, 0" : "=&v"(c) : "a"(wgt), "v"(x));
    else if constexpr (WA) asm volatile("v_mfma_f32_16x16x32_bf16 %0, %1, %2, 0" : "=&v"(c) : "v"(wgt), "v"(x));
    else asm volatile("v_mfma_f32_16x16x32_bf16 %0, %2, %1, 0" : "=&v"(c) : "v"(wgt), "v"(x));
  } else {
    if constexpr (AG && WA) asm volatile("v_mfma_f32_16x16x32_bf16 %0, %1, %2, %0" : "+v"(c) : "a"(wgt), "v"(x));
    else if constexpr (AG) asm volatile("v_mfma_f32_16x16x32_bf16 %0, %2, %1, %0" : "+v"(c) : "a"(wgt), "v"(x));
    else if constexpr (WA) asm volatile("v_mfma_f32_16x16x32_bf16 %0, %1, %2, %0" : "+v"(c) : "v"(wgt), "v"(x));
    else asm volatile("v_mfma_f32_16x16x32_bf16 %0, %2, %1, %0" : "+v"(c) : "v"(wgt), "v"(x));
  }
}
template <bool FIRST> __device__ __forceinline__ void mm32(f32x4& c, const v4u& a, const v4u& b) {
  if constexpr (FIRST) asm volatile("s_nop 1\n\tv_mfma_f32_16x16x32_bf16 %0, %1, %2, 0" : "=&v"(c) : "v"(a), "v"(b));
  else asm volatile("s_nop 1\n\tv_mfma_f32_16x16x32_bf16 %0, %1, %2, %0" : "+v"(c) : "v"(a), "v"(b));
}
__device__ __forceinline__ void mm16(f32x4& c, const v2u& a, const v2u& b) {
  asm volatile("s_nop 1\n\tv_mfma_f32_16x16x16_bf16 %0, %1, %2, %0" : "+v"(c) : "v"(a), "v"(b));
}
template <bool AG> __device__ __forceinline__ void smm(f32x4& c, const v4u& a, const v8u& wgt, int idx) {
  if constexpr (AG) asm volatile("v_smfmac_f32_16x16x64_bf16 %0, %1, %2, %3" : "+v"(c) : "v"(a), "a"(wgt), "v"(idx));
  else asm volatile("v_smfmac_f32_16x16x64_bf16 %0, %1, %2, %3" : "+v"(c) : "v"(a), "v"(wgt), "v"(idx));
}
template <bool AG, class T> __device__ __forceinline__ void pinw(T& x) { if constexpr (AG) asm volatile("" : "+a"(x)); else asm volatile("" : "+v"(x)); }
__device__ __forceinline__ void mdone() { asm volatile("s_nop 7"); }
__device__ __forceinline__ void tie(f32x4& a) { asm volatile("" : "+v"(a)); }
__device__ __forceinline__ void zero_acc(f32x4& a) { a = f32x4{0.f, 0.f, 0.f, 0.f}; asm volatile("" : "+v"(a)); }
__device__ __forceinline__ float halfsum(float x, float y) {
  auto p = __builtin_amdgcn_permlane32_swap(__float_as_uint(x), __float_as_uint(y), false, false);
  return __uint_as_float(p[0]) + __uint_as_float(p[1]);
}
__device__ __forceinline__ f32x4 halfsum4(const f32x4& x, const f32x4& y) {
  return f32x4{halfsum(x[0], y[0]), halfsum(x[1], y[1]), halfsum(x[2], y[2]), halfsum(x[3], y[3])};
}
__device__ __forceinline__ float gmax(float x) {
  auto p = __builtin_amdgcn_permlane32_swap(__float_as_uint(x), __float_as_uint(x), false, false);
  x = fmaxf(__uint_as_float(p[0]), __uint_as_float(p[1]));
  auto q = __builtin_amdgcn_permlane16_swap(__float_as_uint(x), __float_as_uint(x), false, false);
  return fmaxf(__uint_as_float(q[0]), __uint_as_float(q[1]));
}
__device__ __forceinline__ float gsum(float x) {
  auto p = __builtin_amdgcn_permlane32_swap(__float_as_uint(x), __float_as_uint(x), false, false);
  x = __uint_as_float(p[0]) + __uint_as_float(p[1]);
  auto q = __builtin_amdgcn_permlane16_swap(__float_as_uint(x), __float_as_uint(x), false, false);
  return __uint_as_float(q[0]) + __uint_as_float(q[1]);
}
__device__ __forceinline__ v2u trread(LAS unsigned char* p) {
  return __builtin_bit_cast(v2u, __builtin_amdgcn_ds_read_tr16_b64_v4i16((LAS v4s*)p));
}
__device__ __forceinline__ v4u ldsr(LAS unsigned char* p) { return *(LAS v4u*)p; }
__device__ __forceinline__ unsigned umax4(v4u a) { return max(max(a.x, a.y), max(a.z, a.w)); }

template <int GR> __device__ __forceinline__ int sig(int row) {
  if constexpr (GR % 8 == 0) return row & 7;
  else if constexpr (GR % 4 == 0) return (row >> 1) & 3;
  else return (row >> 2) & 1;
}
template <int GR> __device__ __forceinline__ void slab_src(int piece, int lane, int& row, int& byte) {
  const int ch = piece * 64 + lane;
  row = ch / (2 * GR);
  const int cc = ch - row * (2 * GR);
  byte = (((cc >> 1) ^ sig<GR>(row)) << 5) + ((cc & 1) << 4);
}
template <int GR> __device__ __forceinline__ int slab_rd(int lane, int t) {
  const int row = lane >> 2;
  return row * (GR * 32) + ((t ^ sig<GR>(row)) << 5) + (lane & 3) * 8;
}
template <int GR> __device__ __forceinline__ v4u slab_frag(LAS unsigned char* slab, int rd) {
  return cat(trread(slab + rd), trread(slab + rd + 16 * GR * 32));
}

template <int NT, int KS, int DEPTH>
__device__ __forceinline__ void load_weights(Ctx& c, v4u (&W)[NT * KS], v4u srd, int ldb, int row0, int colb) {
  constexpr int GR = 4 * NT, SLAB = GR * 1024;
  static_assert(DEPTH * SLAB <= LDS_BYTES && (DEPTH - 1) * NT <= 63, "ring");
  int src[NT], rd[NT];
  sfor<0, NT>([&](auto j) {
    int row, byte; slab_src<GR>(c.w * NT + j, c.lane, row, byte);
    src[j] = colb + byte < ldb ? (row0 + row) * ldb + colb + byte : (int)OOB;
    rd[j] = slab_rd<GR>(c.lane, c.w * NT + j);
  });
  auto issue = [&](auto ks) {
    sfor<0, NT>([&](auto j) { dma<0, false>(srd, src[j], ks * 32 * ldb, (ks % DEPTH) * SLAB + (c.w * NT + j) * 1024); });
  };
  sfor<0, DEPTH - 1>(issue);
  sfor<0, KS>([&](auto ks) {
    constexpr int ahead = (KS - 1 - ks) < (DEPTH - 2) ? (KS - 1 - ks) : (DEPTH - 2);
    vmwait<ahead * NT>();
    bar();
    if constexpr (ks + DEPTH - 1 < KS) issue(std::integral_constant<int, ks + DEPTH - 1>{});
    sfor<0, NT>([&](auto t) {
      constexpr int id = ks * NT + t;
      W[id] = slab_frag<GR>(c.lds + (ks % DEPTH) * SLAB, rd[t]);
      pinw<(id < 64)>(W[id]);
    });
  });
  bar();
}

__device__ __forceinline__ int xn_off(int q, int g, int tok) { return q * 256 + ((g ^ ((q ^ (q >> 1)) & 1) ^ q) << 6) + tok * 16; }
__device__ __forceinline__ int xn_flag(const Ctx& c, int item) {
  const int q = c.lane & 3;
  return c.layer == 0 ? flag_at((item >> 2) & 1, R_E + q, 4 + (item & 3)) : flag_at(0, R_E + q, 2 + (item & 1));
}

template <int KS> __device__ __forceinline__ void issue_input(const Ctx& c, int src, int buf) {
  constexpr int N = KS / 2;
  const int s0 = src + c.w * N * 1024, l0 = buf + c.w * N * 1024, v = c.lane * 16;
  sfor<0, N>([&](auto j) { dma<(j & 3) * 1024, true>(c.rs, v, s0 + (j >> 2) * 4096, l0 + (j >> 2) * 4096); });
}

template <int NT, int KS, bool WA, int S1, class FO, class EPI>
__device__ __forceinline__ void gemm_loop(Ctx& c, v4u (&W)[NT * KS], int in_base, int in_stride, int rd_off, FO flag_off, EPI epi) {
  constexpr int BUF = 2 * KS * 1024, PD = NT >= 4 ? 2 : 3, C1 = KS / 2, C2 = KS + KS / 2;
  wait_flags(c, flag_off(0), 0);
  issue_input<KS>(c, in_base, 0);
  vmwait<0>();
  bool pend = false;
  for (int i = 0; i < NSEG; ++i) {
    bar();
    STAMP(i, 0);
    const int nx = i + 1 < NSEG ? i + 1 : i;
    bool issued = i + 1 >= NSEG;
    const int fo = flag_off(nx);
    LAS unsigned char* in = c.lds + (i & 1) * BUF + rd_off;
    int f1, f2;
    flag_issue(c, f1, fo);
    v4u b[PD + 1];
    sfor<0, PD>([&](auto n) { b[n] = ldsr(in + n * 1024); });
    f32x4 acc[NT];
    sfor<0, 2 * KS>([&](auto n) {
      constexpr int ks = n % KS;
      if constexpr (n + PD < 2 * KS) b[(n + PD) % (PD + 1)] = ldsr(in + (n + PD) * 1024);
      sfor<0, NT>([&](auto t) { constexpr int id = ks * NT + t; mmw<(id < 64), ks == 0, WA>(acc[t], W[id], b[n % (PD + 1)]); });
      if constexpr (n == C1) {
        flag_land(f1);
        if (pend) { signal(c, i - 1); pend = false; }
        if (!issued && __all(f1 >= nx)) { issue_input<KS>(c, in_base + nx * in_stride, (nx & 1) * BUF); issued = true; }
      }
      if constexpr (n == C2) {
        flag_land(f2);
        if (!issued && __all(f2 >= nx)) { issue_input<KS>(c, in_base + nx * in_stride, (nx & 1) * BUF); issued = true; }
      }
      if constexpr (ks == KS - 1) {
        mdone();
        sfor<0, NT>([&](auto t) { tie(acc[t]); });
        epi(std::integral_constant<int, n / KS>{}, i, acc);
        if constexpr (n == KS - 1) flag_issue(c, f2, fo);
      }
    });
    STAMP(i, 1);
    if (issued && i + 1 < NSEG) {
      vmwait<S1>();
      pend = true;
    } else {
      vmwait<0>();
      signal(c, i);
      if (i + 1 < NSEG) {
        wait_flags(c, fo, nx);
        issue_input<KS>(c, in_base + nx * in_stride, (nx & 1) * BUF);
        vmwait<0>();
      }
    }
  }
}

__device__ __forceinline__ void role_kv(Ctx& c, const Params& p) {
  const int j = c.idx - R_KV, isv = j >= 11, jj = isv ? j - 11 : j, wv = jj * 4 + c.w;
  v4u W[108];
  load_weights<6, 18, 6>(c, W, make_srd(isv ? p.wv : p.wk, DM * HD * 2), HD * 2, 0, jj * 768);
  const int rd = xn_off((c.lane >> 2) & 3, c.lane >> 4, c.lane & 3);
  auto fo = [&](int it) { return xn_flag(c, it); };
  if (!isv) {
    const int v = c.lane * 16;
    gemm_loop<6, 18, true, 3>(c, W, c.lb + L_XN, XN_I, rd, fo, [&](auto m, int it, f32x4 (&acc)[6]) {
      sfor<0, 3>([&](auto e) {
        const int U = wv * 3 + e;
        st16(c, cat(pk4(acc[2 * e]), pk4(acc[2 * e + 1])), U < 128 ? v : OOB, c.lb + L_KF + it * KF_I + (U >> 2) * 8192 + m * 4096 + (U & 3) * 1024);
      });
    });
  } else {
    v2u hold[6];
    const int v = c.lane * 16;
    gemm_loop<6, 18, false, 6>(c, W, c.lb + L_XN, XN_I, rd, fo, [&](auto m, int it, f32x4 (&acc)[6]) {
      sfor<0, 6>([&](auto t) {
        if constexpr (m == 0) hold[t] = pk4(acc[t]);
        else { const int T = wv * 6 + t; st16(c, cat(hold[t], pk4(acc[t])), T < 256 ? v : OOB, c.lb + L_VF + it * KF_I + T * 1024); }
      });
    });
  }
}

template <int KS> __device__ __forceinline__ void role_proj(Ctx& c, const void* wgt, int j, int in_off, int in_item, int out_off, int src_idx) {
  const int kc = j & 3, nc = j >> 2;
  v4u W[3 * KS];
  load_weights<3, KS, 12>(c, W, make_srd(wgt, KS * 128 * DM * 2), DM * 2, kc * KS * 32, nc * 384);
  constexpr int NSRC = KS == 32 ? 4 : 8;
  const int fo = flag_at(c.layer, src_idx + min(c.lane >> 2, NSRC - 1), c.lane & 3);
  const int tk = c.lane & 15, g = c.lane >> 4;
  const int v = (tk >> 2) * 36 * 1024 + kc * 256 + (4 * g + (tk & 3)) * 16;
  v2u hold[3];
  gemm_loop<3, KS, true, 3>(c, W, c.lb + in_off + kc * 2 * KS * 1024, in_item, c.lane * 16, [&](int) { return fo; },
    [&](auto m, int it, f32x4 (&acc)[3]) {
      sfor<0, 3>([&](auto t) {
        if constexpr (m == 0) hold[t] = pk4(acc[t]);
        else st16(c, cat(hold[t], pk4(acc[t])), v, c.lb + out_off + it * PP_I + (nc * 12 + c.w * 3 + t) * 1024);
      });
    });
}

__device__ __forceinline__ float geglu(float g, float u) {
  const float t = g * __builtin_fmaf(g * g, -0.1029432397f, -2.302208198f);
  return g * u * __builtin_amdgcn_rcpf(1.0f + __builtin_amdgcn_exp2f(t));
}

__device__ __forceinline__ void role_c(Ctx& c, const Params& p) {
  const int j = c.idx - R_C;
  v4u W[90];
  {
    constexpr int GR = 10, HALF = GR * 1024, SLAB = 2 * HALF, DEPTH = 8, KS = 18;
    const v4u sg = make_srd(p.wg, DM * DF * 2), su = make_srd(p.wu, DM * DF * 2);
    const v4u srd = c.w < 2 ? sg : su;
    int src[5], rd[3];
    sfor<0, 5>([&](auto q) { int row, byte; slab_src<GR>((c.w & 1) * 5 + q, c.lane, row, byte); src[q] = j * 320 + byte < DF * 2 ? row * (DF * 2) + j * 320 + byte : (int)OOB; });
    rd[0] = slab_rd<GR>(c.lane, 2 * c.w); rd[1] = slab_rd<GR>(c.lane, 2 * c.w + 1); rd[2] = slab_rd<GR>(c.lane, 8 + (c.w >> 1));
    auto issue = [&](auto ks) { sfor<0, 5>([&](auto q) { dma<0, false>(srd, src[q], ks * 32 * DF * 2, (ks % DEPTH) * SLAB + (c.w * 5 + q) * 1024); }); };
    sfor<0, DEPTH - 1>(issue);
    const int srcl = (c.lane & ~3) | ((c.w & 1) * 2) | (c.lane & 1);
    const bool up = c.lane & 2;
    sfor<0, KS>([&](auto ks) {
      constexpr int ahead = (KS - 1 - ks) < (DEPTH - 2) ? (KS - 1 - ks) : (DEPTH - 2);
      vmwait<ahead * 5>();
      bar();
      if constexpr (ks + DEPTH - 1 < KS) issue(std::integral_constant<int, ks + DEPTH - 1>{});
      LAS unsigned char* s = c.lds + (ks % DEPTH) * SLAB;
      W[ks * 5 + 0] = slab_frag<GR>(s, rd[0]); W[ks * 5 + 1] = slab_frag<GR>(s + HALF, rd[0]);
      W[ks * 5 + 2] = slab_frag<GR>(s, rd[1]); W[ks * 5 + 3] = slab_frag<GR>(s + HALF, rd[1]);
      const v4u G = slab_frag<GR>(s, rd[2]), U = slab_frag<GR>(s + HALF, rd[2]);
      v4u X;
      #pragma unroll
      for (int e = 0; e < 4; ++e) { const unsigned a = __shfl(G[e], srcl), b = __shfl(U[e], srcl); X[e] = up ? b : a; }
      W[ks * 5 + 4] = X;
      sfor<0, 5>([&](auto t) { constexpr int id = ks * 5 + t; pinw<(id < 64)>(W[id]); });
    });
    bar();
  }
  const int rd = xn_off((c.lane >> 2) & 3, c.lane >> 4, c.lane & 3);
  const int ka = 5 * j + c.w, kb = 5 * j + 4;
  const int oa = (ka / 36) * 72 * 1024 + (ka % 36) * 1024, ob = (kb / 36) * 72 * 1024 + (kb % 36) * 1024;
  const int va = c.lane * 16, vb = kb < 144 ? c.lane * 16 + c.w * 4 : OOB;
  gemm_loop<5, 18, true, 2>(c, W, c.lb + L_XN1, XN_I, rd, [&](int it) { return flag_at(c.layer, R_E + (c.lane & 3), it & 1); },
    [&](auto m, int it, f32x4 (&acc)[5]) {
      f32x4 h0, h1;
      #pragma unroll
      for (int e = 0; e < 4; ++e) { h0[e] = geglu(acc[0][e], acc[1][e]); h1[e] = geglu(acc[2][e], acc[3][e]); }
      const int base = c.lb + L_HF + it * HF_I + m * 36 * 1024;
      st16(c, cat(pk4(h0), pk4(h1)), va, base + oa);
      st4(c, pk(geglu(acc[4][0], acc[4][2]), geglu(acc[4][1], acc[4][3])), vb, base + ob);
    });
}

constexpr float SCL = 0.08838834764831845f * 1.4426950408889634f;
constexpr float MASKED = -1.0e9f / 0.08838834764831845f;

__device__ __forceinline__ int kv_flag(const Ctx& c, int h) {
  const int sel = c.lane & 3;
  const int wv = (sel & 1) ? (4 * h + 3) / 3 : (4 * h) / 3;
  return flag_at(c.layer, R_KV + (sel >> 1) * 11 + (wv >> 2), wv & 3);
}

__device__ __forceinline__ void role_qa(Ctx& c, const Params& p) {
  constexpr int KS = 18, BUF = 36 * 1024, XCH = 2 * BUF;
  const int j = c.idx - R_QA, head = 2 * j + (c.w >> 1), mm = c.w & 1;
  v4u W[72];
  load_weights<4, 18, 8>(c, W, make_srd(p.wq, DM * HD * 2), HD * 2, 0, j * 512);
  const int rd = xn_off((c.lane >> 2) & 3, c.lane >> 4, c.lane & 3);
  const int cc = c.lane & 15, g = c.lane >> 4, v = c.lane * 16;
  const int fkv = kv_flag(c, head), fch = flag_at(c.layer, R_CH + head, c.lane & 3);
  const int in_base = c.lb + L_XN;
  wait_flags(c, xn_flag(c, 0), 0);
  issue_input<KS>(c, in_base, 0);
  vmwait<0>();
  for (int i = 0; i < NSEG; ++i) {
    bar();
    STAMP(i, 0);
    LAS unsigned char* in = c.lds + (i & 1) * BUF + rd;
    v4u b[3], Q[4];
    sfor<0, 2>([&](auto n) { b[n] = ldsr(in + n * 1024); });
    f32x4 acc[4];
    sfor<0, 2 * KS>([&](auto n) {
      constexpr int ks = n % KS, m = n / KS;
      if constexpr (n + 2 < 2 * KS) b[(n + 2) % 3] = ldsr(in + (n + 2) * 1024);
      sfor<0, 4>([&](auto t) { constexpr int id = ks * 4 + t; mmw<(id < 64), ks == 0, true>(acc[t], W[id], b[n % 3]); });
      if constexpr (n == KS / 2) { if (i > 0) { vmwait<0>(); signal(c, i - 1); } }
      if constexpr (ks == KS - 1) {
        mdone();
        sfor<0, 4>([&](auto t) { tie(acc[t]); });
        sfor<0, 2>([&](auto e) { *(LAS v4u*)(c.lds + XCH + (((c.w >> 1) * 2 + m) * 4 + 2 * mm + e) * 1024 + v) = cat(pk4(acc[2 * e]), pk4(acc[2 * e + 1])); });
      }
    });
    bar();
    STAMP(i, 1);
    sfor<0, 4>([&](auto u) { Q[u] = ldsr(c.lds + XCH + (c.w * 4 + u) * 1024 + v); });
    if (i + 1 < NSEG) {
      wait_flags(c, xn_flag(c, i + 1), i + 1);
      issue_input<KS>(c, in_base + (i + 1) * XN_I, ((i + 1) & 1) * BUF);
    }
    wait_flags(c, fkv, i);
    v4u K[8], V[8];
    sfor<0, 8>([&](auto u) { K[u] = ld16(c, v + (u & 3) * 1024, c.lb + L_KF + i * KF_I + head * 8192 + (u >> 2) * 4096); });
    sfor<0, 8>([&](auto u) { V[u] = ld16(c, v + (u & 3) * 1024, c.lb + L_VF + i * KF_I + head * 8192 + (u >> 2) * 4096); });
    f32x4 s0, s1, sm;
    sfor<0, 4>([&](auto u) { mm32<u == 0>(s0, K[u], Q[u]); mm32<u == 0>(s1, K[4 + u], Q[u]); });
    wait_flags(c, fch, i);
    STAMP(i, 2);
    v4u KM[4]; v2u VM[4];
    const int mb = c.lb + L_KM + i * KM_I + head * 4096;
    sfor<0, 4>([&](auto u) { KM[u] = ld16(c, (g * 8 + (cc & 7)) * 16 + u * 512, mb); });
    sfor<0, 4>([&](auto u) { VM[u] = ld8(c, 2048 + c.lane * 8 + u * 512, mb); });
    sfor<0, 4>([&](auto u) { mm32<u == 0>(sm, KM[u], Q[u]); });
    mdone(); tie(s0); tie(s1); tie(sm);
    float mx = fmaxf(fmaxf(sm[0], sm[1]), fmaxf(sm[2], sm[3]));
    #pragma unroll
    for (int e = 0; e < 4; ++e) {
      const int lim = 16 * mm + cc - 4 * g - e;
      s0[e] = lim >= 0 ? s0[e] : MASKED;
      s1[e] = lim >= 16 ? s1[e] : MASKED;
      mx = fmaxf(mx, fmaxf(s0[e], s1[e]));
    }
    mx = gmax(mx) * SCL;
    float lt = 0.f, lm = 0.f;
    #pragma unroll
    for (int e = 0; e < 4; ++e) {
      s0[e] = __builtin_amdgcn_exp2f(__builtin_fmaf(s0[e], SCL, -mx));
      s1[e] = __builtin_amdgcn_exp2f(__builtin_fmaf(s1[e], SCL, -mx));
      sm[e] = __builtin_amdgcn_exp2f(__builtin_fmaf(sm[e], SCL, -mx));
      lt += s0[e] + s1[e]; lm += sm[e];
    }
    const float rl = __builtin_amdgcn_rcpf(gsum(__builtin_fmaf(lm, 0.5f, lt)));
    const v4u P = cat(pk4(s0), pk4(s1));
    const v2u pm = pk4(sm), z2 = v2u{0u, 0u};
    const v2u plo = g < 2 ? pm : z2, phi = g < 2 ? z2 : pm;
    f32x4 o[8];
    sfor<0, 8>([&](auto t) { mm32<true>(o[t], V[t], P); });
    sfor<0, 8>([&](auto t) { mm16(o[t], VM[t >> 1], (t & 1) ? phi : plo); });
    mdone();
    sfor<0, 8>([&](auto t) { tie(o[t]); });
    const int ob = c.lb + L_OF + i * OF_I + (head >> 3) * 65536 + mm * 32768 + (head & 7) * 4096;
    sfor<0, 4>([&](auto u) { st16(c, cat(pk4(o[2 * u] * rl), pk4(o[2 * u + 1] * rl)), v + u * 1024, ob); });
    STAMP(i, 3);
  }
  vmwait<0>();
  signal(c, NSEG - 1);
}

__device__ __forceinline__ float rowsum(float x) {
  x += __uint_as_float(__builtin_amdgcn_update_dpp(0u, __float_as_uint(x), 0x124, 0xF, 0xF, false));
  x += __uint_as_float(__builtin_amdgcn_update_dpp(0u, __float_as_uint(x), 0x128, 0xF, 0xF, false));
  auto p = __builtin_amdgcn_permlane32_swap(__float_as_uint(x), __float_as_uint(x), false, false);
  return __uint_as_float(p[0]) + __uint_as_float(p[1]);
}
__device__ __forceinline__ void norm_store(const Ctx& c, f32x4 (&a)[18], int q, int dst) {
  float s = 0.f;
  #pragma unroll
  for (int u = 0; u < 18; ++u) s += (a[u][0] + a[u][1]) + (a[u][2] + a[u][3]);
  const float mean = rowsum(s) * (1.0f / DM);
  float s2 = 0.f;
  #pragma unroll
  for (int u = 0; u < 18; ++u) {
    a[u] -= mean;
    s2 += (a[u][0] * a[u][0] + a[u][1] * a[u][1]) + (a[u][2] * a[u][2] + a[u][3] * a[u][3]);
  }
  const float rs = __builtin_amdgcn_rsqf(rowsum(s2) * (1.0f / DM) + 1e-5f);
  const int m = (c.lane >> 4) & 1, hi = c.lane >> 5;
  const int v = (m * 18 + hi * 9) * 1024 + xn_off(q, (c.lane >> 2) & 3, c.lane & 3);
  sfor<0, 9>([&](auto k) { st16(c, cat(pk4(a[2 * k] * rs), pk4(a[2 * k + 1] * rs)), v, dst + k * 1024); });
}
__device__ __forceinline__ void sum_partials(const Ctx& c, f32x4 (&a)[18], int src) {
  v4u lo = {0u, 0u, 0u, 0u}, hi = {0u, 0u, 0u, 0u};
  {
    const int r = c.lane & 15; const unsigned one = (r & 1) ? 0x3F800000u : 0x00003F80u;
    #pragma unroll
    for (int e = 0; e < 4; ++e) { lo[e] = (r >> 1) == e ? one : 0u; hi[e] = (r >> 1) == 4 + e ? one : 0u; }
  }
  v4u X[36];
  sfor<0, 18>([&](auto u) {
    X[u] = ld16(c, c.lane * 16, src + u * 1024);
    X[18 + u] = ld16(c, c.lane * 16, src + (18 + u) * 1024);
  });
  __builtin_amdgcn_sched_barrier(0);
  sfor<0, 18>([&](auto u) { mm32<false>(a[u], lo, X[u]); mm32<false>(a[u], hi, X[18 + u]); });
  mdone();
  sfor<0, 18>([&](auto u) { tie(a[u]); });
}
__device__ __forceinline__ void role_e(Ctx& c, const Params& p) {
  const int q = c.idx - R_E;
  const int m = (c.lane >> 4) & 1, hi = c.lane >> 5;
  const int rowoff = ((16 * m + 4 * q + (c.lane & 3)) * DM + hi * 288 + ((c.lane >> 2) & 3) * 4) * 2;
  volatile LAS int* done2 = (volatile LAS int*)(c.lds + 8 * 18432);
  if (threadIdx.x < 2) done2[threadIdx.x] = -1;
  bar();
  f32x4 a[18];
  {
    const rsrc_t rx = __builtin_amdgcn_make_buffer_rsrc((void*)p.x, 0, 2048 * DM * 2, 0x00020000);
    const int f0 = flag_at(c.layer, c.idx, 4 + c.w);
    for (int i = c.layer * 4 + c.w; i < NSEG; i += 8) {
      v2u x[18];
      sfor<0, 18>([&](auto u) { x[u] = __builtin_amdgcn_raw_buffer_load_b64(rx, rowoff + u * 32, i * 32 * DM * 2, 0); });
      sfor<0, 18>([&](auto u) {
        a[u] = f32x4{__uint_as_float(x[u].x << 16), __uint_as_float(x[u].x & 0xFFFF0000u), __uint_as_float(x[u].y << 16), __uint_as_float(x[u].y & 0xFFFF0000u)};
        st16(c, __builtin_bit_cast(v4u, a[u]), c.lane * 16, BULK_OFF + L_XRES + i * XRES_I + (q * 18 + u) * 1024);
      });
      norm_store(c, a, q, BULK_OFF + L_XN + i * XN_I);
      vmwait<0>();
      st4u(c, (unsigned)i, f0);
    }
  }
  const int fsrc = flag_at(c.layer, (c.w < 2 ? R_B : R_D) + min(c.lane >> 2, 11), c.lane & 3);
  if (c.w < 2) {
    for (int i = c.w; i < NSEG; i += 2) {
      wait_flags(c, xn_flag(c, i), i);
      sfor<0, 18>([&](auto u) { a[u] = __builtin_bit_cast(f32x4, ld16(c, c.lane * 16, c.lb + L_XRES + i * XRES_I + (q * 18 + u) * 1024)); });
      if (!c.dead) for (unsigned spins = 0; done2[c.w] < i - 8;) { if (give_up(c, spins)) break; __builtin_amdgcn_s_sleep(2); }
      wait_flags(c, fsrc, i);
      STAMP(i, 0); STAMP(i, 8);
      sum_partials(c, a, c.lb + L_P1 + i * PP_I + q * 36 * 1024);
      sfor<0, 18>([&](auto u) { *(LAS f32x4*)(c.lds + (i & 7) * 18432 + u * 1024 + c.lane * 16) = a[u]; });
      norm_store(c, a, q, c.lb + L_XN1 + i * XN_I);
      asm volatile("s_waitcnt vmcnt(0) lgkmcnt(0)" ::: "memory");
      signal(c, i);
      STAMP(i, 1); STAMP(i, 9);
    }
  } else {
    const rsrc_t ro = __builtin_amdgcn_make_buffer_rsrc(p.out, 0, 2048 * DM * 2, 0x00020000);
    for (int i = c.w - 2; i < NSEG; i += 2) {
      wait_flags(c, fsrc, i);
      STAMP(i, 18); STAMP(i, 26);
      sfor<0, 18>([&](auto u) { a[u] = *(LAS f32x4*)(c.lds + (i & 7) * 18432 + u * 1024 + c.lane * 16); });
      sum_partials(c, a, c.lb + L_P2 + i * PP_I + q * 36 * 1024);
      if (c.lane == 0) done2[c.w - 2] = i;
      if (c.layer == 0) {
        sfor<0, 18>([&](auto u) { st16(c, __builtin_bit_cast(v4u, a[u]), c.lane * 16, BULK_OFF + L_SIZE + L_XRES + i * XRES_I + (q * 18 + u) * 1024); });
        norm_store(c, a, q, BULK_OFF + L_SIZE + L_XN + i * XN_I);
        vmwait<0>();
        signal(c, i);
      } else {
        sfor<0, 18>([&](auto u) { __builtin_amdgcn_raw_buffer_store_b64(pk4(a[u]), ro, rowoff + u * 32, i * 32 * DM * 2, 0); });
      }
      STAMP(i, 19); STAMP(i, 27);
    }
  }
}

__device__ __forceinline__ int mem_col(int z, int hb) {
  const int w = z / 9, zz = z - 9 * w;
  return zz < 8 ? 144 * w + 16 * (4 * (zz >> 2) + 2 * hb + ((zz >> 1) & 1)) + 8 * (zz & 1) : 144 * w + 128 + 8 * hb;
}
__device__ __forceinline__ void sp_slot(int ks, int rho, int& ga, int& hb, int& s) {
  const int g = (rho >> 2) & 3, jj = (rho >> 4) * 4 + (rho & 3);
  ga = 2 * (ks & 1) + (g >> 1); hb = (jj >> 1) & 1; s = 2 * (2 * (g & 1) + (jj >> 2)) + (jj & 1);
}
__device__ __forceinline__ int qkv_row(int ks, int rho) { int ga, hb, s; sp_slot(ks, rho, ga, hb, s); return mem_col(4 * (ks >> 1) + ga, hb) + s; }
__device__ __forceinline__ int wo_row(int ks, int rho) {
  int ga, hb, s; sp_slot(ks, rho, ga, hb, s);
  return 32 * (2 * (ks >> 1) + (ga >> 1)) + 16 * (s >> 2) + 4 * (2 * (ga & 1) + hb) + (s & 3);
}
__device__ __forceinline__ v8u cat8(v4u a, v4u b) { return v8u{a.x, a.y, a.z, a.w, b.x, b.y, b.z, b.w}; }

#define CH_D1 6
#define CH_D2 3

__device__ __forceinline__ void role_chain(Ctx& c, const Params& p) {
  constexpr int LMEM = 0, LK = LMEM + 9216, LI = LK + 8192, LO = LI + 4096, LV = 24576, LWO = LV + 65536;
  const int h = c.idx, lane = c.lane, w = c.w;
  v8u W[55];
  auto pin = [&](auto id) { pinw<(id < 32)>(W[id]); };
  {
    const v4u sq = make_srd(p.wq, DM * HD * 2), sk = make_srd(p.wk, DM * HD * 2), sv = make_srd(p.wv, DM * HD * 2), so = make_srd(p.wo, DM * HD * 2);
    constexpr int DEPTH = 6, SLAB = 24576;
    int row[2], byte[2], rd[2];
    sfor<0, 2>([&](auto q) { slab_src<8>(w * 2 + q, lane, row[q], byte[q]); byte[q] += h * 256; rd[q] = slab_rd<8>(lane, 2 * w + q); });
    auto issue = [&](auto ks) {
      sfor<0, 2>([&](auto q) {
        const int src = qkv_row(ks, row[q]) * (HD * 2) + byte[q], d = (ks % DEPTH) * SLAB + (w * 2 + q) * 1024;
        dma<0, false>(sq, src, 0, d); dma<0, false>(sk, src, 0, d + 8192);
        dma<0, false>(sv, ks < 10 ? src : (int)OOB, 0, d + 16384);
      });
    };
    sfor<0, DEPTH - 1>(issue);
    v4u lo[6];
    sfor<0, 18>([&](auto ks) {
      constexpr int ahead = (17 - ks) < (DEPTH - 2) ? (17 - ks) : (DEPTH - 2), kk = ks >> 1;
      vmwait<ahead * 6>();
      bar();
      if constexpr (ks + DEPTH - 1 < 18) issue(std::integral_constant<int, ks + DEPTH - 1>{});
      sfor<0, (ks < 10 ? 6 : 4)>([&](auto e) {
        constexpr int mat = e >> 1, t = e & 1, id = mat < 2 ? mat * 18 + t * 9 + kk : 36 + t * 5 + kk;
        const v4u f = slab_frag<8>(c.lds + (ks % DEPTH) * SLAB + mat * 8192, rd[t]);
        if constexpr (!(ks & 1)) lo[e] = f; else { W[id] = cat8(lo[e], f); pin(std::integral_constant<int, id>{}); }
      });
    });
    bar();
    int orow[9], obyte[9];
    sfor<0, 9>([&](auto q) { slab_src<36>(w * 9 + q, lane, orow[q], obyte[q]); });
    auto issue_wo = [&](auto ks) { sfor<0, 9>([&](auto q) { dma<0, false>(so, (h * 128 + wo_row(ks, orow[q])) * (DM * 2) + obyte[q], 0, LWO + (ks & 1) * 36864 + (w * 9 + q) * 1024); }); };
    issue_wo(std::integral_constant<int, 0>{}); issue_wo(std::integral_constant<int, 1>{});
    sfor<10, 18>([&](auto ks) { sfor<0, 2>([&](auto q) { dma<0, false>(sv, qkv_row(ks, row[q]) * (HD * 2) + byte[q], 0, LV + (ks - 10) * 8192 + (w * 2 + q) * 1024); }); });
    vmwait<16>();
    bar();
    sfor<0, 9>([&](auto t) {
      const int r = slab_rd<36>(lane, w * 9 + t);
      W[46 + t] = cat8(slab_frag<36>(c.lds + LWO, r), slab_frag<36>(c.lds + LWO + 36864, r));
      pin(std::integral_constant<int, 46 + t>{});
    });
    vmwait<0>();
    bar();
    issue_wo(std::integral_constant<int, 2>{}); issue_wo(std::integral_constant<int, 3>{});
    sfor<0, 8>([&](auto k8) {
      const v4u f0 = slab_frag<8>(c.lds + LV + k8 * 8192, rd[0]), f1 = slab_frag<8>(c.lds + LV + k8 * 8192, rd[1]);
      bar();
      *(LAS v4u*)(c.lds + LV + k8 * 8192 + w * 2048 + lane * 16) = f0;
      *(LAS v4u*)(c.lds + LV + k8 * 8192 + w * 2048 + 1024 + lane * 16) = f1;
    });
    vmwait<0>();
    bar();
    sfor<0, 2>([&](auto f) {
      v4u x[9];
      sfor<0, 9>([&](auto t) { x[t] = slab_frag<36>(c.lds + LWO + f * 36864, slab_rd<36>(lane, w * 9 + t)); });
      bar();
      sfor<0, 9>([&](auto t) { *(LAS v4u*)(c.lds + LWO + f * 36864 + (w * 9 + t) * 1024 + lane * 16) = x[t]; });
    });
  }
  const int v16 = lane * 16;
  const int pol = c.layer * 8192;
  {
    const rsrc_t rm = __builtin_amdgcn_make_buffer_rsrc((void*)p.mem0, 0, 8 * DM * 2, 0x00020000);
    for (int kk = w; kk < 9; kk += 4) {
      const int r = ((lane >> 3) & 1) * 4 + (lane & 3), hb = (lane >> 2) & 1;
      *(LAS v4u*)(c.lds + LMEM + kk * 1024 + v16) = __builtin_amdgcn_raw_buffer_load_b128(rm, (r * DM + mem_col(4 * kk + (lane >> 4), hb)) * 2, 0, 0);
    }
  }
  if (w >= 2) {
    wait_flags(c, kv_flag(c, h), 0);
    sfor<0, 4>([&](auto q) { dma<q * 1024, true>(c.rs, v16, c.lb + L_KF + h * 8192 + (w - 2) * 4096, LK + (w - 2) * 4096); });
  }
  vmwait<0>();
  const int idx = (lane & 8) ? 0xEEEE : 0x4444;
  LAS unsigned char* aptr = c.lds + LMEM + (lane >> 4) * 256 + ((lane >> 2) & 1) * 128 + ((lane >> 3) & 1) * 64 + (lane & 3) * 16;
  LAS unsigned char* lin = c.lds + v16;
  const int unit = h + 32 * w;
  int slot = 0;
  for (int s = 0;; ++s) {
    f32x4 q0, q1, k0, k1, u0, u1;
    zero_acc(q0); zero_acc(q1); zero_acc(k0); zero_acc(k1); zero_acc(u0); zero_acc(u1);
    bar();
    STAMP(s, 0);
    v4u a[3];
    a[0] = ldsr(aptr); a[1] = ldsr(aptr + 1024);
    v4u V0 = ld16(c, v16, c.lb + L_VF + s * KF_I + h * 8192 + w * 2048);
    v4u V1 = ld16(c, v16 + 1024, c.lb + L_VF + s * KF_I + h * 8192 + w * 2048);
    sfor<0, 9>([&](auto kk) {
      a[(kk + 2) % 3] = ldsr(aptr + ((kk + 2) % 9) * 1024);
      smm<true>(q0, a[kk % 3], W[kk], idx); smm<true>(q1, a[kk % 3], W[9 + kk], idx);
      smm<true>(k0, a[kk % 3], W[18 + kk], idx); smm<(27 + kk < 32)>(k1, a[kk % 3], W[27 + kk], idx);
    });
    LAS unsigned char* iw;
    { const int t = opaque(v16); iw = c.lds + LI + w * 1024 + ((t & 0xF0) << 1) + ((t >> 5) & 0x18); }
    sfor<0, 2>([&](auto kk) {
      a[(kk + 2) % 3] = ldsr(aptr + (kk + 2) * 1024);
      smm<false>(u0, a[kk % 3], W[36 + kk], idx); smm<false>(u1, a[kk % 3], W[41 + kk], idx);
    });
    asm volatile("s_nop 3"); tie(q0); tie(q1); tie(k0); tie(k1);
    *(LAS v2u*)iw = pk4(halfsum4(q0, k0));
    *(LAS v2u*)(iw + 512) = pk4(halfsum4(q1, k1));
    LAS unsigned char* ir = c.lds + LI + (opaque(v16) >> 1);
    bar();
    STAMP(s, 1);
    v4u F[2], KA[2], KB[2];
    auto fetch = [&](auto u) {
      F[u & 1] = cat(trread(ir + u * 1024), trread(ir + u * 1024 + 512));
      KA[u & 1] = ldsr(lin + LK + u * 1024); KB[u & 1] = ldsr(lin + LK + 4096 + u * 1024);
    };
    a[1] = ldsr(aptr + 4 * 1024);
    fetch(std::integral_constant<int, 0>{}); fetch(std::integral_constant<int, 1>{});
    sfor<2, 5>([&](auto kk) { smm<false>(u0, a[kk % 3], W[36 + kk], idx); smm<false>(u1, a[kk % 3], W[41 + kk], idx); });
    f32x4 s0, s1, sm;
    sfor<0, 4>([&](auto u) {
      mmw<false, u == 0, true>(s0, KA[u & 1], F[u & 1]); mmw<false, u == 0, true>(s1, KB[u & 1], F[u & 1]); mmw<false, u == 0, true>(sm, F[u & 1], F[u & 1]);
      if constexpr (u < 2) fetch(std::integral_constant<int, u + 2>{});
    });
    const int mb = c.lb + L_KM + s * KM_I + h * 4096 + w * 512;
    LAS unsigned char* pv = c.lds + LV + w * 2048 + opaque(v16);
    v8u wa, wb;
    auto fetchv = [&](auto kk) {
      a[0] = ldsr(aptr + kk * 1024);
      wa = cat8(ldsr(pv + (kk - 5) * 16384), ldsr(pv + (kk - 5) * 16384 + 8192));
      wb = cat8(ldsr(pv + (kk - 5) * 16384 + 1024), ldsr(pv + (kk - 5) * 16384 + 8192 + 1024));
    };
    auto mulv = [&]() { smm<false>(u0, a[0], wa, idx); smm<false>(u1, a[0], wb, idx); };
    fetchv(std::integral_constant<int, 5>{});
    {
      const v4u fw = cat(trread(ir + w * 1024), trread(ir + w * 1024 + 512));
      const int t = opaque(v16);
      asm volatile("" : "+v"(V0), "+v"(V1));
      st16(c, fw, (t & 0x80) ? ((t >> 1) & 0x180) + (t & 0x70) : (int)OOB, mb);
    }
    tie(s0); tie(s1); tie(sm);
    const bool upper = opaque(v16) >= 512;
    float mx = -__builtin_inff();
    #pragma unroll
    for (int e = 0; e < 4; ++e) { sm[e] = upper ? sm[e] : -__builtin_inff(); mx = fmaxf(mx, fmaxf(sm[e], fmaxf(s0[e], s1[e]))); }
    mulv(); fetchv(std::integral_constant<int, 6>{});
    mx = gmax(mx) * SCL;
    #pragma unroll
    for (int e = 0; e < 4; ++e) s0[e] = __builtin_amdgcn_exp2f(__builtin_fmaf(s0[e], SCL, -mx));
    tie(s0);
    mulv(); fetchv(std::integral_constant<int, 7>{});
    #pragma unroll
    for (int e = 0; e < 4; ++e) s1[e] = __builtin_amdgcn_exp2f(__builtin_fmaf(s1[e], SCL, -mx));
    tie(s1);
    mulv(); fetchv(std::integral_constant<int, 8>{});
    #pragma unroll
    for (int e = 0; e < 4; ++e) sm[e] = __builtin_amdgcn_exp2f(__builtin_fmaf(sm[e], SCL, -mx));
    tie(sm);
    mulv();
    const f32x4 ls = s0 + s1 + sm;
    const float l = (ls[0] + ls[1]) + (ls[2] + ls[3]);
    const v4u P = cat(pk4(s0), pk4(s1));
    const v2u phi = pk4(sm);
    v2u plo;
    { auto x = __builtin_amdgcn_permlane32_swap(phi.x, 0u, false, false); auto y = __builtin_amdgcn_permlane32_swap(phi.y, 0u, false, false); plo = v2u{x[1], y[1]}; }
    f32x4 o0, o1;
    mm32<true>(o0, V0, P); mm32<true>(o1, V1, P);
    asm volatile("s_nop 3"); tie(u0); tie(u1);
    const v2u VM = pk4(halfsum4(u0, u1));
    mm16(o0, VM, plo); mm16(o1, VM, phi);
    st8(c, VM, 2048 + (opaque(v16) >> 1), mb);
    const float rl = __builtin_amdgcn_rcpf(gsum(l));
    LAS unsigned char* pw = c.lds + LWO + w * 9216 + opaque(v16);
    auto wol = [&](auto t) { return cat8(ldsr(pw + t * 1024), ldsr(pw + t * 1024 + 36864)); };
    wa = wol(std::integral_constant<int, 0>{}); wb = wol(std::integral_constant<int, 1>{});
    mdone(); tie(o0); tie(o1);
    {
      const int t = opaque(v16);
      if (!(t & 0x80)) *(LAS v4u*)(c.lds + LO + w * 512 + ((t >> 1) & 0x180) + (t & 0x70)) = cat(pk4(o0 * rl), pk4(o1 * rl));
    }
    if (s == NSEG - 1) break;
    f32x4 d[9];
    zero_acc(d[0]); zero_acc(d[1]); zero_acc(d[2]); zero_acc(d[3]);
    int pst;
    { const int t = opaque(v16); pst = ((t & 0x200) << 6) + ((t & 0x80) << 7) + ((t & 0x100) << 4) + (t & 0x70); }
    bar();
    STAMP(s, 2);
    const v4u A0 = ldsr(lin + LO), A1 = ldsr(lin + LO + 1024);
    const int rb = RING_OFF + slot * RING_SLOT + pol;
    const int pb = rb + 9 * w * 16384 + h * 128;
    v2u hold;
    auto fin = [&](f32x4& x, f32x4& y) { tie(x); tie(y); v2u r = pk4(halfsum4(x, y)); r.x = min(r.x, SENT - 1u); r.y = min(r.y, SENT - 1u); return r; };
    sfor<0, 5>([&](auto pr) {
      constexpr int t = 2 * pr;
      smm<false>(d[t], A0, W[46 + t], idx);
      if constexpr (pr < 4) smm<false>(d[t + 1], A0, W[47 + t], idx);
      smm<false>(d[t], A1, wa, idx);
      if constexpr (pr < 4) { smm<false>(d[t + 1], A1, wb, idx); wa = wol(std::integral_constant<int, t + 2>{}); }
      if constexpr (pr < 3) wb = wol(std::integral_constant<int, t + 3>{});
      if constexpr (pr >= 1) {
        asm volatile("s_nop 5");
        const v2u r = fin(d[t - 2], d[t - 1]);
        if constexpr (pr & 1) hold = r; else st16(c, cat(hold, r), pst, pb + (pr / 2 - 1) * 65536);
        if constexpr (t + 2 < 9) zero_acc(d[t + 2]);
        if constexpr (t + 3 < 9) zero_acc(d[t + 3]);
        asm volatile("s_nop 1");
      }
    });
    {
      const int t = opaque(v16);
      const int p8 = t < 512 ? ((t & 0x100) << 4) + (t & 0x70) + ((t & 0x80) >> 4) : (int)OOB;
      st8(c, fin(d[8], d[8]), p8, pb + 8 * 16384);
    }
    vmwait<3>();
    signal(c, s);
    STAMP(s, 3);
    const int mbn = MEM_OFF + (s + 1) * 32768 + pol;
    if (unit < 72) {
      const int t = opaque(v16), cc = (t >> 4) & 15;
      v4u sel;
      { const unsigned one = (cc & 1) ? 0x3F800000u : 0x00003F80u;
        #pragma unroll
        for (int e = 0; e < 4; ++e) sel[e] = (cc >> 1) == e ? one : 0u; }
      const int mo = (t & 0x280) ? (int)OOB : (t & 0x70) + ((t & 0x100) >> 5);
      const int ra = rb + (unit >> 1) * 16384 + (unit & 1) * 4096;
      __builtin_amdgcn_s_sleep(CH_D1);
      f32x4 r;
      for (unsigned spins = 0;;) {
        asm volatile("" ::: "memory");
        v4u X[4];
        sfor<0, 4>([&](auto q) { X[q] = ld16(c, v16 + q * 1024, ra); });
        __builtin_amdgcn_sched_barrier(0);
        sfor<0, 4>([&](auto q) { mm32<q == 0>(r, X[q], sel); });
        const unsigned mxx = max(max(umax4(X[0]), umax4(X[1])), max(umax4(X[2]), umax4(X[3])));
        if (!__any(mxx == SENT) || c.dead || give_up(c, spins)) break;
      }
      mdone();
      st8(c, fin(r, r), mo, mbn + unit * 128 + (unit >= 64 ? 8192 : 0));
      STAMP(s, 4);
    }
    if (w >= 2) {
      if (s > 0) {
        const int ob = RING_OFF + (slot == 0 ? 2 : slot - 1) * RING_SLOT + pol + (h >> 1) * 16384 + (h & 1) * 4096 + (w - 2) * 2048;
        const unsigned f1 = (unsigned)opaque(-1);
        const v4u ff = {f1, f1, f1, f1};
        st16(c, ff, v16, ob); st16(c, ff, v16, ob + 1024);
        st16(c, ff, v16, ob + 16 * 16384); st16(c, ff, v16, ob + 16 * 16384 + 1024);
        if (h < 8) { st16(c, ff, v16, ob + 32 * 16384); st16(c, ff, v16, ob + 32 * 16384 + 1024); }
      }
      Ctx cf = c; cf.lane = opaque(v16) >> 4;
      wait_flags(c, kv_flag(cf, h), s + 1);
      sfor<0, 4>([&](auto q) { dma<q * 1024, true>(c.rs, v16, c.lb + L_KF + (s + 1) * KF_I + h * 8192 + (w - 2) * 4096, LK + (w - 2) * 4096); });
    }
    LAS unsigned char* gw = c.lds + LMEM + w * 2048 + opaque(v16);
    bar();
    __builtin_amdgcn_s_sleep(CH_D2);
    {
      v4u G0, G1, G2;
      for (unsigned spins = 0;;) {
        asm volatile("" ::: "memory");
        G0 = ld16(c, v16, mbn + w * 2048); G1 = ld16(c, v16 + 1024, mbn + w * 2048);
        G2 = v4u{0u, 0u, 0u, 0u};
        if (w == 3) G2 = ld16(c, v16, mbn + 16384);
        const unsigned mxx = max(max(umax4(G0), umax4(G1)), umax4(G2));
        if (!__any(mxx == SENT) || c.dead || give_up(c, spins)) break;
      }
      *(LAS v4u*)gw = G0; *(LAS v4u*)(gw + 1024) = G1;
      if (w == 3) *(LAS v4u*)(gw + 2048) = G2;
    }
    vmwait<0>();
    slot = slot == 2 ? 0 : slot + 1;
  }
  vmwait<0>();
  signal(c, NSEG - 1);
}

__global__ void __launch_bounds__(256, 1) mega_kernel(Params p) {
  extern __shared__ __attribute__((aligned(16))) unsigned char smem[];
  Ctx c;
  c.r = __builtin_amdgcn_make_buffer_rsrc(p.ws, 0, WS_USED, 0x00020000);
  c.rs = make_srd(p.ws, WS_USED);
  c.lds = (LAS unsigned char*)smem;
  c.lane = threadIdx.x & 63;
  c.w = __builtin_amdgcn_readfirstlane(threadIdx.x >> 6);
  const int b = blockIdx.x;
  c.layer = (b >> 2) & 1;
  c.idx = (b >> 3) * 4 + (b & 3);
  c.lb = BULK_OFF + c.layer * L_SIZE;
  c.myflag = flag_at(c.layer, c.idx, c.w);
  c.dead = 0;
  STAMP(64, 0); STAMPC(64, 2);
  if (c.idx < R_B) role_chain(c, p);
  else if (c.idx < R_D) role_proj<32>(c, p.wo, c.idx - R_B, L_OF, OF_I, L_P1, R_QA + 4 * ((c.idx - R_B) & 3));
  else if (c.idx < R_E) role_proj<36>(c, p.wd, c.idx - R_D, L_HF, HF_I, L_P2, R_C + 7 * ((c.idx - R_D) & 3));
  else if (c.idx < R_QA) role_e(c, p);
  else if (c.idx < R_KV) role_qa(c, p);
  else if (c.idx < R_C) role_kv(c, p);
  else if (c.idx < R_END) role_c(c, p);
  STAMP(64, 1); STAMPC(64, 3);
}

}

extern "C" void kernel_launch(void* const* d_in, const int* in_sizes, int n_in, void* d_out, int out_size, void* d_ws, size_t ws_size,
                              hipStream_t stream) {
  Params p;
  p.x = d_in[0]; p.mem0 = d_in[1]; p.wq = d_in[2]; p.wk = d_in[3]; p.wv = d_in[4]; p.wo = d_in[5];
  p.wg = d_in[8]; p.wu = d_in[9]; p.wd = d_in[10];
  p.out = d_out;
  p.ws = (unsigned char*)(((uintptr_t)d_ws + (2u << 20) - 1) & ~(uintptr_t)((2u << 20) - 1));
  (void)hipFuncSetAttribute((const void*)mega_kernel, hipFuncAttributeMaxDynamicSharedMemorySize, LDS_BYTES);
  (void)hipMemsetAsync(p.ws, 0xFF, INIT_BYTES, stream);
  mega_kernel<<<256, 256, LDS_BYTES, stream>>>(p);
}
```

```cpp
#include <hip/hip_runtime.h>
#include <hip/hip_bf16.h>
#include <type_traits>
#include <cstdint>

#define LAS __attribute__((address_space(3)))
typedef unsigned v2u __attribute__((ext_vector_type(2)));
typedef unsigned v4u __attribute__((ext_vector_type(4)));
typedef unsigned v8u __attribute__((ext_vector_type(8)));
typedef unsigned v16u __attribute__((ext_vector_type(16)));
typedef unsigned v32u __attribute__((ext_vector_type(32)));
typedef float f32x4 __attribute__((ext_vector_type(4)));
typedef float f32x2 __attribute__((ext_vector_type(2)));
typedef short v4s __attribute__((ext_vector_type(4)));
typedef __bf16 v2bf __attribute__((ext_vector_type(2)));
typedef __amdgpu_buffer_rsrc_t rsrc_t;

namespace {

constexpr int DM = 576, HD = 4096, DF = 4608, NSEG = 64;
constexpr int LDS_BYTES = 160 * 1024;
constexpr int AUX_SC1 = 16;
constexpr unsigned OOB = 0x80000000u;
constexpr unsigned SENT = 0xFFFFFFFFu;

constexpr int sp(int x) { return ((x >> 13) << 14) | (x & 8191); }
constexpr int BLK = 2 << 20;
constexpr int RING_OFF = 0;
constexpr int RING_SLOT = 36 * 16384;
constexpr int FLAG_OFF = 3 * RING_SLOT;
constexpr int ABORT_OFF = FLAG_OFF + 384 * 128;
constexpr int MEM_OFF = BLK;
constexpr int INIT_BYTES = 2 * BLK;
constexpr int BLK_A = 2, BLK_B = 66, BLK_C = 98, BLK_END = 114;
constexpr int A_KF = 0, A_VF = sp(256 << 10), A_OF = sp(512 << 10), A_KM = sp(768 << 10), A_VM = sp(896 << 10), A_XN = sp(960 << 10);
constexpr int B_HF = 0, B_XRES = sp(288 << 10), B_XN1 = sp(448 << 10);
constexpr int C_P1 = 0;
constexpr int POL_OFF = BLK_END * BLK;
constexpr int DBG_OFF = POL_OFF + 4096;
constexpr int WS_USED = POL_OFF + 2 * BLK;
static_assert(WS_USED + BLK <= (256 << 20), "workspace");

constexpr int R_CH = 0, R_B = 32, R_D = 44, R_E = 56, R_QA = 60, R_KV = 76, R_C = 98, R_END = 127;
constexpr int F_XN = 128, F_XN1 = 136, F_END = 144;

struct Params {
  const void *x, *mem0, *wq, *wk, *wv, *wo, *wg, *wu, *wd;
  void* out;
  unsigned char* ws;
};

template <int I, int N, class F> __device__ __forceinline__ void sfor(F&& f) {
  if constexpr (I < N) { f(std::integral_constant<int, I>{}); sfor<I + 1, N>(f); }
}

struct Ctx {
  rsrc_t r;
  v4u rs;
  LAS unsigned char* lds;
  int lane, w, layer, idx;
  unsigned long long pa, pb;
  int myflag;
  int dead;
};

#define POLX 0
#define RINGN 24
__device__ __forceinline__ int base_a(const Ctx& c, int i) { i %= RINGN; return (BLK_A + i) * BLK + (int)((c.pa >> i) & 1) * 8192; }
__device__ __forceinline__ int base_b(const Ctx& c, int i) { i %= RINGN; return (BLK_B + (i >> 1)) * BLK + (i & 1) * (BLK / 2) + (int)((c.pb >> (i >> 1)) & 1) * 8192; }
__device__ __forceinline__ int base_c(const Ctx& c, int i) { i %= RINGN; return (BLK_C + (i >> 2)) * BLK + (i & 3) * (BLK / 4) + (int)((c.pb >> (32 + (i >> 2))) & 1) * 8192; }
__device__ __forceinline__ int spd(int x) { return ((x >> 13) << 14) | (x & 8191); }
__device__ __forceinline__ v4u make_srd(const void* p, unsigned bytes) {
  const unsigned long long a = (unsigned long long)p;
  return v4u{(unsigned)a, (unsigned)(a >> 32) & 0xFFFFu, bytes, 0x00020000u};
}
__device__ __forceinline__ unsigned pk(float a, float b) {
  v2bf r; r[0] = (__bf16)a; r[1] = (__bf16)b; return __builtin_bit_cast(unsigned, r);
}
__device__ __forceinline__ v2u pk4(const f32x4& a) { return v2u{pk(a[0], a[1]), pk(a[2], a[3])}; }
__device__ __forceinline__ v4u cat(v2u a, v2u b) { return v4u{a.x, a.y, b.x, b.y}; }
__device__ __forceinline__ void bar() { asm volatile("s_waitcnt lgkmcnt(0)\n\ts_barrier" ::: "memory"); }
template <int N> __device__ __forceinline__ void vmwait() { asm volatile("s_waitcnt vmcnt(%0)" :: "i"(N) : "memory"); }
__device__ __forceinline__ v4u ld16(const Ctx& c, int voff, int soff) { return __builtin_amdgcn_raw_buffer_load_b128(c.r, voff, soff, AUX_SC1); }
__device__ __forceinline__ v2u ld8(const Ctx& c, int voff, int soff) { return __builtin_amdgcn_raw_buffer_load_b64(c.r, voff, soff, AUX_SC1); }
__device__ __forceinline__ void st16(const Ctx& c, v4u v, int voff, int soff) {
  asm volatile("buffer_store_dwordx4 %0, %1, %2, %3 offen sc1\n\ts_nop 1" :: "v"(v), "v"(voff), "s"(c.rs), "s"(soff) : "memory");
}
__device__ __forceinline__ void st8(const Ctx& c, v2u v, int voff, int soff) {
  asm volatile("buffer_store_dwordx2 %0, %1, %2, %3 offen sc1" :: "v"(v), "v"(voff), "s"(c.rs), "s"(soff) : "memory");
}
__device__ __forceinline__ void st4(const Ctx& c, unsigned v, int voff, int soff) {
  asm volatile("buffer_store_dword %0, %1, %2, %3 offen sc1" :: "v"(v), "v"(voff), "s"(c.rs), "s"(soff) : "memory");
}
template <int IMM, bool SC1> __device__ __forceinline__ void dma(v4u srd, int voff, int soff, int ldsa) {
  if constexpr (SC1)
    asm volatile("s_mov_b32 m0, %3\n\ts_nop 0\n\tbuffer_load_dwordx4 %0, %1, %2 offen offset:%4 sc1 lds" :: "v"(voff), "s"(srd), "s"(soff), "s"(ldsa), "i"(IMM) : "memory");
  else
    asm volatile("s_mov_b32 m0, %3\n\ts_nop 0\n\tbuffer_load_dwordx4 %0, %1, %2 offen offset:%4 lds" :: "v"(voff), "s"(srd), "s"(soff), "s"(ldsa), "i"(IMM) : "memory");
}
__device__ __forceinline__ void set_m0(int ldsa) { asm volatile("s_mov_b32 m0, %0" :: "s"(ldsa) : "memory"); }
template <int IMM> __device__ __forceinline__ void dma_m0(const Ctx& c, int voff, int soff) {
  asm volatile("buffer_load_dwordx4 %0, %1, %2 offen offset:%3 sc1 lds" :: "v"(voff), "s"(c.rs), "s"(soff), "i"(IMM) : "memory");
}
constexpr int LDS_NOWHERE = 152 * 1024;
__device__ __forceinline__ void st4u(const Ctx& c, unsigned v, int soff) {
  asm volatile("buffer_store_dword %0, off, %1, %2 sc1" :: "v"(v), "s"(c.rs), "s"(soff) : "memory");
}
__device__ __forceinline__ void signal(const Ctx& c, int item) { st4u(c, (unsigned)item, c.myflag); }
__device__ __forceinline__ int opaque(int x) { asm volatile("" : "+v"(x)); return x; }
__device__ __forceinline__ int flag_in(const Ctx& c, int layer, int idx, int word) {
  return FLAG_OFF + spd(idx * 128) + (int)(((c.pb >> 48) ^ layer ^ c.layer) & 1) * 8192 + word * 4;
}
#define flag_at(layer, idx, word) flag_in(c, layer, idx, word)
__device__ __forceinline__ bool give_up(Ctx& c, unsigned& spins) {
  ++spins;
  if ((spins & 255u) == 0u) {
    if (spins >= (1u << 17)) st4u(c, (unsigned)opaque(1), ABORT_OFF);
    const unsigned a = __builtin_amdgcn_raw_buffer_load_b32(c.r, 0, ABORT_OFF, AUX_SC1);
    if (__builtin_amdgcn_readfirstlane(a) == 1u || spins >= (1u << 17)) { c.dead = 1; return true; }
  }
  return false;
}
__device__ __forceinline__ void wait_flags(Ctx& c, int voff, int item, int soff = 0) {
  if (c.dead) return;
  for (unsigned spins = 0;;) {
    asm volatile("" ::: "memory");
    const int f = (int)__builtin_amdgcn_raw_buffer_load_b32(c.r, voff, soff, AUX_SC1);
    if (__all(f >= item)) break;
    if (give_up(c, spins)) break;
    __builtin_amdgcn_s_sleep(1);
  }
  asm volatile("" ::: "memory");
}
__device__ __forceinline__ void flag_issue(const Ctx& c, int& f, int voff, int soff) {
  asm volatile("buffer_load_dword %0, %1, %2, %3 offen sc1" : "=v"(f) : "v"(voff), "s"(c.rs), "s"(soff) : "memory");
}
template <int N> __device__ __forceinline__ void flag_land(int& f) { asm volatile("s_waitcnt vmcnt(%1)" : "+v"(f) : "i"(N) : "memory"); }
__device__ __forceinline__ bool flag_check(int f, int item) {
  unsigned long long m;
  asm volatile("s_waitcnt vmcnt(0)\n\tv_cmp_le_i32 %0, %2, %1" : "=s"(m) : "v"(f), "s"(item) : "memory");
  return m == ~0ull;
}
__device__ __forceinline__ int fresh_lane() {
  int l;
  asm volatile("v_mbcnt_lo_u32_b32 %0, -1, 0\n\tv_mbcnt_hi_u32_b32 %0, -1, %0" : "=v"(l));
  return l;
}
__device__ __forceinline__ int sopaque(int x) { asm volatile("" : "+s"(x)); return x; }

#define STAMP(i, k)
#define STAMPC(i, k)
#define STAMP_FLUSH(n)
#define STAMPY(i, k)

template <bool AG, bool FIRST, bool WA> __device__ __forceinline__ void mmw(f32x4& c, const v4u& wgt, const v4u& x) {
  if constexpr (FIRST) {
    if constexpr (AG && WA) asm volatile("v_mfma_f32_16x16x32_bf16 %0, %1, %2, 0" : "=&v"(c) : "a"(wgt), "v"(x));
    else if constexpr (AG) asm volatile("v_mfma_f32_16x16x32_bf16 %0, %2, %1, 0" : "=&v"(c) : "a"(wgt), "v"(x));
    else if constexpr (WA) asm volatile("v_mfma_f32_16x16x32_bf16 %0, %1, %2, 0" : "=&v"(c) : "v"(wgt), "v"(x));
    else asm volatile("v_mfma_f32_16x16x32_bf16 %0, %2, %1, 0" : "=&v"(c) : "v"(wgt), "v"(x));
  } else {
    if constexpr (AG && WA) asm volatile("v_mfma_f32_16x16x32_bf16 %0, %1, %2, %0" : "+v"(c) : "a"(wgt), "v"(x));
    else if constexpr (AG) asm volatile("v_mfma_f32_16x16x32_bf16 %0, %2, %1, %0" : "+v"(c) : "a"(wgt), "v"(x));
    else if constexpr (WA) asm volatile("v_mfma_f32_16x16x32_bf16 %0, %1, %2, %0" : "+v"(c) : "v"(wgt), "v"(x));
    else asm volatile("v_mfma_f32_16x16x32_bf16 %0, %2, %1, %0" : "+v"(c) : "v"(wgt), "v"(x));
  }
}
template <bool FIRST> __device__ __forceinline__ void mm32(f32x4& c, const v4u& a, const v4u& b) {
  if constexpr (FIRST) asm volatile("s_nop 1\n\tv_mfma_f32_16x16x32_bf16 %0, %1, %2, 0" : "=&v"(c) : "v"(a), "v"(b));
  else asm volatile("s_nop 1\n\tv_mfma_f32_16x16x32_bf16 %0, %1, %2, %0" : "+v"(c) : "v"(a), "v"(b));
}
__device__ __forceinline__ void mm16(f32x4& c, const v2u& a, const v2u& b) {
  asm volatile("s_nop 1\n\tv_mfma_f32_16x16x16_bf16 %0, %1, %2, %0" : "+v"(c) : "v"(a), "v"(b));
}
__device__ __forceinline__ void mm16n(f32x4& c, const v2u& a, const v2u& b) {
  asm volatile("v_mfma_f32_16x16x16_bf16 %0, %1, %2, %0" : "+v"(c) : "v"(a), "v"(b));
}
template <bool AG> __device__ __forceinline__ void smm(f32x4& c, const v4u& a, const v8u& wgt, int idx) {
  if constexpr (AG) asm volatile("v_smfmac_f32_16x16x64_bf16 %0, %1, %2, %3" : "+v"(c) : "v"(a), "a"(wgt), "v"(idx));
  else asm volatile("v_smfmac_f32_16x16x64_bf16 %0, %1, %2, %3" : "+v"(c) : "v"(a), "v"(wgt), "v"(idx));
}
template <bool AG, class T> __device__ __forceinline__ void pinw(T& x) { if constexpr (AG) asm volatile("" : "+a"(x)); else asm volatile("" : "+v"(x)); }
__device__ __forceinline__ void mdone() { asm volatile("s_nop 7"); }
__device__ __forceinline__ void tie(f32x4& a) { asm volatile("" : "+v"(a)); }
__device__ __forceinline__ void zero_acc(f32x4& a) {
  f32x2 lo, hi;
  asm volatile("v_mov_b64 %0, 0\n\tv_mov_b64 %1, 0" : "=v"(lo), "=v"(hi));
  a = f32x4{lo.x, lo.y, hi.x, hi.y};
}
__device__ __forceinline__ float halfsum(float x, float y) {
  auto p = __builtin_amdgcn_permlane32_swap(__float_as_uint(x), __float_as_uint(y), false, false);
  return __uint_as_float(p[0]) + __uint_as_float(p[1]);
}
__device__ __forceinline__ f32x4 halfsum4(const f32x4& x, const f32x4& y) {
  return f32x4{halfsum(x[0], y[0]), halfsum(x[1], y[1]), halfsum(x[2], y[2]), halfsum(x[3], y[3])};
}
__device__ __forceinline__ float vmax(float a, float b) { float r; asm("v_max_f32 %0, %1, %2" : "=v"(r) : "v"(a), "v"(b)); return r; }
__device__ __forceinline__ float vmax3(float a, float b, float c) { float r; asm("v_max3_f32 %0, %1, %2, %3" : "=v"(r) : "v"(a), "v"(b), "v"(c)); return r; }
__device__ __forceinline__ float gmax(float x) {
  auto p = __builtin_amdgcn_permlane32_swap(__float_as_uint(x), __float_as_uint(x), false, false);
  x = vmax(__uint_as_float(p[0]), __uint_as_float(p[1]));
  auto q = __builtin_amdgcn_permlane16_swap(__float_as_uint(x), __float_as_uint(x), false, false);
  return vmax(__uint_as_float(q[0]), __uint_as_float(q[1]));
}
__device__ __forceinline__ float gsum(float x) {
  auto p = __builtin_amdgcn_permlane32_swap(__float_as_uint(x), __float_as_uint(x), false, false);
  x = __uint_as_float(p[0]) + __uint_as_float(p[1]);
  auto q = __builtin_amdgcn_permlane16_swap(__float_as_uint(x), __float_as_uint(x), false, false);
  return __uint_as_float(q[0]) + __uint_as_float(q[1]);
}
__device__ __forceinline__ v2u trread(LAS unsigned char* p) {
  return __builtin_bit_cast(v2u, __builtin_amdgcn_ds_read_tr16_b64_v4i16((LAS v4s*)p));
}
__device__ __forceinline__ v4u ldsr(LAS unsigned char* p) { return *(LAS v4u*)p; }
__device__ __forceinline__ unsigned umax4(v4u a) { return max(max(a.x, a.y), max(a.z, a.w)); }

template <int GR> __device__ __forceinline__ int sig(int row) {
  if constexpr (GR % 8 == 0) return row & 7;
  else if constexpr (GR % 4 == 0) return (row >> 1) & 3;
  else return (row >> 2) & 1;
}
template <int GR> __device__ __forceinline__ void slab_src(int piece, int lane, int& row, int& byte) {
  const int ch = piece * 64 + lane;
  row = ch / (2 * GR);
  const int cc = ch - row * (2 * GR);
  byte = (((cc >> 1) ^ sig<GR>(row)) << 5) + ((cc & 1) << 4);
}
template <int GR> __device__ __forceinline__ int slab_rd(int lane, int t) {
  const int row = lane >> 2;
  return row * (GR * 32) + ((t ^ sig<GR>(row)) << 5) + (lane & 3) * 8;
}
template <int GR> __device__ __forceinline__ v4u slab_frag(LAS unsigned char* slab, int rd) {
  return cat(trread(slab + rd), trread(slab + rd + 16 * GR * 32));
}

template <int NT, int KS, int DEPTH>
__device__ __forceinline__ void load_weights(Ctx& c, v4u (&W)[NT * KS], v4u srd, int ldb, int row0, int colb) {
  constexpr int GR = 4 * NT, SLAB = GR * 1024;
  static_assert(DEPTH * SLAB <= LDS_BYTES && (DEPTH - 1) * NT <= 63, "ring");
  int src[NT], rd[NT];
  sfor<0, NT>([&](auto j) {
    int row, byte; slab_src<GR>(c.w * NT + j, c.lane, row, byte);
    src[j] = colb + byte < ldb ? (row0 + row) * ldb + colb + byte : (int)OOB;
    rd[j] = slab_rd<GR>(c.lane, c.w * NT + j);
  });
  auto issue = [&](auto ks) {
    sfor<0, NT>([&](auto j) { dma<0, false>(srd, src[j], ks * 32 * ldb, (ks % DEPTH) * SLAB + (c.w * NT + j) * 1024); });
  };
  sfor<0, DEPTH - 1>(issue);
  sfor<0, KS>([&](auto ks) {
    constexpr int ahead = (KS - 1 - ks) < (DEPTH - 2) ? (KS - 1 - ks) : (DEPTH - 2);
    vmwait<ahead * NT>();
    bar();
    if constexpr (ks + DEPTH - 1 < KS) issue(std::integral_constant<int, ks + DEPTH - 1>{});
    sfor<0, NT>([&](auto t) {
      constexpr int id = ks * NT + t;
      W[id] = slab_frag<GR>(c.lds + (ks % DEPTH) * SLAB, rd[t]);
      pinw<(id < 64)>(W[id]);
    });
  });
  bar();
}

__device__ __forceinline__ int xn_off(int q, int g, int tok) { return q * 256 + (tok >> 1) * 128 + ((g ^ ((q ^ (q >> 1)) & 1) ^ q) << 5) + (tok & 1) * 16; }
__device__ __forceinline__ int xn_flag(const Ctx& c) { return flag_at(c.layer, F_XN + (c.lane >> 3), 0); }
__device__ __forceinline__ int part_tile(int t) {
  const int ld = t < 32 ? ((t >> 3) * 2 + (t & 1)) * 2 + ((t >> 2) & 1) : 16 + ((t >> 1) & 1);
  return spd(ld * 1024) + (t < 32 ? (t >> 1) & 1 : t & 1) * 128;
}

#define CLOB_S "s34", "s35", "s36", "s37", "s38", "s39", "s40", "s41", "s42", "s43", "s44", "s45", "s46", "s47", "s48", "s49", "s50", "s51", "s52", "s53", "s54", "s55", "s56", "s57", "s58", "s59", "s60", "s61", "s62", "s63", "s64", "s65", "s66", "s67", "s68", "s69", "s70", "s71", "s72", "s73", "s74", "s75", "s76", "s77", "s78", "s79", "s80", "s81", "s82", "s83", "s84", "s85", "s86", "s87", "vcc", "scc", "memory"
#define CLOB_ALL "v24", "v25", "v26", "v27", "v28", "v29", "v30", "v31", "v32", "v33", "v34", "v35", "v36", "v37", "v38", "v39", "v40", "v41", "v42", "v43", "v44", "v45", "v46", "v47", "v48", "v49", "v50", "v51", "v52", "v53", "v54", "v55", CLOB_HI
#define CLOB_HI "v56", "v57", "v58", "v59", "v60", "v61", "v62", "v63", "v64", "v65", "v66", "v67", "v68", "v69", "v70", "v71", "v72", "v73", "v74", "v75", "v76", "v77", "v78", "v79", "v80", "v81", "v82", "v83", "v84", "v85", "v86", "v87", "v88", "v89", "v90", "v91", "v92", "v93", "v94", "v95", "v96", "v97", "v98", "v99", "v100", "v101", "v102", "v103", "v104", "v105", "v106", "v107", "v108", "v109", "v110", "v111", "v112", "v113", "v114", "v115", "v116", "v117", "v118", "v119", "v120", "v121", "v122", "v123", "v124", "v125", "v126", "v127", "v128", "v129", "v130", "v131", "v132", "v133", "v134", "v135", "v136", "v137", "v138", "v139", "v140", "v141", "v142", "v143", "v144", "v145", "v146", "v147", "v148", "v149", "v150", "v151", "v152", "v153", "v154", "v155", "v156", "v157", "v158", "v159", "v160", "v161", "v162", "v163", "v164", "v165", "v166", "v167", "v168", "v169", "v170", "v171", "v172", "v173", "v174", "v175", "v176", "v177", "v178", "v179", "v180", "v181", "v182", "v183", "v184", "v185", "v186", "v187", "v188", "v189", "v190", "v191", "v192", "v193", "v194", "v195", "v196", "v197", "v198", "v199", "v200", "v201", "v202", "v203", "v204", "v205", "v206", "v207", "v208", "v209", "v210", "v211", "v212", "v213", "v214", "v215", "v216", "v217", "v218", "v219", "v220", "v221", "v222", "v223", "v224", "v225", "v226", "v227", "v228", "v229", "v230", "v231", "v232", "v233", "v234", "v235", "v236", "v237", "v238", "v239", "v240", "v241", "v242", "v243", "v244", "v245", "v246", "v247", "v248", "v249", "v250", "v251", "v252", "v253", "v254", "v255", "a0", "a1", "a2", "a3", "a4", "a5", "a6", "a7", "a8", "a9", "a10", "a11", "a12", "a13", "a14", "a15", "a16", "a17", "a18", "a19", "a20", "a21", "a22", "a23", "a24", "a25", "a26", "a27", "a28", "a29", "a30", "a31", "a32", "a33", "a34", "a35", "a36", "a37", "a38", "a39", "a40", "a41", "a42", "a43", "a44", "a45", "a46", "a47", "a48", "a49", "a50", "a51", "a52", "a53", "a54", "a55", "a56", "a57", "a58", "a59", "a60", "a61", "a62", "a63", "a64", "a65", "a66", "a67", "a68", "a69", "a70", "a71", "a72", "a73", "a74", "a75", "a76", "a77", "a78", "a79", "a80", "a81", "a82", "a83", "a84", "a85", "a86", "a87", "a88", "a89", "a90", "a91", "a92", "a93", "a94", "a95", "a96", "a97", "a98", "a99", "a100", "a101", "a102", "a103", "a104", "a105", "a106", "a107", "a108", "a109", "a110", "a111", "a112", "a113", "a114", "a115", "a116", "a117", "a118", "a119", "a120", "a121", "a122", "a123", "a124", "a125", "a126", "a127", "a128", "a129", "a130", "a131", "a132", "a133", "a134", "a135", "a136", "a137", "a138", "a139", "a140", "a141", "a142", "a143", "a144", "a145", "a146", "a147", "a148", "a149", "a150", "a151", "a152", "a153", "a154", "a155", "a156", "a157", "a158", "a159", "a160", "a161", "a162", "a163", "a164", "a165", "a166", "a167", "a168", "a169", "a170", "a171", "a172", "a173", "a174", "a175", "a176", "a177", "a178", "a179", "a180", "a181", "a182", "a183", "a184", "a185", "a186", "a187", "a188", "a189", "a190", "a191", "a192", "a193", "a194", "a195", "a196", "a197", "a198", "a199", "a200", "a201", "a202", "a203", "a204", "a205", "a206", "a207", "a208", "a209", "a210", "a211", "a212", "a213", "a214", "a215", "a216", "a217", "a218", "a219", "a220", "a221", "a222", "a223", "a224", "a225", "a226", "a227", "a228", "a229", "a230", "a231", "a232", "a233", "a234", "a235", "a236", "a237", "a238", "a239", "a240", "a241", "a242", "a243", "a244", "a245", "a246", "a247", "a248", "a249", "a250", "a251", "a252", "a253", "a254", "a255", CLOB_S
template <int K> __device__ __forceinline__ void put_lane(unsigned& r, int x) { asm("v_writelane_b32 %0, %1, %2" : "+v"(r) : "s"(x), "i"(K)); }
template <int N> __device__ __forceinline__ unsigned lane_table(int, const int (&x)[N]) {
  unsigned r = 0;
  int y[N];
  sfor<0, N>([&](auto k) { y[k] = sopaque(__builtin_amdgcn_readfirstlane(x[k])); });
  asm volatile("s_nop 1" : "+v"(r));
  sfor<0, N>([&](auto k) { put_lane<k>(r, y[k]); });
  return r;
}
__device__ __forceinline__ unsigned stage_scalars(const Ctx& c, const void* wgt, int wbytes, int ldb, int fm, int fsh, int o0, int o1, int o2, int o3, int o4, int o5) {
  const v4u ws = make_srd(wgt, wbytes);
  const int x[20] = {(int)c.rs.x, (int)c.rs.y, (int)c.rs.z, (int)c.rs.w, c.myflag, c.w * 2048, fm, fsh, o0, o1, o2, o3, o4, o5,
                     DBG_OFF + (((c.layer * 128 + c.idx) * 4 + c.w) * 5) * 256, WS_USED, (int)ws.x, (int)ws.y, (int)ws.z, 32 * ldb};
  return lane_table(c.lane, x);
}
template <int NT> __device__ __forceinline__ v16u stage_offsets(const Ctx& c, int ldb, int row0, int colb) {
  v16u r = {};
  #pragma unroll
  for (int j = 0; j < NT; ++j) {
    int row, byte; slab_src<4 * NT>(c.w * NT + j, c.lane, row, byte);
    r[j] = colb + byte < ldb ? (row0 + row) * ldb + colb + byte : OOB;
    r[6 + j] = slab_rd<4 * NT>(c.lane, c.w * NT + j);
  }
  return r;
}

template <bool isv> __device__ __forceinline__ void role_kv(Ctx& c, const Params& p) {
  const int jj = c.idx - R_KV - (isv ? 11 : 0), wv = jj * 4 + c.w;
  v16u so = stage_offsets<6>(c, HD * 2, 0, jj * 768);
  const int rel = A_XN + c.w * 2048, junk = sp(1008 << 10) - rel;
  v8u lc = {(unsigned)(c.lane * 16), (unsigned)xn_off((c.lane >> 2) & 3, c.lane >> 4, c.lane & 3), (unsigned)xn_flag(c), 0u, (unsigned)(base_a(c, c.lane) + rel), 0u, 0u, 0u};
  auto uo = [&](int e) { const int U = wv * 3 + e; return U < 128 ? A_KF + (U >> 2) * 16384 + (U & 3) * 1024 - rel : junk; };
  auto to = [&](int t) { const int T = wv * 6 + t; return T < 256 ? A_VF + spd(T * 1024) - rel : junk; };
  if constexpr (!isv) {
    lc[3] = stage_scalars(c, p.wk, DM * HD * 2, HD * 2, 0, 0, uo(0), uo(1), uo(2), 0, 0, 0);
    asm volatile(
  "s_nop 4\n\t"
  "v_readlane_b32 s36, v3, 0\n\t"
  "v_readlane_b32 s37, v3, 1\n\t"
  "v_readlane_b32 s38, v3, 2\n\t"
  "v_readlane_b32 s39, v3, 3\n\t"
  "v_readlane_b32 s40, v3, 4\n\t"
  "v_readlane_b32 s41, v3, 5\n\t"
  "v_readlane_b32 s42, v3, 6\n\t"
  "v_readlane_b32 s43, v3, 7\n\t"
  "v_readlane_b32 s44, v3, 8\n\t"
  "v_readlane_b32 s45, v3, 9\n\t"
  "v_readlane_b32 s46, v3, 10\n\t"
  "v_readlane_b32 s47, v3, 11\n\t"
  "v_readlane_b32 s48, v3, 12\n\t"
  "v_readlane_b32 s49, v3, 13\n\t"
  "v_readlane_b32 s50, v3, 14\n\t"
  "v_readlane_b32 s51, v3, 15\n\t"
  "s_nop 4\n\t"
  "v_readlane_b32 s56, v3, 16\n\t"
  "v_readlane_b32 s57, v3, 17\n\t"
  "v_readlane_b32 s58, v3, 18\n\t"
  "v_readlane_b32 s60, v3, 19\n\t"
  "s_mov_b32 s59, s39\n\t"
  "s_mul_i32 s61, s41, 6\n\t"
  "s_lshr_b32 s61, s61, 1\n\t"
  "s_mov_b32 s62, 0\n\t"
  "s_nop 2\n\t"
  "s_add_u32 m0, s61, 0\n\t"
  "s_nop 0\n\t"
  "buffer_load_dwordx4 v8, s[56:59], s62 offen lds\n\t"
  "s_add_u32 m0, s61, 1024\n\t"
  "s_nop 0\n\t"
  "buffer_load_dwordx4 v9, s[56:59], s62 offen lds\n\t"
  "s_add_u32 m0, s61, 2048\n\t"
  "s_nop 0\n\t"
  "buffer_load_dwordx4 v10, s[56:59], s62 offen lds\n\t"
  "s_add_u32 m0, s61, 3072\n\t"
  "s_nop 0\n\t"
  "buffer_load_dwordx4 v11, s[56:59], s62 offen lds\n\t"
  "s_add_u32 m0, s61, 4096\n\t"
  "s_nop 0\n\t"
  "buffer_load_dwordx4 v12, s[56:59], s62 offen lds\n\t"
  "s_add_u32 m0, s61, 5120\n\t"
  "s_nop 0\n\t"
  "buffer_load_dwordx4 v13, s[56:59], s62 offen lds\n\t"
  "s_add_u32 s62, s62, s60\n\t"
  "s_add_u32 m0, s61, 24576\n\t"
  "s_nop 0\n\t"
  "buffer_load_dwordx4 v8, s[56:59], s62 offen lds\n\t"
  "s_add_u32 m0, s61, 25600\n\t"
  "s_nop 0\n\t"
  "buffer_load_dwordx4 v9, s[56:59], s62 offen lds\n\t"
  "s_add_u32 m0, s61, 26624\n\t"
  "s_nop 0\n\t"
  "buffer_load_dwordx4 v10, s[56:59], s62 offen lds\n\t"
  "s_add_u32 m0, s61, 27648\n\t"
  "s_nop 0\n\t"
  "buffer_load_dwordx4 v11, s[56:59], s62 offen lds\n\t"
  "s_add_u32 m0, s61, 28672\n\t"
  "s_nop 0\n\t"
  "buffer_load_dwordx4 v12, s[56:59], s62 offen lds\n\t"
  "s_add_u32 m0, s61, 29696\n\t"
  "s_nop 0\n\t"
  "buffer_load_dwordx4 v13, s[56:59], s62 offen lds\n\t"
  "s_add_u32 s62, s62, s60\n\t"
  "s_add_u32 m0, s61, 49152\n\t"
  "s_nop 0\n\t"
  "buffer_load_dwordx4 v8, s[56:59], s62 offen lds\n\t"
  "s_add_u32 m0, s61, 50176\n\t"
  "s_nop 0\n\t"
  "buffer_load_dwordx4 v9, s[56:59], s62 offen lds\n\t"
  "s_add_u32 m0, s61, 51200\n\t"
  "s_nop 0\n\t"
  "buffer_load_dwordx4 v10, s[56:59], s62 offen lds\n\t"
  "s_add_u32 m0, s61, 52224\n\t"
  "s_nop 0\n\t"
  "buffer_load_dwordx4 v11, s[56:59], s62 offen lds\n\t"
  "s_add_u32 m0, s61, 53248\n\t"
  "s_nop 0\n\t"
  "buffer_load_dwordx4 v12, s[56:59], s62 offen lds\n\t"
  "s_add_u32 m0, s61, 54272\n\t"
  "s_nop 0\n\t"
  "buffer_load_dwordx4 v13, s[56:59], s62 offen lds\n\t"
  "s_add_u32 s62, s62, s60\n\t"
  "s_add_u32 m0, s61, 73728\n\t"
  "s_nop 0\n\t"
  "buffer_load_dwordx4 v8, s[56:59], s62 offen lds\n\t"
  "s_add_u32 m0, s61, 74752\n\t"
  "s_nop 0\n\t"
  "buffer_load_dwordx4 v9, s[56:59], s62 offen lds\n\t"
  "s_add_u32 m0, s61, 75776\n\t"
  "s_nop 0\n\t"
  "buffer_load_dwordx4 v10, s[56:59], s62 offen lds\n\t"
  "s_add_u32 m0, s61, 76800\n\t"
  "s_nop 0\n\t"
  "buffer_load_dwordx4 v11, s[56:59], s62 offen lds\n\t"
  "s_add_u32 m0, s61, 77824\n\t"
  "s_nop 0\n\t"
  "buffer_load_dwordx4 v12, s[56:59], s62 offen lds\n\t"
  "s_add_u32 m0, s61, 78848\n\t"
  "s_nop 0\n\t"
  "buffer_load_dwordx4 v13, s[56:59], s62 offen lds\n\t"
  "s_add_u32 s62, s62, s60\n\t"
  "s_add_u32 m0, s61, 98304\n\t"
  "s_nop 0\n\t"
  "buffer_load_dwordx4 v8, s[56:59], s62 offen lds\n\t"
  "s_add_u32 m0, s61, 99328\n\t"
  "s_nop 0\n\t"
  "buffer_load_dwordx4 v9, s[56:59], s62 offen lds\n\t"
  "s_add_u32 m0, s61, 100352\n\t"
  "s_nop 0\n\t"
  "buffer_load_dwordx4 v10, s[56:59], s62 offen lds\n\t"
  "s_add_u32 m0, s61, 101376\n\t"
  "s_nop 0\n\t"
  "buffer_load_dwordx4 v11, s[56:59], s62 offen lds\n\t"
  "s_add_u32 m0, s61, 102400\n\t"
  "s_nop 0\n\t"
  "buffer_load_dwordx4 v12, s[56:59], s62 offen lds\n\t"
  "s_add_u32 m0, s61, 103424\n\t"
  "s_nop 0\n\t"
  "buffer_load_dwordx4 v13, s[56:59], s62 offen lds\n\t"
  "s_add_u32 s62, s62, s60\n\t"
  "s_waitcnt vmcnt(24) lgkmcnt(0)\n\t"
  "s_barrier\n\t"
  "s_add_u32 m0, s61, 122880\n\t"
  "s_nop 0\n\t"
  "buffer_load_dwordx4 v8, s[56:59], s62 offen lds\n\t"
  "s_add_u32 m0, s61, 123904\n\t"
  "s_nop 0\n\t"
  "buffer_load_dwordx4 v9, s[56:59], s62 offen lds\n\t"
  "s_add_u32 m0, s61, 124928\n\t"
  "s_nop 0\n\t"
  "buffer_load_dwordx4 v10, s[56:59], s62 offen lds\n\t"
  "s_add_u32 m0, s61, 125952\n\t"
  "s_nop 0\n\t"
  "buffer_load_dwordx4 v11, s[56:59], s62 offen lds\n\t"
  "s_add_u32 m0, s61, 126976\n\t"
  "s_nop 0\n\t"
  "buffer_load_dwordx4 v12, s[56:59], s62 offen lds\n\t"
  "s_add_u32 m0, s61, 128000\n\t"
  "s_nop 0\n\t"
  "buffer_load_dwordx4 v13, s[56:59], s62 offen lds\n\t"
  "s_add_u32 s62, s62, s60\n\t"
  "v_add_u32 v20, 0, v14\n\t"
  "ds_read_b64_tr_b16 a[0:1], v20\n\t"
  "ds_read_b64_tr_b16 a[2:3], v20 offset:12288\n\t"
  "v_add_u32 v20, 0, v15\n\t"
  "ds_read_b64_tr_b16 a[4:5], v20\n\t"
  "ds_read_b64_tr_b16 a[6:7], v20 offset:12288\n\t"
  "v_add_u32 v20, 0, v16\n\t"
  "ds_read_b64_tr_b16 a[8:9], v20\n\t"
  "ds_read_b64_tr_b16 a[10:11], v20 offset:12288\n\t"
  "v_add_u32 v20, 0, v17\n\t"
  "ds_read_b64_tr_b16 a[12:13], v20\n\t"
  "ds_read_b64_tr_b16 a[14:15], v20 offset:12288\n\t"
  "v_add_u32 v20, 0, v18\n\t"
  "ds_read_b64_tr_b16 a[16:17], v20\n\t"
  "ds_read_b64_tr_b16 a[18:19], v20 offset:12288\n\t"
  "v_add_u32 v20, 0, v19\n\t"
  "ds_read_b64_tr_b16 a[20:21], v20\n\t"
  "ds_read_b64_tr_b16 a[22:23], v20 offset:12288\n\t"
  "s_waitcnt vmcnt(24) lgkmcnt(0)\n\t"
  "s_barrier\n\t"
  "s_add_u32 m0, s61, 0\n\t"
  "s_nop 0\n\t"
  "buffer_load_dwordx4 v8, s[56:59], s62 offen lds\n\t"
  "s_add_u32 m0, s61, 1024\n\t"
  "s_nop 0\n\t"
  "buffer_load_dwordx4 v9, s[56:59], s62 offen lds\n\t"
  "s_add_u32 m0, s61, 2048\n\t"
  "s_nop 0\n\t"
  "buffer_load_dwordx4 v10, s[56:59], s62 offen lds\n\t"
  "s_add_u32 m0, s61, 3072\n\t"
  "s_nop 0\n\t"
  "buffer_load_dwordx4 v11, s[56:59], s62 offen lds\n\t"
  "s_add_u32 m0, s61, 4096\n\t"
  "s_nop 0\n\t"
  "buffer_load_dwordx4 v12, s[56:59], s62 offen lds\n\t"
  "s_add_u32 m0, s61, 5120\n\t"
  "s_nop 0\n\t"
  "buffer_load_dwordx4 v13, s[56:59], s62 offen lds\n\t"
  "s_add_u32 s62, s62, s60\n\t"
  "v_add_u32 v20, 24576, v14\n\t"
  "ds_read_b64_tr_b16 a[24:25], v20\n\t"
  "ds_read_b64_tr_b16 a[26:27], v20 offset:12288\n\t"
  "v_add_u32 v20, 24576, v15\n\t"
  "ds_read_b64_tr_b16 a[28:29], v20\n\t"
  "ds_read_b64_tr_b16 a[30:31], v20 offset:12288\n\t"
  "v_add_u32 v20, 24576, v16\n\t"
  "ds_read_b64_tr_b16 a[32:33], v20\n\t"
  "ds_read_b64_tr_b16 a[34:35], v20 offset:12288\n\t"
  "v_add_u32 v20, 24576, v17\n\t"
  "ds_read_b64_tr_b16 a[36:37], v20\n\t"
  "ds_read_b64_tr_b16 a[38:39], v20 offset:12288\n\t"
  "v_add_u32 v20, 24576, v18\n\t"
  "ds_read_b64_tr_b16 a[40:41], v20\n\t"
  "ds_read_b64_tr_b16 a[42:43], v20 offset:12288\n\t"
  "v_add_u32 v20, 24576, v19\n\t"
  "ds_read_b64_tr_b16 a[44:45], v20\n\t"
  "ds_read_b64_tr_b16 a[46:47], v20 offset:12288\n\t"
  "s_waitcnt vmcnt(24) lgkmcnt(0)\n\t"
  "s_barrier\n\t"
  "s_add_u32 m0, s61, 24576\n\t"
  "s_nop 0\n\t"
  "buffer_load_dwordx4 v8, s[56:59], s62 offen lds\n\t"
  "s_add_u32 m0, s61, 25600\n\t"
  "s_nop 0\n\t"
  "buffer_load_dwordx4 v9, s[56:59], s62 offen lds\n\t"
  "s_add_u32 m0, s61, 26624\n\t"
  "s_nop 0\n\t"
  "buffer_load_dwordx4 v10, s[56:59], s62 offen lds\n\t"
  "s_add_u32 m0, s61, 27648\n\t"
  "s_nop 0\n\t"
  "buffer_load_dwordx4 v11, s[56:59], s62 offen lds\n\t"
  "s_add_u32 m0, s61, 28672\n\t"
  "s_nop 0\n\t"
  "buffer_load_dwordx4 v12, s[56:59], s62 offen lds\n\t"
  "s_add_u32 m0, s61, 29696\n\t"
  "s_nop 0\n\t"
  "buffer_load_dwordx4 v13, s[56:59], s62 offen lds\n\t"
  "s_add_u32 s62, s62, s60\n\t"
  "v_add_u32 v20, 49152, v14\n\t"
  "ds_read_b64_tr_b16 a[48:49], v20\n\t"
  "ds_read_b64_tr_b16 a[50:51], v20 offset:12288\n\t"
  "v_add_u32 v20, 49152, v15\n\t"
  "ds_read_b64_tr_b16 a[52:53], v20\n\t"
  "ds_read_b64_tr_b16 a[54:55], v20 offset:12288\n\t"
  "v_add_u32 v20, 49152, v16\n\t"
  "ds_read_b64_tr_b16 a[56:57], v20\n\t"
  "ds_read_b64_tr_b16 a[58:59], v20 offset:12288\n\t"
  "v_add_u32 v20, 49152, v17\n\t"
  "ds_read_b64_tr_b16 a[60:61], v20\n\t"
  "ds_read_b64_tr_b16 a[62:63], v20 offset:12288\n\t"
  "v_add_u32 v20, 49152, v18\n\t"
  "ds_read_b64_tr_b16 a[64:65], v20\n\t"
  "ds_read_b64_tr_b16 a[66:67], v20 offset:12288\n\t"
  "v_add_u32 v20, 49152, v19\n\t"
  "ds_read_b64_tr_b16 a[68:69], v20\n\t"
  "ds_read_b64_tr_b16 a[70:71], v20 offset:12288\n\t"
  "s_waitcnt vmcnt(24) lgkmcnt(0)\n\t"
  "s_barrier\n\t"
  "s_add_u32 m0, s61, 49152\n\t"
  "s_nop 0\n\t"
  "buffer_load_dwordx4 v8, s[56:59], s62 offen lds\n\t"
  "s_add_u32 m0, s61, 50176\n\t"
  "s_nop 0\n\t"
  "buffer_load_dwordx4 v9, s[56:59], s62 offen lds\n\t"
  "s_add_u32 m0, s61, 51200\n\t"
  "s_nop 0\n\t"
  "buffer_load_dwordx4 v10, s[56:59], s62 offen lds\n\t"
  "s_add_u32 m0, s61, 52224\n\t"
  "s_nop 0\n\t"
  "buffer_load_dwordx4 v11, s[56:59], s62 offen lds\n\t"
  "s_add_u32 m0, s61, 53248\n\t"
  "s_nop 0\n\t"
  "buffer_load_dwordx4 v12, s[56:59], s62 offen lds\n\t"
  "s_add_u32 m0, s61, 54272\n\t"
  "s_nop 0\n\t"
  "buffer_load_dwordx4 v13, s[56:59], s62 offen lds\n\t"
  "s_add_u32 s62, s62, s60\n\t"
  "v_add_u32 v20, 73728, v14\n\t"
  "ds_read_b64_tr_b16 a[72:73], v20\n\t"
  "ds_read_b64_tr_b16 a[74:75], v20 offset:12288\n\t"
  "v_add_u32 v20, 73728, v15\n\t"
  "ds_read_b64_tr_b16 a[76:77], v20\n\t"
  "ds_read_b64_tr_b16 a[78:79], v20 offset:12288\n\t"
  "v_add_u32 v20, 73728, v16\n\t"
  "ds_read_b64_tr_b16 a[80:81], v20\n\t"
  "ds_read_b64_tr_b16 a[82:83], v20 offset:12288\n\t"
  "v_add_u32 v20, 73728, v17\n\t"
  "ds_read_b64_tr_b16 a[84:85], v20\n\t"
  "ds_read_b64_tr_b16 a[86:87], v20 offset:12288\n\t"
  "v_add_u32 v20, 73728, v18\n\t"
  "ds_read_b64_tr_b16 a[88:89], v20\n\t"
  "ds_read_b64_tr_b16 a[90:91], v20 offset:12288\n\t"
  "v_add_u32 v20, 73728, v19\n\t"
  "ds_read_b64_tr_b16 a[92:93], v20\n\t"
  "ds_read_b64_tr_b16 a[94:95], v20 offset:12288\n\t"
  "s_waitcnt vmcnt(24) lgkmcnt(0)\n\t"
  "s_barrier\n\t"
  "s_add_u32 m0, s61, 73728\n\t"
  "s_nop 0\n\t"
  "buffer_load_dwordx4 v8, s[56:59], s62 offen lds\n\t"
  "s_add_u32 m0, s61, 74752\n\t"
  "s_nop 0\n\t"
  "buffer_load_dwordx4 v9, s[56:59], s62 offen lds\n\t"
  "s_add_u32 m0, s61, 75776\n\t"
  "s_nop 0\n\t"
  "buffer_load_dwordx4 v10, s[56:59], s62 offen lds\n\t"
  "s_add_u32 m0, s61, 76800\n\t"
  "s_nop 0\n\t"
  "buffer_load_dwordx4 v11, s[56:59], s62 offen lds\n\t"
  "s_add_u32 m0, s61, 77824\n\t"
  "s_nop 0\n\t"
  "buffer_load_dwordx4 v12, s[56:59], s62 offen lds\n\t"
  "s_add_u32 m0, s61, 78848\n\t"
  "s_nop 0\n\t"
  "buffer_load_dwordx4 v13, s[56:59], s62 offen lds\n\t"
  "s_add_u32 s62, s62, s60\n\t"
  "v_add_u32 v20, 98304, v14\n\t"
  "ds_read_b64_tr_b16 a[96:97], v20\n\t"
  "ds_read_b64_tr_b16 a[98:99], v20 offset:12288\n\t"
  "v_add_u32 v20, 98304, v15\n\t"
  "ds_read_b64_tr_b16 a[100:101], v20\n\t"
  "ds_read_b64_tr_b16 a[102:103], v20 offset:12288\n\t"
  "v_add_u32 v20, 98304, v16\n\t"
  "ds_read_b64_tr_b16 a[104:105], v20\n\t"
  "ds_read_b64_tr_b16 a[106:107], v20 offset:12288\n\t"
  "v_add_u32 v20, 98304, v17\n\t"
  "ds_read_b64_tr_b16 a[108:109], v20\n\t"
  "ds_read_b64_tr_b16 a[110:111], v20 offset:12288\n\t"
  "v_add_u32 v20, 98304, v18\n\t"
  "ds_read_b64_tr_b16 a[112:113], v20\n\t"
  "ds_read_b64_tr_b16 a[114:115], v20 offset:12288\n\t"
  "v_add_u32 v20, 98304, v19\n\t"
  "ds_read_b64_tr_b16 a[116:117], v20\n\t"
  "ds_read_b64_tr_b16 a[118:119], v20 offset:12288\n\t"
  "s_waitcnt vmcnt(24) lgkmcnt(0)\n\t"
  "s_barrier\n\t"
  "s_add_u32 m0, s61, 98304\n\t"
  "s_nop 0\n\t"
  "buffer_load_dwordx4 v8, s[56:59], s62 offen lds\n\t"
  "s_add_u32 m0, s61, 99328\n\t"
  "s_nop 0\n\t"
  "buffer_load_dwordx4 v9, s[56:59], s62 offen lds\n\t"
  "s_add_u32 m0, s61, 100352\n\t"
  "s_nop 0\n\t"
  "buffer_load_dwordx4 v10, s[56:59], s62 offen lds\n\t"
  "s_add_u32 m0, s61, 101376\n\t"
  "s_nop 0\n\t"
  "buffer_load_dwordx4 v11, s[56:59], s62 offen lds\n\t"
  "s_add_u32 m0, s61, 102400\n\t"
  "s_nop 0\n\t"
  "buffer_load_dwordx4 v12, s[56:59], s62 offen lds\n\t"
  "s_add_u32 m0, s61, 103424\n\t"
  "s_nop 0\n\t"
  "buffer_load_dwordx4 v13, s[56:59], s62 offen lds\n\t"
  "s_add_u32 s62, s62, s60\n\t"
  "v_add_u32 v20, 122880, v14\n\t"
  "ds_read_b64_tr_b16 a[120:121], v20\n\t"
  "ds_read_b64_tr_b16 a[122:123], v20 offset:12288\n\t"
  "v_add_u32 v20, 122880, v15\n\t"
  "ds_read_b64_tr_b16 a[124:125], v20\n\t"
  "ds_read_b64_tr_b16 a[126:127], v20 offset:12288\n\t"
  "v_add_u32 v20, 122880, v16\n\t"
  "ds_read_b64_tr_b16 a[128:129], v20\n\t"
  "ds_read_b64_tr_b16 a[130:131], v20 offset:12288\n\t"
  "v_add_u32 v20, 122880, v17\n\t"
  "ds_read_b64_tr_b16 a[132:133], v20\n\t"
  "ds_read_b64_tr_b16 a[134:135], v20 offset:12288\n\t"
  "v_add_u32 v20, 122880, v18\n\t"
  "ds_read_b64_tr_b16 a[136:137], v20\n\t"
  "ds_read_b64_tr_b16 a[138:139], v20 offset:12288\n\t"
  "v_add_u32 v20, 122880, v19\n\t"
  "ds_read_b64_tr_b16 a[140:141], v20\n\t"
  "ds_read_b64_tr_b16 a[142:143], v20 offset:12288\n\t"
  "s_waitcnt vmcnt(24) lgkmcnt(0)\n\t"
  "s_barrier\n\t"
  "s_add_u32 m0, s61, 122880\n\t"
  "s_nop 0\n\t"
  "buffer_load_dwordx4 v8, s[56:59], s62 offen lds\n\t"
  "s_add_u32 m0, s61, 123904\n\t"
  "s_nop 0\n\t"
  "buffer_load_dwordx4 v9, s[56:59], s62 offen lds\n\t"
  "s_add_u32 m0, s61, 124928\n\t"
  "s_nop 0\n\t"
  "buffer_load_dwordx4 v10, s[56:59], s62 offen lds\n\t"
  "s_add_u32 m0, s61, 125952\n\t"
  "s_nop 0\n\t"
  "buffer_load_dwordx4 v11, s[56:59], s62 offen lds\n\t"
  "s_add_u32 m0, s61, 126976\n\t"
  "s_nop 0\n\t"
  "buffer_load_dwordx4 v12, s[56:59], s62 offen lds\n\t"
  "s_add_u32 m0, s61, 128000\n\t"
  "s_nop 0\n\t"
  "buffer_load_dwordx4 v13, s[56:59], s62 offen lds\n\t"
  "s_add_u32 s62, s62, s60\n\t"
  "v_add_u32 v20, 0, v14\n\t"
  "ds_read_b64_tr_b16 a[144:145], v20\n\t"
  "ds_read_b64_tr_b16 a[146:147], v20 offset:12288\n\t"
  "v_add_u32 v20, 0, v15\n\t"
  "ds_read_b64_tr_b16 a[148:149], v20\n\t"
  "ds_read_b64_tr_b16 a[150:151], v20 offset:12288\n\t"
  "v_add_u32 v20, 0, v16\n\t"
  "ds_read_b64_tr_b16 a[152:153], v20\n\t"
  "ds_read_b64_tr_b16 a[154:155], v20 offset:12288\n\t"
  "v_add_u32 v20, 0, v17\n\t"
  "ds_read_b64_tr_b16 a[156:157], v20\n\t"
  "ds_read_b64_tr_b16 a[158:159], v20 offset:12288\n\t"
  "v_add_u32 v20, 0, v18\n\t"
  "ds_read_b64_tr_b16 a[160:161], v20\n\t"
  "ds_read_b64_tr_b16 a[162:163], v20 offset:12288\n\t"
  "v_add_u32 v20, 0, v19\n\t"
  "ds_read_b64_tr_b16 a[164:165], v20\n\t"
  "ds_read_b64_tr_b16 a[166:167], v20 offset:12288\n\t"
  "s_waitcnt vmcnt(24) lgkmcnt(0)\n\t"
  "s_barrier\n\t"
  "s_add_u32 m0, s61, 0\n\t"
  "s_nop 0\n\t"
  "buffer_load_dwordx4 v8, s[56:59], s62 offen lds\n\t"
  "s_add_u32 m0, s61, 1024\n\t"
  "s_nop 0\n\t"
  "buffer_load_dwordx4 v9, s[56:59], s62 offen lds\n\t"
  "s_add_u32 m0, s61, 2048\n\t"
  "s_nop 0\n\t"
  "buffer_load_dwordx4 v10, s[56:59], s62 offen lds\n\t"
  "s_add_u32 m0, s61, 3072\n\t"
  "s_nop 0\n\t"
  "buffer_load_dwordx4 v11, s[56:59], s62 offen lds\n\t"
  "s_add_u32 m0, s61, 4096\n\t"
  "s_nop 0\n\t"
  "buffer_load_dwordx4 v12, s[56:59], s62 offen lds\n\t"
  "s_add_u32 m0, s61, 5120\n\t"
  "s_nop 0\n\t"
  "buffer_load_dwordx4 v13, s[56:59], s62 offen lds\n\t"
  "s_add_u32 s62, s62, s60\n\t"
  "v_add_u32 v20, 24576, v14\n\t"
  "ds_read_b64_tr_b16 a[168:169], v20\n\t"
  "ds_read_b64_tr_b16 a[170:171], v20 offset:12288\n\t"
  "v_add_u32 v20, 24576, v15\n\t"
  "ds_read_b64_tr_b16 a[172:173], v20\n\t"
  "ds_read_b64_tr_b16 a[174:175], v20 offset:12288\n\t"
  "v_add_u32 v20, 24576, v16\n\t"
  "ds_read_b64_tr_b16 a[176:177], v20\n\t"
  "ds_read_b64_tr_b16 a[178:179], v20 offset:12288\n\t"
  "v_add_u32 v20, 24576, v17\n\t"
  "ds_read_b64_tr_b16 a[180:181], v20\n\t"
  "ds_read_b64_tr_b16 a[182:183], v20 offset:12288\n\t"
  "v_add_u32 v20, 24576, v18\n\t"
  "ds_read_b64_tr_b16 a[184:185], v20\n\t"
  "ds_read_b64_tr_b16 a[186:187], v20 offset:12288\n\t"
  "v_add_u32 v20, 24576, v19\n\t"
  "ds_read_b64_tr_b16 a[188:189], v20\n\t"
  "ds_read_b64_tr_b16 a[190:191], v20 offset:12288\n\t"
  "s_waitcnt vmcnt(24) lgkmcnt(0)\n\t"
  "s_barrier\n\t"
  "s_add_u32 m0, s61, 24576\n\t"
  "s_nop 0\n\t"
  "buffer_load_dwordx4 v8, s[56:59], s62 offen lds\n\t"
  "s_add_u32 m0, s61, 25600\n\t"
  "s_nop 0\n\t"
  "buffer_load_dwordx4 v9, s[56:59], s62 offen lds\n\t"
  "s_add_u32 m0, s61, 26624\n\t"
  "s_nop 0\n\t"
  "buffer_load_dwordx4 v10, s[56:59], s62 offen lds\n\t"
  "s_add_u32 m0, s61, 27648\n\t"
  "s_nop 0\n\t"
  "buffer_load_dwordx4 v11, s[56:59], s62 offen lds\n\t"
  "s_add_u32 m0, s61, 28672\n\t"
  "s_nop 0\n\t"
  "buffer_load_dwordx4 v12, s[56:59], s62 offen lds\n\t"
  "s_add_u32 m0, s61, 29696\n\t"
  "s_nop 0\n\t"
  "buffer_load_dwordx4 v13, s[56:59], s62 offen lds\n\t"
  "s_add_u32 s62, s62, s60\n\t"
  "v_add_u32 v20, 49152, v14\n\t"
  "ds_read_b64_tr_b16 a[192:193], v20\n\t"
  "ds_read_b64_tr_b16 a[194:195], v20 offset:12288\n\t"
  "v_add_u32 v20, 49152, v15\n\t"
  "ds_read_b64_tr_b16 a[196:197], v20\n\t"
  "ds_read_b64_tr_b16 a[198:199], v20 offset:12288\n\t"
  "v_add_u32 v20, 49152, v16\n\t"
  "ds_read_b64_tr_b16 a[200:201], v20\n\t"
  "ds_read_b64_tr_b16 a[202:203], v20 offset:12288\n\t"
  "v_add_u32 v20, 49152, v17\n\t"
  "ds_read_b64_tr_b16 a[204:205], v20\n\t"
  "ds_read_b64_tr_b16 a[206:207], v20 offset:12288\n\t"
  "v_add_u32 v20, 49152, v18\n\t"
  "ds_read_b64_tr_b16 a[208:209], v20\n\t"
  "ds_read_b64_tr_b16 a[210:211], v20 offset:12288\n\t"
  "v_add_u32 v20, 49152, v19\n\t"
  "ds_read_b64_tr_b16 a[212:213], v20\n\t"
  "ds_read_b64_tr_b16 a[214:215], v20 offset:12288\n\t"
  "s_waitcnt vmcnt(24) lgkmcnt(0)\n\t"
  "s_barrier\n\t"
  "s_add_u32 m0, s61, 49152\n\t"
  "s_nop 0\n\t"
  "buffer_load_dwordx4 v8, s[56:59], s62 offen lds\n\t"
  "s_add_u32 m0, s61, 50176\n\t"
  "s_nop 0\n\t"
  "buffer_load_dwordx4 v9, s[56:59], s62 offen lds\n\t"
  "s_add_u32 m0, s61, 51200\n\t"
  "s_nop 0\n\t"
  "buffer_load_dwordx4 v10, s[56:59], s62 offen lds\n\t"
  "s_add_u32 m0, s61, 52224\n\t"
  "s_nop 0\n\t"
  "buffer_load_dwordx4 v11, s[56:59], s62 offen lds\n\t"
  "s_add_u32 m0, s61, 53248\n\t"
  "s_nop 0\n\t"
  "buffer_load_dwordx4 v12, s[56:59], s62 offen lds\n\t"
  "s_add_u32 m0, s61, 54272\n\t"
  "s_nop 0\n\t"
  "buffer_load_dwordx4 v13, s[56:59], s62 offen lds\n\t"
  "s_add_u32 s62, s62, s60\n\t"
  "v_add_u32 v20, 73728, v14\n\t"
  "ds_read_b64_tr_b16 a[216:217], v20\n\t"
  "ds_read_b64_tr_b16 a[218:219], v20 offset:12288\n\t"
  "v_add_u32 v20, 73728, v15\n\t"
  "ds_read_b64_tr_b16 a[220:221], v20\n\t"
  "ds_read_b64_tr_b16 a[222:223], v20 offset:12288\n\t"
  "v_add_u32 v20, 73728, v16\n\t"
  "ds_read_b64_tr_b16 a[224:225], v20\n\t"
  "ds_read_b64_tr_b16 a[226:227], v20 offset:12288\n\t"
  "v_add_u32 v20, 73728, v17\n\t"
  "ds_read_b64_tr_b16 a[228:229], v20\n\t"
  "ds_read_b64_tr_b16 a[230:231], v20 offset:12288\n\t"
  "v_add_u32 v20, 73728, v18\n\t"
  "ds_read_b64_tr_b16 a[232:233], v20\n\t"
  "ds_read_b64_tr_b16 a[234:235], v20 offset:12288\n\t"
  "v_add_u32 v20, 73728, v19\n\t"
  "ds_read_b64_tr_b16 a[236:237], v20\n\t"
  "ds_read_b64_tr_b16 a[238:239], v20 offset:12288\n\t"
  "s_waitcnt vmcnt(24) lgkmcnt(0)\n\t"
  "s_barrier\n\t"
  "s_add_u32 m0, s61, 73728\n\t"
  "s_nop 0\n\t"
  "buffer_load_dwordx4 v8, s[56:59], s62 offen lds\n\t"
  "s_add_u32 m0, s61, 74752\n\t"
  "s_nop 0\n\t"
  "buffer_load_dwordx4 v9, s[56:59], s62 offen lds\n\t"
  "s_add_u32 m0, s61, 75776\n\t"
  "s_nop 0\n\t"
  "buffer_load_dwordx4 v10, s[56:59], s62 offen lds\n\t"
  "s_add_u32 m0, s61, 76800\n\t"
  "s_nop 0\n\t"
  "buffer_load_dwordx4 v11, s[56:59], s62 offen lds\n\t"
  "s_add_u32 m0, s61, 77824\n\t"
  "s_nop 0\n\t"
  "buffer_load_dwordx4 v12, s[56:59], s62 offen lds\n\t"
  "s_add_u32 m0, s61, 78848\n\t"
  "s_nop 0\n\t"
  "buffer_load_dwordx4 v13, s[56:59], s62 offen lds\n\t"
  "s_add_u32 s62, s62, s60\n\t"
  "v_add_u32 v20, 98304, v14\n\t"
  "ds_read_b64_tr_b16 a[240:241], v20\n\t"
  "ds_read_b64_tr_b16 a[242:243], v20 offset:12288\n\t"
  "v_add_u32 v20, 98304, v15\n\t"
  "ds_read_b64_tr_b16 a[244:245], v20\n\t"
  "ds_read_b64_tr_b16 a[246:247], v20 offset:12288\n\t"
  "v_add_u32 v20, 98304, v16\n\t"
  "ds_read_b64_tr_b16 a[248:249], v20\n\t"
  "ds_read_b64_tr_b16 a[250:251], v20 offset:12288\n\t"
  "v_add_u32 v20, 98304, v17\n\t"
  "ds_read_b64_tr_b16 a[252:253], v20\n\t"
  "ds_read_b64_tr_b16 a[254:255], v20 offset:12288\n\t"
  "v_add_u32 v20, 98304, v18\n\t"
  "ds_read_b64_tr_b16 v[224:225], v20\n\t"
  "ds_read_b64_tr_b16 v[226:227], v20 offset:12288\n\t"
  "v_add_u32 v20, 98304, v19\n\t"
  "ds_read_b64_tr_b16 v[228:229], v20\n\t"
  "ds_read_b64_tr_b16 v[230:231], v20 offset:12288\n\t"
  "s_waitcnt vmcnt(24) lgkmcnt(0)\n\t"
  "s_barrier\n\t"
  "s_add_u32 m0, s61, 98304\n\t"
  "s_nop 0\n\t"
  "buffer_load_dwordx4 v8, s[56:59], s62 offen lds\n\t"
  "s_add_u32 m0, s61, 99328\n\t"
  "s_nop 0\n\t"
  "buffer_load_dwordx4 v9, s[56:59], s62 offen lds\n\t"
  "s_add_u32 m0, s61, 100352\n\t"
  "s_nop 0\n\t"
  "buffer_load_dwordx4 v10, s[56:59], s62 offen lds\n\t"
  "s_add_u32 m0, s61, 101376\n\t"
  "s_nop 0\n\t"
  "buffer_load_dwordx4 v11, s[56:59], s62 offen lds\n\t"
  "s_add_u32 m0, s61, 102400\n\t"
  "s_nop 0\n\t"
  "buffer_load_dwordx4 v12, s[56:59], s62 offen lds\n\t"
  "s_add_u32 m0, s61, 103424\n\t"
  "s_nop 0\n\t"
  "buffer_load_dwordx4 v13, s[56:59], s62 offen lds\n\t"
  "s_add_u32 s62, s62, s60\n\t"
  "v_add_u32 v20, 122880, v14\n\t"
  "ds_read_b64_tr_b16 v[232:233], v20\n\t"
  "ds_read_b64_tr_b16 v[234:235], v20 offset:12288\n\t"
  "v_add_u32 v20, 122880, v15\n\t"
  "ds_read_b64_tr_b16 v[236:237], v20\n\t"
  "ds_read_b64_tr_b16 v[238:239], v20 offset:12288\n\t"
  "v_add_u32 v20, 122880, v16\n\t"
  "ds_read_b64_tr_b16 v[240:241], v20\n\t"
  "ds_read_b64_tr_b16 v[242:243], v20 offset:12288\n\t"
  "v_add_u32 v20, 122880, v17\n\t"
  "ds_read_b64_tr_b16 v[244:245], v20\n\t"
  "ds_read_b64_tr_b16 v[246:247], v20 offset:12288\n\t"
  "v_add_u32 v20, 122880, v18\n\t"
  "ds_read_b64_tr_b16 v[248:249], v20\n\t"
  "ds_read_b64_tr_b16 v[250:251], v20 offset:12288\n\t"
  "v_add_u32 v20, 122880, v19\n\t"
  "ds_read_b64_tr_b16 v[252:253], v20\n\t"
  "ds_read_b64_tr_b16 v[254:255], v20 offset:12288\n\t"
  "s_waitcnt vmcnt(24) lgkmcnt(0)\n\t"
  "s_barrier\n\t"
  "s_add_u32 m0, s61, 122880\n\t"
  "s_nop 0\n\t"
  "buffer_load_dwordx4 v8, s[56:59], s62 offen lds\n\t"
  "s_add_u32 m0, s61, 123904\n\t"
  "s_nop 0\n\t"
  "buffer_load_dwordx4 v9, s[56:59], s62 offen lds\n\t"
  "s_add_u32 m0, s61, 124928\n\t"
  "s_nop 0\n\t"
  "buffer_load_dwordx4 v10, s[56:59], s62 offen lds\n\t"
  "s_add_u32 m0, s61, 125952\n\t"
  "s_nop 0\n\t"
  "buffer_load_dwordx4 v11, s[56:59], s62 offen lds\n\t"
  "s_add_u32 m0, s61, 126976\n\t"
  "s_nop 0\n\t"
  "buffer_load_dwordx4 v12, s[56:59], s62 offen lds\n\t"
  "s_add_u32 m0, s61, 128000\n\t"
  "s_nop 0\n\t"
  "buffer_load_dwordx4 v13, s[56:59], s62 offen lds\n\t"
  "s_add_u32 s62, s62, s60\n\t"
  "v_add_u32 v20, 0, v14\n\t"
  "ds_read_b64_tr_b16 v[192:193], v20\n\t"
  "ds_read_b64_tr_b16 v[194:195], v20 offset:12288\n\t"
  "v_add_u32 v20, 0, v15\n\t"
  "ds_read_b64_tr_b16 v[196:197], v20\n\t"
  "ds_read_b64_tr_b16 v[198:199], v20 offset:12288\n\t"
  "v_add_u32 v20, 0, v16\n\t"
  "ds_read_b64_tr_b16 v[200:201], v20\n\t"
  "ds_read_b64_tr_b16 v[202:203], v20 offset:12288\n\t"
  "v_add_u32 v20, 0, v17\n\t"
  "ds_read_b64_tr_b16 v[204:205], v20\n\t"
  "ds_read_b64_tr_b16 v[206:207], v20 offset:12288\n\t"
  "v_add_u32 v20, 0, v18\n\t"
  "ds_read_b64_tr_b16 v[208:209], v20\n\t"
  "ds_read_b64_tr_b16 v[210:211], v20 offset:12288\n\t"
  "v_add_u32 v20, 0, v19\n\t"
  "ds_read_b64_tr_b16 v[212:213], v20\n\t"
  "ds_read_b64_tr_b16 v[214:215], v20 offset:12288\n\t"
  "s_waitcnt vmcnt(24) lgkmcnt(0)\n\t"
  "s_barrier\n\t"
  "v_add_u32 v20, 24576, v14\n\t"
  "ds_read_b64_tr_b16 v[216:217], v20\n\t"
  "ds_read_b64_tr_b16 v[218:219], v20 offset:12288\n\t"
  "v_add_u32 v20, 24576, v15\n\t"
  "ds_read_b64_tr_b16 v[220:221], v20\n\t"
  "ds_read_b64_tr_b16 v[222:223], v20 offset:12288\n\t"
  "v_add_u32 v20, 24576, v16\n\t"
  "ds_read_b64_tr_b16 v[160:161], v20\n\t"
  "ds_read_b64_tr_b16 v[162:163], v20 offset:12288\n\t"
  "v_add_u32 v20, 24576, v17\n\t"
  "ds_read_b64_tr_b16 v[164:165], v20\n\t"
  "ds_read_b64_tr_b16 v[166:167], v20 offset:12288\n\t"
  "v_add_u32 v20, 24576, v18\n\t"
  "ds_read_b64_tr_b16 v[168:169], v20\n\t"
  "ds_read_b64_tr_b16 v[170:171], v20 offset:12288\n\t"
  "v_add_u32 v20, 24576, v19\n\t"
  "ds_read_b64_tr_b16 v[172:173], v20\n\t"
  "ds_read_b64_tr_b16 v[174:175], v20 offset:12288\n\t"
  "s_waitcnt vmcnt(18) lgkmcnt(0)\n\t"
  "s_barrier\n\t"
  "v_add_u32 v20, 49152, v14\n\t"
  "ds_read_b64_tr_b16 v[176:177], v20\n\t"
  "ds_read_b64_tr_b16 v[178:179], v20 offset:12288\n\t"
  "v_add_u32 v20, 49152, v15\n\t"
  "ds_read_b64_tr_b16 v[180:181], v20\n\t"
  "ds_read_b64_tr_b16 v[182:183], v20 offset:12288\n\t"
  "v_add_u32 v20, 49152, v16\n\t"
  "ds_read_b64_tr_b16 v[184:185], v20\n\t"
  "ds_read_b64_tr_b16 v[186:187], v20 offset:12288\n\t"
  "v_add_u32 v20, 49152, v17\n\t"
  "ds_read_b64_tr_b16 v[188:189], v20\n\t"
  "ds_read_b64_tr_b16 v[190:191], v20 offset:12288\n\t"
  "v_add_u32 v20, 49152, v18\n\t"
  "ds_read_b64_tr_b16 v[128:129], v20\n\t"
  "ds_read_b64_tr_b16 v[130:131], v20 offset:12288\n\t"
  "v_add_u32 v20, 49152, v19\n\t"
  "ds_read_b64_tr_b16 v[132:133], v20\n\t"
  "ds_read_b64_tr_b16 v[134:135], v20 offset:12288\n\t"
  "s_waitcnt vmcnt(12) lgkmcnt(0)\n\t"
  "s_barrier\n\t"
  "v_add_u32 v20, 73728, v14\n\t"
  "ds_read_b64_tr_b16 v[136:137], v20\n\t"
  "ds_read_b64_tr_b16 v[138:139], v20 offset:12288\n\t"
  "v_add_u32 v20, 73728, v15\n\t"
  "ds_read_b64_tr_b16 v[140:141], v20\n\t"
  "ds_read_b64_tr_b16 v[142:143], v20 offset:12288\n\t"
  "v_add_u32 v20, 73728, v16\n\t"
  "ds_read_b64_tr_b16 v[144:145], v20\n\t"
  "ds_read_b64_tr_b16 v[146:147], v20 offset:12288\n\t"
  "v_add_u32 v20, 73728, v17\n\t"
  "ds_read_b64_tr_b16 v[148:149], v20\n\t"
  "ds_read_b64_tr_b16 v[150:151], v20 offset:12288\n\t"
  "v_add_u32 v20, 73728, v18\n\t"
  "ds_read_b64_tr_b16 v[152:153], v20\n\t"
  "ds_read_b64_tr_b16 v[154:155], v20 offset:12288\n\t"
  "v_add_u32 v20, 73728, v19\n\t"
  "ds_read_b64_tr_b16 v[156:157], v20\n\t"
  "ds_read_b64_tr_b16 v[158:159], v20 offset:12288\n\t"
  "s_waitcnt vmcnt(6) lgkmcnt(0)\n\t"
  "s_barrier\n\t"
  "v_add_u32 v20, 98304, v14\n\t"
  "ds_read_b64_tr_b16 v[96:97], v20\n\t"
  "ds_read_b64_tr_b16 v[98:99], v20 offset:12288\n\t"
  "v_add_u32 v20, 98304, v15\n\t"
  "ds_read_b64_tr_b16 v[100:101], v20\n\t"
  "ds_read_b64_tr_b16 v[102:103], v20 offset:12288\n\t"
  "v_add_u32 v20, 98304, v16\n\t"
  "ds_read_b64_tr_b16 v[104:105], v20\n\t"
  "ds_read_b64_tr_b16 v[106:107], v20 offset:12288\n\t"
  "v_add_u32 v20, 98304, v17\n\t"
  "ds_read_b64_tr_b16 v[108:109], v20\n\t"
  "ds_read_b64_tr_b16 v[110:111], v20 offset:12288\n\t"
  "v_add_u32 v20, 98304, v18\n\t"
  "ds_read_b64_tr_b16 v[112:113], v20\n\t"
  "ds_read_b64_tr_b16 v[114:115], v20 offset:12288\n\t"
  "v_add_u32 v20, 98304, v19\n\t"
  "ds_read_b64_tr_b16 v[116:117], v20\n\t"
  "ds_read_b64_tr_b16 v[118:119], v20 offset:12288\n\t"
  "s_waitcnt vmcnt(0) lgkmcnt(0)\n\t"
  "s_barrier\n\t"
  "v_add_u32 v20, 122880, v14\n\t"
  "ds_read_b64_tr_b16 v[120:121], v20\n\t"
  "ds_read_b64_tr_b16 v[122:123], v20 offset:12288\n\t"
  "v_add_u32 v20, 122880, v15\n\t"
  "ds_read_b64_tr_b16 v[124:125], v20\n\t"
  "ds_read_b64_tr_b16 v[126:127], v20 offset:12288\n\t"
  "v_add_u32 v20, 122880, v16\n\t"
  "ds_read_b64_tr_b16 v[80:81], v20\n\t"
  "ds_read_b64_tr_b16 v[82:83], v20 offset:12288\n\t"
  "v_add_u32 v20, 122880, v17\n\t"
  "ds_read_b64_tr_b16 v[84:85], v20\n\t"
  "ds_read_b64_tr_b16 v[86:87], v20 offset:12288\n\t"
  "v_add_u32 v20, 122880, v18\n\t"
  "ds_read_b64_tr_b16 v[88:89], v20\n\t"
  "ds_read_b64_tr_b16 v[90:91], v20 offset:12288\n\t"
  "v_add_u32 v20, 122880, v19\n\t"
  "ds_read_b64_tr_b16 v[92:93], v20\n\t"
  "ds_read_b64_tr_b16 v[94:95], v20 offset:12288\n\t"
  "s_waitcnt lgkmcnt(0)\n\t"
  "s_barrier\n\t"
  "s_mov_b64 s[60:61], s[36:37]\n\t"
  "s_mov_b32 s63, s39\n\t"
  "s_mov_b64 s[64:65], s[36:37]\n\t"
  "s_mov_b32 s67, s39\n\t"
  "s_mov_b64 s[84:85], s[36:37]\n\t"
  "s_mov_b32 s87, s39\n\t"
  "s_mov_b32 s86, 0\n\t"
  "s_mov_b32 s57, 0\n\t"
  "s_mov_b32 s56, -1\n\t"
  "s_mov_b32 s80, 0x20000\n\t"
  "s_lshr_b32 s78, s41, 9\n\t"
  "s_mov_b32 s71, s41\n\t"
  "s_mov_b32 s74, -40960\n\t"
  "v_readlane_b32 s68, v4, 0\n\t"
  "v_mov_b32 v3, -1\n\t"
  "s_branch 3f\n\t"
  "1:\n\t"
  "ds_read_b128 v[16:19], v1 offset:2048\n\t"
  "s_waitcnt lgkmcnt(2)\n\t"
  "v_mfma_f32_16x16x32_bf16 v[20:23], a[0:3], v[8:11], 0\n\t"
  "s_add_u32 m0, s71, 16384\n\t"
  "s_add_u32 s76, s68, 32768\n\t"
  "buffer_load_dwordx4 v0, s[36:39], s76 offen sc1 lds\n\t"
  "v_mfma_f32_16x16x32_bf16 v[24:27], a[4:7], v[8:11], 0\n\t"
  "s_mov_b32 s70, s69\n\t"
  "s_mov_b32 s56, s57\n\t"
  "s_add_u32 s57, s57, 1\n\t"
  "v_mfma_f32_16x16x32_bf16 v[28:31], a[8:11], v[8:11], 0\n\t"
  "v_mfma_f32_16x16x32_bf16 v[32:35], a[12:15], v[8:11], 0\n\t"
  "v_mfma_f32_16x16x32_bf16 v[36:39], a[16:19], v[8:11], 0\n\t"
  "v_mfma_f32_16x16x32_bf16 v[40:43], a[20:23], v[8:11], 0\n\t"
  "ds_read_b128 v[8:11], v1 offset:3072\n\t"
  "s_waitcnt lgkmcnt(2)\n\t"
  "v_mfma_f32_16x16x32_bf16 v[20:23], a[24:27], v[12:15], v[20:23]\n\t"
  "buffer_load_dwordx4 v0, s[36:39], s76 offen offset:1024 sc1 lds\n\t"
  "v_mfma_f32_16x16x32_bf16 v[24:27], a[28:31], v[12:15], v[24:27]\n\t"
  "v_mfma_f32_16x16x32_bf16 v[28:31], a[32:35], v[12:15], v[28:31]\n\t"
  "v_mfma_f32_16x16x32_bf16 v[32:35], a[36:39], v[12:15], v[32:35]\n\t"
  "v_mfma_f32_16x16x32_bf16 v[36:39], a[40:43], v[12:15], v[36:39]\n\t"
  "v_mfma_f32_16x16x32_bf16 v[40:43], a[44:47], v[12:15], v[40:43]\n\t"
  "ds_read_b128 v[12:15], v1 offset:4096\n\t"
  "s_waitcnt lgkmcnt(2)\n\t"
  "v_mfma_f32_16x16x32_bf16 v[20:23], a[48:51], v[16:19], v[20:23]\n\t"
  "s_add_u32 m0, s71, 24576\n\t"
  "s_add_u32 s76, s68, 49152\n\t"
  "buffer_load_dwordx4 v0, s[36:39], s76 offen sc1 lds\n\t"
  "v_mfma_f32_16x16x32_bf16 v[24:27], a[52:55], v[16:19], v[24:27]\n\t"
  "v_mfma_f32_16x16x32_bf16 v[28:31], a[56:59], v[16:19], v[28:31]\n\t"
  "v_mfma_f32_16x16x32_bf16 v[32:35], a[60:63], v[16:19], v[32:35]\n\t"
  "v_mfma_f32_16x16x32_bf16 v[36:39], a[64:67], v[16:19], v[36:39]\n\t"
  "v_mfma_f32_16x16x32_bf16 v[40:43], a[68:71], v[16:19], v[40:43]\n\t"
  "ds_read_b128 v[16:19], v1 offset:5120\n\t"
  "s_waitcnt lgkmcnt(2)\n\t"
  "v_mfma_f32_16x16x32_bf16 v[20:23], a[72:75], v[8:11], v[20:23]\n\t"
  "buffer_load_dwordx4 v0, s[36:39], s76 offen offset:1024 sc1 lds\n\t"
  "v_mfma_f32_16x16x32_bf16 v[24:27], a[76:79], v[8:11], v[24:27]\n\t"
  "v_mfma_f32_16x16x32_bf16 v[28:31], a[80:83], v[8:11], v[28:31]\n\t"
  "v_mfma_f32_16x16x32_bf16 v[32:35], a[84:87], v[8:11], v[32:35]\n\t"
  "v_mfma_f32_16x16x32_bf16 v[36:39], a[88:91], v[8:11], v[36:39]\n\t"
  "v_mfma_f32_16x16x32_bf16 v[40:43], a[92:95], v[8:11], v[40:43]\n\t"
  "ds_read_b128 v[8:11], v1 offset:6144\n\t"
  "s_waitcnt lgkmcnt(2)\n\t"
  "v_mfma_f32_16x16x32_bf16 v[20:23], a[96:99], v[12:15], v[20:23]\n\t"
  "v_mov_b32 v6, s56\n\t"
  "v_mfma_f32_16x16x32_bf16 v[24:27], a[100:103], v[12:15], v[24:27]\n\t"
  "s_add_u32 m0, s71, 32768\n\t"
  "s_add_u32 s76, s68, 65536\n\t"
  "buffer_load_dwordx4 v0, s[36:39], s76 offen sc1 lds\n\t"
  "v_mfma_f32_16x16x32_bf16 v[28:31], a[104:107], v[12:15], v[28:31]\n\t"
  "v_mfma_f32_16x16x32_bf16 v[32:35], a[108:111], v[12:15], v[32:35]\n\t"
  "v_mfma_f32_16x16x32_bf16 v[36:39], a[112:115], v[12:15], v[36:39]\n\t"
  "v_mfma_f32_16x16x32_bf16 v[40:43], a[116:119], v[12:15], v[40:43]\n\t"
  "ds_read_b128 v[12:15], v1 offset:7168\n\t"
  "s_waitcnt lgkmcnt(2)\n\t"
  "v_mfma_f32_16x16x32_bf16 v[20:23], a[120:123], v[16:19], v[20:23]\n\t"
  "v_mfma_f32_16x16x32_bf16 v[24:27], a[124:127], v[16:19], v[24:27]\n\t"
  "buffer_load_dwordx4 v0, s[36:39], s76 offen offset:1024 sc1 lds\n\t"
  "v_mfma_f32_16x16x32_bf16 v[28:31], a[128:131], v[16:19], v[28:31]\n\t"
  "v_mfma_f32_16x16x32_bf16 v[32:35], a[132:135], v[16:19], v[32:35]\n\t"
  "v_mfma_f32_16x16x32_bf16 v[36:39], a[136:139], v[16:19], v[36:39]\n\t"
  "v_mfma_f32_16x16x32_bf16 v[40:43], a[140:143], v[16:19], v[40:43]\n\t"
  "ds_read_b128 v[16:19], v1 offset:8192\n\t"
  "s_waitcnt lgkmcnt(2)\n\t"
  "v_mfma_f32_16x16x32_bf16 v[20:23], a[144:147], v[8:11], v[20:23]\n\t"
  "v_mfma_f32_16x16x32_bf16 v[24:27], a[148:151], v[8:11], v[24:27]\n\t"
  "v_mfma_f32_16x16x32_bf16 v[28:31], a[152:155], v[8:11], v[28:31]\n\t"
  "v_mfma_f32_16x16x32_bf16 v[32:35], a[156:159], v[8:11], v[32:35]\n\t"
  "v_subrev_u32 v6, 1, v6\n\t"
  "s_waitcnt vmcnt(6)\n\t"
  "buffer_store_dword v6, off, s[84:87], s40 sc1\n\t"
  "v_mfma_f32_16x16x32_bf16 v[36:39], a[160:163], v[8:11], v[36:39]\n\t"
  "v_mfma_f32_16x16x32_bf16 v[40:43], a[164:167], v[8:11], v[40:43]\n\t"
  "ds_read_b128 v[8:11], v1 offset:9216\n\t"
  "s_waitcnt lgkmcnt(2)\n\t"
  "v_mfma_f32_16x16x32_bf16 v[20:23], a[168:171], v[12:15], v[20:23]\n\t"
  "v_mfma_f32_16x16x32_bf16 v[24:27], a[172:175], v[12:15], v[24:27]\n\t"
  "v_mfma_f32_16x16x32_bf16 v[28:31], a[176:179], v[12:15], v[28:31]\n\t"
  "v_mfma_f32_16x16x32_bf16 v[32:35], a[180:183], v[12:15], v[32:35]\n\t"
  "v_mfma_f32_16x16x32_bf16 v[36:39], a[184:187], v[12:15], v[36:39]\n\t"
  "v_mfma_f32_16x16x32_bf16 v[40:43], a[188:191], v[12:15], v[40:43]\n\t"
  "ds_read_b128 v[12:15], v1 offset:10240\n\t"
  "s_waitcnt lgkmcnt(2)\n\t"
  "v_mfma_f32_16x16x32_bf16 v[20:23], a[192:195], v[16:19], v[20:23]\n\t"
  "v_mfma_f32_16x16x32_bf16 v[24:27], a[196:199], v[16:19], v[24:27]\n\t"
  "v_mfma_f32_16x16x32_bf16 v[28:31], a[200:203], v[16:19], v[28:31]\n\t"
  "v_mfma_f32_16x16x32_bf16 v[32:35], a[204:207], v[16:19], v[32:35]\n\t"
  "v_mfma_f32_16x16x32_bf16 v[36:39], a[208:211], v[16:19], v[36:39]\n\t"
  "v_mfma_f32_16x16x32_bf16 v[40:43], a[212:215], v[16:19], v[40:43]\n\t"
  "ds_read_b128 v[16:19], v1 offset:11264\n\t"
  "s_waitcnt lgkmcnt(2)\n\t"
  "v_mfma_f32_16x16x32_bf16 v[20:23], a[216:219], v[8:11], v[20:23]\n\t"
  "v_mfma_f32_16x16x32_bf16 v[24:27], a[220:223], v[8:11], v[24:27]\n\t"
  "v_mfma_f32_16x16x32_bf16 v[28:31], a[224:227], v[8:11], v[28:31]\n\t"
  "v_mfma_f32_16x16x32_bf16 v[32:35], a[228:231], v[8:11], v[32:35]\n\t"
  "v_mfma_f32_16x16x32_bf16 v[36:39], a[232:235], v[8:11], v[36:39]\n\t"
  "v_mfma_f32_16x16x32_bf16 v[40:43], a[236:239], v[8:11], v[40:43]\n\t"
  "ds_read_b128 v[8:11], v1 offset:12288\n\t"
  "s_waitcnt lgkmcnt(2)\n\t"
  "v_mfma_f32_16x16x32_bf16 v[20:23], a[240:243], v[12:15], v[20:23]\n\t"
  "v_mfma_f32_16x16x32_bf16 v[24:27], a[244:247], v[12:15], v[24:27]\n\t"
  "v_mfma_f32_16x16x32_bf16 v[28:31], a[248:251], v[12:15], v[28:31]\n\t"
  "v_mfma_f32_16x16x32_bf16 v[32:35], a[252:255], v[12:15], v[32:35]\n\t"
  "v_mfma_f32_16x16x32_bf16 v[36:39], v[224:227], v[12:15], v[36:39]\n\t"
  "v_mfma_f32_16x16x32_bf16 v[40:43], v[228:231], v[12:15], v[40:43]\n\t"
  "ds_read_b128 v[12:15], v1 offset:13312\n\t"
  "s_waitcnt lgkmcnt(2)\n\t"
  "v_mfma_f32_16x16x32_bf16 v[20:23], v[232:235], v[16:19], v[20:23]\n\t"
  "v_mfma_f32_16x16x32_bf16 v[24:27], v[236:239], v[16:19], v[24:27]\n\t"
  "v_mfma_f32_16x16x32_bf16 v[28:31], v[240:243], v[16:19], v[28:31]\n\t"
  "v_mfma_f32_16x16x32_bf16 v[32:35], v[244:247], v[16:19], v[32:35]\n\t"
  "v_mfma_f32_16x16x32_bf16 v[36:39], v[248:251], v[16:19], v[36:39]\n\t"
  "v_mfma_f32_16x16x32_bf16 v[40:43], v[252:255], v[16:19], v[40:43]\n\t"
  "ds_read_b128 v[16:19], v1 offset:14336\n\t"
  "s_waitcnt lgkmcnt(2)\n\t"
  "v_mfma_f32_16x16x32_bf16 v[20:23], v[192:195], v[8:11], v[20:23]\n\t"
  "v_mfma_f32_16x16x32_bf16 v[24:27], v[196:199], v[8:11], v[24:27]\n\t"
  "v_mfma_f32_16x16x32_bf16 v[28:31], v[200:203], v[8:11], v[28:31]\n\t"
  "v_mfma_f32_16x16x32_bf16 v[32:35], v[204:207], v[8:11], v[32:35]\n\t"
  "v_mfma_f32_16x16x32_bf16 v[36:39], v[208:211], v[8:11], v[36:39]\n\t"
  "v_mfma_f32_16x16x32_bf16 v[40:43], v[212:215], v[8:11], v[40:43]\n\t"
  "ds_read_b128 v[8:11], v1 offset:15360\n\t"
  "s_waitcnt lgkmcnt(2)\n\t"
  "v_mfma_f32_16x16x32_bf16 v[20:23], v[216:219], v[12:15], v[20:23]\n\t"
  "v_mfma_f32_16x16x32_bf16 v[24:27], v[220:223], v[12:15], v[24:27]\n\t"
  "v_mfma_f32_16x16x32_bf16 v[28:31], v[160:163], v[12:15], v[28:31]\n\t"
  "v_mfma_f32_16x16x32_bf16 v[32:35], v[164:167], v[12:15], v[32:35]\n\t"
  "v_mfma_f32_16x16x32_bf16 v[36:39], v[168:171], v[12:15], v[36:39]\n\t"
  "s_waitcnt vmcnt(1)\n\t"
  "s_barrier\n\t"
  "v_mfma_f32_16x16x32_bf16 v[40:43], v[172:175], v[12:15], v[40:43]\n\t"
  "ds_read_b128 v[12:15], v1 offset:16384\n\t"
  "s_waitcnt lgkmcnt(2)\n\t"
  "v_mfma_f32_16x16x32_bf16 v[20:23], v[176:179], v[16:19], v[20:23]\n\t"
  "v_mfma_f32_16x16x32_bf16 v[24:27], v[180:183], v[16:19], v[24:27]\n\t"
  "v_mfma_f32_16x16x32_bf16 v[28:31], v[184:187], v[16:19], v[28:31]\n\t"
  "v_mfma_f32_16x16x32_bf16 v[32:35], v[188:191], v[16:19], v[32:35]\n\t"
  "v_mfma_f32_16x16x32_bf16 v[36:39], v[128:131], v[16:19], v[36:39]\n\t"
  "v_mfma_f32_16x16x32_bf16 v[40:43], v[132:135], v[16:19], v[40:43]\n\t"
  "ds_read_b128 v[16:19], v1 offset:17408\n\t"
  "s_waitcnt lgkmcnt(2)\n\t"
  "v_mfma_f32_16x16x32_bf16 v[20:23], v[136:139], v[8:11], v[20:23]\n\t"
  "v_mfma_f32_16x16x32_bf16 v[24:27], v[140:143], v[8:11], v[24:27]\n\t"
  "v_mfma_f32_16x16x32_bf16 v[28:31], v[144:147], v[8:11], v[28:31]\n\t"
  "v_mfma_f32_16x16x32_bf16 v[32:35], v[148:151], v[8:11], v[32:35]\n\t"
  "v_mfma_f32_16x16x32_bf16 v[36:39], v[152:155], v[8:11], v[36:39]\n\t"
  "v_mfma_f32_16x16x32_bf16 v[40:43], v[156:159], v[8:11], v[40:43]\n\t"
  "ds_read_b128 v[8:11], v1 offset:18432\n\t"
  "s_waitcnt lgkmcnt(2)\n\t"
  "v_mfma_f32_16x16x32_bf16 v[20:23], v[96:99], v[12:15], v[20:23]\n\t"
  "v_mfma_f32_16x16x32_bf16 v[24:27], v[100:103], v[12:15], v[24:27]\n\t"
  "s_mov_b32 s58, 0\n\t"
  "v_readlane_b32 s69, v4, s56\n\t"
  "v_mfma_f32_16x16x32_bf16 v[28:31], v[104:107], v[12:15], v[28:31]\n\t"
  "v_mfma_f32_16x16x32_bf16 v[32:35], v[108:111], v[12:15], v[32:35]\n\t"
  "v_mfma_f32_16x16x32_bf16 v[36:39], v[112:115], v[12:15], v[36:39]\n\t"
  "v_mfma_f32_16x16x32_bf16 v[40:43], v[116:119], v[12:15], v[40:43]\n\t"
  "ds_read_b128 v[12:15], v1 offset:19456\n\t"
  "s_waitcnt lgkmcnt(2)\n\t"
  "v_mfma_f32_16x16x32_bf16 v[20:23], v[120:123], v[16:19], v[20:23]\n\t"
  "v_mfma_f32_16x16x32_bf16 v[24:27], v[124:127], v[16:19], v[24:27]\n\t"
  "v_mfma_f32_16x16x32_bf16 v[28:31], v[80:83], v[16:19], v[28:31]\n\t"
  "v_mfma_f32_16x16x32_bf16 v[32:35], v[84:87], v[16:19], v[32:35]\n\t"
  "v_mfma_f32_16x16x32_bf16 v[36:39], v[88:91], v[16:19], v[36:39]\n\t"
  "v_mfma_f32_16x16x32_bf16 v[40:43], v[92:95], v[16:19], v[40:43]\n\t"
  "ds_read_b128 v[16:19], v1 offset:20480\n\t"
  "s_waitcnt lgkmcnt(2)\n\t"
  "v_mfma_f32_16x16x32_bf16 v[44:47], a[0:3], v[8:11], 0\n\t"
  "buffer_load_dword v3, v2, s[36:39], 0 offen sc1\n\t"
  "v_mfma_f32_16x16x32_bf16 v[48:51], a[4:7], v[8:11], 0\n\t"
  "v_cvt_pk_bf16_f32 v20, v20, v21\n\t"
  "v_cvt_pk_bf16_f32 v21, v22, v23\n\t"
  "v_mfma_f32_16x16x32_bf16 v[52:55], a[8:11], v[8:11], 0\n\t"
  "v_cvt_pk_bf16_f32 v22, v24, v25\n\t"
  "v_cvt_pk_bf16_f32 v23, v26, v27\n\t"
  "s_add_u32 s77, s69, s44\n\t"
  "v_mfma_f32_16x16x32_bf16 v[56:59], a[12:15], v[8:11], 0\n\t"
  "v_mfma_f32_16x16x32_bf16 v[60:63], a[16:19], v[8:11], 0\n\t"
  "v_mfma_f32_16x16x32_bf16 v[64:67], a[20:23], v[8:11], 0\n\t"
  "ds_read_b128 v[8:11], v1 offset:21504\n\t"
  "s_waitcnt lgkmcnt(2)\n\t"
  "v_mfma_f32_16x16x32_bf16 v[44:47], a[24:27], v[12:15], v[44:47]\n\t"
  "s_sub_u32 s71, s71, s74\n\t"
  "s_sub_u32 s74, 0, s74\n\t"
  "v_readlane_b32 s68, v4, s57\n\t"
  "v_mfma_f32_16x16x32_bf16 v[48:51], a[28:31], v[12:15], v[48:51]\n\t"
  "buffer_store_dwordx4 v[20:23], v0, s[36:39], s77 offen sc1\n\t"
  "v_mfma_f32_16x16x32_bf16 v[52:55], a[32:35], v[12:15], v[52:55]\n\t"
  "v_cvt_pk_bf16_f32 v28, v28, v29\n\t"
  "v_cvt_pk_bf16_f32 v29, v30, v31\n\t"
  "v_mfma_f32_16x16x32_bf16 v[56:59], a[36:39], v[12:15], v[56:59]\n\t"
  "v_cvt_pk_bf16_f32 v30, v32, v33\n\t"
  "v_cvt_pk_bf16_f32 v31, v34, v35\n\t"
  "s_add_u32 s77, s69, s45\n\t"
  "v_mfma_f32_16x16x32_bf16 v[60:63], a[40:43], v[12:15], v[60:63]\n\t"
  "v_mfma_f32_16x16x32_bf16 v[64:67], a[44:47], v[12:15], v[64:67]\n\t"
  "ds_read_b128 v[12:15], v1 offset:22528\n\t"
  "s_waitcnt lgkmcnt(2)\n\t"
  "v_mfma_f32_16x16x32_bf16 v[44:47], a[48:51], v[16:19], v[44:47]\n\t"
  "v_mfma_f32_16x16x32_bf16 v[48:51], a[52:55], v[16:19], v[48:51]\n\t"
  "buffer_store_dwordx4 v[28:31], v0, s[36:39], s77 offen sc1\n\t"
  "v_mfma_f32_16x16x32_bf16 v[52:55], a[56:59], v[16:19], v[52:55]\n\t"
  "v_cvt_pk_bf16_f32 v36, v36, v37\n\t"
  "v_cvt_pk_bf16_f32 v37, v38, v39\n\t"
  "v_mfma_f32_16x16x32_bf16 v[56:59], a[60:63], v[16:19], v[56:59]\n\t"
  "v_cvt_pk_bf16_f32 v38, v40, v41\n\t"
  "v_cvt_pk_bf16_f32 v39, v42, v43\n\t"
  "s_add_u32 s77, s69, s46\n\t"
  "v_mfma_f32_16x16x32_bf16 v[60:63], a[64:67], v[16:19], v[60:63]\n\t"
  "v_mfma_f32_16x16x32_bf16 v[64:67], a[68:71], v[16:19], v[64:67]\n\t"
  "ds_read_b128 v[16:19], v1 offset:23552\n\t"
  "s_waitcnt lgkmcnt(2)\n\t"
  "v_mfma_f32_16x16x32_bf16 v[44:47], a[72:75], v[8:11], v[44:47]\n\t"
  "v_mfma_f32_16x16x32_bf16 v[48:51], a[76:79], v[8:11], v[48:51]\n\t"
  "buffer_store_dwordx4 v[36:39], v0, s[36:39], s77 offen sc1\n\t"
  "v_mfma_f32_16x16x32_bf16 v[52:55], a[80:83], v[8:11], v[52:55]\n\t"
  "v_mfma_f32_16x16x32_bf16 v[56:59], a[84:87], v[8:11], v[56:59]\n\t"
  "v_mfma_f32_16x16x32_bf16 v[60:63], a[88:91], v[8:11], v[60:63]\n\t"
  "v_mfma_f32_16x16x32_bf16 v[64:67], a[92:95], v[8:11], v[64:67]\n\t"
  "ds_read_b128 v[8:11], v1 offset:24576\n\t"
  "s_waitcnt lgkmcnt(2)\n\t"
  "v_mfma_f32_16x16x32_bf16 v[44:47], a[96:99], v[12:15], v[44:47]\n\t"
  "v_cmp_le_i32 vcc, s57, v3\n\t"
  "v_mfma_f32_16x16x32_bf16 v[48:51], a[100:103], v[12:15], v[48:51]\n\t"
  "s_cmp_eq_u64 vcc, -1\n\t"
  "s_cselect_b32 s75, 1, 0\n\t"
  "s_mov_b32 s59, s75\n\t"
  "v_mfma_f32_16x16x32_bf16 v[52:55], a[104:107], v[12:15], v[52:55]\n\t"
  "s_andn2_b32 s75, s75, s58\n\t"
  "s_cselect_b32 s62, s51, 0\n\t"
  "s_cselect_b32 s72, s71, 155648\n\t"
  "v_mfma_f32_16x16x32_bf16 v[56:59], a[108:111], v[12:15], v[56:59]\n\t"
  "s_lshl_b32 s73, s75, 13\n\t"
  "s_or_b32 s58, s58, s75\n\t"
  "v_mfma_f32_16x16x32_bf16 v[60:63], a[112:115], v[12:15], v[60:63]\n\t"
  "s_mov_b32 m0, s72\n\t"
  "s_add_u32 s76, s68, 0\n\t"
  "buffer_load_dwordx4 v0, s[60:63], s76 offen sc1 lds\n\t"
  "v_mfma_f32_16x16x32_bf16 v[64:67], a[116:119], v[12:15], v[64:67]\n\t"
  "ds_read_b128 v[12:15], v1 offset:25600\n\t"
  "s_waitcnt lgkmcnt(2)\n\t"
  "v_mfma_f32_16x16x32_bf16 v[44:47], a[120:123], v[16:19], v[44:47]\n\t"
  "v_mfma_f32_16x16x32_bf16 v[48:51], a[124:127], v[16:19], v[48:51]\n\t"
  "v_mfma_f32_16x16x32_bf16 v[52:55], a[128:131], v[16:19], v[52:55]\n\t"
  "v_mfma_f32_16x16x32_bf16 v[56:59], a[132:135], v[16:19], v[56:59]\n\t"
  "v_mfma_f32_16x16x32_bf16 v[60:63], a[136:139], v[16:19], v[60:63]\n\t"
  "s_add_u32 s72, s72, s73\n\t"
  "buffer_load_dwordx4 v0, s[60:63], s76 offen offset:1024 sc1 lds\n\t"
  "v_mfma_f32_16x16x32_bf16 v[64:67], a[140:143], v[16:19], v[64:67]\n\t"
  "ds_read_b128 v[16:19], v1 offset:26624\n\t"
  "s_waitcnt lgkmcnt(2)\n\t"
  "v_mfma_f32_16x16x32_bf16 v[44:47], a[144:147], v[8:11], v[44:47]\n\t"
  "v_mfma_f32_16x16x32_bf16 v[48:51], a[148:151], v[8:11], v[48:51]\n\t"
  "v_mfma_f32_16x16x32_bf16 v[52:55], a[152:155], v[8:11], v[52:55]\n\t"
  "v_mfma_f32_16x16x32_bf16 v[56:59], a[156:159], v[8:11], v[56:59]\n\t"
  "v_mfma_f32_16x16x32_bf16 v[60:63], a[160:163], v[8:11], v[60:63]\n\t"
  "s_mov_b32 m0, s72\n\t"
  "s_add_u32 s76, s68, 16384\n\t"
  "buffer_load_dwordx4 v0, s[60:63], s76 offen sc1 lds\n\t"
  "v_mfma_f32_16x16x32_bf16 v[64:67], a[164:167], v[8:11], v[64:67]\n\t"
  "ds_read_b128 v[8:11], v1 offset:27648\n\t"
  "s_waitcnt lgkmcnt(2)\n\t"
  "v_mfma_f32_16x16x32_bf16 v[44:47], a[168:171], v[12:15], v[44:47]\n\t"
  "v_mfma_f32_16x16x32_bf16 v[48:51], a[172:175], v[12:15], v[48:51]\n\t"
  "v_mfma_f32_16x16x32_bf16 v[52:55], a[176:179], v[12:15], v[52:55]\n\t"
  "v_mfma_f32_16x16x32_bf16 v[56:59], a[180:183], v[12:15], v[56:59]\n\t"
  "v_mfma_f32_16x16x32_bf16 v[60:63], a[184:187], v[12:15], v[60:63]\n\t"
  "s_add_u32 s72, s72, s73\n\t"
  "buffer_load_dwordx4 v0, s[60:63], s76 offen offset:1024 sc1 lds\n\t"
  "v_mfma_f32_16x16x32_bf16 v[64:67], a[188:191], v[12:15], v[64:67]\n\t"
  "ds_read_b128 v[12:15], v1 offset:28672\n\t"
  "s_waitcnt lgkmcnt(2)\n\t"
  "v_mfma_f32_16x16x32_bf16 v[44:47], a[192:195], v[16:19], v[44:47]\n\t"
  "v_mfma_f32_16x16x32_bf16 v[48:51], a[196:199], v[16:19], v[48:51]\n\t"
  "v_mfma_f32_16x16x32_bf16 v[52:55], a[200:203], v[16:19], v[52:55]\n\t"
  "v_mfma_f32_16x16x32_bf16 v[56:59], a[204:207], v[16:19], v[56:59]\n\t"
  "v_mfma_f32_16x16x32_bf16 v[60:63], a[208:211], v[16:19], v[60:63]\n\t"
  "buffer_load_dword v3, v2, s[36:39], 0 offen sc1\n\t"
  "v_mfma_f32_16x16x32_bf16 v[64:67], a[212:215], v[16:19], v[64:67]\n\t"
  "ds_read_b128 v[16:19], v1 offset:29696\n\t"
  "s_waitcnt lgkmcnt(2)\n\t"
  "v_mfma_f32_16x16x32_bf16 v[44:47], a[216:219], v[8:11], v[44:47]\n\t"
  "v_mfma_f32_16x16x32_bf16 v[48:51], a[220:223], v[8:11], v[48:51]\n\t"
  "v_mfma_f32_16x16x32_bf16 v[52:55], a[224:227], v[8:11], v[52:55]\n\t"
  "v_mfma_f32_16x16x32_bf16 v[56:59], a[228:231], v[8:11], v[56:59]\n\t"
  "v_mfma_f32_16x16x32_bf16 v[60:63], a[232:235], v[8:11], v[60:63]\n\t"
  "v_mfma_f32_16x16x32_bf16 v[64:67], a[236:239], v[8:11], v[64:67]\n\t"
  "ds_read_b128 v[8:11], v1 offset:30720\n\t"
  "s_waitcnt lgkmcnt(2)\n\t"
  "v_mfma_f32_16x16x32_bf16 v[44:47], a[240:243], v[12:15], v[44:47]\n\t"
  "v_mfma_f32_16x16x32_bf16 v[48:51], a[244:247], v[12:15], v[48:51]\n\t"
  "v_mfma_f32_16x16x32_bf16 v[52:55], a[248:251], v[12:15], v[52:55]\n\t"
  "v_mfma_f32_16x16x32_bf16 v[56:59], a[252:255], v[12:15], v[56:59]\n\t"
  "v_mfma_f32_16x16x32_bf16 v[60:63], v[224:227], v[12:15], v[60:63]\n\t"
  "v_mfma_f32_16x16x32_bf16 v[64:67], v[228:231], v[12:15], v[64:67]\n\t"
  "ds_read_b128 v[12:15], v1 offset:31744\n\t"
  "s_waitcnt lgkmcnt(2)\n\t"
  "v_mfma_f32_16x16x32_bf16 v[44:47], v[232:235], v[16:19], v[44:47]\n\t"
  "v_mfma_f32_16x16x32_bf16 v[48:51], v[236:239], v[16:19], v[48:51]\n\t"
  "v_mfma_f32_16x16x32_bf16 v[52:55], v[240:243], v[16:19], v[52:55]\n\t"
  "v_mfma_f32_16x16x32_bf16 v[56:59], v[244:247], v[16:19], v[56:59]\n\t"
  "v_mfma_f32_16x16x32_bf16 v[60:63], v[248:251], v[16:19], v[60:63]\n\t"
  "v_mfma_f32_16x16x32_bf16 v[64:67], v[252:255], v[16:19], v[64:67]\n\t"
  "ds_read_b128 v[16:19], v1 offset:32768\n\t"
  "s_waitcnt lgkmcnt(2)\n\t"
  "v_mfma_f32_16x16x32_bf16 v[44:47], v[192:195], v[8:11], v[44:47]\n\t"
  "v_cmp_le_i32 vcc, s57, v3\n\t"
  "v_mfma_f32_16x16x32_bf16 v[48:51], v[196:199], v[8:11], v[48:51]\n\t"
  "s_cmp_eq_u64 vcc, -1\n\t"
  "s_cselect_b32 s75, 1, 0\n\t"
  "v_mfma_f32_16x16x32_bf16 v[52:55], v[200:203], v[8:11], v[52:55]\n\t"
  "s_andn2_b32 s75, s75, s58\n\t"
  "s_cselect_b32 s62, s51, 0\n\t"
  "s_cselect_b32 s72, s71, 155648\n\t"
  "v_mfma_f32_16x16x32_bf16 v[56:59], v[204:207], v[8:11], v[56:59]\n\t"
  "s_lshl_b32 s73, s75, 13\n\t"
  "s_or_b32 s58, s58, s75\n\t"
  "v_mfma_f32_16x16x32_bf16 v[60:63], v[208:211], v[8:11], v[60:63]\n\t"
  "s_mov_b32 m0, s72\n\t"
  "s_add_u32 s76, s68, 0\n\t"
  "buffer_load_dwordx4 v0, s[60:63], s76 offen sc1 lds\n\t"
  "v_mfma_f32_16x16x32_bf16 v[64:67], v[212:215], v[8:11], v[64:67]\n\t"
  "ds_read_b128 v[8:11], v1 offset:33792\n\t"
  "s_waitcnt lgkmcnt(2)\n\t"
  "v_mfma_f32_16x16x32_bf16 v[44:47], v[216:219], v[12:15], v[44:47]\n\t"
  "v_mfma_f32_16x16x32_bf16 v[48:51], v[220:223], v[12:15], v[48:51]\n\t"
  "v_mfma_f32_16x16x32_bf16 v[52:55], v[160:163], v[12:15], v[52:55]\n\t"
  "v_mfma_f32_16x16x32_bf16 v[56:59], v[164:167], v[12:15], v[56:59]\n\t"
  "v_mfma_f32_16x16x32_bf16 v[60:63], v[168:171], v[12:15], v[60:63]\n\t"
  "s_add_u32 s72, s72, s73\n\t"
  "buffer_load_dwordx4 v0, s[60:63], s76 offen offset:1024 sc1 lds\n\t"
  "v_mfma_f32_16x16x32_bf16 v[64:67], v[172:175], v[12:15], v[64:67]\n\t"
  "ds_read_b128 v[12:15], v1 offset:34816\n\t"
  "s_waitcnt lgkmcnt(2)\n\t"
  "v_mfma_f32_16x16x32_bf16 v[44:47], v[176:179], v[16:19], v[44:47]\n\t"
  "v_mfma_f32_16x16x32_bf16 v[48:51], v[180:183], v[16:19], v[48:51]\n\t"
  "v_mfma_f32_16x16x32_bf16 v[52:55], v[184:187], v[16:19], v[52:55]\n\t"
  "v_mfma_f32_16x16x32_bf16 v[56:59], v[188:191], v[16:19], v[56:59]\n\t"
  "v_mfma_f32_16x16x32_bf16 v[60:63], v[128:131], v[16:19], v[60:63]\n\t"
  "buffer_load_dword v3, v2, s[36:39], 0 offen sc1\n\t"
  "v_mfma_f32_16x16x32_bf16 v[64:67], v[132:135], v[16:19], v[64:67]\n\t"
  "ds_read_b128 v[16:19], v1 offset:35840\n\t"
  "s_waitcnt lgkmcnt(2)\n\t"
  "v_mfma_f32_16x16x32_bf16 v[44:47], v[136:139], v[8:11], v[44:47]\n\t"
  "v_add_u32 v1, s74, v1\n\t"
  "v_mfma_f32_16x16x32_bf16 v[48:51], v[140:143], v[8:11], v[48:51]\n\t"
  "v_mfma_f32_16x16x32_bf16 v[52:55], v[144:147], v[8:11], v[52:55]\n\t"
  "v_mfma_f32_16x16x32_bf16 v[56:59], v[148:151], v[8:11], v[56:59]\n\t"
  "v_mfma_f32_16x16x32_bf16 v[60:63], v[152:155], v[8:11], v[60:63]\n\t"
  "s_mov_b32 m0, s72\n\t"
  "s_add_u32 s76, s68, 16384\n\t"
  "buffer_load_dwordx4 v0, s[60:63], s76 offen sc1 lds\n\t"
  "v_mfma_f32_16x16x32_bf16 v[64:67], v[156:159], v[8:11], v[64:67]\n\t"
  "s_waitcnt lgkmcnt(1)\n\t"
  "v_mfma_f32_16x16x32_bf16 v[44:47], v[96:99], v[12:15], v[44:47]\n\t"
  "v_mfma_f32_16x16x32_bf16 v[48:51], v[100:103], v[12:15], v[48:51]\n\t"
  "s_lshl_b32 s52, s58, 7\n\t"
  "s_add_u32 s52, s52, s57\n\t"
  "v_mfma_f32_16x16x32_bf16 v[52:55], v[104:107], v[12:15], v[52:55]\n\t"
  "v_mfma_f32_16x16x32_bf16 v[56:59], v[108:111], v[12:15], v[56:59]\n\t"
  "v_mfma_f32_16x16x32_bf16 v[60:63], v[112:115], v[12:15], v[60:63]\n\t"
  "s_add_u32 s72, s72, s73\n\t"
  "buffer_load_dwordx4 v0, s[60:63], s76 offen offset:1024 sc1 lds\n\t"
  "v_mfma_f32_16x16x32_bf16 v[64:67], v[116:119], v[12:15], v[64:67]\n\t"
  "s_waitcnt lgkmcnt(0)\n\t"
  "v_mfma_f32_16x16x32_bf16 v[44:47], v[120:123], v[16:19], v[44:47]\n\t"
  "v_mfma_f32_16x16x32_bf16 v[48:51], v[124:127], v[16:19], v[48:51]\n\t"
  "v_mfma_f32_16x16x32_bf16 v[52:55], v[80:83], v[16:19], v[52:55]\n\t"
  "v_mfma_f32_16x16x32_bf16 v[56:59], v[84:87], v[16:19], v[56:59]\n\t"
  "v_mfma_f32_16x16x32_bf16 v[60:63], v[88:91], v[16:19], v[60:63]\n\t"
  "v_mfma_f32_16x16x32_bf16 v[64:67], v[92:95], v[16:19], v[64:67]\n\t"
  "s_nop 0\n\t"
  "v_cmp_le_i32 vcc, s52, v3\n\t"
  "s_cmp_eq_u64 vcc, -1\n\t"
  "s_cbranch_scc1 80f\n\t"
  "70:\n\t"
  "s_nop 3\n\t"
  "v_cvt_pk_bf16_f32 v44, v44, v45\n\t"
  "v_cvt_pk_bf16_f32 v45, v46, v47\n\t"
  "v_cvt_pk_bf16_f32 v46, v48, v49\n\t"
  "v_cvt_pk_bf16_f32 v47, v50, v51\n\t"
  "s_add_u32 s77, s69, s44\n\t"
  "s_add_u32 s77, s77, 4096\n\t"
  "buffer_store_dwordx4 v[44:47], v0, s[36:39], s77 offen sc1\n\t"
  "v_cvt_pk_bf16_f32 v52, v52, v53\n\t"
  "v_cvt_pk_bf16_f32 v53, v54, v55\n\t"
  "v_cvt_pk_bf16_f32 v54, v56, v57\n\t"
  "v_cvt_pk_bf16_f32 v55, v58, v59\n\t"
  "s_add_u32 s77, s69, s45\n\t"
  "s_add_u32 s77, s77, 4096\n\t"
  "buffer_store_dwordx4 v[52:55], v0, s[36:39], s77 offen sc1\n\t"
  "v_cvt_pk_bf16_f32 v60, v60, v61\n\t"
  "v_cvt_pk_bf16_f32 v61, v62, v63\n\t"
  "v_cvt_pk_bf16_f32 v62, v64, v65\n\t"
  "v_cvt_pk_bf16_f32 v63, v66, v67\n\t"
  "s_add_u32 s77, s69, s46\n\t"
  "s_add_u32 s77, s77, 4096\n\t"
  "buffer_store_dwordx4 v[60:63], v0, s[36:39], s77 offen sc1\n\t"
  "s_mov_b32 s86, s51\n\t"
  "s_mov_b32 s66, 0\n\t"
  "s_cmp_lg_u32 s58, 0\n\t"
  "s_cbranch_scc1 5f\n\t"
  "v_cmp_le_i32 vcc, s57, v3\n\t"
  "s_cmp_eq_u64 vcc, -1\n\t"
  "s_cbranch_scc1 41f\n\t"
  "buffer_load_dword v3, v2, s[36:39], 0 offen sc1\n\t"
  "s_sleep 2\n\t"
  "v_cmp_le_i32 vcc, s57, v3\n\t"
  "s_cmp_eq_u64 vcc, -1\n\t"
  "s_cbranch_scc1 41f\n\t"
  "buffer_load_dword v3, v2, s[36:39], 0 offen sc1\n\t"
  "s_sleep 2\n\t"
  "v_cmp_le_i32 vcc, s57, v3\n\t"
  "s_cmp_eq_u64 vcc, -1\n\t"
  "s_cbranch_scc1 41f\n\t"
  "buffer_load_dword v3, v2, s[36:39], 0 offen sc1\n\t"
  "s_sleep 2\n\t"
  "v_cmp_le_i32 vcc, s57, v3\n\t"
  "s_cmp_eq_u64 vcc, -1\n\t"
  "s_cbranch_scc1 41f\n\t"
  "buffer_load_dword v3, v2, s[36:39], 0 offen sc1\n\t"
  "s_sleep 2\n\t"
  "s_mov_b32 s86, 0\n\t"
  "v_mov_b32 v20, s56\n\t"
  "s_waitcnt vmcnt(4)\n\t"
  "buffer_store_dword v20, off, s[36:39], s40 sc1\n\t"
  "s_cmp_eq_u32 s56, 63\n\t"
  "s_cbranch_scc1 9f\n\t"
  "3:\n\t"
  "s_mov_b32 s79, 0\n\t"
  "s_mov_b32 s66, 0\n\t"
  "4:\n\t"
  "v_cmp_le_i32 vcc, s57, v3\n\t"
  "s_cmp_eq_u64 vcc, -1\n\t"
  "s_cbranch_scc1 41f\n\t"
  "s_waitcnt vmcnt(8)\n\t"
  "buffer_load_dword v3, v2, s[36:39], 0 offen sc1\n\t"
  "s_sleep 4\n\t"
  "s_add_u32 s79, s79, 1\n\t"
  "s_cmp_lt_u32 s79, s80\n\t"
  "s_cbranch_scc1 4b\n\t"
  "s_mov_b32 s80, 0\n\t"
  "41:\n\t"
  "s_add_u32 m0, s71, 0\n\t"
  "s_add_u32 s76, s68, 0\n\t"
  "buffer_load_dwordx4 v0, s[36:39], s76 offen sc1 lds\n\t"
  "buffer_load_dwordx4 v0, s[36:39], s76 offen offset:1024 sc1 lds\n\t"
  "s_add_u32 m0, s71, 8192\n\t"
  "s_add_u32 s76, s68, 16384\n\t"
  "buffer_load_dwordx4 v0, s[36:39], s76 offen sc1 lds\n\t"
  "buffer_load_dwordx4 v0, s[36:39], s76 offen offset:1024 sc1 lds\n\t"
  "v_mov_b32 v20, s56\n\t"
  "buffer_store_dword v20, off, s[64:67], s40 sc1\n\t"
  "buffer_store_dword v20, off, s[64:67], s40 sc1\n\t"
  "s_waitcnt vmcnt(6)\n\t"
  "buffer_store_dword v20, off, s[84:87], s40 sc1\n\t"
  "s_mov_b32 s86, 0\n\t"
  "s_waitcnt vmcnt(3)\n\t"
  "s_branch 6f\n\t"
  "5:\n\t"
  "s_waitcnt vmcnt(3)\n\t"
  "6:\n\t"
  "s_waitcnt lgkmcnt(0)\n\t"
  "s_barrier\n\t"
  "ds_read_b128 v[8:11], v1 offset:0\n\t"
  "ds_read_b128 v[12:15], v1 offset:1024\n\t"
  "s_branch 1b\n\t"
  "80:\n\t"
  "s_add_u32 m0, s71, 0\n\t"
  "s_add_u32 s76, s68, 0\n\t"
  "buffer_load_dwordx4 v0, s[36:39], s76 offen sc1 lds\n\t"
  "buffer_load_dwordx4 v0, s[36:39], s76 offen offset:1024 sc1 lds\n\t"
  "s_add_u32 m0, s71, 8192\n\t"
  "s_add_u32 s76, s68, 16384\n\t"
  "buffer_load_dwordx4 v0, s[36:39], s76 offen sc1 lds\n\t"
  "buffer_load_dwordx4 v0, s[36:39], s76 offen offset:1024 sc1 lds\n\t"
  "s_mov_b32 s58, 1\n\t"
  "s_mov_b32 s52, 127\n\t"
  "s_branch 70b\n\t"
  "9:\n\t"
  ".Lws_end_K:\n\t"
  "s_waitcnt vmcnt(0)\n\t"
  "s_waitcnt vmcnt(0) lgkmcnt(0)\n\t"
    : "+{v[0:7]}"(lc), "+{v[8:23]}"(so) :: CLOB_ALL);
  } else {
    lc[3] = stage_scalars(c, p.wv, DM * HD * 2, HD * 2, 0, 0, to(0), to(1), to(2), to(3), to(4), to(5));
    asm volatile(
  "s_nop 4\n\t"
  "v_readlane_b32 s36, v3, 0\n\t"
  "v_readlane_b32 s37, v3, 1\n\t"
  "v_readlane_b32 s38, v3, 2\n\t"
  "v_readlane_b32 s39, v3, 3\n\t"
  "v_readlane_b32 s40, v3, 4\n\t"
  "v_readlane_b32 s41, v3, 5\n\t"
  "v_readlane_b32 s42, v3, 6\n\t"
  "v_readlane_b32 s43, v3, 7\n\t"
  "v_readlane_b32 s44, v3, 8\n\t"
  "v_readlane_b32 s45, v3, 9\n\t"
  "v_readlane_b32 s46, v3, 10\n\t"
  "v_readlane_b32 s47, v3, 11\n\t"
  "v_readlane_b32 s48, v3, 12\n\t"
  "v_readlane_b32 s49, v3, 13\n\t"
  "v_readlane_b32 s50, v3, 14\n\t"
  "v_readlane_b32 s51, v3, 15\n\t"
  "s_nop 4\n\t"
  "v_readlane_b32 s56, v3, 16\n\t"
  "v_readlane_b32 s57, v3, 17\n\t"
  "v_readlane_b32 s58, v3, 18\n\t"
  "v_readlane_b32 s60, v3, 19\n\t"
  "s_mov_b32 s59, s39\n\t"
  "s_mul_i32 s61, s41, 6\n\t"
  "s_lshr_b32 s61, s61, 1\n\t"
  "s_mov_b32 s62, 0\n\t"
  "s_nop 2\n\t"
  "s_add_u32 m0, s61, 0\n\t"
  "s_nop 0\n\t"
  "buffer_load_dwordx4 v8, s[56:59], s62 offen lds\n\t"
  "s_add_u32 m0, s61, 1024\n\t"
  "s_nop 0\n\t"
  "buffer_load_dwordx4 v9, s[56:59], s62 offen lds\n\t"
  "s_add_u32 m0, s61, 2048\n\t"
  "s_nop 0\n\t"
  "buffer_load_dwordx4 v10, s[56:59], s62 offen lds\n\t"
  "s_add_u32 m0, s61, 3072\n\t"
  "s_nop 0\n\t"
  "buffer_load_dwordx4 v11, s[56:59], s62 offen lds\n\t"
  "s_add_u32 m0, s61, 4096\n\t"
  "s_nop 0\n\t"
  "buffer_load_dwordx4 v12, s[56:59], s62 offen lds\n\t"
  "s_add_u32 m0, s61, 5120\n\t"
  "s_nop 0\n\t"
  "buffer_load_dwordx4 v13, s[56:59], s62 offen lds\n\t"
  "s_add_u32 s62, s62, s60\n\t"
  "s_add_u32 m0, s61, 24576\n\t"
  "s_nop 0\n\t"
  "buffer_load_dwordx4 v8, s[56:59], s62 offen lds\n\t"
  "s_add_u32 m0, s61, 25600\n\t"
  "s_nop 0\n\t"
  "buffer_load_dwordx4 v9, s[56:59], s62 offen lds\n\t"
  "s_add_u32 m0, s61, 26624\n\t"
  "s_nop 0\n\t"
  "buffer_load_dwordx4 v10, s[56:59], s62 offen lds\n\t"
  "s_add_u32 m0, s61, 27648\n\t"
  "s_nop 0\n\t"
  "buffer_load_dwordx4 v11, s[56:59], s62 offen lds\n\t"
  "s_add_u32 m0, s61, 28672\n\t"
  "s_nop 0\n\t"
  "buffer_load_dwordx4 v12, s[56:59], s62 offen lds\n\t"
  "s_add_u32 m0, s61, 29696\n\t"
  "s_nop 0\n\t"
  "buffer_load_dwordx4 v13, s[56:59], s62 offen lds\n\t"
  "s_add_u32 s62, s62, s60\n\t"
  "s_add_u32 m0, s61, 49152\n\t"
  "s_nop 0\n\t"
  "buffer_load_dwordx4 v8, s[56:59], s62 offen lds\n\t"
  "s_add_u32 m0, s61, 50176\n\t"
  "s_nop 0\n\t"
  "buffer_load_dwordx4 v9, s[56:59], s62 offen lds\n\t"
  "s_add_u32 m0, s61, 51200\n\t"
  "s_nop 0\n\t"
  "buffer_load_dwordx4 v10, s[56:59], s62 offen lds\n\t"
  "s_add_u32 m0, s61, 52224\n\t"
  "s_nop 0\n\t"
  "buffer_load_dwordx4 v11, s[56:59], s62 offen lds\n\t"
  "s_add_u32 m0, s61, 53248\n\t"
  "s_nop 0\n\t"
  "buffer_load_dwordx4 v12, s[56:59], s62 offen lds\n\t"
  "s_add_u32 m0, s61, 54272\n\t"
  "s_nop 0\n\t"
  "buffer_load_dwordx4 v13, s[56:59], s62 offen lds\n\t"
  "s_add_u32 s62, s62, s60\n\t"
  "s_add_u32 m0, s61, 73728\n\t"
  "s_nop 0\n\t"
  "buffer_load_dwordx4 v8, s[56:59], s62 offen lds\n\t"
  "s_add_u32 m0, s61, 74752\n\t"
  "s_nop 0\n\t"
  "buffer_load_dwordx4 v9, s[56:59], s62 offen lds\n\t"
  "s_add_u32 m0, s61, 75776\n\t"
  "s_nop 0\n\t"
  "buffer_load_dwordx4 v10, s[56:59], s62 offen lds\n\t"
  "s_add_u32 m0, s61, 76800\n\t"
  "s_nop 0\n\t"
  "buffer_load_dwordx4 v11, s[56:59], s62 offen lds\n\t"
  "s_add_u32 m0, s61, 77824\n\t"
  "s_nop 0\n\t"
  "buffer_load_dwordx4 v12, s[56:59], s62 offen lds\n\t"
  "s_add_u32 m0, s61, 78848\n\t"
  "s_nop 0\n\t"
  "buffer_load_dwordx4 v13, s[56:59], s62 offen lds\n\t"
  "s_add_u32 s62, s62, s60\n\t"
  "s_add_u32 m0, s61, 98304\n\t"
  "s_nop 0\n\t"
  "buffer_load_dwordx4 v8, s[56:59], s62 offen lds\n\t"
  "s_add_u32 m0, s61, 99328\n\t"
  "s_nop 0\n\t"
  "buffer_load_dwordx4 v9, s[56:59], s62 offen lds\n\t"
  "s_add_u32 m0, s61, 100352\n\t"
  "s_nop 0\n\t"
  "buffer_load_dwordx4 v10, s[56:59], s62 offen lds\n\t"
  "s_add_u32 m0, s61, 101376\n\t"
  "s_nop 0\n\t"
  "buffer_load_dwordx4 v11, s[56:59], s62 offen lds\n\t"
  "s_add_u32 m0, s61, 102400\n\t"
  "s_nop 0\n\t"
  "buffer_load_dwordx4 v12, s[56:59], s62 offen lds\n\t"
  "s_add_u32 m0, s61, 103424\n\t"
  "s_nop 0\n\t"
  "buffer_load_dwordx4 v13, s[56:59], s62 offen lds\n\t"
  "s_add_u32 s62, s62, s60\n\t"
  "s_waitcnt vmcnt(24) lgkmcnt(0)\n\t"
  "s_barrier\n\t"
  "s_add_u32 m0, s61, 122880\n\t"
  "s_nop 0\n\t"
  "buffer_load_dwordx4 v8, s[56:59], s62 offen lds\n\t"
  "s_add_u32 m0, s61, 123904\n\t"
  "s_nop 0\n\t"
  "buffer_load_dwordx4 v9, s[56:59], s62 offen lds\n\t"
  "s_add_u32 m0, s61, 124928\n\t"
  "s_nop 0\n\t"
  "buffer_load_dwordx4 v10, s[56:59], s62 offen lds\n\t"
  "s_add_u32 m0, s61, 125952\n\t"
  "s_nop 0\n\t"
  "buffer_load_dwordx4 v11, s[56:59], s62 offen lds\n\t"
  "s_add_u32 m0, s61, 126976\n\t"
  "s_nop 0\n\t"
  "buffer_load_dwordx4 v12, s[56:59], s62 offen lds\n\t"
  "s_add_u32 m0, s61, 128000\n\t"
  "s_nop 0\n\t"
  "buffer_load_dwordx4 v13, s[56:59], s62 offen lds\n\t"
  "s_add_u32 s62, s62, s60\n\t"
  "v_add_u32 v20, 0, v14\n\t"
  "ds_read_b64_tr_b16 a[0:1], v20\n\t"
  "ds_read_b64_tr_b16 a[2:3], v20 offset:12288\n\t"
  "v_add_u32 v20, 0, v15\n\t"
  "ds_read_b64_tr_b16 a[4:5], v20\n\t"
  "ds_read_b64_tr_b16 a[6:7], v20 offset:12288\n\t"
  "v_add_u32 v20, 0, v16\n\t"
  "ds_read_b64_tr_b16 a[8:9], v20\n\t"
  "ds_read_b64_tr_b16 a[10:11], v20 offset:12288\n\t"
  "v_add_u32 v20, 0, v17\n\t"
  "ds_read_b64_tr_b16 a[12:13], v20\n\t"
  "ds_read_b64_tr_b16 a[14:15], v20 offset:12288\n\t"
  "v_add_u32 v20, 0, v18\n\t"
  "ds_read_b64_tr_b16 a[16:17], v20\n\t"
  "ds_read_b64_tr_b16 a[18:19], v20 offset:12288\n\t"
  "v_add_u32 v20, 0, v19\n\t"
  "ds_read_b64_tr_b16 a[20:21], v20\n\t"
  "ds_read_b64_tr_b16 a[22:23], v20 offset:12288\n\t"
  "s_waitcnt vmcnt(24) lgkmcnt(0)\n\t"
  "s_barrier\n\t"
  "s_add_u32 m0, s61, 0\n\t"
  "s_nop 0\n\t"
  "buffer_load_dwordx4 v8, s[56:59], s62 offen lds\n\t"
  "s_add_u32 m0, s61, 1024\n\t"
  "s_nop 0\n\t"
  "buffer_load_dwordx4 v9, s[56:59], s62 offen lds\n\t"
  "s_add_u32 m0, s61, 2048\n\t"
  "s_nop 0\n\t"
  "buffer_load_dwordx4 v10, s[56:59], s62 offen lds\n\t"
  "s_add_u32 m0, s61, 3072\n\t"
  "s_nop 0\n\t"
  "buffer_load_dwordx4 v11, s[56:59], s62 offen lds\n\t"
  "s_add_u32 m0, s61, 4096\n\t"
  "s_nop 0\n\t"
  "buffer_load_dwordx4 v12, s[56:59], s62 offen lds\n\t"
  "s_add_u32 m0, s61, 5120\n\t"
  "s_nop 0\n\t"
  "buffer_load_dwordx4 v13, s[56:59], s62 offen lds\n\t"
  "s_add_u32 s62, s62, s60\n\t"
  "v_add_u32 v20, 24576, v14\n\t"
  "ds_read_b64_tr_b16 a[24:25], v20\n\t"
  "ds_read_b64_tr_b16 a[26:27], v20 offset:12288\n\t"
  "v_add_u32 v20, 24576, v15\n\t"
  "ds_read_b64_tr_b16 a[28:29], v20\n\t"
  "ds_read_b64_tr_b16 a[30:31], v20 offset:12288\n\t"
  "v_add_u32 v20, 24576, v16\n\t"
  "ds_read_b64_tr_b16 a[32:33], v20\n\t"
  "ds_read_b64_tr_b16 a[34:35], v20 offset:12288\n\t"
  "v_add_u32 v20, 24576, v17\n\t"
  "ds_read_b64_tr_b16 a[36:37], v20\n\t"
  "ds_read_b64_tr_b16 a[38:39], v20 offset:12288\n\t"
  "v_add_u32 v20, 24576, v18\n\t"
  "ds_read_b64_tr_b16 a[40:41], v20\n\t"
  "ds_read_b64_tr_b16 a[42:43], v20 offset:12288\n\t"
  "v_add_u32 v20, 24576, v19\n\t"
  "ds_read_b64_tr_b16 a[44:45], v20\n\t"
  "ds_read_b64_tr_b16 a[46:47], v20 offset:12288\n\t"
  "s_waitcnt vmcnt(24) lgkmcnt(0)\n\t"
  "s_barrier\n\t"
  "s_add_u32 m0, s61, 24576\n\t"
  "s_nop 0\n\t"
  "buffer_load_dwordx4 v8, s[56:59], s62 offen lds\n\t"
  "s_add_u32 m0, s61, 25600\n\t"
  "s_nop 0\n\t"
  "buffer_load_dwordx4 v9, s[56:59], s62 offen lds\n\t"
  "s_add_u32 m0, s61, 26624\n\t"
  "s_nop 0\n\t"
  "buffer_load_dwordx4 v10, s[56:59], s62 offen lds\n\t"
  "s_add_u32 m0, s61, 27648\n\t"
  "s_nop 0\n\t"
  "buffer_load_dwordx4 v11, s[56:59], s62 offen lds\n\t"
  "s_add_u32 m0, s61, 28672\n\t"
  "s_nop 0\n\t"
  "buffer_load_dwordx4 v12, s[56:59], s62 offen lds\n\t"
  "s_add_u32 m0, s61, 29696\n\t"
  "s_nop 0\n\t"
  "buffer_load_dwordx4 v13, s[56:59], s62 offen lds\n\t"
  "s_add_u32 s62, s62, s60\n\t"
  "v_add_u32 v20, 49152, v14\n\t"
  "ds_read_b64_tr_b16 a[48:49], v20\n\t"
  "ds_read_b64_tr_b16 a[50:51], v20 offset:12288\n\t"
  "v_add_u32 v20, 49152, v15\n\t"
  "ds_read_b64_tr_b16 a[52:53], v20\n\t"
  "ds_read_b64_tr_b16 a[54:55], v20 offset:12288\n\t"
  "v_add_u32 v20, 49152, v16\n\t"
  "ds_read_b64_tr_b16 a[56:57], v20\n\t"
  "ds_read_b64_tr_b16 a[58:59], v20 offset:12288\n\t"
  "v_add_u32 v20, 49152, v17\n\t"
  "ds_read_b64_tr_b16 a[60:61], v20\n\t"
  "ds_read_b64_tr_b16 a[62:63], v20 offset:12288\n\t"
  "v_add_u32 v20, 49152, v18\n\t"
  "ds_read_b64_tr_b16 a[64:65], v20\n\t"
  "ds_read_b64_tr_b16 a[66:67], v20 offset:12288\n\t"
  "v_add_u32 v20, 49152, v19\n\t"
  "ds_read_b64_tr_b16 a[68:69], v20\n\t"
  "ds_read_b64_tr_b16 a[70:71], v20 offset:12288\n\t"
  "s_waitcnt vmcnt(24) lgkmcnt(0)\n\t"
  "s_barrier\n\t"
  "s_add_u32 m0, s61, 49152\n\t"
  "s_nop 0\n\t"
  "buffer_load_dwordx4 v8, s[56:59], s62 offen lds\n\t"
  "s_add_u32 m0, s61, 50176\n\t"
  "s_nop 0\n\t"
  "buffer_load_dwordx4 v9, s[56:59], s62 offen lds\n\t"
  "s_add_u32 m0, s61, 51200\n\t"
  "s_nop 0\n\t"
  "buffer_load_dwordx4 v10, s[56:59], s62 offen lds\n\t"
  "s_add_u32 m0, s61, 52224\n\t"
  "s_nop 0\n\t"
  "buffer_load_dwordx4 v11, s[56:59], s62 offen lds\n\t"
  "s_add_u32 m0, s61, 53248\n\t"
  "s_nop 0\n\t"
  "buffer_load_dwordx4 v12, s[56:59], s62 offen lds\n\t"
  "s_add_u32 m0, s61, 54272\n\t"
  "s_nop 0\n\t"
  "buffer_load_dwordx4 v13, s[56:59], s62 offen lds\n\t"
  "s_add_u32 s62, s62, s60\n\t"
  "v_add_u32 v20, 73728, v14\n\t"
  "ds_read_b64_tr_b16 a[72:73], v20\n\t"
  "ds_read_b64_tr_b16 a[74:75], v20 offset:12288\n\t"
  "v_add_u32 v20, 73728, v15\n\t"
  "ds_read_b64_tr_b16 a[76:77], v20\n\t"
  "ds_read_b64_tr_b16 a[78:79], v20 offset:12288\n\t"
  "v_add_u32 v20, 73728, v16\n\t"
  "ds_read_b64_tr_b16 a[80:81], v20\n\t"
  "ds_read_b64_tr_b16 a[82:83], v20 offset:12288\n\t"
  "v_add_u32 v20, 73728, v17\n\t"
  "ds_read_b64_tr_b16 a[84:85], v20\n\t"
  "ds_read_b64_tr_b16 a[86:87], v20 offset:12288\n\t"
  "v_add_u32 v20, 73728, v18\n\t"
  "ds_read_b64_tr_b16 a[88:89], v20\n\t"
  "ds_read_b64_tr_b16 a[90:91], v20 offset:12288\n\t"
  "v_add_u32 v20, 73728, v19\n\t"
  "ds_read_b64_tr_b16 a[92:93], v20\n\t"
  "ds_read_b64_tr_b16 a[94:95], v20 offset:12288\n\t"
  "s_waitcnt vmcnt(24) lgkmcnt(0)\n\t"
  "s_barrier\n\t"
  "s_add_u32 m0, s61, 73728\n\t"
  "s_nop 0\n\t"
  "buffer_load_dwordx4 v8, s[56:59], s62 offen lds\n\t"
  "s_add_u32 m0, s61, 74752\n\t"
  "s_nop 0\n\t"
  "buffer_load_dwordx4 v9, s[56:59], s62 offen lds\n\t"
  "s_add_u32 m0, s61, 75776\n\t"
  "s_nop 0\n\t"
  "buffer_load_dwordx4 v10, s[56:59], s62 offen lds\n\t"
  "s_add_u32 m0, s61, 76800\n\t"
  "s_nop 0\n\t"
  "buffer_load_dwordx4 v11, s[56:59], s62 offen lds\n\t"
  "s_add_u32 m0, s61, 77824\n\t"
  "s_nop 0\n\t"
  "buffer_load_dwordx4 v12, s[56:59], s62 offen lds\n\t"
  "s_add_u32 m0, s61, 78848\n\t"
  "s_nop 0\n\t"
  "buffer_load_dwordx4 v13, s[56:59], s62 offen lds\n\t"
  "s_add_u32 s62, s62, s60\n\t"
  "v_add_u32 v20, 98304, v14\n\t"
  "ds_read_b64_tr_b16 a[96:97], v20\n\t"
  "ds_read_b64_tr_b16 a[98:99], v20 offset:12288\n\t"
  "v_add_u32 v20, 98304, v15\n\t"
  "ds_read_b64_tr_b16 a[100:101], v20\n\t"
  "ds_read_b64_tr_b16 a[102:103], v20 offset:12288\n\t"
  "v_add_u32 v20, 98304, v16\n\t"
  "ds_read_b64_tr_b16 a[104:105], v20\n\t"
  "ds_read_b64_tr_b16 a[106:107], v20 offset:12288\n\t"
  "v_add_u32 v20, 98304, v17\n\t"
  "ds_read_b64_tr_b16 a[108:109], v20\n\t"
  "ds_read_b64_tr_b16 a[110:111], v20 offset:12288\n\t"
  "v_add_u32 v20, 98304, v18\n\t"
  "ds_read_b64_tr_b16 a[112:113], v20\n\t"
  "ds_read_b64_tr_b16 a[114:115], v20 offset:12288\n\t"
  "v_add_u32 v20, 98304, v19\n\t"
  "ds_read_b64_tr_b16 a[116:117], v20\n\t"
  "ds_read_b64_tr_b16 a[118:119], v20 offset:12288\n\t"
  "s_waitcnt vmcnt(24) lgkmcnt(0)\n\t"
  "s_barrier\n\t"
  "s_add_u32 m0, s61, 98304\n\t"
  "s_nop 0\n\t"
  "buffer_load_dwordx4 v8, s[56:59], s62 offen lds\n\t"
  "s_add_u32 m0, s61, 99328\n\t"
  "s_nop 0\n\t"
  "buffer_load_dwordx4 v9, s[56:59], s62 offen lds\n\t"
  "s_add_u32 m0, s61, 100352\n\t"
  "s_nop 0\n\t"
  "buffer_load_dwordx4 v10, s[56:59], s62 offen lds\n\t"
  "s_add_u32 m0, s61, 101376\n\t"
  "s_nop 0\n\t"
  "buffer_load_dwordx4 v11, s[56:59], s62 offen lds\n\t"
  "s_add_u32 m0, s61, 102400\n\t"
  "s_nop 0\n\t"
  "buffer_load_dwordx4 v12, s[56:59], s62 offen lds\n\t"
  "s_add_u32 m0, s61, 103424\n\t"
  "s_nop 0\n\t"
  "buffer_load_dwordx4 v13, s[56:59], s62 offen lds\n\t"
  "s_add_u32 s62, s62, s60\n\t"
  "v_add_u32 v20, 122880, v14\n\t"
  "ds_read_b64_tr_b16 a[120:121], v20\n\t"
  "ds_read_b64_tr_b16 a[122:123], v20 offset:12288\n\t"
  "v_add_u32 v20, 122880, v15\n\t"
  "ds_read_b64_tr_b16 a[124:125], v20\n\t"
  "ds_read_b64_tr_b16 a[126:127], v20 offset:12288\n\t"
  "v_add_u32 v20, 122880, v16\n\t"
  "ds_read_b64_tr_b16 a[128:129], v20\n\t"
  "ds_read_b64_tr_b16 a[130:131], v20 offset:12288\n\t"
  "v_add_u32 v20, 122880, v17\n\t"
  "ds_read_b64_tr_b16 a[132:133], v20\n\t"
  "ds_read_b64_tr_b16 a[134:135], v20 offset:12288\n\t"
  "v_add_u32 v20, 122880, v18\n\t"
  "ds_read_b64_tr_b16 a[136:137], v20\n\t"
  "ds_read_b64_tr_b16 a[138:139], v20 offset:12288\n\t"
  "v_add_u32 v20, 122880, v19\n\t"
  "ds_read_b64_tr_b16 a[140:141], v20\n\t"
  "ds_read_b64_tr_b16 a[142:143], v20 offset:12288\n\t"
  "s_waitcnt vmcnt(24) lgkmcnt(0)\n\t"
  "s_barrier\n\t"
  "s_add_u32 m0, s61, 122880\n\t"
  "s_nop 0\n\t"
  "buffer_load_dwordx4 v8, s[56:59], s62 offen lds\n\t"
  "s_add_u32 m0, s61, 123904\n\t"
  "s_nop 0\n\t"
  "buffer_load_dwordx4 v9, s[56:59], s62 offen lds\n\t"
  "s_add_u32 m0, s61, 124928\n\t"
  "s_nop 0\n\t"
  "buffer_load_dwordx4 v10, s[56:59], s62 offen lds\n\t"
  "s_add_u32 m0, s61, 125952\n\t"
  "s_nop 0\n\t"
  "buffer_load_dwordx4 v11, s[56:59], s62 offen lds\n\t"
  "s_add_u32 m0, s61, 126976\n\t"
  "s_nop 0\n\t"
  "buffer_load_dwordx4 v12, s[56:59], s62 offen lds\n\t"
  "s_add_u32 m0, s61, 128000\n\t"
  "s_nop 0\n\t"
  "buffer_load_dwordx4 v13, s[56:59], s62 offen lds\n\t"
  "s_add_u32 s62, s62, s60\n\t"
  "v_add_u32 v20, 0, v14\n\t"
  "ds_read_b64_tr_b16 a[144:145], v20\n\t"
  "ds_read_b64_tr_b16 a[146:147], v20 offset:12288\n\t"
  "v_add_u32 v20, 0, v15\n\t"
  "ds_read_b64_tr_b16 a[148:149], v20\n\t"
  "ds_read_b64_tr_b16 a[150:151], v20 offset:12288\n\t"
  "v_add_u32 v20, 0, v16\n\t"
  "ds_read_b64_tr_b16 a[152:153], v20\n\t"
  "ds_read_b64_tr_b16 a[154:155], v20 offset:12288\n\t"
  "v_add_u32 v20, 0, v17\n\t"
  "ds_read_b64_tr_b16 a[156:157], v20\n\t"
  "ds_read_b64_tr_b16 a[158:159], v20 offset:12288\n\t"
  "v_add_u32 v20, 0, v18\n\t"
  "ds_read_b64_tr_b16 a[160:161], v20\n\t"
  "ds_read_b64_tr_b16 a[162:163], v20 offset:12288\n\t"
  "v_add_u32 v20, 0, v19\n\t"
  "ds_read_b64_tr_b16 a[164:165], v20\n\t"
  "ds_read_b64_tr_b16 a[166:167], v20 offset:12288\n\t"
  "s_waitcnt vmcnt(24) lgkmcnt(0)\n\t"
  "s_barrier\n\t"
  "s_add_u32 m0, s61, 0\n\t"
  "s_nop 0\n\t"
  "buffer_load_dwordx4 v8, s[56:59], s62 offen lds\n\t"
  "s_add_u32 m0, s61, 1024\n\t"
  "s_nop 0\n\t"
  "buffer_load_dwordx4 v9, s[56:59], s62 offen lds\n\t"
  "s_add_u32 m0, s61, 2048\n\t"
  "s_nop 0\n\t"
  "buffer_load_dwordx4 v10, s[56:59], s62 offen lds\n\t"
  "s_add_u32 m0, s61, 3072\n\t"
  "s_nop 0\n\t"
  "buffer_load_dwordx4 v11, s[56:59], s62 offen lds\n\t"
  "s_add_u32 m0, s61, 4096\n\t"
  "s_nop 0\n\t"
  "buffer_load_dwordx4 v12, s[56:59], s62 offen lds\n\t"
  "s_add_u32 m0, s61, 5120\n\t"
  "s_nop 0\n\t"
  "buffer_load_dwordx4 v13, s[56:59], s62 offen lds\n\t"
  "s_add_u32 s62, s62, s60\n\t"
  "v_add_u32 v20, 24576, v14\n\t"
  "ds_read_b64_tr_b16 a[168:169], v20\n\t"
  "ds_read_b64_tr_b16 a[170:171], v20 offset:12288\n\t"
  "v_add_u32 v20, 24576, v15\n\t"
  "ds_read_b64_tr_b16 a[172:173], v20\n\t"
  "ds_read_b64_tr_b16 a[174:175], v20 offset:12288\n\t"
  "v_add_u32 v20, 24576, v16\n\t"
  "ds_read_b64_tr_b16 a[176:177], v20\n\t"
  "ds_read_b64_tr_b16 a[178:179], v20 offset:12288\n\t"
  "v_add_u32 v20, 24576, v17\n\t"
  "ds_read_b64_tr_b16 a[180:181], v20\n\t"
  "ds_read_b64_tr_b16 a[182:183], v20 offset:12288\n\t"
  "v_add_u32 v20, 24576, v18\n\t"
  "ds_read_b64_tr_b16 a[184:185], v20\n\t"
  "ds_read_b64_tr_b16 a[186:187], v20 offset:12288\n\t"
  "v_add_u32 v20, 24576, v19\n\t"
  "ds_read_b64_tr_b16 a[188:189], v20\n\t"
  "ds_read_b64_tr_b16 a[190:191], v20 offset:12288\n\t"
  "s_waitcnt vmcnt(24) lgkmcnt(0)\n\t"
  "s_barrier\n\t"
  "s_add_u32 m0, s61, 24576\n\t"
  "s_nop 0\n\t"
  "buffer_load_dwordx4 v8, s[56:59], s62 offen lds\n\t"
  "s_add_u32 m0, s61, 25600\n\t"
  "s_nop 0\n\t"
  "buffer_load_dwordx4 v9, s[56:59], s62 offen lds\n\t"
  "s_add_u32 m0, s61, 26624\n\t"
  "s_nop 0\n\t"
  "buffer_load_dwordx4 v10, s[56:59], s62 offen lds\n\t"
  "s_add_u32 m0, s61, 27648\n\t"
  "s_nop 0\n\t"
  "buffer_load_dwordx4 v11, s[56:59], s62 offen lds\n\t"
  "s_add_u32 m0, s61, 28672\n\t"
  "s_nop 0\n\t"
  "buffer_load_dwordx4 v12, s[56:59], s62 offen lds\n\t"
  "s_add_u32 m0, s61, 29696\n\t"
  "s_nop 0\n\t"
  "buffer_load_dwordx4 v13, s[56:59], s62 offen lds\n\t"
  "s_add_u32 s62, s62, s60\n\t"
  "v_add_u32 v20, 49152, v14\n\t"
  "ds_read_b64_tr_b16 a[192:193], v20\n\t"
  "ds_read_b64_tr_b16 a[194:195], v20 offset:12288\n\t"
  "v_add_u32 v20, 49152, v15\n\t"
  "ds_read_b64_tr_b16 a[196:197], v20\n\t"
  "ds_read_b64_tr_b16 a[198:199], v20 offset:12288\n\t"
  "v_add_u32 v20, 49152, v16\n\t"
  "ds_read_b64_tr_b16 a[200:201], v20\n\t"
  "ds_read_b64_tr_b16 a[202:203], v20 offset:12288\n\t"
  "v_add_u32 v20, 49152, v17\n\t"
  "ds_read_b64_tr_b16 a[204:205], v20\n\t"
  "ds_read_b64_tr_b16 a[206:207], v20 offset:12288\n\t"
  "v_add_u32 v20, 49152, v18\n\t"
  "ds_read_b64_tr_b16 a[208:209], v20\n\t"
  "ds_read_b64_tr_b16 a[210:211], v20 offset:12288\n\t"
  "v_add_u32 v20, 49152, v19\n\t"
  "ds_read_b64_tr_b16 a[212:213], v20\n\t"
  "ds_read_b64_tr_b16 a[214:215], v20 offset:12288\n\t"
  "s_waitcnt vmcnt(24) lgkmcnt(0)\n\t"
  "s_barrier\n\t"
  "s_add_u32 m0, s61, 49152\n\t"
  "s_nop 0\n\t"
  "buffer_load_dwordx4 v8, s[56:59], s62 offen lds\n\t"
  "s_add_u32 m0, s61, 50176\n\t"
  "s_nop 0\n\t"
  "buffer_load_dwordx4 v9, s[56:59], s62 offen lds\n\t"
  "s_add_u32 m0, s61, 51200\n\t"
  "s_nop 0\n\t"
  "buffer_load_dwordx4 v10, s[56:59], s62 offen lds\n\t"
  "s_add_u32 m0, s61, 52224\n\t"
  "s_nop 0\n\t"
  "buffer_load_dwordx4 v11, s[56:59], s62 offen lds\n\t"
  "s_add_u32 m0, s61, 53248\n\t"
  "s_nop 0\n\t"
  "buffer_load_dwordx4 v12, s[56:59], s62 offen lds\n\t"
  "s_add_u32 m0, s61, 54272\n\t"
  "s_nop 0\n\t"
  "buffer_load_dwordx4 v13, s[56:59], s62 offen lds\n\t"
  "s_add_u32 s62, s62, s60\n\t"
  "v_add_u32 v20, 73728, v14\n\t"
  "ds_read_b64_tr_b16 a[216:217], v20\n\t"
  "ds_read_b64_tr_b16 a[218:219], v20 offset:12288\n\t"
  "v_add_u32 v20, 73728, v15\n\t"
  "ds_read_b64_tr_b16 a[220:221], v20\n\t"
  "ds_read_b64_tr_b16 a[222:223], v20 offset:12288\n\t"
  "v_add_u32 v20, 73728, v16\n\t"
  "ds_read_b64_tr_b16 a[224:225], v20\n\t"
  "ds_read_b64_tr_b16 a[226:227], v20 offset:12288\n\t"
  "v_add_u32 v20, 73728, v17\n\t"
  "ds_read_b64_tr_b16 a[228:229], v20\n\t"
  "ds_read_b64_tr_b16 a[230:231], v20 offset:12288\n\t"
  "v_add_u32 v20, 73728, v18\n\t"
  "ds_read_b64_tr_b16 a[232:233], v20\n\t"
  "ds_read_b64_tr_b16 a[234:235], v20 offset:12288\n\t"
  "v_add_u32 v20, 73728, v19\n\t"
  "ds_read_b64_tr_b16 a[236:237], v20\n\t"
  "ds_read_b64_tr_b16 a[238:239], v20 offset:12288\n\t"
  "s_waitcnt vmcnt(24) lgkmcnt(0)\n\t"
  "s_barrier\n\t"
  "s_add_u32 m0, s61, 73728\n\t"
  "s_nop 0\n\t"
  "buffer_load_dwordx4 v8, s[56:59], s62 offen lds\n\t"
  "s_add_u32 m0, s61, 74752\n\t"
  "s_nop 0\n\t"
  "buffer_load_dwordx4 v9, s[56:59], s62 offen lds\n\t"
  "s_add_u32 m0, s61, 75776\n\t"
  "s_nop 0\n\t"
  "buffer_load_dwordx4 v10, s[56:59], s62 offen lds\n\t"
  "s_add_u32 m0, s61, 76800\n\t"
  "s_nop 0\n\t"
  "buffer_load_dwordx4 v11, s[56:59], s62 offen lds\n\t"
  "s_add_u32 m0, s61, 77824\n\t"
  "s_nop 0\n\t"
  "buffer_load_dwordx4 v12, s[56:59], s62 offen lds\n\t"
  "s_add_u32 m0, s61, 78848\n\t"
  "s_nop 0\n\t"
  "buffer_load_dwordx4 v13, s[56:59], s62 offen lds\n\t"
  "s_add_u32 s62, s62, s60\n\t"
  "v_add_u32 v20, 98304, v14\n\t"
  "ds_read_b64_tr_b16 a[240:241], v20\n\t"
  "ds_read_b64_tr_b16 a[242:243], v20 offset:12288\n\t"
  "v_add_u32 v20, 98304, v15\n\t"
  "ds_read_b64_tr_b16 a[244:245], v20\n\t"
  "ds_read_b64_tr_b16 a[246:247], v20 offset:12288\n\t"
  "v_add_u32 v20, 98304, v16\n\t"
  "ds_read_b64_tr_b16 a[248:249], v20\n\t"
  "ds_read_b64_tr_b16 a[250:251], v20 offset:12288\n\t"
  "v_add_u32 v20, 98304, v17\n\t"
  "ds_read_b64_tr_b16 a[252:253], v20\n\t"
  "ds_read_b64_tr_b16 a[254:255], v20 offset:12288\n\t"
  "v_add_u32 v20, 98304, v18\n\t"
  "ds_read_b64_tr_b16 v[224:225], v20\n\t"
  "ds_read_b64_tr_b16 v[226:227], v20 offset:12288\n\t"
  "v_add_u32 v20, 98304, v19\n\t"
  "ds_read_b64_tr_b16 v[228:229], v20\n\t"
  "ds_read_b64_tr_b16 v[230:231], v20 offset:12288\n\t"
  "s_waitcnt vmcnt(24) lgkmcnt(0)\n\t"
  "s_barrier\n\t"
  "s_add_u32 m0, s61, 98304\n\t"
  "s_nop 0\n\t"
  "buffer_load_dwordx4 v8, s[56:59], s62 offen lds\n\t"
  "s_add_u32 m0, s61, 99328\n\t"
  "s_nop 0\n\t"
  "buffer_load_dwordx4 v9, s[56:59], s62 offen lds\n\t"
  "s_add_u32 m0, s61, 100352\n\t"
  "s_nop 0\n\t"
  "buffer_load_dwordx4 v10, s[56:59], s62 offen lds\n\t"
  "s_add_u32 m0, s61, 101376\n\t"
  "s_nop 0\n\t"
  "buffer_load_dwordx4 v11, s[56:59], s62 offen lds\n\t"
  "s_add_u32 m0, s61, 102400\n\t"
  "s_nop 0\n\t"
  "buffer_load_dwordx4 v12, s[56:59], s62 offen lds\n\t"
  "s_add_u32 m0, s61, 103424\n\t"
  "s_nop 0\n\t"
  "buffer_load_dwordx4 v13, s[56:59], s62 offen lds\n\t"
  "s_add_u32 s62, s62, s60\n\t"
  "v_add_u32 v20, 122880, v14\n\t"
  "ds_read_b64_tr_b16 v[232:233], v20\n\t"
  "ds_read_b64_tr_b16 v[234:235], v20 offset:12288\n\t"
  "v_add_u32 v20, 122880, v15\n\t"
  "ds_read_b64_tr_b16 v[236:237], v20\n\t"
  "ds_read_b64_tr_b16 v[238:239], v20 offset:12288\n\t"
  "v_add_u32 v20, 122880, v16\n\t"
  "ds_read_b64_tr_b16 v[240:241], v20\n\t"
  "ds_read_b64_tr_b16 v[242:243], v20 offset:12288\n\t"
  "v_add_u32 v20, 122880, v17\n\t"
  "ds_read_b64_tr_b16 v[244:245], v20\n\t"
  "ds_read_b64_tr_b16 v[246:247], v20 offset:12288\n\t"
  "v_add_u32 v20, 122880, v18\n\t"
  "ds_read_b64_tr_b16 v[248:249], v20\n\t"
  "ds_read_b64_tr_b16 v[250:251], v20 offset:12288\n\t"
  "v_add_u32 v20, 122880, v19\n\t"
  "ds_read_b64_tr_b16 v[252:253], v20\n\t"
  "ds_read_b64_tr_b16 v[254:255], v20 offset:12288\n\t"
  "s_waitcnt vmcnt(24) lgkmcnt(0)\n\t"
  "s_barrier\n\t"
  "s_add_u32 m0, s61, 122880\n\t"
  "s_nop 0\n\t"
  "buffer_load_dwordx4 v8, s[56:59], s62 offen lds\n\t"
  "s_add_u32 m0, s61, 123904\n\t"
  "s_nop 0\n\t"
  "buffer_load_dwordx4 v9, s[56:59], s62 offen lds\n\t"
  "s_add_u32 m0, s61, 124928\n\t"
  "s_nop 0\n\t"
  "buffer_load_dwordx4 v10, s[56:59], s62 offen lds\n\t"
  "s_add_u32 m0, s61, 125952\n\t"
  "s_nop 0\n\t"
  "buffer_load_dwordx4 v11, s[56:59], s62 offen lds\n\t"
  "s_add_u32 m0, s61, 126976\n\t"
  "s_nop 0\n\t"
  "buffer_load_dwordx4 v12, s[56:59], s62 offen lds\n\t"
  "s_add_u32 m0, s61, 128000\n\t"
  "s_nop 0\n\t"
  "buffer_load_dwordx4 v13, s[56:59], s62 offen lds\n\t"
  "s_add_u32 s62, s62, s60\n\t"
  "v_add_u32 v20, 0, v14\n\t"
  "ds_read_b64_tr_b16 v[192:193], v20\n\t"
  "ds_read_b64_tr_b16 v[194:195], v20 offset:12288\n\t"
  "v_add_u32 v20, 0, v15\n\t"
  "ds_read_b64_tr_b16 v[196:197], v20\n\t"
  "ds_read_b64_tr_b16 v[198:199], v20 offset:12288\n\t"
  "v_add_u32 v20, 0, v16\n\t"
  "ds_read_b64_tr_b16 v[200:201], v20\n\t"
  "ds_read_b64_tr_b16 v[202:203], v20 offset:12288\n\t"
  "v_add_u32 v20, 0, v17\n\t"
  "ds_read_b64_tr_b16 v[204:205], v20\n\t"
  "ds_read_b64_tr_b16 v[206:207], v20 offset:12288\n\t"
  "v_add_u32 v20, 0, v18\n\t"
  "ds_read_b64_tr_b16 v[208:209], v20\n\t"
  "ds_read_b64_tr_b16 v[210:211], v20 offset:12288\n\t"
  "v_add_u32 v20, 0, v19\n\t"
  "ds_read_b64_tr_b16 v[212:213], v20\n\t"
  "ds_read_b64_tr_b16 v[214:215], v20 offset:12288\n\t"
  "s_waitcnt vmcnt(24) lgkmcnt(0)\n\t"
  "s_barrier\n\t"
  "v_add_u32 v20, 24576, v14\n\t"
  "ds_read_b64_tr_b16 v[216:217], v20\n\t"
  "ds_read_b64_tr_b16 v[218:219], v20 offset:12288\n\t"
  "v_add_u32 v20, 24576, v15\n\t"
  "ds_read_b64_tr_b16 v[220:221], v20\n\t"
  "ds_read_b64_tr_b16 v[222:223], v20 offset:12288\n\t"
  "v_add_u32 v20, 24576, v16\n\t"
  "ds_read_b64_tr_b16 v[160:161], v20\n\t"
  "ds_read_b64_tr_b16 v[162:163], v20 offset:12288\n\t"
  "v_add_u32 v20, 24576, v17\n\t"
  "ds_read_b64_tr_b16 v[164:165], v20\n\t"
  "ds_read_b64_tr_b16 v[166:167], v20 offset:12288\n\t"
  "v_add_u32 v20, 24576, v18\n\t"
  "ds_read_b64_tr_b16 v[168:169], v20\n\t"
  "ds_read_b64_tr_b16 v[170:171], v20 offset:12288\n\t"
  "v_add_u32 v20, 24576, v19\n\t"
  "ds_read_b64_tr_b16 v[172:173], v20\n\t"
  "ds_read_b64_tr_b16 v[174:175], v20 offset:12288\n\t"
  "s_waitcnt vmcnt(18) lgkmcnt(0)\n\t"
  "s_barrier\n\t"
  "v_add_u32 v20, 49152, v14\n\t"
  "ds_read_b64_tr_b16 v[176:177], v20\n\t"
  "ds_read_b64_tr_b16 v[178:179], v20 offset:12288\n\t"
  "v_add_u32 v20, 49152, v15\n\t"
  "ds_read_b64_tr_b16 v[180:181], v20\n\t"
  "ds_read_b64_tr_b16 v[182:183], v20 offset:12288\n\t"
  "v_add_u32 v20, 49152, v16\n\t"
  "ds_read_b64_tr_b16 v[184:185], v20\n\t"
  "ds_read_b64_tr_b16 v[186:187], v20 offset:12288\n\t"
  "v_add_u32 v20, 49152, v17\n\t"
  "ds_read_b64_tr_b16 v[188:189], v20\n\t"
  "ds_read_b64_tr_b16 v[190:191], v20 offset:12288\n\t"
  "v_add_u32 v20, 49152, v18\n\t"
  "ds_read_b64_tr_b16 v[128:129], v20\n\t"
  "ds_read_b64_tr_b16 v[130:131], v20 offset:12288\n\t"
  "v_add_u32 v20, 49152, v19\n\t"
  "ds_read_b64_tr_b16 v[132:133], v20\n\t"
  "ds_read_b64_tr_b16 v[134:135], v20 offset:12288\n\t"
  "s_waitcnt vmcnt(12) lgkmcnt(0)\n\t"
  "s_barrier\n\t"
  "v_add_u32 v20, 73728, v14\n\t"
  "ds_read_b64_tr_b16 v[136:137], v20\n\t"
  "ds_read_b64_tr_b16 v[138:139], v20 offset:12288\n\t"
  "v_add_u32 v20, 73728, v15\n\t"
  "ds_read_b64_tr_b16 v[140:141], v20\n\t"
  "ds_read_b64_tr_b16 v[142:143], v20 offset:12288\n\t"
  "v_add_u32 v20, 73728, v16\n\t"
  "ds_read_b64_tr_b16 v[144:145], v20\n\t"
  "ds_read_b64_tr_b16 v[146:147], v20 offset:12288\n\t"
  "v_add_u32 v20, 73728, v17\n\t"
  "ds_read_b64_tr_b16 v[148:149], v20\n\t"
  "ds_read_b64_tr_b16 v[150:151], v20 offset:12288\n\t"
  "v_add_u32 v20, 73728, v18\n\t"
  "ds_read_b64_tr_b16 v[152:153], v20\n\t"
  "ds_read_b64_tr_b16 v[154:155], v20 offset:12288\n\t"
  "v_add_u32 v20, 73728, v19\n\t"
  "ds_read_b64_tr_b16 v[156:157], v20\n\t"
  "ds_read_b64_tr_b16 v[158:159], v20 offset:12288\n\t"
  "s_waitcnt vmcnt(6) lgkmcnt(0)\n\t"
  "s_barrier\n\t"
  "v_add_u32 v20, 98304, v14\n\t"
  "ds_read_b64_tr_b16 v[96:97], v20\n\t"
  "ds_read_b64_tr_b16 v[98:99], v20 offset:12288\n\t"
  "v_add_u32 v20, 98304, v15\n\t"
  "ds_read_b64_tr_b16 v[100:101], v20\n\t"
  "ds_read_b64_tr_b16 v[102:103], v20 offset:12288\n\t"
  "v_add_u32 v20, 98304, v16\n\t"
  "ds_read_b64_tr_b16 v[104:105], v20\n\t"
  "ds_read_b64_tr_b16 v[106:107], v20 offset:12288\n\t"
  "v_add_u32 v20, 98304, v17\n\t"
  "ds_read_b64_tr_b16 v[108:109], v20\n\t"
  "ds_read_b64_tr_b16 v[110:111], v20 offset:12288\n\t"
  "v_add_u32 v20, 98304, v18\n\t"
  "ds_read_b64_tr_b16 v[112:113], v20\n\t"
  "ds_read_b64_tr_b16 v[114:115], v20 offset:12288\n\t"
  "v_add_u32 v20, 98304, v19\n\t"
  "ds_read_b64_tr_b16 v[116:117], v20\n\t"
  "ds_read_b64_tr_b16 v[118:119], v20 offset:12288\n\t"
  "s_waitcnt vmcnt(0) lgkmcnt(0)\n\t"
  "s_barrier\n\t"
  "v_add_u32 v20, 122880, v14\n\t"
  "ds_read_b64_tr_b16 v[120:121], v20\n\t"
  "ds_read_b64_tr_b16 v[122:123], v20 offset:12288\n\t"
  "v_add_u32 v20, 122880, v15\n\t"
  "ds_read_b64_tr_b16 v[124:125], v20\n\t"
  "ds_read_b64_tr_b16 v[126:127], v20 offset:12288\n\t"
  "v_add_u32 v20, 122880, v16\n\t"
  "ds_read_b64_tr_b16 v[80:81], v20\n\t"
  "ds_read_b64_tr_b16 v[82:83], v20 offset:12288\n\t"
  "v_add_u32 v20, 122880, v17\n\t"
  "ds_read_b64_tr_b16 v[84:85], v20\n\t"
  "ds_read_b64_tr_b16 v[86:87], v20 offset:12288\n\t"
  "v_add_u32 v20, 122880, v18\n\t"
  "ds_read_b64_tr_b16 v[88:89], v20\n\t"
  "ds_read_b64_tr_b16 v[90:91], v20 offset:12288\n\t"
  "v_add_u32 v20, 122880, v19\n\t"
  "ds_read_b64_tr_b16 v[92:93], v20\n\t"
  "ds_read_b64_tr_b16 v[94:95], v20 offset:12288\n\t"
  "s_waitcnt lgkmcnt(0)\n\t"
  "s_barrier\n\t"
  "s_mov_b64 s[60:61], s[36:37]\n\t"
  "s_mov_b32 s63, s39\n\t"
  "s_mov_b64 s[64:65], s[36:37]\n\t"
  "s_mov_b32 s67, s39\n\t"
  "s_mov_b64 s[84:85], s[36:37]\n\t"
  "s_mov_b32 s87, s39\n\t"
  "s_mov_b32 s86, 0\n\t"
  "s_mov_b32 s57, 0\n\t"
  "s_mov_b32 s56, -1\n\t"
  "s_mov_b32 s80, 0x20000\n\t"
  "s_lshr_b32 s78, s41, 9\n\t"
  "s_mov_b32 s71, s41\n\t"
  "s_mov_b32 s74, -40960\n\t"
  "v_readlane_b32 s68, v4, 0\n\t"
  "v_mov_b32 v3, -1\n\t"
  "s_branch 3f\n\t"
  "1:\n\t"
  "ds_read_b128 v[16:19], v1 offset:2048\n\t"
  "s_waitcnt lgkmcnt(2)\n\t"
  "v_mfma_f32_16x16x32_bf16 v[32:35], v[8:11], a[0:3], 0\n\t"
  "s_add_u32 m0, s71, 16384\n\t"
  "s_add_u32 s76, s68, 32768\n\t"
  "buffer_load_dwordx4 v0, s[36:39], s76 offen sc1 lds\n\t"
  "v_mfma_f32_16x16x32_bf16 v[36:39], v[8:11], a[4:7], 0\n\t"
  "s_mov_b32 s70, s69\n\t"
  "s_mov_b32 s56, s57\n\t"
  "s_add_u32 s57, s57, 1\n\t"
  "v_mfma_f32_16x16x32_bf16 v[40:43], v[8:11], a[8:11], 0\n\t"
  "v_mfma_f32_16x16x32_bf16 v[44:47], v[8:11], a[12:15], 0\n\t"
  "v_mfma_f32_16x16x32_bf16 v[48:51], v[8:11], a[16:19], 0\n\t"
  "v_mfma_f32_16x16x32_bf16 v[52:55], v[8:11], a[20:23], 0\n\t"
  "ds_read_b128 v[8:11], v1 offset:3072\n\t"
  "s_waitcnt lgkmcnt(2)\n\t"
  "v_mfma_f32_16x16x32_bf16 v[32:35], v[12:15], a[24:27], v[32:35]\n\t"
  "buffer_load_dwordx4 v0, s[36:39], s76 offen offset:1024 sc1 lds\n\t"
  "v_mfma_f32_16x16x32_bf16 v[36:39], v[12:15], a[28:31], v[36:39]\n\t"
  "v_mfma_f32_16x16x32_bf16 v[40:43], v[12:15], a[32:35], v[40:43]\n\t"
  "v_mfma_f32_16x16x32_bf16 v[44:47], v[12:15], a[36:39], v[44:47]\n\t"
  "v_mfma_f32_16x16x32_bf16 v[48:51], v[12:15], a[40:43], v[48:51]\n\t"
  "v_mfma_f32_16x16x32_bf16 v[52:55], v[12:15], a[44:47], v[52:55]\n\t"
  "ds_read_b128 v[12:15], v1 offset:4096\n\t"
  "s_waitcnt lgkmcnt(2)\n\t"
  "v_mfma_f32_16x16x32_bf16 v[32:35], v[16:19], a[48:51], v[32:35]\n\t"
  "s_add_u32 m0, s71, 24576\n\t"
  "s_add_u32 s76, s68, 49152\n\t"
  "buffer_load_dwordx4 v0, s[36:39], s76 offen sc1 lds\n\t"
  "v_mfma_f32_16x16x32_bf16 v[36:39], v[16:19], a[52:55], v[36:39]\n\t"
  "v_mfma_f32_16x16x32_bf16 v[40:43], v[16:19], a[56:59], v[40:43]\n\t"
  "v_mfma_f32_16x16x32_bf16 v[44:47], v[16:19], a[60:63], v[44:47]\n\t"
  "v_mfma_f32_16x16x32_bf16 v[48:51], v[16:19], a[64:67], v[48:51]\n\t"
  "v_mfma_f32_16x16x32_bf16 v[52:55], v[16:19], a[68:71], v[52:55]\n\t"
  "ds_read_b128 v[16:19], v1 offset:5120\n\t"
  "s_waitcnt lgkmcnt(2)\n\t"
  "v_mfma_f32_16x16x32_bf16 v[32:35], v[8:11], a[72:75], v[32:35]\n\t"
  "buffer_load_dwordx4 v0, s[36:39], s76 offen offset:1024 sc1 lds\n\t"
  "v_mfma_f32_16x16x32_bf16 v[36:39], v[8:11], a[76:79], v[36:39]\n\t"
  "v_mfma_f32_16x16x32_bf16 v[40:43], v[8:11], a[80:83], v[40:43]\n\t"
  "v_mfma_f32_16x16x32_bf16 v[44:47], v[8:11], a[84:87], v[44:47]\n\t"
  "v_mfma_f32_16x16x32_bf16 v[48:51], v[8:11], a[88:91], v[48:51]\n\t"
  "v_mfma_f32_16x16x32_bf16 v[52:55], v[8:11], a[92:95], v[52:55]\n\t"
  "ds_read_b128 v[8:11], v1 offset:6144\n\t"
  "s_waitcnt lgkmcnt(2)\n\t"
  "v_mfma_f32_16x16x32_bf16 v[32:35], v[12:15], a[96:99], v[32:35]\n\t"
  "v_mov_b32 v6, s56\n\t"
  "v_mfma_f32_16x16x32_bf16 v[36:39], v[12:15], a[100:103], v[36:39]\n\t"
  "s_add_u32 m0, s71, 32768\n\t"
  "s_add_u32 s76, s68, 65536\n\t"
  "buffer_load_dwordx4 v0, s[36:39], s76 offen sc1 lds\n\t"
  "v_mfma_f32_16x16x32_bf16 v[40:43], v[12:15], a[104:107], v[40:43]\n\t"
  "v_mfma_f32_16x16x32_bf16 v[44:47], v[12:15], a[108:111], v[44:47]\n\t"
  "v_mfma_f32_16x16x32_bf16 v[48:51], v[12:15], a[112:115], v[48:51]\n\t"
  "v_mfma_f32_16x16x32_bf16 v[52:55], v[12:15], a[116:119], v[52:55]\n\t"
  "ds_read_b128 v[12:15], v1 offset:7168\n\t"
  "s_waitcnt lgkmcnt(2)\n\t"
  "v_mfma_f32_16x16x32_bf16 v[32:35], v[16:19], a[120:123], v[32:35]\n\t"
  "v_mfma_f32_16x16x32_bf16 v[36:39], v[16:19], a[124:127], v[36:39]\n\t"
  "buffer_load_dwordx4 v0, s[36:39], s76 offen offset:1024 sc1 lds\n\t"
  "v_mfma_f32_16x16x32_bf16 v[40:43], v[16:19], a[128:131], v[40:43]\n\t"
  "v_mfma_f32_16x16x32_bf16 v[44:47], v[16:19], a[132:135], v[44:47]\n\t"
  "v_mfma_f32_16x16x32_bf16 v[48:51], v[16:19], a[136:139], v[48:51]\n\t"
  "v_mfma_f32_16x16x32_bf16 v[52:55], v[16:19], a[140:143], v[52:55]\n\t"
  "ds_read_b128 v[16:19], v1 offset:8192\n\t"
  "s_waitcnt lgkmcnt(2)\n\t"
  "v_mfma_f32_16x16x32_bf16 v[32:35], v[8:11], a[144:147], v[32:35]\n\t"
  "v_mfma_f32_16x16x32_bf16 v[36:39], v[8:11], a[148:151], v[36:39]\n\t"
  "v_mfma_f32_16x16x32_bf16 v[40:43], v[8:11], a[152:155], v[40:43]\n\t"
  "v_mfma_f32_16x16x32_bf16 v[44:47], v[8:11], a[156:159], v[44:47]\n\t"
  "v_subrev_u32 v6, 1, v6\n\t"
  "s_waitcnt vmcnt(6)\n\t"
  "buffer_store_dword v6, off, s[84:87], s40 sc1\n\t"
  "v_mfma_f32_16x16x32_bf16 v[48:51], v[8:11], a[160:163], v[48:51]\n\t"
  "v_mfma_f32_16x16x32_bf16 v[52:55], v[8:11], a[164:167], v[52:55]\n\t"
  "ds_read_b128 v[8:11], v1 offset:9216\n\t"
  "s_waitcnt lgkmcnt(2)\n\t"
  "v_mfma_f32_16x16x32_bf16 v[32:35], v[12:15], a[168:171], v[32:35]\n\t"
  "v_mfma_f32_16x16x32_bf16 v[36:39], v[12:15], a[172:175], v[36:39]\n\t"
  "v_mfma_f32_16x16x32_bf16 v[40:43], v[12:15], a[176:179], v[40:43]\n\t"
  "v_mfma_f32_16x16x32_bf16 v[44:47], v[12:15], a[180:183], v[44:47]\n\t"
  "v_mfma_f32_16x16x32_bf16 v[48:51], v[12:15], a[184:187], v[48:51]\n\t"
  "v_mfma_f32_16x16x32_bf16 v[52:55], v[12:15], a[188:191], v[52:55]\n\t"
  "ds_read_b128 v[12:15], v1 offset:10240\n\t"
  "s_waitcnt lgkmcnt(2)\n\t"
  "v_mfma_f32_16x16x32_bf16 v[32:35], v[16:19], a[192:195], v[32:35]\n\t"
  "v_mfma_f32_16x16x32_bf16 v[36:39], v[16:19], a[196:199], v[36:39]\n\t"
  "v_mfma_f32_16x16x32_bf16 v[40:43], v[16:19], a[200:203], v[40:43]\n\t"
  "v_mfma_f32_16x16x32_bf16 v[44:47], v[16:19], a[204:207], v[44:47]\n\t"
  "v_mfma_f32_16x16x32_bf16 v[48:51], v[16:19], a[208:211], v[48:51]\n\t"
  "v_mfma_f32_16x16x32_bf16 v[52:55], v[16:19], a[212:215], v[52:55]\n\t"
  "ds_read_b128 v[16:19], v1 offset:11264\n\t"
  "s_waitcnt lgkmcnt(2)\n\t"
  "v_mfma_f32_16x16x32_bf16 v[32:35], v[8:11], a[216:219], v[32:35]\n\t"
  "v_mfma_f32_16x16x32_bf16 v[36:39], v[8:11], a[220:223], v[36:39]\n\t"
  "v_mfma_f32_16x16x32_bf16 v[40:43], v[8:11], a[224:227], v[40:43]\n\t"
  "v_mfma_f32_16x16x32_bf16 v[44:47], v[8:11], a[228:231], v[44:47]\n\t"
  "v_mfma_f32_16x16x32_bf16 v[48:51], v[8:11], a[232:235], v[48:51]\n\t"
  "v_mfma_f32_16x16x32_bf16 v[52:55], v[8:11], a[236:239], v[52:55]\n\t"
  "ds_read_b128 v[8:11], v1 offset:12288\n\t"
  "s_waitcnt lgkmcnt(2)\n\t"
  "v_mfma_f32_16x16x32_bf16 v[32:35], v[12:15], a[240:243], v[32:35]\n\t"
  "v_mfma_f32_16x16x32_bf16 v[36:39], v[12:15], a[244:247], v[36:39]\n\t"
  "v_mfma_f32_16x16x32_bf16 v[40:43], v[12:15], a[248:251], v[40:43]\n\t"
  "v_mfma_f32_16x16x32_bf16 v[44:47], v[12:15], a[252:255], v[44:47]\n\t"
  "v_mfma_f32_16x16x32_bf16 v[48:51], v[12:15], v[224:227], v[48:51]\n\t"
  "v_mfma_f32_16x16x32_bf16 v[52:55], v[12:15], v[228:231], v[52:55]\n\t"
  "ds_read_b128 v[12:15], v1 offset:13312\n\t"
  "s_waitcnt lgkmcnt(2)\n\t"
  "v_mfma_f32_16x16x32_bf16 v[32:35], v[16:19], v[232:235], v[32:35]\n\t"
  "v_mfma_f32_16x16x32_bf16 v[36:39], v[16:19], v[236:239], v[36:39]\n\t"
  "v_mfma_f32_16x16x32_bf16 v[40:43], v[16:19], v[240:243], v[40:43]\n\t"
  "v_mfma_f32_16x16x32_bf16 v[44:47], v[16:19], v[244:247], v[44:47]\n\t"
  "v_mfma_f32_16x16x32_bf16 v[48:51], v[16:19], v[248:251], v[48:51]\n\t"
  "v_mfma_f32_16x16x32_bf16 v[52:55], v[16:19], v[252:255], v[52:55]\n\t"
  "ds_read_b128 v[16:19], v1 offset:14336\n\t"
  "s_waitcnt lgkmcnt(2)\n\t"
  "v_mfma_f32_16x16x32_bf16 v[32:35], v[8:11], v[192:195], v[32:35]\n\t"
  "v_mfma_f32_16x16x32_bf16 v[36:39], v[8:11], v[196:199], v[36:39]\n\t"
  "v_mfma_f32_16x16x32_bf16 v[40:43], v[8:11], v[200:203], v[40:43]\n\t"
  "v_mfma_f32_16x16x32_bf16 v[44:47], v[8:11], v[204:207], v[44:47]\n\t"
  "v_mfma_f32_16x16x32_bf16 v[48:51], v[8:11], v[208:211], v[48:51]\n\t"
  "v_mfma_f32_16x16x32_bf16 v[52:55], v[8:11], v[212:215], v[52:55]\n\t"
  "ds_read_b128 v[8:11], v1 offset:15360\n\t"
  "s_waitcnt lgkmcnt(2)\n\t"
  "v_mfma_f32_16x16x32_bf16 v[32:35], v[12:15], v[216:219], v[32:35]\n\t"
  "v_mfma_f32_16x16x32_bf16 v[36:39], v[12:15], v[220:223], v[36:39]\n\t"
  "v_mfma_f32_16x16x32_bf16 v[40:43], v[12:15], v[160:163], v[40:43]\n\t"
  "v_mfma_f32_16x16x32_bf16 v[44:47], v[12:15], v[164:167], v[44:47]\n\t"
  "v_mfma_f32_16x16x32_bf16 v[48:51], v[12:15], v[168:171], v[48:51]\n\t"
  "s_waitcnt vmcnt(1)\n\t"
  "s_barrier\n\t"
  "v_mfma_f32_16x16x32_bf16 v[52:55], v[12:15], v[172:175], v[52:55]\n\t"
  "ds_read_b128 v[12:15], v1 offset:16384\n\t"
  "s_waitcnt lgkmcnt(2)\n\t"
  "v_mfma_f32_16x16x32_bf16 v[32:35], v[16:19], v[176:179], v[32:35]\n\t"
  "v_mfma_f32_16x16x32_bf16 v[36:39], v[16:19], v[180:183], v[36:39]\n\t"
  "v_mfma_f32_16x16x32_bf16 v[40:43], v[16:19], v[184:187], v[40:43]\n\t"
  "v_mfma_f32_16x16x32_bf16 v[44:47], v[16:19], v[188:191], v[44:47]\n\t"
  "v_mfma_f32_16x16x32_bf16 v[48:51], v[16:19], v[128:131], v[48:51]\n\t"
  "v_mfma_f32_16x16x32_bf16 v[52:55], v[16:19], v[132:135], v[52:55]\n\t"
  "ds_read_b128 v[16:19], v1 offset:17408\n\t"
  "s_waitcnt lgkmcnt(2)\n\t"
  "v_mfma_f32_16x16x32_bf16 v[32:35], v[8:11], v[136:139], v[32:35]\n\t"
  "v_mfma_f32_16x16x32_bf16 v[36:39], v[8:11], v[140:143], v[36:39]\n\t"
  "v_mfma_f32_16x16x32_bf16 v[40:43], v[8:11], v[144:147], v[40:43]\n\t"
  "v_mfma_f32_16x16x32_bf16 v[44:47], v[8:11], v[148:151], v[44:47]\n\t"
  "v_mfma_f32_16x16x32_bf16 v[48:51], v[8:11], v[152:155], v[48:51]\n\t"
  "v_mfma_f32_16x16x32_bf16 v[52:55], v[8:11], v[156:159], v[52:55]\n\t"
  "ds_read_b128 v[8:11], v1 offset:18432\n\t"
  "s_waitcnt lgkmcnt(2)\n\t"
  "v_mfma_f32_16x16x32_bf16 v[32:35], v[12:15], v[96:99], v[32:35]\n\t"
  "v_mfma_f32_16x16x32_bf16 v[36:39], v[12:15], v[100:103], v[36:39]\n\t"
  "s_mov_b32 s58, 0\n\t"
  "v_readlane_b32 s69, v4, s56\n\t"
  "v_mfma_f32_16x16x32_bf16 v[40:43], v[12:15], v[104:107], v[40:43]\n\t"
  "v_mfma_f32_16x16x32_bf16 v[44:47], v[12:15], v[108:111], v[44:47]\n\t"
  "v_mfma_f32_16x16x32_bf16 v[48:51], v[12:15], v[112:115], v[48:51]\n\t"
  "v_mfma_f32_16x16x32_bf16 v[52:55], v[12:15], v[116:119], v[52:55]\n\t"
  "ds_read_b128 v[12:15], v1 offset:19456\n\t"
  "s_waitcnt lgkmcnt(2)\n\t"
  "v_mfma_f32_16x16x32_bf16 v[32:35], v[16:19], v[120:123], v[32:35]\n\t"
  "v_mfma_f32_16x16x32_bf16 v[36:39], v[16:19], v[124:127], v[36:39]\n\t"
  "v_mfma_f32_16x16x32_bf16 v[40:43], v[16:19], v[80:83], v[40:43]\n\t"
  "v_mfma_f32_16x16x32_bf16 v[44:47], v[16:19], v[84:87], v[44:47]\n\t"
  "v_mfma_f32_16x16x32_bf16 v[48:51], v[16:19], v[88:91], v[48:51]\n\t"
  "v_mfma_f32_16x16x32_bf16 v[52:55], v[16:19], v[92:95], v[52:55]\n\t"
  "ds_read_b128 v[16:19], v1 offset:20480\n\t"
  "s_waitcnt lgkmcnt(2)\n\t"
  "v_mfma_f32_16x16x32_bf16 v[56:59], v[8:11], a[0:3], 0\n\t"
  "buffer_load_dword v3, v2, s[36:39], 0 offen sc1\n\t"
  "v_mfma_f32_16x16x32_bf16 v[60:63], v[8:11], a[4:7], 0\n\t"
  "v_cvt_pk_bf16_f32 v20, v32, v33\n\t"
  "v_cvt_pk_bf16_f32 v21, v34, v35\n\t"
  "v_mfma_f32_16x16x32_bf16 v[64:67], v[8:11], a[8:11], 0\n\t"
  "v_cvt_pk_bf16_f32 v22, v36, v37\n\t"
  "v_cvt_pk_bf16_f32 v23, v38, v39\n\t"
  "v_mfma_f32_16x16x32_bf16 v[68:71], v[8:11], a[12:15], 0\n\t"
  "v_cvt_pk_bf16_f32 v24, v40, v41\n\t"
  "v_cvt_pk_bf16_f32 v25, v42, v43\n\t"
  "v_mfma_f32_16x16x32_bf16 v[72:75], v[8:11], a[16:19], 0\n\t"
  "v_cvt_pk_bf16_f32 v26, v44, v45\n\t"
  "v_cvt_pk_bf16_f32 v27, v46, v47\n\t"
  "v_mfma_f32_16x16x32_bf16 v[76:79], v[8:11], a[20:23], 0\n\t"
  "ds_read_b128 v[8:11], v1 offset:21504\n\t"
  "s_waitcnt lgkmcnt(2)\n\t"
  "v_mfma_f32_16x16x32_bf16 v[56:59], v[12:15], a[24:27], v[56:59]\n\t"
  "s_sub_u32 s71, s71, s74\n\t"
  "s_sub_u32 s74, 0, s74\n\t"
  "v_readlane_b32 s68, v4, s57\n\t"
  "v_mfma_f32_16x16x32_bf16 v[60:63], v[12:15], a[28:31], v[60:63]\n\t"
  "v_cvt_pk_bf16_f32 v28, v48, v49\n\t"
  "v_cvt_pk_bf16_f32 v29, v50, v51\n\t"
  "v_mfma_f32_16x16x32_bf16 v[64:67], v[12:15], a[32:35], v[64:67]\n\t"
  "v_cvt_pk_bf16_f32 v30, v52, v53\n\t"
  "v_cvt_pk_bf16_f32 v31, v54, v55\n\t"
  "v_mfma_f32_16x16x32_bf16 v[68:71], v[12:15], a[36:39], v[68:71]\n\t"
  "v_mfma_f32_16x16x32_bf16 v[72:75], v[12:15], a[40:43], v[72:75]\n\t"
  "v_mfma_f32_16x16x32_bf16 v[76:79], v[12:15], a[44:47], v[76:79]\n\t"
  "ds_read_b128 v[12:15], v1 offset:22528\n\t"
  "s_waitcnt lgkmcnt(2)\n\t"
  "v_mfma_f32_16x16x32_bf16 v[56:59], v[16:19], a[48:51], v[56:59]\n\t"
  "v_mfma_f32_16x16x32_bf16 v[60:63], v[16:19], a[52:55], v[60:63]\n\t"
  "v_mfma_f32_16x16x32_bf16 v[64:67], v[16:19], a[56:59], v[64:67]\n\t"
  "v_mfma_f32_16x16x32_bf16 v[68:71], v[16:19], a[60:63], v[68:71]\n\t"
  "v_mfma_f32_16x16x32_bf16 v[72:75], v[16:19], a[64:67], v[72:75]\n\t"
  "v_mfma_f32_16x16x32_bf16 v[76:79], v[16:19], a[68:71], v[76:79]\n\t"
  "ds_read_b128 v[16:19], v1 offset:23552\n\t"
  "s_waitcnt lgkmcnt(2)\n\t"
  "v_mfma_f32_16x16x32_bf16 v[56:59], v[8:11], a[72:75], v[56:59]\n\t"
  "v_mfma_f32_16x16x32_bf16 v[60:63], v[8:11], a[76:79], v[60:63]\n\t"
  "v_mfma_f32_16x16x32_bf16 v[64:67], v[8:11], a[80:83], v[64:67]\n\t"
  "v_mfma_f32_16x16x32_bf16 v[68:71], v[8:11], a[84:87], v[68:71]\n\t"
  "v_mfma_f32_16x16x32_bf16 v[72:75], v[8:11], a[88:91], v[72:75]\n\t"
  "v_mfma_f32_16x16x32_bf16 v[76:79], v[8:11], a[92:95], v[76:79]\n\t"
  "ds_read_b128 v[8:11], v1 offset:24576\n\t"
  "s_waitcnt lgkmcnt(2)\n\t"
  "v_mfma_f32_16x16x32_bf16 v[56:59], v[12:15], a[96:99], v[56:59]\n\t"
  "v_cmp_le_i32 vcc, s57, v3\n\t"
  "v_mfma_f32_16x16x32_bf16 v[60:63], v[12:15], a[100:103], v[60:63]\n\t"
  "s_cmp_eq_u64 vcc, -1\n\t"
  "s_cselect_b32 s75, 1, 0\n\t"
  "s_mov_b32 s59, s75\n\t"
  "v_mfma_f32_16x16x32_bf16 v[64:67], v[12:15], a[104:107], v[64:67]\n\t"
  "s_andn2_b32 s75, s75, s58\n\t"
  "s_cselect_b32 s62, s51, 0\n\t"
  "s_cselect_b32 s72, s71, 155648\n\t"
  "v_mfma_f32_16x16x32_bf16 v[68:71], v[12:15], a[108:111], v[68:71]\n\t"
  "s_lshl_b32 s73, s75, 13\n\t"
  "s_or_b32 s58, s58, s75\n\t"
  "v_mfma_f32_16x16x32_bf16 v[72:75], v[12:15], a[112:115], v[72:75]\n\t"
  "s_mov_b32 m0, s72\n\t"
  "s_add_u32 s76, s68, 0\n\t"
  "buffer_load_dwordx4 v0, s[60:63], s76 offen sc1 lds\n\t"
  "v_mfma_f32_16x16x32_bf16 v[76:79], v[12:15], a[116:119], v[76:79]\n\t"
  "ds_read_b128 v[12:15], v1 offset:25600\n\t"
  "s_waitcnt lgkmcnt(2)\n\t"
  "v_mfma_f32_16x16x32_bf16 v[56:59], v[16:19], a[120:123], v[56:59]\n\t"
  "v_mfma_f32_16x16x32_bf16 v[60:63], v[16:19], a[124:127], v[60:63]\n\t"
  "v_mfma_f32_16x16x32_bf16 v[64:67], v[16:19], a[128:131], v[64:67]\n\t"
  "v_mfma_f32_16x16x32_bf16 v[68:71], v[16:19], a[132:135], v[68:71]\n\t"
  "v_mfma_f32_16x16x32_bf16 v[72:75], v[16:19], a[136:139], v[72:75]\n\t"
  "s_add_u32 s72, s72, s73\n\t"
  "buffer_load_dwordx4 v0, s[60:63], s76 offen offset:1024 sc1 lds\n\t"
  "v_mfma_f32_16x16x32_bf16 v[76:79], v[16:19], a[140:143], v[76:79]\n\t"
  "ds_read_b128 v[16:19], v1 offset:26624\n\t"
  "s_waitcnt lgkmcnt(2)\n\t"
  "v_mfma_f32_16x16x32_bf16 v[56:59], v[8:11], a[144:147], v[56:59]\n\t"
  "v_mfma_f32_16x16x32_bf16 v[60:63], v[8:11], a[148:151], v[60:63]\n\t"
  "v_mfma_f32_16x16x32_bf16 v[64:67], v[8:11], a[152:155], v[64:67]\n\t"
  "v_mfma_f32_16x16x32_bf16 v[68:71], v[8:11], a[156:159], v[68:71]\n\t"
  "v_mfma_f32_16x16x32_bf16 v[72:75], v[8:11], a[160:163], v[72:75]\n\t"
  "s_mov_b32 m0, s72\n\t"
  "s_add_u32 s76, s68, 16384\n\t"
  "buffer_load_dwordx4 v0, s[60:63], s76 offen sc1 lds\n\t"
  "v_mfma_f32_16x16x32_bf16 v[76:79], v[8:11], a[164:167], v[76:79]\n\t"
  "ds_read_b128 v[8:11], v1 offset:27648\n\t"
  "s_waitcnt lgkmcnt(2)\n\t"
  "v_mfma_f32_16x16x32_bf16 v[56:59], v[12:15], a[168:171], v[56:59]\n\t"
  "v_mfma_f32_16x16x32_bf16 v[60:63], v[12:15], a[172:175], v[60:63]\n\t"
  "v_mfma_f32_16x16x32_bf16 v[64:67], v[12:15], a[176:179], v[64:67]\n\t"
  "v_mfma_f32_16x16x32_bf16 v[68:71], v[12:15], a[180:183], v[68:71]\n\t"
  "v_mfma_f32_16x16x32_bf16 v[72:75], v[12:15], a[184:187], v[72:75]\n\t"
  "s_add_u32 s72, s72, s73\n\t"
  "buffer_load_dwordx4 v0, s[60:63], s76 offen offset:1024 sc1 lds\n\t"
  "v_mfma_f32_16x16x32_bf16 v[76:79], v[12:15], a[188:191], v[76:79]\n\t"
  "ds_read_b128 v[12:15], v1 offset:28672\n\t"
  "s_waitcnt lgkmcnt(2)\n\t"
  "v_mfma_f32_16x16x32_bf16 v[56:59], v[16:19], a[192:195], v[56:59]\n\t"
  "v_mfma_f32_16x16x32_bf16 v[60:63], v[16:19], a[196:199], v[60:63]\n\t"
  "v_mfma_f32_16x16x32_bf16 v[64:67], v[16:19], a[200:203], v[64:67]\n\t"
  "v_mfma_f32_16x16x32_bf16 v[68:71], v[16:19], a[204:207], v[68:71]\n\t"
  "v_mfma_f32_16x16x32_bf16 v[72:75], v[16:19], a[208:211], v[72:75]\n\t"
  "buffer_load_dword v3, v2, s[36:39], 0 offen sc1\n\t"
  "v_mfma_f32_16x16x32_bf16 v[76:79], v[16:19], a[212:215], v[76:79]\n\t"
  "ds_read_b128 v[16:19], v1 offset:29696\n\t"
  "s_waitcnt lgkmcnt(2)\n\t"
  "v_mfma_f32_16x16x32_bf16 v[56:59], v[8:11], a[216:219], v[56:59]\n\t"
  "v_mfma_f32_16x16x32_bf16 v[60:63], v[8:11], a[220:223], v[60:63]\n\t"
  "v_mfma_f32_16x16x32_bf16 v[64:67], v[8:11], a[224:227], v[64:67]\n\t"
  "v_mfma_f32_16x16x32_bf16 v[68:71], v[8:11], a[228:231], v[68:71]\n\t"
  "v_mfma_f32_16x16x32_bf16 v[72:75], v[8:11], a[232:235], v[72:75]\n\t"
  "v_mfma_f32_16x16x32_bf16 v[76:79], v[8:11], a[236:239], v[76:79]\n\t"
  "ds_read_b128 v[8:11], v1 offset:30720\n\t"
  "s_waitcnt lgkmcnt(2)\n\t"
  "v_mfma_f32_16x16x32_bf16 v[56:59], v[12:15], a[240:243], v[56:59]\n\t"
  "v_mfma_f32_16x16x32_bf16 v[60:63], v[12:15], a[244:247], v[60:63]\n\t"
  "v_mfma_f32_16x16x32_bf16 v[64:67], v[12:15], a[248:251], v[64:67]\n\t"
  "v_mfma_f32_16x16x32_bf16 v[68:71], v[12:15], a[252:255], v[68:71]\n\t"
  "v_mfma_f32_16x16x32_bf16 v[72:75], v[12:15], v[224:227], v[72:75]\n\t"
  "v_mfma_f32_16x16x32_bf16 v[76:79], v[12:15], v[228:231], v[76:79]\n\t"
  "ds_read_b128 v[12:15], v1 offset:31744\n\t"
  "s_waitcnt lgkmcnt(2)\n\t"
  "v_mfma_f32_16x16x32_bf16 v[56:59], v[16:19], v[232:235], v[56:59]\n\t"
  "v_mfma_f32_16x16x32_bf16 v[60:63], v[16:19], v[236:239], v[60:63]\n\t"
  "v_mfma_f32_16x16x32_bf16 v[64:67], v[16:19], v[240:243], v[64:67]\n\t"
  "v_mfma_f32_16x16x32_bf16 v[68:71], v[16:19], v[244:247], v[68:71]\n\t"
  "v_mfma_f32_16x16x32_bf16 v[72:75], v[16:19], v[248:251], v[72:75]\n\t"
  "v_mfma_f32_16x16x32_bf16 v[76:79], v[16:19], v[252:255], v[76:79]\n\t"
  "ds_read_b128 v[16:19], v1 offset:32768\n\t"
  "s_waitcnt lgkmcnt(2)\n\t"
  "v_mfma_f32_16x16x32_bf16 v[56:59], v[8:11], v[192:195], v[56:59]\n\t"
  "v_cmp_le_i32 vcc, s57, v3\n\t"
  "v_mfma_f32_16x16x32_bf16 v[60:63], v[8:11], v[196:199], v[60:63]\n\t"
  "s_cmp_eq_u64 vcc, -1\n\t"
  "s_cselect_b32 s75, 1, 0\n\t"
  "v_mfma_f32_16x16x32_bf16 v[64:67], v[8:11], v[200:203], v[64:67]\n\t"
  "s_andn2_b32 s75, s75, s58\n\t"
  "s_cselect_b32 s62, s51, 0\n\t"
  "s_cselect_b32 s72, s71, 155648\n\t"
  "v_mfma_f32_16x16x32_bf16 v[68:71], v[8:11], v[204:207], v[68:71]\n\t"
  "s_lshl_b32 s73, s75, 13\n\t"
  "s_or_b32 s58, s58, s75\n\t"
  "v_mfma_f32_16x16x32_bf16 v[72:75], v[8:11], v[208:211], v[72:75]\n\t"
  "s_mov_b32 m0, s72\n\t"
  "s_add_u32 s76, s68, 0\n\t"
  "buffer_load_dwordx4 v0, s[60:63], s76 offen sc1 lds\n\t"
  "v_mfma_f32_16x16x32_bf16 v[76:79], v[8:11], v[212:215], v[76:79]\n\t"
  "ds_read_b128 v[8:11], v1 offset:33792\n\t"
  "s_waitcnt lgkmcnt(2)\n\t"
  "v_mfma_f32_16x16x32_bf16 v[56:59], v[12:15], v[216:219], v[56:59]\n\t"
  "v_mfma_f32_16x16x32_bf16 v[60:63], v[12:15], v[220:223], v[60:63]\n\t"
  "v_mfma_f32_16x16x32_bf16 v[64:67], v[12:15], v[160:163], v[64:67]\n\t"
  "v_mfma_f32_16x16x32_bf16 v[68:71], v[12:15], v[164:167], v[68:71]\n\t"
  "v_mfma_f32_16x16x32_bf16 v[72:75], v[12:15], v[168:171], v[72:75]\n\t"
  "s_add_u32 s72, s72, s73\n\t"
  "buffer_load_dwordx4 v0, s[60:63], s76 offen offset:1024 sc1 lds\n\t"
  "v_mfma_f32_16x16x32_bf16 v[76:79], v[12:15], v[172:175], v[76:79]\n\t"
  "ds_read_b128 v[12:15], v1 offset:34816\n\t"
  "s_waitcnt lgkmcnt(2)\n\t"
  "v_mfma_f32_16x16x32_bf16 v[56:59], v[16:19], v[176:179], v[56:59]\n\t"
  "v_mfma_f32_16x16x32_bf16 v[60:63], v[16:19], v[180:183], v[60:63]\n\t"
  "v_mfma_f32_16x16x32_bf16 v[64:67], v[16:19], v[184:187], v[64:67]\n\t"
  "v_mfma_f32_16x16x32_bf16 v[68:71], v[16:19], v[188:191], v[68:71]\n\t"
  "v_mfma_f32_16x16x32_bf16 v[72:75], v[16:19], v[128:131], v[72:75]\n\t"
  "buffer_load_dword v3, v2, s[36:39], 0 offen sc1\n\t"
  "v_mfma_f32_16x16x32_bf16 v[76:79], v[16:19], v[132:135], v[76:79]\n\t"
  "ds_read_b128 v[16:19], v1 offset:35840\n\t"
  "s_waitcnt lgkmcnt(2)\n\t"
  "v_mfma_f32_16x16x32_bf16 v[56:59], v[8:11], v[136:139], v[56:59]\n\t"
  "v_add_u32 v1, s74, v1\n\t"
  "v_mfma_f32_16x16x32_bf16 v[60:63], v[8:11], v[140:143], v[60:63]\n\t"
  "v_mfma_f32_16x16x32_bf16 v[64:67], v[8:11], v[144:147], v[64:67]\n\t"
  "v_mfma_f32_16x16x32_bf16 v[68:71], v[8:11], v[148:151], v[68:71]\n\t"
  "v_mfma_f32_16x16x32_bf16 v[72:75], v[8:11], v[152:155], v[72:75]\n\t"
  "s_mov_b32 m0, s72\n\t"
  "s_add_u32 s76, s68, 16384\n\t"
  "buffer_load_dwordx4 v0, s[60:63], s76 offen sc1 lds\n\t"
  "v_mfma_f32_16x16x32_bf16 v[76:79], v[8:11], v[156:159], v[76:79]\n\t"
  "s_waitcnt lgkmcnt(1)\n\t"
  "v_mfma_f32_16x16x32_bf16 v[56:59], v[12:15], v[96:99], v[56:59]\n\t"
  "v_mfma_f32_16x16x32_bf16 v[60:63], v[12:15], v[100:103], v[60:63]\n\t"
  "s_lshl_b32 s52, s58, 7\n\t"
  "s_add_u32 s52, s52, s57\n\t"
  "v_mfma_f32_16x16x32_bf16 v[64:67], v[12:15], v[104:107], v[64:67]\n\t"
  "v_mfma_f32_16x16x32_bf16 v[68:71], v[12:15], v[108:111], v[68:71]\n\t"
  "v_mfma_f32_16x16x32_bf16 v[72:75], v[12:15], v[112:115], v[72:75]\n\t"
  "s_add_u32 s72, s72, s73\n\t"
  "buffer_load_dwordx4 v0, s[60:63], s76 offen offset:1024 sc1 lds\n\t"
  "v_mfma_f32_16x16x32_bf16 v[76:79], v[12:15], v[116:119], v[76:79]\n\t"
  "s_waitcnt lgkmcnt(0)\n\t"
  "v_mfma_f32_16x16x32_bf16 v[56:59], v[16:19], v[120:123], v[56:59]\n\t"
  "v_mfma_f32_16x16x32_bf16 v[60:63], v[16:19], v[124:127], v[60:63]\n\t"
  "v_mfma_f32_16x16x32_bf16 v[64:67], v[16:19], v[80:83], v[64:67]\n\t"
  "v_mfma_f32_16x16x32_bf16 v[68:71], v[16:19], v[84:87], v[68:71]\n\t"
  "v_mfma_f32_16x16x32_bf16 v[72:75], v[16:19], v[88:91], v[72:75]\n\t"
  "v_mfma_f32_16x16x32_bf16 v[76:79], v[16:19], v[92:95], v[76:79]\n\t"
  "s_nop 0\n\t"
  "v_cmp_le_i32 vcc, s52, v3\n\t"
  "s_cmp_eq_u64 vcc, -1\n\t"
  "s_cbranch_scc1 80f\n\t"
  "70:\n\t"
  "s_nop 3\n\t"
  "v_cvt_pk_bf16_f32 v59, v58, v59\n\t"
  "v_cvt_pk_bf16_f32 v58, v56, v57\n\t"
  "v_mov_b64 v[56:57], v[20:21]\n\t"
  "s_add_u32 s77, s69, s44\n\t"
  "buffer_store_dwordx4 v[56:59], v0, s[36:39], s77 offen sc1\n\t"
  "v_cvt_pk_bf16_f32 v63, v62, v63\n\t"
  "v_cvt_pk_bf16_f32 v62, v60, v61\n\t"
  "v_mov_b64 v[60:61], v[22:23]\n\t"
  "s_add_u32 s77, s69, s45\n\t"
  "buffer_store_dwordx4 v[60:63], v0, s[36:39], s77 offen sc1\n\t"
  "v_cvt_pk_bf16_f32 v67, v66, v67\n\t"
  "v_cvt_pk_bf16_f32 v66, v64, v65\n\t"
  "v_mov_b64 v[64:65], v[24:25]\n\t"
  "s_add_u32 s77, s69, s46\n\t"
  "buffer_store_dwordx4 v[64:67], v0, s[36:39], s77 offen sc1\n\t"
  "v_cvt_pk_bf16_f32 v71, v70, v71\n\t"
  "v_cvt_pk_bf16_f32 v70, v68, v69\n\t"
  "v_mov_b64 v[68:69], v[26:27]\n\t"
  "s_add_u32 s77, s69, s47\n\t"
  "buffer_store_dwordx4 v[68:71], v0, s[36:39], s77 offen sc1\n\t"
  "v_cvt_pk_bf16_f32 v75, v74, v75\n\t"
  "v_cvt_pk_bf16_f32 v74, v72, v73\n\t"
  "v_mov_b64 v[72:73], v[28:29]\n\t"
  "s_add_u32 s77, s69, s48\n\t"
  "buffer_store_dwordx4 v[72:75], v0, s[36:39], s77 offen sc1\n\t"
  "v_cvt_pk_bf16_f32 v79, v78, v79\n\t"
  "v_cvt_pk_bf16_f32 v78, v76, v77\n\t"
  "v_mov_b64 v[76:77], v[30:31]\n\t"
  "s_add_u32 s77, s69, s49\n\t"
  "buffer_store_dwordx4 v[76:79], v0, s[36:39], s77 offen sc1\n\t"
  "s_mov_b32 s86, s51\n\t"
  "s_mov_b32 s66, 0\n\t"
  "s_cmp_lg_u32 s58, 0\n\t"
  "s_cbranch_scc1 5f\n\t"
  "v_cmp_le_i32 vcc, s57, v3\n\t"
  "s_cmp_eq_u64 vcc, -1\n\t"
  "s_cbranch_scc1 41f\n\t"
  "buffer_load_dword v3, v2, s[36:39], 0 offen sc1\n\t"
  "s_sleep 2\n\t"
  "v_cmp_le_i32 vcc, s57, v3\n\t"
  "s_cmp_eq_u64 vcc, -1\n\t"
  "s_cbranch_scc1 41f\n\t"
  "buffer_load_dword v3, v2, s[36:39], 0 offen sc1\n\t"
  "s_sleep 2\n\t"
  "v_cmp_le_i32 vcc, s57, v3\n\t"
  "s_cmp_eq_u64 vcc, -1\n\t"
  "s_cbranch_scc1 41f\n\t"
  "buffer_load_dword v3, v2, s[36:39], 0 offen sc1\n\t"
  "s_sleep 2\n\t"
  "v_cmp_le_i32 vcc, s57, v3\n\t"
  "s_cmp_eq_u64 vcc, -1\n\t"
  "s_cbranch_scc1 41f\n\t"
  "buffer_load_dword v3, v2, s[36:39], 0 offen sc1\n\t"
  "s_sleep 2\n\t"
  "s_mov_b32 s86, 0\n\t"
  "v_mov_b32 v32, s56\n\t"
  "s_waitcnt vmcnt(4)\n\t"
  "buffer_store_dword v32, off, s[36:39], s40 sc1\n\t"
  "s_cmp_eq_u32 s56, 63\n\t"
  "s_cbranch_scc1 9f\n\t"
  "3:\n\t"
  "s_mov_b32 s79, 0\n\t"
  "s_mov_b32 s66, 0\n\t"
  "4:\n\t"
  "v_cmp_le_i32 vcc, s57, v3\n\t"
  "s_cmp_eq_u64 vcc, -1\n\t"
  "s_cbranch_scc1 41f\n\t"
  "s_waitcnt vmcnt(8)\n\t"
  "buffer_load_dword v3, v2, s[36:39], 0 offen sc1\n\t"
  "s_sleep 4\n\t"
  "s_add_u32 s79, s79, 1\n\t"
  "s_cmp_lt_u32 s79, s80\n\t"
  "s_cbranch_scc1 4b\n\t"
  "s_mov_b32 s80, 0\n\t"
  "41:\n\t"
  "s_add_u32 m0, s71, 0\n\t"
  "s_add_u32 s76, s68, 0\n\t"
  "buffer_load_dwordx4 v0, s[36:39], s76 offen sc1 lds\n\t"
  "buffer_load_dwordx4 v0, s[36:39], s76 offen offset:1024 sc1 lds\n\t"
  "s_add_u32 m0, s71, 8192\n\t"
  "s_add_u32 s76, s68, 16384\n\t"
  "buffer_load_dwordx4 v0, s[36:39], s76 offen sc1 lds\n\t"
  "buffer_load_dwordx4 v0, s[36:39], s76 offen offset:1024 sc1 lds\n\t"
  "v_mov_b32 v32, s56\n\t"
  "buffer_store_dword v32, off, s[64:67], s40 sc1\n\t"
  "buffer_store_dword v32, off, s[64:67], s40 sc1\n\t"
  "buffer_store_dword v32, off, s[64:67], s40 sc1\n\t"
  "buffer_store_dword v32, off, s[64:67], s40 sc1\n\t"
  "buffer_store_dword v32, off, s[64:67], s40 sc1\n\t"
  "s_waitcnt vmcnt(9)\n\t"
  "buffer_store_dword v32, off, s[84:87], s40 sc1\n\t"
  "s_mov_b32 s86, 0\n\t"
  "s_waitcnt vmcnt(6)\n\t"
  "s_branch 6f\n\t"
  "5:\n\t"
  "s_waitcnt vmcnt(6)\n\t"
  "6:\n\t"
  "s_waitcnt lgkmcnt(0)\n\t"
  "s_barrier\n\t"
  "ds_read_b128 v[8:11], v1 offset:0\n\t"
  "ds_read_b128 v[12:15], v1 offset:1024\n\t"
  "s_branch 1b\n\t"
  "80:\n\t"
  "s_add_u32 m0, s71, 0\n\t"
  "s_add_u32 s76, s68, 0\n\t"
  "buffer_load_dwordx4 v0, s[36:39], s76 offen sc1 lds\n\t"
  "buffer_load_dwordx4 v0, s[36:39], s76 offen offset:1024 sc1 lds\n\t"
  "s_add_u32 m0, s71, 8192\n\t"
  "s_add_u32 s76, s68, 16384\n\t"
  "buffer_load_dwordx4 v0, s[36:39], s76 offen sc1 lds\n\t"
  "buffer_load_dwordx4 v0, s[36:39], s76 offen offset:1024 sc1 lds\n\t"
  "s_mov_b32 s58, 1\n\t"
  "s_mov_b32 s52, 127\n\t"
  "s_branch 70b\n\t"
  "9:\n\t"
  ".Lws_end_V:\n\t"
  "s_waitcnt vmcnt(0)\n\t"
  "s_waitcnt vmcnt(0) lgkmcnt(0)\n\t"
    : "+{v[0:7]}"(lc), "+{v[8:23]}"(so) :: CLOB_ALL);
  }
}

template <int KS> __device__ __forceinline__ void role_proj(Ctx& c, const void* wgt) {
  const int j = c.idx - (KS == 32 ? R_B : R_D),
    kc = j < 9 ? j % 3 : 3, nc = j < 9 ? j / 3 : j - 9;
  const int src_idx = KS == 32 ? R_QA + 4 * kc : R_C + 7 * kc;
  v16u so = stage_offsets<3>(c, DM * 2, kc * KS * 32, nc * 384);
  constexpr int NSRC = KS == 32 ? 4 : 8;
  const int tk = c.lane & 15, g = c.lane >> 4, t0 = nc * 12 + c.w * 3;
  v8u lc = {(unsigned)(c.lane * 16), (unsigned)(c.lane * 16), (unsigned)flag_at(c.layer, src_idx + min(c.lane >> 2, NSRC - 1), c.lane & 3), 0u,
            (unsigned)((KS == 32 ? base_a(c, c.lane) + A_OF : base_b(c, c.lane) + B_HF) + kc * sp(2 * KS * 1024) + c.w * 2048),
            (unsigned)(KS == 32 ? base_c(c, c.lane) + C_P1 : base_a(c, c.lane) + A_OF),
            (unsigned)((tk >> 1) * sp(24 * 1024) + kc * 256 + g * 32 + (tk & 1) * 16), (unsigned)(c.lane * 16 + KS * 1024)};
  lc[3] = stage_scalars(c, wgt, KS * 128 * DM * 2, DM * 2, 0, 0, part_tile(t0), part_tile(t0 + 1), part_tile(t0 + 2), 0, 0, 0);
  if constexpr (KS == 32) {
    asm volatile(
  "s_nop 4\n\t"
  "v_readlane_b32 s36, v3, 0\n\t"
  "v_readlane_b32 s37, v3, 1\n\t"
  "v_readlane_b32 s38, v3, 2\n\t"
  "v_readlane_b32 s39, v3, 3\n\t"
  "v_readlane_b32 s40, v3, 4\n\t"
  "v_readlane_b32 s41, v3, 5\n\t"
  "v_readlane_b32 s42, v3, 6\n\t"
  "v_readlane_b32 s43, v3, 7\n\t"
  "v_readlane_b32 s44, v3, 8\n\t"
  "v_readlane_b32 s45, v3, 9\n\t"
  "v_readlane_b32 s46, v3, 10\n\t"
  "v_readlane_b32 s47, v3, 11\n\t"
  "v_readlane_b32 s48, v3, 12\n\t"
  "v_readlane_b32 s49, v3, 13\n\t"
  "v_readlane_b32 s50, v3, 14\n\t"
  "v_readlane_b32 s51, v3, 15\n\t"
  "s_nop 4\n\t"
  "v_readlane_b32 s56, v3, 16\n\t"
  "v_readlane_b32 s57, v3, 17\n\t"
  "v_readlane_b32 s58, v3, 18\n\t"
  "v_readlane_b32 s60, v3, 19\n\t"
  "s_mov_b32 s59, s39\n\t"
  "s_mul_i32 s61, s41, 3\n\t"
  "s_lshr_b32 s61, s61, 1\n\t"
  "s_mov_b32 s62, 0\n\t"
  "s_nop 2\n\t"
  "s_add_u32 m0, s61, 0\n\t"
  "s_nop 0\n\t"
  "buffer_load_dwordx4 v8, s[56:59], s62 offen lds\n\t"
  "s_add_u32 m0, s61, 1024\n\t"
  "s_nop 0\n\t"
  "buffer_load_dwordx4 v9, s[56:59], s62 offen lds\n\t"
  "s_add_u32 m0, s61, 2048\n\t"
  "s_nop 0\n\t"
  "buffer_load_dwordx4 v10, s[56:59], s62 offen lds\n\t"
  "s_add_u32 s62, s62, s60\n\t"
  "s_add_u32 m0, s61, 12288\n\t"
  "s_nop 0\n\t"
  "buffer_load_dwordx4 v8, s[56:59], s62 offen lds\n\t"
  "s_add_u32 m0, s61, 13312\n\t"
  "s_nop 0\n\t"
  "buffer_load_dwordx4 v9, s[56:59], s62 offen lds\n\t"
  "s_add_u32 m0, s61, 14336\n\t"
  "s_nop 0\n\t"
  "buffer_load_dwordx4 v10, s[56:59], s62 offen lds\n\t"
  "s_add_u32 s62, s62, s60\n\t"
  "s_add_u32 m0, s61, 24576\n\t"
  "s_nop 0\n\t"
  "buffer_load_dwordx4 v8, s[56:59], s62 offen lds\n\t"
  "s_add_u32 m0, s61, 25600\n\t"
  "s_nop 0\n\t"
  "buffer_load_dwordx4 v9, s[56:59], s62 offen lds\n\t"
  "s_add_u32 m0, s61, 26624\n\t"
  "s_nop 0\n\t"
  "buffer_load_dwordx4 v10, s[56:59], s62 offen lds\n\t"
  "s_add_u32 s62, s62, s60\n\t"
  "s_add_u32 m0, s61, 36864\n\t"
  "s_nop 0\n\t"
  "buffer_load_dwordx4 v8, s[56:59], s62 offen lds\n\t"
  "s_add_u32 m0, s61, 37888\n\t"
  "s_nop 0\n\t"
  "buffer_load_dwordx4 v9, s[56:59], s62 offen lds\n\t"
  "s_add_u32 m0, s61, 38912\n\t"
  "s_nop 0\n\t"
  "buffer_load_dwordx4 v10, s[56:59], s62 offen lds\n\t"
  "s_add_u32 s62, s62, s60\n\t"
  "s_add_u32 m0, s61, 49152\n\t"
  "s_nop 0\n\t"
  "buffer_load_dwordx4 v8, s[56:59], s62 offen lds\n\t"
  "s_add_u32 m0, s61, 50176\n\t"
  "s_nop 0\n\t"
  "buffer_load_dwordx4 v9, s[56:59], s62 offen lds\n\t"
  "s_add_u32 m0, s61, 51200\n\t"
  "s_nop 0\n\t"
  "buffer_load_dwordx4 v10, s[56:59], s62 offen lds\n\t"
  "s_add_u32 s62, s62, s60\n\t"
  "s_add_u32 m0, s61, 61440\n\t"
  "s_nop 0\n\t"
  "buffer_load_dwordx4 v8, s[56:59], s62 offen lds\n\t"
  "s_add_u32 m0, s61, 62464\n\t"
  "s_nop 0\n\t"
  "buffer_load_dwordx4 v9, s[56:59], s62 offen lds\n\t"
  "s_add_u32 m0, s61, 63488\n\t"
  "s_nop 0\n\t"
  "buffer_load_dwordx4 v10, s[56:59], s62 offen lds\n\t"
  "s_add_u32 s62, s62, s60\n\t"
  "s_add_u32 m0, s61, 73728\n\t"
  "s_nop 0\n\t"
  "buffer_load_dwordx4 v8, s[56:59], s62 offen lds\n\t"
  "s_add_u32 m0, s61, 74752\n\t"
  "s_nop 0\n\t"
  "buffer_load_dwordx4 v9, s[56:59], s62 offen lds\n\t"
  "s_add_u32 m0, s61, 75776\n\t"
  "s_nop 0\n\t"
  "buffer_load_dwordx4 v10, s[56:59], s62 offen lds\n\t"
  "s_add_u32 s62, s62, s60\n\t"
  "s_add_u32 m0, s61, 86016\n\t"
  "s_nop 0\n\t"
  "buffer_load_dwordx4 v8, s[56:59], s62 offen lds\n\t"
  "s_add_u32 m0, s61, 87040\n\t"
  "s_nop 0\n\t"
  "buffer_load_dwordx4 v9, s[56:59], s62 offen lds\n\t"
  "s_add_u32 m0, s61, 88064\n\t"
  "s_nop 0\n\t"
  "buffer_load_dwordx4 v10, s[56:59], s62 offen lds\n\t"
  "s_add_u32 s62, s62, s60\n\t"
  "s_add_u32 m0, s61, 98304\n\t"
  "s_nop 0\n\t"
  "buffer_load_dwordx4 v8, s[56:59], s62 offen lds\n\t"
  "s_add_u32 m0, s61, 99328\n\t"
  "s_nop 0\n\t"
  "buffer_load_dwordx4 v9, s[56:59], s62 offen lds\n\t"
  "s_add_u32 m0, s61, 100352\n\t"
  "s_nop 0\n\t"
  "buffer_load_dwordx4 v10, s[56:59], s62 offen lds\n\t"
  "s_add_u32 s62, s62, s60\n\t"
  "s_add_u32 m0, s61, 110592\n\t"
  "s_nop 0\n\t"
  "buffer_load_dwordx4 v8, s[56:59], s62 offen lds\n\t"
  "s_add_u32 m0, s61, 111616\n\t"
  "s_nop 0\n\t"
  "buffer_load_dwordx4 v9, s[56:59], s62 offen lds\n\t"
  "s_add_u32 m0, s61, 112640\n\t"
  "s_nop 0\n\t"
  "buffer_load_dwordx4 v10, s[56:59], s62 offen lds\n\t"
  "s_add_u32 s62, s62, s60\n\t"
  "s_add_u32 m0, s61, 122880\n\t"
  "s_nop 0\n\t"
  "buffer_load_dwordx4 v8, s[56:59], s62 offen lds\n\t"
  "s_add_u32 m0, s61, 123904\n\t"
  "s_nop 0\n\t"
  "buffer_load_dwordx4 v9, s[56:59], s62 offen lds\n\t"
  "s_add_u32 m0, s61, 124928\n\t"
  "s_nop 0\n\t"
  "buffer_load_dwordx4 v10, s[56:59], s62 offen lds\n\t"
  "s_add_u32 s62, s62, s60\n\t"
  "s_waitcnt vmcnt(30) lgkmcnt(0)\n\t"
  "s_barrier\n\t"
  "s_add_u32 m0, s61, 135168\n\t"
  "s_nop 0\n\t"
  "buffer_load_dwordx4 v8, s[56:59], s62 offen lds\n\t"
  "s_add_u32 m0, s61, 136192\n\t"
  "s_nop 0\n\t"
  "buffer_load_dwordx4 v9, s[56:59], s62 offen lds\n\t"
  "s_add_u32 m0, s61, 137216\n\t"
  "s_nop 0\n\t"
  "buffer_load_dwordx4 v10, s[56:59], s62 offen lds\n\t"
  "s_add_u32 s62, s62, s60\n\t"
  "v_add_u32 v20, 0, v14\n\t"
  "ds_read_b64_tr_b16 a[0:1], v20\n\t"
  "ds_read_b64_tr_b16 a[2:3], v20 offset:6144\n\t"
  "v_add_u32 v20, 0, v15\n\t"
  "ds_read_b64_tr_b16 a[4:5], v20\n\t"
  "ds_read_b64_tr_b16 a[6:7], v20 offset:6144\n\t"
  "v_add_u32 v20, 0, v16\n\t"
  "ds_read_b64_tr_b16 a[8:9], v20\n\t"
  "ds_read_b64_tr_b16 a[10:11], v20 offset:6144\n\t"
  "s_waitcnt vmcnt(30) lgkmcnt(0)\n\t"
  "s_barrier\n\t"
  "s_add_u32 m0, s61, 0\n\t"
  "s_nop 0\n\t"
  "buffer_load_dwordx4 v8, s[56:59], s62 offen lds\n\t"
  "s_add_u32 m0, s61, 1024\n\t"
  "s_nop 0\n\t"
  "buffer_load_dwordx4 v9, s[56:59], s62 offen lds\n\t"
  "s_add_u32 m0, s61, 2048\n\t"
  "s_nop 0\n\t"
  "buffer_load_dwordx4 v10, s[56:59], s62 offen lds\n\t"
  "s_add_u32 s62, s62, s60\n\t"
  "v_add_u32 v20, 12288, v14\n\t"
  "ds_read_b64_tr_b16 a[12:13], v20\n\t"
  "ds_read_b64_tr_b16 a[14:15], v20 offset:6144\n\t"
  "v_add_u32 v20, 12288, v15\n\t"
  "ds_read_b64_tr_b16 a[16:17], v20\n\t"
  "ds_read_b64_tr_b16 a[18:19], v20 offset:6144\n\t"
  "v_add_u32 v20, 12288, v16\n\t"
  "ds_read_b64_tr_b16 a[20:21], v20\n\t"
  "ds_read_b64_tr_b16 a[22:23], v20 offset:6144\n\t"
  "s_waitcnt vmcnt(30) lgkmcnt(0)\n\t"
  "s_barrier\n\t"
  "s_add_u32 m0, s61, 12288\n\t"
  "s_nop 0\n\t"
  "buffer_load_dwordx4 v8, s[56:59], s62 offen lds\n\t"
  "s_add_u32 m0, s61, 13312\n\t"
  "s_nop 0\n\t"
  "buffer_load_dwordx4 v9, s[56:59], s62 offen lds\n\t"
  "s_add_u32 m0, s61, 14336\n\t"
  "s_nop 0\n\t"
  "buffer_load_dwordx4 v10, s[56:59], s62 offen lds\n\t"
  "s_add_u32 s62, s62, s60\n\t"
  "v_add_u32 v20, 24576, v14\n\t"
  "ds_read_b64_tr_b16 a[24:25], v20\n\t"
  "ds_read_b64_tr_b16 a[26:27], v20 offset:6144\n\t"
  "v_add_u32 v20, 24576, v15\n\t"
  "ds_read_b64_tr_b16 a[28:29], v20\n\t"
  "ds_read_b64_tr_b16 a[30:31], v20 offset:6144\n\t"
  "v_add_u32 v20, 24576, v16\n\t"
  "ds_read_b64_tr_b16 a[32:33], v20\n\t"
  "ds_read_b64_tr_b16 a[34:35], v20 offset:6144\n\t"
  "s_waitcnt vmcnt(30) lgkmcnt(0)\n\t"
  "s_barrier\n\t"
  "s_add_u32 m0, s61, 24576\n\t"
  "s_nop 0\n\t"
  "buffer_load_dwordx4 v8, s[56:59], s62 offen lds\n\t"
  "s_add_u32 m0, s61, 25600\n\t"
  "s_nop 0\n\t"
  "buffer_load_dwordx4 v9, s[56:59], s62 offen lds\n\t"
  "s_add_u32 m0, s61, 26624\n\t"
  "s_nop 0\n\t"
  "buffer_load_dwordx4 v10, s[56:59], s62 offen lds\n\t"
  "s_add_u32 s62, s62, s60\n\t"
  "v_add_u32 v20, 36864, v14\n\t"
  "ds_read_b64_tr_b16 a[36:37], v20\n\t"
  "ds_read_b64_tr_b16 a[38:39], v20 offset:6144\n\t"
  "v_add_u32 v20, 36864, v15\n\t"
  "ds_read_b64_tr_b16 a[40:41], v20\n\t"
  "ds_read_b64_tr_b16 a[42:43], v20 offset:6144\n\t"
  "v_add_u32 v20, 36864, v16\n\t"
  "ds_read_b64_tr_b16 a[44:45], v20\n\t"
  "ds_read_b64_tr_b16 a[46:47], v20 offset:6144\n\t"
  "s_waitcnt vmcnt(30) lgkmcnt(0)\n\t"
  "s_barrier\n\t"
  "s_add_u32 m0, s61, 36864\n\t"
  "s_nop 0\n\t"
  "buffer_load_dwordx4 v8, s[56:59], s62 offen lds\n\t"
  "s_add_u32 m0, s61, 37888\n\t"
  "s_nop 0\n\t"
  "buffer_load_dwordx4 v9, s[56:59], s62 offen lds\n\t"
  "s_add_u32 m0, s61, 38912\n\t"
  "s_nop 0\n\t"
  "buffer_load_dwordx4 v10, s[56:59], s62 offen lds\n\t"
  "s_add_u32 s62, s62, s60\n\t"
  "v_add_u32 v20, 49152, v14\n\t"
  "ds_read_b64_tr_b16 a[48:49], v20\n\t"
  "ds_read_b64_tr_b16 a[50:51], v20 offset:6144\n\t"
  "v_add_u32 v20, 49152, v15\n\t"
  "ds_read_b64_tr_b16 a[52:53], v20\n\t"
  "ds_read_b64_tr_b16 a[54:55], v20 offset:6144\n\t"
  "v_add_u32 v20, 49152, v16\n\t"
  "ds_read_b64_tr_b16 a[56:57], v20\n\t"
  "ds_read_b64_tr_b16 a[58:59], v20 offset:6144\n\t"
  "s_waitcnt vmcnt(30) lgkmcnt(0)\n\t"
  "s_barrier\n\t"
  "s_add_u32 m0, s61, 49152\n\t"
  "s_nop 0\n\t"
  "buffer_load_dwordx4 v8, s[56:59], s62 offen lds\n\t"
  "s_add_u32 m0, s61, 50176\n\t"
  "s_nop 0\n\t"
  "buffer_load_dwordx4 v9, s[56:59], s62 offen lds\n\t"
  "s_add_u32 m0, s61, 51200\n\t"
  "s_nop 0\n\t"
  "buffer_load_dwordx4 v10, s[56:59], s62 offen lds\n\t"
  "s_add_u32 s62, s62, s60\n\t"
  "v_add_u32 v20, 61440, v14\n\t"
  "ds_read_b64_tr_b16 a[60:61], v20\n\t"
  "ds_read_b64_tr_b16 a[62:63], v20 offset:6144\n\t"
  "v_add_u32 v20, 61440, v15\n\t"
  "ds_read_b64_tr_b16 a[64:65], v20\n\t"
  "ds_read_b64_tr_b16 a[66:67], v20 offset:6144\n\t"
  "v_add_u32 v20, 61440, v16\n\t"
  "ds_read_b64_tr_b16 a[68:69], v20\n\t"
  "ds_read_b64_tr_b16 a[70:71], v20 offset:6144\n\t"
  "s_waitcnt vmcnt(30) lgkmcnt(0)\n\t"
  "s_barrier\n\t"
  "s_add_u32 m0, s61, 61440\n\t"
  "s_nop 0\n\t"
  "buffer_load_dwordx4 v8, s[56:59], s62 offen lds\n\t"
  "s_add_u32 m0, s61, 62464\n\t"
  "s_nop 0\n\t"
  "buffer_load_dwordx4 v9, s[56:59], s62 offen lds\n\t"
  "s_add_u32 m0, s61, 63488\n\t"
  "s_nop 0\n\t"
  "buffer_load_dwordx4 v10, s[56:59], s62 offen lds\n\t"
  "s_add_u32 s62, s62, s60\n\t"
  "v_add_u32 v20, 73728, v14\n\t"
  "ds_read_b64_tr_b16 a[72:73], v20\n\t"
  "ds_read_b64_tr_b16 a[74:75], v20 offset:6144\n\t"
  "v_add_u32 v20, 73728, v15\n\t"
  "ds_read_b64_tr_b16 a[76:77], v20\n\t"
  "ds_read_b64_tr_b16 a[78:79], v20 offset:6144\n\t"
  "v_add_u32 v20, 73728, v16\n\t"
  "ds_read_b64_tr_b16 a[80:81], v20\n\t"
  "ds_read_b64_tr_b16 a[82:83], v20 offset:6144\n\t"
  "s_waitcnt vmcnt(30) lgkmcnt(0)\n\t"
  "s_barrier\n\t"
  "s_add_u32 m0, s61, 73728\n\t"
  "s_nop 0\n\t"
  "buffer_load_dwordx4 v8, s[56:59], s62 offen lds\n\t"
  "s_add_u32 m0, s61, 74752\n\t"
  "s_nop 0\n\t"
  "buffer_load_dwordx4 v9, s[56:59], s62 offen lds\n\t"
  "s_add_u32 m0, s61, 75776\n\t"
  "s_nop 0\n\t"
  "buffer_load_dwordx4 v10, s[56:59], s62 offen lds\n\t"
  "s_add_u32 s62, s62, s60\n\t"
  "v_add_u32 v20, 86016, v14\n\t"
  "ds_read_b64_tr_b16 a[84:85], v20\n\t"
  "ds_read_b64_tr_b16 a[86:87], v20 offset:6144\n\t"
  "v_add_u32 v20, 86016, v15\n\t"
  "ds_read_b64_tr_b16 a[88:89], v20\n\t"
  "ds_read_b64_tr_b16 a[90:91], v20 offset:6144\n\t"
  "v_add_u32 v20, 86016, v16\n\t"
  "ds_read_b64_tr_b16 a[92:93], v20\n\t"
  "ds_read_b64_tr_b16 a[94:95], v20 offset:6144\n\t"
  "s_waitcnt vmcnt(30) lgkmcnt(0)\n\t"
  "s_barrier\n\t"
  "s_add_u32 m0, s61, 86016\n\t"
  "s_nop 0\n\t"
  "buffer_load_dwordx4 v8, s[56:59], s62 offen lds\n\t"
  "s_add_u32 m0, s61, 87040\n\t"
  "s_nop 0\n\t"
  "buffer_load_dwordx4 v9, s[56:59], s62 offen lds\n\t"
  "s_add_u32 m0, s61, 88064\n\t"
  "s_nop 0\n\t"
  "buffer_load_dwordx4 v10, s[56:59], s62 offen lds\n\t"
  "s_add_u32 s62, s62, s60\n\t"
  "v_add_u32 v20, 98304, v14\n\t"
  "ds_read_b64_tr_b16 a[96:97], v20\n\t"
  "ds_read_b64_tr_b16 a[98:99], v20 offset:6144\n\t"
  "v_add_u32 v20, 98304, v15\n\t"
  "ds_read_b64_tr_b16 a[100:101], v20\n\t"
  "ds_read_b64_tr_b16 a[102:103], v20 offset:6144\n\t"
  "v_add_u32 v20, 98304, v16\n\t"
  "ds_read_b64_tr_b16 a[104:105], v20\n\t"
  "ds_read_b64_tr_b16 a[106:107], v20 offset:6144\n\t"
  "s_waitcnt vmcnt(30) lgkmcnt(0)\n\t"
  "s_barrier\n\t"
  "s_add_u32 m0, s61, 98304\n\t"
  "s_nop 0\n\t"
  "buffer_load_dwordx4 v8, s[56:59], s62 offen lds\n\t"
  "s_add_u32 m0, s61, 99328\n\t"
  "s_nop 0\n\t"
  "buffer_load_dwordx4 v9, s[56:59], s62 offen lds\n\t"
  "s_add_u32 m0, s61, 100352\n\t"
  "s_nop 0\n\t"
  "buffer_load_dwordx4 v10, s[56:59], s62 offen lds\n\t"
  "s_add_u32 s62, s62, s60\n\t"
  "v_add_u32 v20, 110592, v14\n\t"
  "ds_read_b64_tr_b16 a[108:109], v20\n\t"
  "ds_read_b64_tr_b16 a[110:111], v20 offset:6144\n\t"
  "v_add_u32 v20, 110592, v15\n\t"
  "ds_read_b64_tr_b16 a[112:113], v20\n\t"
  "ds_read_b64_tr_b16 a[114:115], v20 offset:6144\n\t"
  "v_add_u32 v20, 110592, v16\n\t"
  "ds_read_b64_tr_b16 a[116:117], v20\n\t"
  "ds_read_b64_tr_b16 a[118:119], v20 offset:6144\n\t"
  "s_waitcnt vmcnt(30) lgkmcnt(0)\n\t"
  "s_barrier\n\t"
  "s_add_u32 m0, s61, 110592\n\t"
  "s_nop 0\n\t"
  "buffer_load_dwordx4 v8, s[56:59], s62 offen lds\n\t"
  "s_add_u32 m0, s61, 111616\n\t"
  "s_nop 0\n\t"
  "buffer_load_dwordx4 v9, s[56:59], s62 offen lds\n\t"
  "s_add_u32 m0, s61, 112640\n\t"
  "s_nop 0\n\t"
  "buffer_load_dwordx4 v10, s[56:59], s62 offen lds\n\t"
  "s_add_u32 s62, s62, s60\n\t"
  "v_add_u32 v20, 122880, v14\n\t"
  "ds_read_b64_tr_b16 a[120:121], v20\n\t"
  "ds_read_b64_tr_b16 a[122:123], v20 offset:6144\n\t"
  "v_add_u32 v20, 122880, v15\n\t"
  "ds_read_b64_tr_b16 a[124:125], v20\n\t"
  "ds_read_b64_tr_b16 a[126:127], v20 offset:6144\n\t"
  "v_add_u32 v20, 122880, v16\n\t"
  "ds_read_b64_tr_b16 a[128:129], v20\n\t"
  "ds_read_b64_tr_b16 a[130:131], v20 offset:6144\n\t"
  "s_waitcnt vmcnt(30) lgkmcnt(0)\n\t"
  "s_barrier\n\t"
  "s_add_u32 m0, s61, 122880\n\t"
  "s_nop 0\n\t"
  "buffer_load_dwordx4 v8, s[56:59], s62 offen lds\n\t"
  "s_add_u32 m0, s61, 123904\n\t"
  "s_nop 0\n\t"
  "buffer_load_dwordx4 v9, s[56:59], s62 offen lds\n\t"
  "s_add_u32 m0, s61, 124928\n\t"
  "s_nop 0\n\t"
  "buffer_load_dwordx4 v10, s[56:59], s62 offen lds\n\t"
  "s_add_u32 s62, s62, s60\n\t"
  "v_add_u32 v20, 135168, v14\n\t"
  "ds_read_b64_tr_b16 a[132:133], v20\n\t"
  "ds_read_b64_tr_b16 a[134:135], v20 offset:6144\n\t"
  "v_add_u32 v20, 135168, v15\n\t"
  "ds_read_b64_tr_b16 a[136:137], v20\n\t"
  "ds_read_b64_tr_b16 a[138:139], v20 offset:6144\n\t"
  "v_add_u32 v20, 135168, v16\n\t"
  "ds_read_b64_tr_b16 a[140:141], v20\n\t"
  "ds_read_b64_tr_b16 a[142:143], v20 offset:6144\n\t"
  "s_waitcnt vmcnt(30) lgkmcnt(0)\n\t"
  "s_barrier\n\t"
  "s_add_u32 m0, s61, 135168\n\t"
  "s_nop 0\n\t"
  "buffer_load_dwordx4 v8, s[56:59], s62 offen lds\n\t"
  "s_add_u32 m0, s61, 136192\n\t"
  "s_nop 0\n\t"
  "buffer_load_dwordx4 v9, s[56:59], s62 offen lds\n\t"
  "s_add_u32 m0, s61, 137216\n\t"
  "s_nop 0\n\t"
  "buffer_load_dwordx4 v10, s[56:59], s62 offen lds\n\t"
  "s_add_u32 s62, s62, s60\n\t"
  "v_add_u32 v20, 0, v14\n\t"
  "ds_read_b64_tr_b16 a[144:145], v20\n\t"
  "ds_read_b64_tr_b16 a[146:147], v20 offset:6144\n\t"
  "v_add_u32 v20, 0, v15\n\t"
  "ds_read_b64_tr_b16 a[148:149], v20\n\t"
  "ds_read_b64_tr_b16 a[150:151], v20 offset:6144\n\t"
  "v_add_u32 v20, 0, v16\n\t"
  "ds_read_b64_tr_b16 a[152:153], v20\n\t"
  "ds_read_b64_tr_b16 a[154:155], v20 offset:6144\n\t"
  "s_waitcnt vmcnt(30) lgkmcnt(0)\n\t"
  "s_barrier\n\t"
  "s_add_u32 m0, s61, 0\n\t"
  "s_nop 0\n\t"
  "buffer_load_dwordx4 v8, s[56:59], s62 offen lds\n\t"
  "s_add_u32 m0, s61, 1024\n\t"
  "s_nop 0\n\t"
  "buffer_load_dwordx4 v9, s[56:59], s62 offen lds\n\t"
  "s_add_u32 m0, s61, 2048\n\t"
  "s_nop 0\n\t"
  "buffer_load_dwordx4 v10, s[56:59], s62 offen lds\n\t"
  "s_add_u32 s62, s62, s60\n\t"
  "v_add_u32 v20, 12288, v14\n\t"
  "ds_read_b64_tr_b16 a[156:157], v20\n\t"
  "ds_read_b64_tr_b16 a[158:159], v20 offset:6144\n\t"
  "v_add_u32 v20, 12288, v15\n\t"
  "ds_read_b64_tr_b16 a[160:161], v20\n\t"
  "ds_read_b64_tr_b16 a[162:163], v20 offset:6144\n\t"
  "v_add_u32 v20, 12288, v16\n\t"
  "ds_read_b64_tr_b16 a[164:165], v20\n\t"
  "ds_read_b64_tr_b16 a[166:167], v20 offset:6144\n\t"
  "s_waitcnt vmcnt(30) lgkmcnt(0)\n\t"
  "s_barrier\n\t"
  "s_add_u32 m0, s61, 12288\n\t"
  "s_nop 0\n\t"
  "buffer_load_dwordx4 v8, s[56:59], s62 offen lds\n\t"
  "s_add_u32 m0, s61, 13312\n\t"
  "s_nop 0\n\t"
  "buffer_load_dwordx4 v9, s[56:59], s62 offen lds\n\t"
  "s_add_u32 m0, s61, 14336\n\t"
  "s_nop 0\n\t"
  "buffer_load_dwordx4 v10, s[56:59], s62 offen lds\n\t"
  "s_add_u32 s62, s62, s60\n\t"
  "v_add_u32 v20, 24576, v14\n\t"
  "ds_read_b64_tr_b16 a[168:169], v20\n\t"
  "ds_read_b64_tr_b16 a[170:171], v20 offset:6144\n\t"
  "v_add_u32 v20, 24576, v15\n\t"
  "ds_read_b64_tr_b16 a[172:173], v20\n\t"
  "ds_read_b64_tr_b16 a[174:175], v20 offset:6144\n\t"
  "v_add_u32 v20, 24576, v16\n\t"
  "ds_read_b64_tr_b16 a[176:177], v20\n\t"
  "ds_read_b64_tr_b16 a[178:179], v20 offset:6144\n\t"
  "s_waitcnt vmcnt(30) lgkmcnt(0)\n\t"
  "s_barrier\n\t"
  "s_add_u32 m0, s61, 24576\n\t"
  "s_nop 0\n\t"
  "buffer_load_dwordx4 v8, s[56:59], s62 offen lds\n\t"
  "s_add_u32 m0, s61, 25600\n\t"
  "s_nop 0\n\t"
  "buffer_load_dwordx4 v9, s[56:59], s62 offen lds\n\t"
  "s_add_u32 m0, s61, 26624\n\t"
  "s_nop 0\n\t"
  "buffer_load_dwordx4 v10, s[56:59], s62 offen lds\n\t"
  "s_add_u32 s62, s62, s60\n\t"
  "v_add_u32 v20, 36864, v14\n\t"
  "ds_read_b64_tr_b16 a[180:181], v20\n\t"
  "ds_read_b64_tr_b16 a[182:183], v20 offset:6144\n\t"
  "v_add_u32 v20, 36864, v15\n\t"
  "ds_read_b64_tr_b16 a[184:185], v20\n\t"
  "ds_read_b64_tr_b16 a[186:187], v20 offset:6144\n\t"
  "v_add_u32 v20, 36864, v16\n\t"
  "ds_read_b64_tr_b16 a[188:189], v20\n\t"
  "ds_read_b64_tr_b16 a[190:191], v20 offset:6144\n\t"
  "s_waitcnt vmcnt(30) lgkmcnt(0)\n\t"
  "s_barrier\n\t"
  "s_add_u32 m0, s61, 36864\n\t"
  "s_nop 0\n\t"
  "buffer_load_dwordx4 v8, s[56:59], s62 offen lds\n\t"
  "s_add_u32 m0, s61, 37888\n\t"
  "s_nop 0\n\t"
  "buffer_load_dwordx4 v9, s[56:59], s62 offen lds\n\t"
  "s_add_u32 m0, s61, 38912\n\t"
  "s_nop 0\n\t"
  "buffer_load_dwordx4 v10, s[56:59], s62 offen lds\n\t"
  "s_add_u32 s62, s62, s60\n\t"
  "v_add_u32 v20, 49152, v14\n\t"
  "ds_read_b64_tr_b16 a[192:193], v20\n\t"
  "ds_read_b64_tr_b16 a[194:195], v20 offset:6144\n\t"
  "v_add_u32 v20, 49152, v15\n\t"
  "ds_read_b64_tr_b16 a[196:197], v20\n\t"
  "ds_read_b64_tr_b16 a[198:199], v20 offset:6144\n\t"
  "v_add_u32 v20, 49152, v16\n\t"
  "ds_read_b64_tr_b16 a[200:201], v20\n\t"
  "ds_read_b64_tr_b16 a[202:203], v20 offset:6144\n\t"
  "s_waitcnt vmcnt(30) lgkmcnt(0)\n\t"
  "s_barrier\n\t"
  "s_add_u32 m0, s61, 49152\n\t"
  "s_nop 0\n\t"
  "buffer_load_dwordx4 v8, s[56:59], s62 offen lds\n\t"
  "s_add_u32 m0, s61, 50176\n\t"
  "s_nop 0\n\t"
  "buffer_load_dwordx4 v9, s[56:59], s62 offen lds\n\t"
  "s_add_u32 m0, s61, 51200\n\t"
  "s_nop 0\n\t"
  "buffer_load_dwordx4 v10, s[56:59], s62 offen lds\n\t"
  "s_add_u32 s62, s62, s60\n\t"
  "v_add_u32 v20, 61440, v14\n\t"
  "ds_read_b64_tr_b16 a[204:205], v20\n\t"
  "ds_read_b64_tr_b16 a[206:207], v20 offset:6144\n\t"
  "v_add_u32 v20, 61440, v15\n\t"
  "ds_read_b64_tr_b16 a[208:209], v20\n\t"
  "ds_read_b64_tr_b16 a[210:211], v20 offset:6144\n\t"
  "v_add_u32 v20, 61440, v16\n\t"
  "ds_read_b64_tr_b16 a[212:213], v20\n\t"
  "ds_read_b64_tr_b16 a[214:215], v20 offset:6144\n\t"
  "s_waitcnt vmcnt(30) lgkmcnt(0)\n\t"
  "s_barrier\n\t"
  "s_add_u32 m0, s61, 61440\n\t"
  "s_nop 0\n\t"
  "buffer_load_dwordx4 v8, s[56:59], s62 offen lds\n\t"
  "s_add_u32 m0, s61, 62464\n\t"
  "s_nop 0\n\t"
  "buffer_load_dwordx4 v9, s[56:59], s62 offen lds\n\t"
  "s_add_u32 m0, s61, 63488\n\t"
  "s_nop 0\n\t"
  "buffer_load_dwordx4 v10, s[56:59], s62 offen lds\n\t"
  "s_add_u32 s62, s62, s60\n\t"
  "v_add_u32 v20, 73728, v14\n\t"
  "ds_read_b64_tr_b16 a[216:217], v20\n\t"
  "ds_read_b64_tr_b16 a[218:219], v20 offset:6144\n\t"
  "v_add_u32 v20, 73728, v15\n\t"
  "ds_read_b64_tr_b16 a[220:221], v20\n\t"
  "ds_read_b64_tr_b16 a[222:223], v20 offset:6144\n\t"
  "v_add_u32 v20, 73728, v16\n\t"
  "ds_read_b64_tr_b16 a[224:225], v20\n\t"
  "ds_read_b64_tr_b16 a[226:227], v20 offset:6144\n\t"
  "s_waitcnt vmcnt(30) lgkmcnt(0)\n\t"
  "s_barrier\n\t"
  "s_add_u32 m0, s61, 73728\n\t"
  "s_nop 0\n\t"
  "buffer_load_dwordx4 v8, s[56:59], s62 offen lds\n\t"
  "s_add_u32 m0, s61, 74752\n\t"
  "s_nop 0\n\t"
  "buffer_load_dwordx4 v9, s[56:59], s62 offen lds\n\t"
  "s_add_u32 m0, s61, 75776\n\t"
  "s_nop 0\n\t"
  "buffer_load_dwordx4 v10, s[56:59], s62 offen lds\n\t"
  "s_add_u32 s62, s62, s60\n\t"
  "v_add_u32 v20, 86016, v14\n\t"
  "ds_read_b64_tr_b16 a[228:229], v20\n\t"
  "ds_read_b64_tr_b16 a[230:231], v20 offset:6144\n\t"
  "v_add_u32 v20, 86016, v15\n\t"
  "ds_read_b64_tr_b16 a[232:233], v20\n\t"
  "ds_read_b64_tr_b16 a[234:235], v20 offset:6144\n\t"
  "v_add_u32 v20, 86016, v16\n\t"
  "ds_read_b64_tr_b16 a[236:237], v20\n\t"
  "ds_read_b64_tr_b16 a[238:239], v20 offset:6144\n\t"
  "s_waitcnt vmcnt(30) lgkmcnt(0)\n\t"
  "s_barrier\n\t"
  "s_add_u32 m0, s61, 86016\n\t"
  "s_nop 0\n\t"
  "buffer_load_dwordx4 v8, s[56:59], s62 offen lds\n\t"
  "s_add_u32 m0, s61, 87040\n\t"
  "s_nop 0\n\t"
  "buffer_load_dwordx4 v9, s[56:59], s62 offen lds\n\t"
  "s_add_u32 m0, s61, 88064\n\t"
  "s_nop 0\n\t"
  "buffer_load_dwordx4 v10, s[56:59], s62 offen lds\n\t"
  "s_add_u32 s62, s62, s60\n\t"
  "v_add_u32 v20, 98304, v14\n\t"
  "ds_read_b64_tr_b16 a[240:241], v20\n\t"
  "ds_read_b64_tr_b16 a[242:243], v20 offset:6144\n\t"
  "v_add_u32 v20, 98304, v15\n\t"
  "ds_read_b64_tr_b16 a[244:245], v20\n\t"
  "ds_read_b64_tr_b16 a[246:247], v20 offset:6144\n\t"
  "v_add_u32 v20, 98304, v16\n\t"
  "ds_read_b64_tr_b16 a[248:249], v20\n\t"
  "ds_read_b64_tr_b16 a[250:251], v20 offset:6144\n\t"
  "s_waitcnt vmcnt(30) lgkmcnt(0)\n\t"
  "s_barrier\n\t"
  "v_add_u32 v20, 110592, v14\n\t"
  "ds_read_b64_tr_b16 a[252:253], v20\n\t"
  "ds_read_b64_tr_b16 a[254:255], v20 offset:6144\n\t"
  "v_add_u32 v20, 110592, v15\n\t"
  "ds_read_b64_tr_b16 v[224:225], v20\n\t"
  "ds_read_b64_tr_b16 v[226:227], v20 offset:6144\n\t"
  "v_add_u32 v20, 110592, v16\n\t"
  "ds_read_b64_tr_b16 v[228:229], v20\n\t"
  "ds_read_b64_tr_b16 v[230:231], v20 offset:6144\n\t"
  "s_waitcnt vmcnt(27) lgkmcnt(0)\n\t"
  "s_barrier\n\t"
  "v_add_u32 v20, 122880, v14\n\t"
  "ds_read_b64_tr_b16 v[232:233], v20\n\t"
  "ds_read_b64_tr_b16 v[234:235], v20 offset:6144\n\t"
  "v_add_u32 v20, 122880, v15\n\t"
  "ds_read_b64_tr_b16 v[236:237], v20\n\t"
  "ds_read_b64_tr_b16 v[238:239], v20 offset:6144\n\t"
  "v_add_u32 v20, 122880, v16\n\t"
  "ds_read_b64_tr_b16 v[240:241], v20\n\t"
  "ds_read_b64_tr_b16 v[242:243], v20 offset:6144\n\t"
  "s_waitcnt vmcnt(24) lgkmcnt(0)\n\t"
  "s_barrier\n\t"
  "v_add_u32 v20, 135168, v14\n\t"
  "ds_read_b64_tr_b16 v[244:245], v20\n\t"
  "ds_read_b64_tr_b16 v[246:247], v20 offset:6144\n\t"
  "v_add_u32 v20, 135168, v15\n\t"
  "ds_read_b64_tr_b16 v[248:249], v20\n\t"
  "ds_read_b64_tr_b16 v[250:251], v20 offset:6144\n\t"
  "v_add_u32 v20, 135168, v16\n\t"
  "ds_read_b64_tr_b16 v[252:253], v20\n\t"
  "ds_read_b64_tr_b16 v[254:255], v20 offset:6144\n\t"
  "s_waitcnt vmcnt(21) lgkmcnt(0)\n\t"
  "s_barrier\n\t"
  "v_add_u32 v20, 0, v14\n\t"
  "ds_read_b64_tr_b16 v[192:193], v20\n\t"
  "ds_read_b64_tr_b16 v[194:195], v20 offset:6144\n\t"
  "v_add_u32 v20, 0, v15\n\t"
  "ds_read_b64_tr_b16 v[196:197], v20\n\t"
  "ds_read_b64_tr_b16 v[198:199], v20 offset:6144\n\t"
  "v_add_u32 v20, 0, v16\n\t"
  "ds_read_b64_tr_b16 v[200:201], v20\n\t"
  "ds_read_b64_tr_b16 v[202:203], v20 offset:6144\n\t"
  "s_waitcnt vmcnt(18) lgkmcnt(0)\n\t"
  "s_barrier\n\t"
  "v_add_u32 v20, 12288, v14\n\t"
  "ds_read_b64_tr_b16 v[204:205], v20\n\t"
  "ds_read_b64_tr_b16 v[206:207], v20 offset:6144\n\t"
  "v_add_u32 v20, 12288, v15\n\t"
  "ds_read_b64_tr_b16 v[208:209], v20\n\t"
  "ds_read_b64_tr_b16 v[210:211], v20 offset:6144\n\t"
  "v_add_u32 v20, 12288, v16\n\t"
  "ds_read_b64_tr_b16 v[212:213], v20\n\t"
  "ds_read_b64_tr_b16 v[214:215], v20 offset:6144\n\t"
  "s_waitcnt vmcnt(15) lgkmcnt(0)\n\t"
  "s_barrier\n\t"
  "v_add_u32 v20, 24576, v14\n\t"
  "ds_read_b64_tr_b16 v[216:217], v20\n\t"
  "ds_read_b64_tr_b16 v[218:219], v20 offset:6144\n\t"
  "v_add_u32 v20, 24576, v15\n\t"
  "ds_read_b64_tr_b16 v[220:221], v20\n\t"
  "ds_read_b64_tr_b16 v[222:223], v20 offset:6144\n\t"
  "v_add_u32 v20, 24576, v16\n\t"
  "ds_read_b64_tr_b16 v[160:161], v20\n\t"
  "ds_read_b64_tr_b16 v[162:163], v20 offset:6144\n\t"
  "s_waitcnt vmcnt(12) lgkmcnt(0)\n\t"
  "s_barrier\n\t"
  "v_add_u32 v20, 36864, v14\n\t"
  "ds_read_b64_tr_b16 v[164:165], v20\n\t"
  "ds_read_b64_tr_b16 v[166:167], v20 offset:6144\n\t"
  "v_add_u32 v20, 36864, v15\n\t"
  "ds_read_b64_tr_b16 v[168:169], v20\n\t"
  "ds_read_b64_tr_b16 v[170:171], v20 offset:6144\n\t"
  "v_add_u32 v20, 36864, v16\n\t"
  "ds_read_b64_tr_b16 v[172:173], v20\n\t"
  "ds_read_b64_tr_b16 v[174:175], v20 offset:6144\n\t"
  "s_waitcnt vmcnt(9) lgkmcnt(0)\n\t"
  "s_barrier\n\t"
  "v_add_u32 v20, 49152, v14\n\t"
  "ds_read_b64_tr_b16 v[176:177], v20\n\t"
  "ds_read_b64_tr_b16 v[178:179], v20 offset:6144\n\t"
  "v_add_u32 v20, 49152, v15\n\t"
  "ds_read_b64_tr_b16 v[180:181], v20\n\t"
  "ds_read_b64_tr_b16 v[182:183], v20 offset:6144\n\t"
  "v_add_u32 v20, 49152, v16\n\t"
  "ds_read_b64_tr_b16 v[184:185], v20\n\t"
  "ds_read_b64_tr_b16 v[186:187], v20 offset:6144\n\t"
  "s_waitcnt vmcnt(6) lgkmcnt(0)\n\t"
  "s_barrier\n\t"
  "v_add_u32 v20, 61440, v14\n\t"
  "ds_read_b64_tr_b16 v[188:189], v20\n\t"
  "ds_read_b64_tr_b16 v[190:191], v20 offset:6144\n\t"
  "v_add_u32 v20, 61440, v15\n\t"
  "ds_read_b64_tr_b16 v[128:129], v20\n\t"
  "ds_read_b64_tr_b16 v[130:131], v20 offset:6144\n\t"
  "v_add_u32 v20, 61440, v16\n\t"
  "ds_read_b64_tr_b16 v[132:133], v20\n\t"
  "ds_read_b64_tr_b16 v[134:135], v20 offset:6144\n\t"
  "s_waitcnt vmcnt(3) lgkmcnt(0)\n\t"
  "s_barrier\n\t"
  "v_add_u32 v20, 73728, v14\n\t"
  "ds_read_b64_tr_b16 v[136:137], v20\n\t"
  "ds_read_b64_tr_b16 v[138:139], v20 offset:6144\n\t"
  "v_add_u32 v20, 73728, v15\n\t"
  "ds_read_b64_tr_b16 v[140:141], v20\n\t"
  "ds_read_b64_tr_b16 v[142:143], v20 offset:6144\n\t"
  "v_add_u32 v20, 73728, v16\n\t"
  "ds_read_b64_tr_b16 v[144:145], v20\n\t"
  "ds_read_b64_tr_b16 v[146:147], v20 offset:6144\n\t"
  "s_waitcnt vmcnt(0) lgkmcnt(0)\n\t"
  "s_barrier\n\t"
  "v_add_u32 v20, 86016, v14\n\t"
  "ds_read_b64_tr_b16 v[148:149], v20\n\t"
  "ds_read_b64_tr_b16 v[150:151], v20 offset:6144\n\t"
  "v_add_u32 v20, 86016, v15\n\t"
  "ds_read_b64_tr_b16 v[152:153], v20\n\t"
  "ds_read_b64_tr_b16 v[154:155], v20 offset:6144\n\t"
  "v_add_u32 v20, 86016, v16\n\t"
  "ds_read_b64_tr_b16 v[156:157], v20\n\t"
  "ds_read_b64_tr_b16 v[158:159], v20 offset:6144\n\t"
  "s_waitcnt lgkmcnt(0)\n\t"
  "s_barrier\n\t"
  "s_mov_b64 s[60:61], s[36:37]\n\t"
  "s_mov_b32 s63, s39\n\t"
  "s_mov_b64 s[64:65], s[36:37]\n\t"
  "s_mov_b32 s67, s39\n\t"
  "s_mov_b64 s[84:85], s[36:37]\n\t"
  "s_mov_b32 s87, s39\n\t"
  "s_mov_b32 s86, 0\n\t"
  "s_mov_b32 s57, 0\n\t"
  "s_mov_b32 s56, -1\n\t"
  "s_mov_b32 s80, 0x20000\n\t"
  "s_lshr_b32 s78, s41, 9\n\t"
  "s_mov_b32 s71, s41\n\t"
  "s_mov_b32 s74, -65536\n\t"
  "v_readlane_b32 s68, v4, 0\n\t"
  "v_mov_b32 v3, -1\n\t"
  "s_branch 3f\n\t"
  "1:\n\t"
  "ds_read_b128 v[20:23], v1 offset:3072\n\t"
  "s_waitcnt lgkmcnt(3)\n\t"
  "v_mfma_f32_16x16x32_bf16 v[30:33], a[0:3], v[8:11], 0\n\t"
  "s_add_u32 m0, s71, 32768\n\t"
  "s_add_u32 s76, s68, 65536\n\t"
  "buffer_load_dwordx4 v0, s[36:39], s76 offen sc1 lds\n\t"
  "v_mfma_f32_16x16x32_bf16 v[34:37], a[4:7], v[8:11], 0\n\t"
  "s_mov_b32 s70, s69\n\t"
  "s_mov_b32 s56, s57\n\t"
  "s_add_u32 s57, s57, 1\n\t"
  "v_mfma_f32_16x16x32_bf16 v[38:41], a[8:11], v[8:11], 0\n\t"
  "ds_read_b128 v[8:11], v1 offset:4096\n\t"
  "s_waitcnt lgkmcnt(3)\n\t"
  "v_mfma_f32_16x16x32_bf16 v[30:33], a[12:15], v[12:15], v[30:33]\n\t"
  "v_mfma_f32_16x16x32_bf16 v[34:37], a[16:19], v[12:15], v[34:37]\n\t"
  "buffer_load_dwordx4 v0, s[36:39], s76 offen offset:1024 sc1 lds\n\t"
  "v_mfma_f32_16x16x32_bf16 v[38:41], a[20:23], v[12:15], v[38:41]\n\t"
  "ds_read_b128 v[12:15], v1 offset:5120\n\t"
  "s_waitcnt lgkmcnt(3)\n\t"
  "v_mfma_f32_16x16x32_bf16 v[30:33], a[24:27], v[16:19], v[30:33]\n\t"
  "v_mfma_f32_16x16x32_bf16 v[34:37], a[28:31], v[16:19], v[34:37]\n\t"
  "v_mfma_f32_16x16x32_bf16 v[38:41], a[32:35], v[16:19], v[38:41]\n\t"
  "ds_read_b128 v[16:19], v1 offset:6144\n\t"
  "s_waitcnt lgkmcnt(3)\n\t"
  "v_mfma_f32_16x16x32_bf16 v[30:33], a[36:39], v[20:23], v[30:33]\n\t"
  "s_add_u32 m0, s71, 40960\n\t"
  "s_add_u32 s76, s68, 81920\n\t"
  "buffer_load_dwordx4 v0, s[36:39], s76 offen sc1 lds\n\t"
  "v_mfma_f32_16x16x32_bf16 v[34:37], a[40:43], v[20:23], v[34:37]\n\t"
  "v_mfma_f32_16x16x32_bf16 v[38:41], a[44:47], v[20:23], v[38:41]\n\t"
  "ds_read_b128 v[20:23], v1 offset:7168\n\t"
  "s_waitcnt lgkmcnt(3)\n\t"
  "v_mfma_f32_16x16x32_bf16 v[30:33], a[48:51], v[8:11], v[30:33]\n\t"
  "v_mfma_f32_16x16x32_bf16 v[34:37], a[52:55], v[8:11], v[34:37]\n\t"
  "buffer_load_dwordx4 v0, s[36:39], s76 offen offset:1024 sc1 lds\n\t"
  "v_mfma_f32_16x16x32_bf16 v[38:41], a[56:59], v[8:11], v[38:41]\n\t"
  "ds_read_b128 v[8:11], v1 offset:8192\n\t"
  "s_waitcnt lgkmcnt(3)\n\t"
  "v_mfma_f32_16x16x32_bf16 v[30:33], a[60:63], v[12:15], v[30:33]\n\t"
  "v_mov_b32 v76, s56\n\t"
  "v_mfma_f32_16x16x32_bf16 v[34:37], a[64:67], v[12:15], v[34:37]\n\t"
  "v_mfma_f32_16x16x32_bf16 v[38:41], a[68:71], v[12:15], v[38:41]\n\t"
  "ds_read_b128 v[12:15], v1 offset:9216\n\t"
  "s_waitcnt lgkmcnt(3)\n\t"
  "v_mfma_f32_16x16x32_bf16 v[30:33], a[72:75], v[16:19], v[30:33]\n\t"
  "s_add_u32 m0, s71, 49152\n\t"
  "s_add_u32 s76, s68, 98304\n\t"
  "buffer_load_dwordx4 v0, s[36:39], s76 offen sc1 lds\n\t"
  "v_mfma_f32_16x16x32_bf16 v[34:37], a[76:79], v[16:19], v[34:37]\n\t"
  "v_mfma_f32_16x16x32_bf16 v[38:41], a[80:83], v[16:19], v[38:41]\n\t"
  "ds_read_b128 v[16:19], v1 offset:10240\n\t"
  "s_waitcnt lgkmcnt(3)\n\t"
  "v_mfma_f32_16x16x32_bf16 v[30:33], a[84:87], v[20:23], v[30:33]\n\t"
  "v_mfma_f32_16x16x32_bf16 v[34:37], a[88:91], v[20:23], v[34:37]\n\t"
  "buffer_load_dwordx4 v0, s[36:39], s76 offen offset:1024 sc1 lds\n\t"
  "v_mfma_f32_16x16x32_bf16 v[38:41], a[92:95], v[20:23], v[38:41]\n\t"
  "ds_read_b128 v[20:23], v1 offset:11264\n\t"
  "s_waitcnt lgkmcnt(3)\n\t"
  "v_mfma_f32_16x16x32_bf16 v[30:33], a[96:99], v[8:11], v[30:33]\n\t"
  "v_mfma_f32_16x16x32_bf16 v[34:37], a[100:103], v[8:11], v[34:37]\n\t"
  "v_mfma_f32_16x16x32_bf16 v[38:41], a[104:107], v[8:11], v[38:41]\n\t"
  "ds_read_b128 v[8:11], v1 offset:12288\n\t"
  "s_waitcnt lgkmcnt(3)\n\t"
  "v_mfma_f32_16x16x32_bf16 v[30:33], a[108:111], v[12:15], v[30:33]\n\t"
  "s_add_u32 m0, s71, 57344\n\t"
  "s_add_u32 s76, s68, 114688\n\t"
  "buffer_load_dwordx4 v0, s[36:39], s76 offen sc1 lds\n\t"
  "v_mfma_f32_16x16x32_bf16 v[34:37], a[112:115], v[12:15], v[34:37]\n\t"
  "v_mfma_f32_16x16x32_bf16 v[38:41], a[116:119], v[12:15], v[38:41]\n\t"
  "ds_read_b128 v[12:15], v1 offset:13312\n\t"
  "s_waitcnt lgkmcnt(3)\n\t"
  "v_mfma_f32_16x16x32_bf16 v[30:33], a[120:123], v[16:19], v[30:33]\n\t"
  "v_mfma_f32_16x16x32_bf16 v[34:37], a[124:127], v[16:19], v[34:37]\n\t"
  "buffer_load_dwordx4 v0, s[36:39], s76 offen offset:1024 sc1 lds\n\t"
  "v_mfma_f32_16x16x32_bf16 v[38:41], a[128:131], v[16:19], v[38:41]\n\t"
  "ds_read_b128 v[16:19], v1 offset:14336\n\t"
  "s_waitcnt lgkmcnt(3)\n\t"
  "v_mfma_f32_16x16x32_bf16 v[30:33], a[132:135], v[20:23], v[30:33]\n\t"
  "v_mfma_f32_16x16x32_bf16 v[34:37], a[136:139], v[20:23], v[34:37]\n\t"
  "v_mfma_f32_16x16x32_bf16 v[38:41], a[140:143], v[20:23], v[38:41]\n\t"
  "ds_read_b128 v[20:23], v1 offset:15360\n\t"
  "s_waitcnt lgkmcnt(3)\n\t"
  "v_mfma_f32_16x16x32_bf16 v[30:33], a[144:147], v[8:11], v[30:33]\n\t"
  "v_mfma_f32_16x16x32_bf16 v[34:37], a[148:151], v[8:11], v[34:37]\n\t"
  "s_waitcnt vmcnt(11)\n\t"
  "s_barrier\n\t"
  "v_mfma_f32_16x16x32_bf16 v[38:41], a[152:155], v[8:11], v[38:41]\n\t"
  "ds_read_b128 v[8:11], v1 offset:16384\n\t"
  "s_waitcnt lgkmcnt(3)\n\t"
  "v_mfma_f32_16x16x32_bf16 v[30:33], a[156:159], v[12:15], v[30:33]\n\t"
  "v_subrev_u32 v76, 1, v76\n\t"
  "s_waitcnt vmcnt(8)\n\t"
  "buffer_store_dword v76, off, s[84:87], s40 sc1\n\t"
  "v_mfma_f32_16x16x32_bf16 v[34:37], a[160:163], v[12:15], v[34:37]\n\t"
  "v_mfma_f32_16x16x32_bf16 v[38:41], a[164:167], v[12:15], v[38:41]\n\t"
  "ds_read_b128 v[12:15], v1 offset:17408\n\t"
  "s_waitcnt lgkmcnt(3)\n\t"
  "v_mfma_f32_16x16x32_bf16 v[30:33], a[168:171], v[16:19], v[30:33]\n\t"
  "v_mfma_f32_16x16x32_bf16 v[34:37], a[172:175], v[16:19], v[34:37]\n\t"
  "v_mfma_f32_16x16x32_bf16 v[38:41], a[176:179], v[16:19], v[38:41]\n\t"
  "ds_read_b128 v[16:19], v1 offset:18432\n\t"
  "s_waitcnt lgkmcnt(3)\n\t"
  "v_mfma_f32_16x16x32_bf16 v[30:33], a[180:183], v[20:23], v[30:33]\n\t"
  "v_mfma_f32_16x16x32_bf16 v[34:37], a[184:187], v[20:23], v[34:37]\n\t"
  "v_mfma_f32_16x16x32_bf16 v[38:41], a[188:191], v[20:23], v[38:41]\n\t"
  "ds_read_b128 v[20:23], v1 offset:19456\n\t"
  "s_waitcnt lgkmcnt(3)\n\t"
  "v_mfma_f32_16x16x32_bf16 v[30:33], a[192:195], v[8:11], v[30:33]\n\t"
  "v_mfma_f32_16x16x32_bf16 v[34:37], a[196:199], v[8:11], v[34:37]\n\t"
  "v_mfma_f32_16x16x32_bf16 v[38:41], a[200:203], v[8:11], v[38:41]\n\t"
  "ds_read_b128 v[8:11], v1 offset:20480\n\t"
  "s_waitcnt lgkmcnt(3)\n\t"
  "v_mfma_f32_16x16x32_bf16 v[30:33], a[204:207], v[12:15], v[30:33]\n\t"
  "v_mfma_f32_16x16x32_bf16 v[34:37], a[208:211], v[12:15], v[34:37]\n\t"
  "v_mfma_f32_16x16x32_bf16 v[38:41], a[212:215], v[12:15], v[38:41]\n\t"
  "ds_read_b128 v[12:15], v1 offset:21504\n\t"
  "s_waitcnt lgkmcnt(3)\n\t"
  "v_mfma_f32_16x16x32_bf16 v[30:33], a[216:219], v[16:19], v[30:33]\n\t"
  "v_mfma_f32_16x16x32_bf16 v[34:37], a[220:223], v[16:19], v[34:37]\n\t"
  "v_mfma_f32_16x16x32_bf16 v[38:41], a[224:227], v[16:19], v[38:41]\n\t"
  "ds_read_b128 v[16:19], v1 offset:22528\n\t"
  "s_waitcnt lgkmcnt(3)\n\t"
  "v_mfma_f32_16x16x32_bf16 v[30:33], a[228:231], v[20:23], v[30:33]\n\t"
  "v_mfma_f32_16x16x32_bf16 v[34:37], a[232:235], v[20:23], v[34:37]\n\t"
  "v_mfma_f32_16x16x32_bf16 v[38:41], a[236:239], v[20:23], v[38:41]\n\t"
  "ds_read_b128 v[20:23], v1 offset:23552\n\t"
  "s_waitcnt lgkmcnt(3)\n\t"
  "v_mfma_f32_16x16x32_bf16 v[30:33], a[240:243], v[8:11], v[30:33]\n\t"
  "v_mfma_f32_16x16x32_bf16 v[34:37], a[244:247], v[8:11], v[34:37]\n\t"
  "v_mfma_f32_16x16x32_bf16 v[38:41], a[248:251], v[8:11], v[38:41]\n\t"
  "ds_read_b128 v[8:11], v1 offset:24576\n\t"
  "s_waitcnt lgkmcnt(3)\n\t"
  "v_mfma_f32_16x16x32_bf16 v[30:33], a[252:255], v[12:15], v[30:33]\n\t"
  "v_mfma_f32_16x16x32_bf16 v[34:37], v[224:227], v[12:15], v[34:37]\n\t"
  "v_mfma_f32_16x16x32_bf16 v[38:41], v[228:231], v[12:15], v[38:41]\n\t"
  "ds_read_b128 v[12:15], v1 offset:25600\n\t"
  "s_waitcnt lgkmcnt(3)\n\t"
  "v_mfma_f32_16x16x32_bf16 v[30:33], v[232:235], v[16:19], v[30:33]\n\t"
  "v_mfma_f32_16x16x32_bf16 v[34:37], v[236:239], v[16:19], v[34:37]\n\t"
  "v_mfma_f32_16x16x32_bf16 v[38:41], v[240:243], v[16:19], v[38:41]\n\t"
  "ds_read_b128 v[16:19], v1 offset:26624\n\t"
  "s_waitcnt lgkmcnt(3)\n\t"
  "v_mfma_f32_16x16x32_bf16 v[30:33], v[244:247], v[20:23], v[30:33]\n\t"
  "buffer_load_dword v3, v2, s[36:39], 0 offen sc1\n\t"
  "v_mfma_f32_16x16x32_bf16 v[34:37], v[248:251], v[20:23], v[34:37]\n\t"
  "v_mfma_f32_16x16x32_bf16 v[38:41], v[252:255], v[20:23], v[38:41]\n\t"
  "ds_read_b128 v[20:23], v1 offset:27648\n\t"
  "s_waitcnt lgkmcnt(3)\n\t"
  "v_mfma_f32_16x16x32_bf16 v[30:33], v[192:195], v[8:11], v[30:33]\n\t"
  "v_mfma_f32_16x16x32_bf16 v[34:37], v[196:199], v[8:11], v[34:37]\n\t"
  "v_mfma_f32_16x16x32_bf16 v[38:41], v[200:203], v[8:11], v[38:41]\n\t"
  "ds_read_b128 v[8:11], v1 offset:28672\n\t"
  "s_waitcnt lgkmcnt(3)\n\t"
  "v_mfma_f32_16x16x32_bf16 v[30:33], v[204:207], v[12:15], v[30:33]\n\t"
  "v_mfma_f32_16x16x32_bf16 v[34:37], v[208:211], v[12:15], v[34:37]\n\t"
  "v_mfma_f32_16x16x32_bf16 v[38:41], v[212:215], v[12:15], v[38:41]\n\t"
  "ds_read_b128 v[12:15], v1 offset:29696\n\t"
  "s_waitcnt lgkmcnt(3)\n\t"
  "v_mfma_f32_16x16x32_bf16 v[30:33], v[216:219], v[16:19], v[30:33]\n\t"
  "v_mfma_f32_16x16x32_bf16 v[34:37], v[220:223], v[16:19], v[34:37]\n\t"
  "v_mfma_f32_16x16x32_bf16 v[38:41], v[160:163], v[16:19], v[38:41]\n\t"
  "ds_read_b128 v[16:19], v1 offset:30720\n\t"
  "s_waitcnt lgkmcnt(3)\n\t"
  "v_mfma_f32_16x16x32_bf16 v[30:33], v[164:167], v[20:23], v[30:33]\n\t"
  "s_sub_u32 s71, s71, s74\n\t"
  "s_sub_u32 s74, 0, s74\n\t"
  "v_readlane_b32 s68, v4, s57\n\t"
  "v_mfma_f32_16x16x32_bf16 v[34:37], v[168:171], v[20:23], v[34:37]\n\t"
  "s_mov_b32 s58, 0\n\t"
  "v_readlane_b32 s69, v5, s56\n\t"
  "v_mfma_f32_16x16x32_bf16 v[38:41], v[172:175], v[20:23], v[38:41]\n\t"
  "ds_read_b128 v[20:23], v1 offset:31744\n\t"
  "s_waitcnt lgkmcnt(3)\n\t"
  "v_mfma_f32_16x16x32_bf16 v[30:33], v[176:179], v[8:11], v[30:33]\n\t"
  "v_mfma_f32_16x16x32_bf16 v[34:37], v[180:183], v[8:11], v[34:37]\n\t"
  "s_waitcnt vmcnt(2)\n\t"
  "s_barrier\n\t"
  "v_mfma_f32_16x16x32_bf16 v[38:41], v[184:187], v[8:11], v[38:41]\n\t"
  "ds_read_b128 v[8:11], v1 offset:32768\n\t"
  "s_waitcnt lgkmcnt(3)\n\t"
  "v_mfma_f32_16x16x32_bf16 v[30:33], v[188:191], v[12:15], v[30:33]\n\t"
  "v_cmp_le_i32 vcc, s57, v3\n\t"
  "v_mfma_f32_16x16x32_bf16 v[34:37], v[128:131], v[12:15], v[34:37]\n\t"
  "s_cmp_eq_u64 vcc, -1\n\t"
  "s_cselect_b32 s75, 1, 0\n\t"
  "s_mov_b32 s59, s75\n\t"
  "v_mfma_f32_16x16x32_bf16 v[38:41], v[132:135], v[12:15], v[38:41]\n\t"
  "ds_read_b128 v[12:15], v1 offset:33792\n\t"
  "s_waitcnt lgkmcnt(3)\n\t"
  "v_mfma_f32_16x16x32_bf16 v[30:33], v[136:139], v[16:19], v[30:33]\n\t"
  "s_andn2_b32 s75, s75, s58\n\t"
  "s_cselect_b32 s62, s51, 0\n\t"
  "s_cselect_b32 s72, s71, 155648\n\t"
  "v_mfma_f32_16x16x32_bf16 v[34:37], v[140:143], v[16:19], v[34:37]\n\t"
  "s_lshl_b32 s73, s75, 13\n\t"
  "s_or_b32 s58, s58, s75\n\t"
  "v_mfma_f32_16x16x32_bf16 v[38:41], v[144:147], v[16:19], v[38:41]\n\t"
  "ds_read_b128 v[16:19], v1 offset:34816\n\t"
  "s_waitcnt lgkmcnt(3)\n\t"
  "v_mfma_f32_16x16x32_bf16 v[30:33], v[148:151], v[20:23], v[30:33]\n\t"
  "s_mov_b32 m0, s72\n\t"
  "s_add_u32 s76, s68, 0\n\t"
  "buffer_load_dwordx4 v0, s[60:63], s76 offen sc1 lds\n\t"
  "v_mfma_f32_16x16x32_bf16 v[34:37], v[152:155], v[20:23], v[34:37]\n\t"
  "v_mfma_f32_16x16x32_bf16 v[38:41], v[156:159], v[20:23], v[38:41]\n\t"
  "ds_read_b128 v[20:23], v1 offset:35840\n\t"
  "s_waitcnt lgkmcnt(3)\n\t"
  "v_mfma_f32_16x16x32_bf16 v[42:45], a[0:3], v[8:11], 0\n\t"
  "v_mfma_f32_16x16x32_bf16 v[46:49], a[4:7], v[8:11], 0\n\t"
  "s_add_u32 s72, s72, s73\n\t"
  "buffer_load_dwordx4 v0, s[60:63], s76 offen offset:1024 sc1 lds\n\t"
  "v_cvt_pk_bf16_f32 v24, v30, v31\n\t"
  "v_mfma_f32_16x16x32_bf16 v[50:53], a[8:11], v[8:11], 0\n\t"
  "ds_read_b128 v[8:11], v1 offset:36864\n\t"
  "s_waitcnt lgkmcnt(3)\n\t"
  "v_mfma_f32_16x16x32_bf16 v[42:45], a[12:15], v[12:15], v[42:45]\n\t"
  "v_cvt_pk_bf16_f32 v25, v32, v33\n\t"
  "v_cvt_pk_bf16_f32 v26, v34, v35\n\t"
  "v_mfma_f32_16x16x32_bf16 v[46:49], a[16:19], v[12:15], v[46:49]\n\t"
  "v_cvt_pk_bf16_f32 v27, v36, v37\n\t"
  "v_cvt_pk_bf16_f32 v28, v38, v39\n\t"
  "v_mfma_f32_16x16x32_bf16 v[50:53], a[20:23], v[12:15], v[50:53]\n\t"
  "ds_read_b128 v[12:15], v1 offset:37888\n\t"
  "s_waitcnt lgkmcnt(3)\n\t"
  "v_mfma_f32_16x16x32_bf16 v[42:45], a[24:27], v[16:19], v[42:45]\n\t"
  "s_mov_b32 m0, s72\n\t"
  "s_add_u32 s76, s68, 16384\n\t"
  "buffer_load_dwordx4 v0, s[60:63], s76 offen sc1 lds\n\t"
  "v_mfma_f32_16x16x32_bf16 v[46:49], a[28:31], v[16:19], v[46:49]\n\t"
  "v_cvt_pk_bf16_f32 v29, v40, v41\n\t"
  "v_mfma_f32_16x16x32_bf16 v[50:53], a[32:35], v[16:19], v[50:53]\n\t"
  "ds_read_b128 v[16:19], v1 offset:38912\n\t"
  "s_waitcnt lgkmcnt(3)\n\t"
  "v_mfma_f32_16x16x32_bf16 v[42:45], a[36:39], v[20:23], v[42:45]\n\t"
  "v_mfma_f32_16x16x32_bf16 v[46:49], a[40:43], v[20:23], v[46:49]\n\t"
  "s_add_u32 s72, s72, s73\n\t"
  "buffer_load_dwordx4 v0, s[60:63], s76 offen offset:1024 sc1 lds\n\t"
  "v_mfma_f32_16x16x32_bf16 v[50:53], a[44:47], v[20:23], v[50:53]\n\t"
  "ds_read_b128 v[20:23], v1 offset:39936\n\t"
  "s_waitcnt lgkmcnt(3)\n\t"
  "v_mfma_f32_16x16x32_bf16 v[42:45], a[48:51], v[8:11], v[42:45]\n\t"
  "v_mfma_f32_16x16x32_bf16 v[46:49], a[52:55], v[8:11], v[46:49]\n\t"
  "v_mfma_f32_16x16x32_bf16 v[50:53], a[56:59], v[8:11], v[50:53]\n\t"
  "ds_read_b128 v[8:11], v1 offset:40960\n\t"
  "s_waitcnt lgkmcnt(3)\n\t"
  "v_mfma_f32_16x16x32_bf16 v[42:45], a[60:63], v[12:15], v[42:45]\n\t"
  "s_mov_b32 m0, s72\n\t"
  "s_add_u32 s76, s68, 32768\n\t"
  "buffer_load_dwordx4 v0, s[60:63], s76 offen sc1 lds\n\t"
  "v_mfma_f32_16x16x32_bf16 v[46:49], a[64:67], v[12:15], v[46:49]\n\t"
  "v_mfma_f32_16x16x32_bf16 v[50:53], a[68:71], v[12:15], v[50:53]\n\t"
  "ds_read_b128 v[12:15], v1 offset:41984\n\t"
  "s_waitcnt lgkmcnt(3)\n\t"
  "v_mfma_f32_16x16x32_bf16 v[42:45], a[72:75], v[16:19], v[42:45]\n\t"
  "v_mfma_f32_16x16x32_bf16 v[46:49], a[76:79], v[16:19], v[46:49]\n\t"
  "s_add_u32 s72, s72, s73\n\t"
  "buffer_load_dwordx4 v0, s[60:63], s76 offen offset:1024 sc1 lds\n\t"
  "v_mfma_f32_16x16x32_bf16 v[50:53], a[80:83], v[16:19], v[50:53]\n\t"
  "ds_read_b128 v[16:19], v1 offset:43008\n\t"
  "s_waitcnt lgkmcnt(3)\n\t"
  "v_mfma_f32_16x16x32_bf16 v[42:45], a[84:87], v[20:23], v[42:45]\n\t"
  "v_mfma_f32_16x16x32_bf16 v[46:49], a[88:91], v[20:23], v[46:49]\n\t"
  "v_mfma_f32_16x16x32_bf16 v[50:53], a[92:95], v[20:23], v[50:53]\n\t"
  "ds_read_b128 v[20:23], v1 offset:44032\n\t"
  "s_waitcnt lgkmcnt(3)\n\t"
  "v_mfma_f32_16x16x32_bf16 v[42:45], a[96:99], v[8:11], v[42:45]\n\t"
  "s_mov_b32 m0, s72\n\t"
  "s_add_u32 s76, s68, 49152\n\t"
  "buffer_load_dwordx4 v0, s[60:63], s76 offen sc1 lds\n\t"
  "v_mfma_f32_16x16x32_bf16 v[46:49], a[100:103], v[8:11], v[46:49]\n\t"
  "v_mfma_f32_16x16x32_bf16 v[50:53], a[104:107], v[8:11], v[50:53]\n\t"
  "ds_read_b128 v[8:11], v1 offset:45056\n\t"
  "s_waitcnt lgkmcnt(3)\n\t"
  "v_mfma_f32_16x16x32_bf16 v[42:45], a[108:111], v[12:15], v[42:45]\n\t"
  "v_mfma_f32_16x16x32_bf16 v[46:49], a[112:115], v[12:15], v[46:49]\n\t"
  "buffer_load_dword v3, v2, s[36:39], 0 offen sc1\n\t"
  "v_mfma_f32_16x16x32_bf16 v[50:53], a[116:119], v[12:15], v[50:53]\n\t"
  "ds_read_b128 v[12:15], v1 offset:46080\n\t"
  "s_waitcnt lgkmcnt(3)\n\t"
  "v_mfma_f32_16x16x32_bf16 v[42:45], a[120:123], v[16:19], v[42:45]\n\t"
  "v_mfma_f32_16x16x32_bf16 v[46:49], a[124:127], v[16:19], v[46:49]\n\t"
  "v_mfma_f32_16x16x32_bf16 v[50:53], a[128:131], v[16:19], v[50:53]\n\t"
  "ds_read_b128 v[16:19], v1 offset:47104\n\t"
  "s_waitcnt lgkmcnt(3)\n\t"
  "v_mfma_f32_16x16x32_bf16 v[42:45], a[132:135], v[20:23], v[42:45]\n\t"
  "s_add_u32 s72, s72, s73\n\t"
  "buffer_load_dwordx4 v0, s[60:63], s76 offen offset:1024 sc1 lds\n\t"
  "v_mfma_f32_16x16x32_bf16 v[46:49], a[136:139], v[20:23], v[46:49]\n\t"
  "v_mfma_f32_16x16x32_bf16 v[50:53], a[140:143], v[20:23], v[50:53]\n\t"
  "ds_read_b128 v[20:23], v1 offset:48128\n\t"
  "s_waitcnt lgkmcnt(3)\n\t"
  "v_mfma_f32_16x16x32_bf16 v[42:45], a[144:147], v[8:11], v[42:45]\n\t"
  "v_mfma_f32_16x16x32_bf16 v[46:49], a[148:151], v[8:11], v[46:49]\n\t"
  "v_mfma_f32_16x16x32_bf16 v[50:53], a[152:155], v[8:11], v[50:53]\n\t"
  "ds_read_b128 v[8:11], v1 offset:49152\n\t"
  "s_waitcnt lgkmcnt(3)\n\t"
  "v_mfma_f32_16x16x32_bf16 v[42:45], a[156:159], v[12:15], v[42:45]\n\t"
  "v_mfma_f32_16x16x32_bf16 v[46:49], a[160:163], v[12:15], v[46:49]\n\t"
  "v_mfma_f32_16x16x32_bf16 v[50:53], a[164:167], v[12:15], v[50:53]\n\t"
  "ds_read_b128 v[12:15], v1 offset:50176\n\t"
  "s_waitcnt lgkmcnt(3)\n\t"
  "v_mfma_f32_16x16x32_bf16 v[42:45], a[168:171], v[16:19], v[42:45]\n\t"
  "v_mfma_f32_16x16x32_bf16 v[46:49], a[172:175], v[16:19], v[46:49]\n\t"
  "v_mfma_f32_16x16x32_bf16 v[50:53], a[176:179], v[16:19], v[50:53]\n\t"
  "ds_read_b128 v[16:19], v1 offset:51200\n\t"
  "s_waitcnt lgkmcnt(3)\n\t"
  "v_mfma_f32_16x16x32_bf16 v[42:45], a[180:183], v[20:23], v[42:45]\n\t"
  "v_cmp_le_i32 vcc, s57, v3\n\t"
  "v_mfma_f32_16x16x32_bf16 v[46:49], a[184:187], v[20:23], v[46:49]\n\t"
  "s_cmp_eq_u64 vcc, -1\n\t"
  "s_cselect_b32 s75, 1, 0\n\t"
  "v_mfma_f32_16x16x32_bf16 v[50:53], a[188:191], v[20:23], v[50:53]\n\t"
  "ds_read_b128 v[20:23], v1 offset:52224\n\t"
  "s_waitcnt lgkmcnt(3)\n\t"
  "v_mfma_f32_16x16x32_bf16 v[42:45], a[192:195], v[8:11], v[42:45]\n\t"
  "s_andn2_b32 s75, s75, s58\n\t"
  "s_cselect_b32 s62, s51, 0\n\t"
  "s_cselect_b32 s72, s71, 155648\n\t"
  "v_mfma_f32_16x16x32_bf16 v[46:49], a[196:199], v[8:11], v[46:49]\n\t"
  "s_lshl_b32 s73, s75, 13\n\t"
  "s_or_b32 s58, s58, s75\n\t"
  "v_mfma_f32_16x16x32_bf16 v[50:53], a[200:203], v[8:11], v[50:53]\n\t"
  "ds_read_b128 v[8:11], v1 offset:53248\n\t"
  "s_waitcnt lgkmcnt(3)\n\t"
  "v_mfma_f32_16x16x32_bf16 v[42:45], a[204:207], v[12:15], v[42:45]\n\t"
  "s_mov_b32 m0, s72\n\t"
  "s_add_u32 s76, s68, 0\n\t"
  "buffer_load_dwordx4 v0, s[60:63], s76 offen sc1 lds\n\t"
  "v_mfma_f32_16x16x32_bf16 v[46:49], a[208:211], v[12:15], v[46:49]\n\t"
  "v_mfma_f32_16x16x32_bf16 v[50:53], a[212:215], v[12:15], v[50:53]\n\t"
  "ds_read_b128 v[12:15], v1 offset:54272\n\t"
  "s_waitcnt lgkmcnt(3)\n\t"
  "v_mfma_f32_16x16x32_bf16 v[42:45], a[216:219], v[16:19], v[42:45]\n\t"
  "v_mfma_f32_16x16x32_bf16 v[46:49], a[220:223], v[16:19], v[46:49]\n\t"
  "s_add_u32 s72, s72, s73\n\t"
  "buffer_load_dwordx4 v0, s[60:63], s76 offen offset:1024 sc1 lds\n\t"
  "v_mfma_f32_16x16x32_bf16 v[50:53], a[224:227], v[16:19], v[50:53]\n\t"
  "ds_read_b128 v[16:19], v1 offset:55296\n\t"
  "s_waitcnt lgkmcnt(3)\n\t"
  "v_mfma_f32_16x16x32_bf16 v[42:45], a[228:231], v[20:23], v[42:45]\n\t"
  "v_mfma_f32_16x16x32_bf16 v[46:49], a[232:235], v[20:23], v[46:49]\n\t"
  "v_mfma_f32_16x16x32_bf16 v[50:53], a[236:239], v[20:23], v[50:53]\n\t"
  "ds_read_b128 v[20:23], v1 offset:56320\n\t"
  "s_waitcnt lgkmcnt(3)\n\t"
  "v_mfma_f32_16x16x32_bf16 v[42:45], a[240:243], v[8:11], v[42:45]\n\t"
  "s_mov_b32 m0, s72\n\t"
  "s_add_u32 s76, s68, 16384\n\t"
  "buffer_load_dwordx4 v0, s[60:63], s76 offen sc1 lds\n\t"
  "v_mfma_f32_16x16x32_bf16 v[46:49], a[244:247], v[8:11], v[46:49]\n\t"
  "v_mfma_f32_16x16x32_bf16 v[50:53], a[248:251], v[8:11], v[50:53]\n\t"
  "ds_read_b128 v[8:11], v1 offset:57344\n\t"
  "s_waitcnt lgkmcnt(3)\n\t"
  "v_mfma_f32_16x16x32_bf16 v[42:45], a[252:255], v[12:15], v[42:45]\n\t"
  "v_mfma_f32_16x16x32_bf16 v[46:49], v[224:227], v[12:15], v[46:49]\n\t"
  "s_add_u32 s72, s72, s73\n\t"
  "buffer_load_dwordx4 v0, s[60:63], s76 offen offset:1024 sc1 lds\n\t"
  "v_mfma_f32_16x16x32_bf16 v[50:53], v[228:231], v[12:15], v[50:53]\n\t"
  "ds_read_b128 v[12:15], v1 offset:58368\n\t"
  "s_waitcnt lgkmcnt(3)\n\t"
  "v_mfma_f32_16x16x32_bf16 v[42:45], v[232:235], v[16:19], v[42:45]\n\t"
  "v_mfma_f32_16x16x32_bf16 v[46:49], v[236:239], v[16:19], v[46:49]\n\t"
  "v_mfma_f32_16x16x32_bf16 v[50:53], v[240:243], v[16:19], v[50:53]\n\t"
  "ds_read_b128 v[16:19], v1 offset:59392\n\t"
  "s_waitcnt lgkmcnt(3)\n\t"
  "v_mfma_f32_16x16x32_bf16 v[42:45], v[244:247], v[20:23], v[42:45]\n\t"
  "s_mov_b32 m0, s72\n\t"
  "s_add_u32 s76, s68, 32768\n\t"
  "buffer_load_dwordx4 v0, s[60:63], s76 offen sc1 lds\n\t"
  "v_mfma_f32_16x16x32_bf16 v[46:49], v[248:251], v[20:23], v[46:49]\n\t"
  "v_mfma_f32_16x16x32_bf16 v[50:53], v[252:255], v[20:23], v[50:53]\n\t"
  "ds_read_b128 v[20:23], v1 offset:60416\n\t"
  "s_waitcnt lgkmcnt(3)\n\t"
  "v_mfma_f32_16x16x32_bf16 v[42:45], v[192:195], v[8:11], v[42:45]\n\t"
  "v_mfma_f32_16x16x32_bf16 v[46:49], v[196:199], v[8:11], v[46:49]\n\t"
  "buffer_load_dword v3, v2, s[36:39], 0 offen sc1\n\t"
  "v_mfma_f32_16x16x32_bf16 v[50:53], v[200:203], v[8:11], v[50:53]\n\t"
  "ds_read_b128 v[8:11], v1 offset:61440\n\t"
  "s_waitcnt lgkmcnt(3)\n\t"
  "v_mfma_f32_16x16x32_bf16 v[42:45], v[204:207], v[12:15], v[42:45]\n\t"
  "v_mfma_f32_16x16x32_bf16 v[46:49], v[208:211], v[12:15], v[46:49]\n\t"
  "v_mfma_f32_16x16x32_bf16 v[50:53], v[212:215], v[12:15], v[50:53]\n\t"
  "ds_read_b128 v[12:15], v1 offset:62464\n\t"
  "s_waitcnt lgkmcnt(3)\n\t"
  "v_mfma_f32_16x16x32_bf16 v[42:45], v[216:219], v[16:19], v[42:45]\n\t"
  "s_add_u32 s72, s72, s73\n\t"
  "buffer_load_dwordx4 v0, s[60:63], s76 offen offset:1024 sc1 lds\n\t"
  "v_mfma_f32_16x16x32_bf16 v[46:49], v[220:223], v[16:19], v[46:49]\n\t"
  "v_mfma_f32_16x16x32_bf16 v[50:53], v[160:163], v[16:19], v[50:53]\n\t"
  "ds_read_b128 v[16:19], v1 offset:63488\n\t"
  "s_waitcnt lgkmcnt(3)\n\t"
  "v_mfma_f32_16x16x32_bf16 v[42:45], v[164:167], v[20:23], v[42:45]\n\t"
  "v_mfma_f32_16x16x32_bf16 v[46:49], v[168:171], v[20:23], v[46:49]\n\t"
  "s_mov_b32 m0, s72\n\t"
  "s_add_u32 s76, s68, 49152\n\t"
  "buffer_load_dwordx4 v0, s[60:63], s76 offen sc1 lds\n\t"
  "v_mfma_f32_16x16x32_bf16 v[50:53], v[172:175], v[20:23], v[50:53]\n\t"
  "ds_read_b128 v[20:23], v1 offset:64512\n\t"
  "s_waitcnt lgkmcnt(3)\n\t"
  "v_mfma_f32_16x16x32_bf16 v[42:45], v[176:179], v[8:11], v[42:45]\n\t"
  "v_add_u32 v1, s74, v1\n\t"
  "v_mfma_f32_16x16x32_bf16 v[46:49], v[180:183], v[8:11], v[46:49]\n\t"
  "v_mfma_f32_16x16x32_bf16 v[50:53], v[184:187], v[8:11], v[50:53]\n\t"
  "s_waitcnt lgkmcnt(2)\n\t"
  "v_mfma_f32_16x16x32_bf16 v[42:45], v[188:191], v[12:15], v[42:45]\n\t"
  "s_add_u32 s72, s72, s73\n\t"
  "buffer_load_dwordx4 v0, s[60:63], s76 offen offset:1024 sc1 lds\n\t"
  "v_mfma_f32_16x16x32_bf16 v[46:49], v[128:131], v[12:15], v[46:49]\n\t"
  "v_mfma_f32_16x16x32_bf16 v[50:53], v[132:135], v[12:15], v[50:53]\n\t"
  "s_waitcnt lgkmcnt(1)\n\t"
  "v_mfma_f32_16x16x32_bf16 v[42:45], v[136:139], v[16:19], v[42:45]\n\t"
  "v_mfma_f32_16x16x32_bf16 v[46:49], v[140:143], v[16:19], v[46:49]\n\t"
  "s_lshl_b32 s52, s58, 7\n\t"
  "s_add_u32 s52, s52, s57\n\t"
  "v_mfma_f32_16x16x32_bf16 v[50:53], v[144:147], v[16:19], v[50:53]\n\t"
  "s_waitcnt lgkmcnt(0)\n\t"
  "v_mfma_f32_16x16x32_bf16 v[42:45], v[148:151], v[20:23], v[42:45]\n\t"
  "v_mfma_f32_16x16x32_bf16 v[46:49], v[152:155], v[20:23], v[46:49]\n\t"
  "v_mfma_f32_16x16x32_bf16 v[50:53], v[156:159], v[20:23], v[50:53]\n\t"
  "s_nop 0\n\t"
  "v_cmp_le_i32 vcc, s52, v3\n\t"
  "s_cmp_eq_u64 vcc, -1\n\t"
  "s_cbranch_scc1 80f\n\t"
  "70:\n\t"
  "s_nop 3\n\t"
  "v_cvt_pk_bf16_f32 v45, v44, v45\n\t"
  "v_cvt_pk_bf16_f32 v44, v42, v43\n\t"
  "v_mov_b64 v[42:43], v[24:25]\n\t"
  "s_add_u32 s77, s69, s44\n\t"
  "buffer_store_dwordx4 v[42:45], v6, s[36:39], s77 offen sc1\n\t"
  "v_cvt_pk_bf16_f32 v49, v48, v49\n\t"
  "v_cvt_pk_bf16_f32 v48, v46, v47\n\t"
  "v_mov_b64 v[46:47], v[26:27]\n\t"
  "s_add_u32 s77, s69, s45\n\t"
  "buffer_store_dwordx4 v[46:49], v6, s[36:39], s77 offen sc1\n\t"
  "v_cvt_pk_bf16_f32 v53, v52, v53\n\t"
  "v_cvt_pk_bf16_f32 v52, v50, v51\n\t"
  "v_mov_b64 v[50:51], v[28:29]\n\t"
  "s_add_u32 s77, s69, s46\n\t"
  "buffer_store_dwordx4 v[50:53], v6, s[36:39], s77 offen sc1\n\t"
  "s_mov_b32 s86, s51\n\t"
  "s_mov_b32 s66, 0\n\t"
  "s_cmp_lg_u32 s58, 0\n\t"
  "s_cbranch_scc1 5f\n\t"
  "v_cmp_le_i32 vcc, s57, v3\n\t"
  "s_cmp_eq_u64 vcc, -1\n\t"
  "s_cbranch_scc1 41f\n\t"
  "buffer_load_dword v3, v2, s[36:39], 0 offen sc1\n\t"
  "s_sleep 2\n\t"
  "v_cmp_le_i32 vcc, s57, v3\n\t"
  "s_cmp_eq_u64 vcc, -1\n\t"
  "s_cbranch_scc1 41f\n\t"
  "buffer_load_dword v3, v2, s[36:39], 0 offen sc1\n\t"
  "s_sleep 2\n\t"
  "v_cmp_le_i32 vcc, s57, v3\n\t"
  "s_cmp_eq_u64 vcc, -1\n\t"
  "s_cbranch_scc1 41f\n\t"
  "buffer_load_dword v3, v2, s[36:39], 0 offen sc1\n\t"
  "s_sleep 2\n\t"
  "v_cmp_le_i32 vcc, s57, v3\n\t"
  "s_cmp_eq_u64 vcc, -1\n\t"
  "s_cbranch_scc1 41f\n\t"
  "buffer_load_dword v3, v2, s[36:39], 0 offen sc1\n\t"
  "s_sleep 2\n\t"
  "s_mov_b32 s86, 0\n\t"
  "v_mov_b32 v30, s56\n\t"
  "s_waitcnt vmcnt(4)\n\t"
  "buffer_store_dword v30, off, s[36:39], s40 sc1\n\t"
  "s_cmp_eq_u32 s56, 63\n\t"
  "s_cbranch_scc1 9f\n\t"
  "3:\n\t"
  "s_mov_b32 s79, 0\n\t"
  "s_mov_b32 s66, 0\n\t"
  "4:\n\t"
  "v_cmp_le_i32 vcc, s57, v3\n\t"
  "s_cmp_eq_u64 vcc, -1\n\t"
  "s_cbranch_scc1 41f\n\t"
  "s_waitcnt vmcnt(8)\n\t"
  "buffer_load_dword v3, v2, s[36:39], 0 offen sc1\n\t"
  "s_sleep 4\n\t"
  "s_add_u32 s79, s79, 1\n\t"
  "s_cmp_lt_u32 s79, s80\n\t"
  "s_cbranch_scc1 4b\n\t"
  "s_mov_b32 s80, 0\n\t"
  "41:\n\t"
  "s_add_u32 m0, s71, 0\n\t"
  "s_add_u32 s76, s68, 0\n\t"
  "buffer_load_dwordx4 v0, s[36:39], s76 offen sc1 lds\n\t"
  "buffer_load_dwordx4 v0, s[36:39], s76 offen offset:1024 sc1 lds\n\t"
  "s_add_u32 m0, s71, 8192\n\t"
  "s_add_u32 s76, s68, 16384\n\t"
  "buffer_load_dwordx4 v0, s[36:39], s76 offen sc1 lds\n\t"
  "buffer_load_dwordx4 v0, s[36:39], s76 offen offset:1024 sc1 lds\n\t"
  "s_add_u32 m0, s71, 16384\n\t"
  "s_add_u32 s76, s68, 32768\n\t"
  "buffer_load_dwordx4 v0, s[36:39], s76 offen sc1 lds\n\t"
  "buffer_load_dwordx4 v0, s[36:39], s76 offen offset:1024 sc1 lds\n\t"
  "s_add_u32 m0, s71, 24576\n\t"
  "s_add_u32 s76, s68, 49152\n\t"
  "buffer_load_dwordx4 v0, s[36:39], s76 offen sc1 lds\n\t"
  "buffer_load_dwordx4 v0, s[36:39], s76 offen offset:1024 sc1 lds\n\t"
  "v_mov_b32 v30, s56\n\t"
  "buffer_store_dword v30, off, s[64:67], s40 sc1\n\t"
  "buffer_store_dword v30, off, s[64:67], s40 sc1\n\t"
  "s_waitcnt vmcnt(10)\n\t"
  "buffer_store_dword v30, off, s[84:87], s40 sc1\n\t"
  "s_mov_b32 s86, 0\n\t"
  "s_waitcnt vmcnt(7)\n\t"
  "s_branch 6f\n\t"
  "5:\n\t"
  "s_waitcnt vmcnt(7)\n\t"
  "6:\n\t"
  "s_waitcnt lgkmcnt(0)\n\t"
  "s_barrier\n\t"
  "ds_read_b128 v[8:11], v1 offset:0\n\t"
  "ds_read_b128 v[12:15], v1 offset:1024\n\t"
  "ds_read_b128 v[16:19], v1 offset:2048\n\t"
  "s_branch 1b\n\t"
  "80:\n\t"
  "s_add_u32 m0, s71, 0\n\t"
  "s_add_u32 s76, s68, 0\n\t"
  "buffer_load_dwordx4 v0, s[36:39], s76 offen sc1 lds\n\t"
  "buffer_load_dwordx4 v0, s[36:39], s76 offen offset:1024 sc1 lds\n\t"
  "s_add_u32 m0, s71, 8192\n\t"
  "s_add_u32 s76, s68, 16384\n\t"
  "buffer_load_dwordx4 v0, s[36:39], s76 offen sc1 lds\n\t"
  "buffer_load_dwordx4 v0, s[36:39], s76 offen offset:1024 sc1 lds\n\t"
  "s_add_u32 m0, s71, 16384\n\t"
  "s_add_u32 s76, s68, 32768\n\t"
  "buffer_load_dwordx4 v0, s[36:39], s76 offen sc1 lds\n\t"
  "buffer_load_dwordx4 v0, s[36:39], s76 offen offset:1024 sc1 lds\n\t"
  "s_add_u32 m0, s71, 24576\n\t"
  "s_add_u32 s76, s68, 49152\n\t"
  "buffer_load_dwordx4 v0, s[36:39], s76 offen sc1 lds\n\t"
  "buffer_load_dwordx4 v0, s[36:39], s76 offen offset:1024 sc1 lds\n\t"
  "s_mov_b32 s58, 1\n\t"
  "s_mov_b32 s52, 127\n\t"
  "s_branch 70b\n\t"
  "9:\n\t"
  ".Lws_end_B:\n\t"
  "s_waitcnt vmcnt(0)\n\t"
  "s_waitcnt vmcnt(0) lgkmcnt(0)\n\t"
    : "+{v[0:7]}"(lc), "+{v[8:23]}"(so) :: CLOB_ALL);
  } else {
    asm volatile(
  "s_nop 4\n\t"
  "v_readlane_b32 s36, v3, 0\n\t"
  "v_readlane_b32 s37, v3, 1\n\t"
  "v_readlane_b32 s38, v3, 2\n\t"
  "v_readlane_b32 s39, v3, 3\n\t"
  "v_readlane_b32 s40, v3, 4\n\t"
  "v_readlane_b32 s41, v3, 5\n\t"
  "v_readlane_b32 s42, v3, 6\n\t"
  "v_readlane_b32 s43, v3, 7\n\t"
  "v_readlane_b32 s44, v3, 8\n\t"
  "v_readlane_b32 s45, v3, 9\n\t"
  "v_readlane_b32 s46, v3, 10\n\t"
  "v_readlane_b32 s47, v3, 11\n\t"
  "v_readlane_b32 s48, v3, 12\n\t"
  "v_readlane_b32 s49, v3, 13\n\t"
  "v_readlane_b32 s50, v3, 14\n\t"
  "v_readlane_b32 s51, v3, 15\n\t"
  "s_nop 4\n\t"
  "v_readlane_b32 s56, v3, 16\n\t"
  "v_readlane_b32 s57, v3, 17\n\t"
  "v_readlane_b32 s58, v3, 18\n\t"
  "v_readlane_b32 s60, v3, 19\n\t"
  "s_mov_b32 s59, s39\n\t"
  "s_mul_i32 s61, s41, 3\n\t"
  "s_lshr_b32 s61, s61, 1\n\t"
  "s_mov_b32 s62, 0\n\t"
  "s_nop 2\n\t"
  "s_add_u32 m0, s61, 0\n\t"
  "s_nop 0\n\t"
  "buffer_load_dwordx4 v8, s[56:59], s62 offen lds\n\t"
  "s_add_u32 m0, s61, 1024\n\t"
  "s_nop 0\n\t"
  "buffer_load_dwordx4 v9, s[56:59], s62 offen lds\n\t"
  "s_add_u32 m0, s61, 2048\n\t"
  "s_nop 0\n\t"
  "buffer_load_dwordx4 v10, s[56:59], s62 offen lds\n\t"
  "s_add_u32 s62, s62, s60\n\t"
  "s_add_u32 m0, s61, 12288\n\t"
  "s_nop 0\n\t"
  "buffer_load_dwordx4 v8, s[56:59], s62 offen lds\n\t"
  "s_add_u32 m0, s61, 13312\n\t"
  "s_nop 0\n\t"
  "buffer_load_dwordx4 v9, s[56:59], s62 offen lds\n\t"
  "s_add_u32 m0, s61, 14336\n\t"
  "s_nop 0\n\t"
  "buffer_load_dwordx4 v10, s[56:59], s62 offen lds\n\t"
  "s_add_u32 s62, s62, s60\n\t"
  "s_add_u32 m0, s61, 24576\n\t"
  "s_nop 0\n\t"
  "buffer_load_dwordx4 v8, s[56:59], s62 offen lds\n\t"
  "s_add_u32 m0, s61, 25600\n\t"
  "s_nop 0\n\t"
  "buffer_load_dwordx4 v9, s[56:59], s62 offen lds\n\t"
  "s_add_u32 m0, s61, 26624\n\t"
  "s_nop 0\n\t"
  "buffer_load_dwordx4 v10, s[56:59], s62 offen lds\n\t"
  "s_add_u32 s62, s62, s60\n\t"
  "s_add_u32 m0, s61, 36864\n\t"
  "s_nop 0\n\t"
  "buffer_load_dwordx4 v8, s[56:59], s62 offen lds\n\t"
  "s_add_u32 m0, s61, 37888\n\t"
  "s_nop 0\n\t"
  "buffer_load_dwordx4 v9, s[56:59], s62 offen lds\n\t"
  "s_add_u32 m0, s61, 38912\n\t"
  "s_nop 0\n\t"
  "buffer_load_dwordx4 v10, s[56:59], s62 offen lds\n\t"
  "s_add_u32 s62, s62, s60\n\t"
  "s_add_u32 m0, s61, 49152\n\t"
  "s_nop 0\n\t"
  "buffer_load_dwordx4 v8, s[56:59], s62 offen lds\n\t"
  "s_add_u32 m0, s61, 50176\n\t"
  "s_nop 0\n\t"
  "buffer_load_dwordx4 v9, s[56:59], s62 offen lds\n\t"
  "s_add_u32 m0, s61, 51200\n\t"
  "s_nop 0\n\t"
  "buffer_load_dwordx4 v10, s[56:59], s62 offen lds\n\t"
  "s_add_u32 s62, s62, s60\n\t"
  "s_add_u32 m0, s61, 61440\n\t"
  "s_nop 0\n\t"
  "buffer_load_dwordx4 v8, s[56:59], s62 offen lds\n\t"
  "s_add_u32 m0, s61, 62464\n\t"
  "s_nop 0\n\t"
  "buffer_load_dwordx4 v9, s[56:59], s62 offen lds\n\t"
  "s_add_u32 m0, s61, 63488\n\t"
  "s_nop 0\n\t"
  "buffer_load_dwordx4 v10, s[56:59], s62 offen lds\n\t"
  "s_add_u32 s62, s62, s60\n\t"
  "s_add_u32 m0, s61, 73728\n\t"
  "s_nop 0\n\t"
  "buffer_load_dwordx4 v8, s[56:59], s62 offen lds\n\t"
  "s_add_u32 m0, s61, 74752\n\t"
  "s_nop 0\n\t"
  "buffer_load_dwordx4 v9, s[56:59], s62 offen lds\n\t"
  "s_add_u32 m0, s61, 75776\n\t"
  "s_nop 0\n\t"
  "buffer_load_dwordx4 v10, s[56:59], s62 offen lds\n\t"
  "s_add_u32 s62, s62, s60\n\t"
  "s_add_u32 m0, s61, 86016\n\t"
  "s_nop 0\n\t"
  "buffer_load_dwordx4 v8, s[56:59], s62 offen lds\n\t"
  "s_add_u32 m0, s61, 87040\n\t"
  "s_nop 0\n\t"
  "buffer_load_dwordx4 v9, s[56:59], s62 offen lds\n\t"
  "s_add_u32 m0, s61, 88064\n\t"
  "s_nop 0\n\t"
  "buffer_load_dwordx4 v10, s[56:59], s62 offen lds\n\t"
  "s_add_u32 s62, s62, s60\n\t"
  "s_add_u32 m0, s61, 98304\n\t"
  "s_nop 0\n\t"
  "buffer_load_dwordx4 v8, s[56:59], s62 offen lds\n\t"
  "s_add_u32 m0, s61, 99328\n\t"
  "s_nop 0\n\t"
  "buffer_load_dwordx4 v9, s[56:59], s62 offen lds\n\t"
  "s_add_u32 m0, s61, 100352\n\t"
  "s_nop 0\n\t"
  "buffer_load_dwordx4 v10, s[56:59], s62 offen lds\n\t"
  "s_add_u32 s62, s62, s60\n\t"
  "s_add_u32 m0, s61, 110592\n\t"
  "s_nop 0\n\t"
  "buffer_load_dwordx4 v8, s[56:59], s62 offen lds\n\t"
  "s_add_u32 m0, s61, 111616\n\t"
  "s_nop 0\n\t"
  "buffer_load_dwordx4 v9, s[56:59], s62 offen lds\n\t"
  "s_add_u32 m0, s61, 112640\n\t"
  "s_nop 0\n\t"
  "buffer_load_dwordx4 v10, s[56:59], s62 offen lds\n\t"
  "s_add_u32 s62, s62, s60\n\t"
  "s_add_u32 m0, s61, 122880\n\t"
  "s_nop 0\n\t"
  "buffer_load_dwordx4 v8, s[56:59], s62 offen lds\n\t"
  "s_add_u32 m0, s61, 123904\n\t"
  "s_nop 0\n\t"
  "buffer_load_dwordx4 v9, s[56:59], s62 offen lds\n\t"
  "s_add_u32 m0, s61, 124928\n\t"
  "s_nop 0\n\t"
  "buffer_load_dwordx4 v10, s[56:59], s62 offen lds\n\t"
  "s_add_u32 s62, s62, s60\n\t"
  "s_waitcnt vmcnt(30) lgkmcnt(0)\n\t"
  "s_barrier\n\t"
  "s_add_u32 m0, s61, 135168\n\t"
  "s_nop 0\n\t"
  "buffer_load_dwordx4 v8, s[56:59], s62 offen lds\n\t"
  "s_add_u32 m0, s61, 136192\n\t"
  "s_nop 0\n\t"
  "buffer_load_dwordx4 v9, s[56:59], s62 offen lds\n\t"
  "s_add_u32 m0, s61, 137216\n\t"
  "s_nop 0\n\t"
  "buffer_load_dwordx4 v10, s[56:59], s62 offen lds\n\t"
  "s_add_u32 s62, s62, s60\n\t"
  "v_add_u32 v20, 0, v14\n\t"
  "ds_read_b64_tr_b16 a[0:1], v20\n\t"
  "ds_read_b64_tr_b16 a[2:3], v20 offset:6144\n\t"
  "v_add_u32 v20, 0, v15\n\t"
  "ds_read_b64_tr_b16 a[4:5], v20\n\t"
  "ds_read_b64_tr_b16 a[6:7], v20 offset:6144\n\t"
  "v_add_u32 v20, 0, v16\n\t"
  "ds_read_b64_tr_b16 a[8:9], v20\n\t"
  "ds_read_b64_tr_b16 a[10:11], v20 offset:6144\n\t"
  "s_waitcnt vmcnt(30) lgkmcnt(0)\n\t"
  "s_barrier\n\t"
  "s_add_u32 m0, s61, 0\n\t"
  "s_nop 0\n\t"
  "buffer_load_dwordx4 v8, s[56:59], s62 offen lds\n\t"
  "s_add_u32 m0, s61, 1024\n\t"
  "s_nop 0\n\t"
  "buffer_load_dwordx4 v9, s[56:59], s62 offen lds\n\t"
  "s_add_u32 m0, s61, 2048\n\t"
  "s_nop 0\n\t"
  "buffer_load_dwordx4 v10, s[56:59], s62 offen lds\n\t"
  "s_add_u32 s62, s62, s60\n\t"
  "v_add_u32 v20, 12288, v14\n\t"
  "ds_read_b64_tr_b16 a[12:13], v20\n\t"
  "ds_read_b64_tr_b16 a[14:15], v20 offset:6144\n\t"
  "v_add_u32 v20, 12288, v15\n\t"
  "ds_read_b64_tr_b16 a[16:17], v20\n\t"
  "ds_read_b64_tr_b16 a[18:19], v20 offset:6144\n\t"
  "v_add_u32 v20, 12288, v16\n\t"
  "ds_read_b64_tr_b16 a[20:21], v20\n\t"
  "ds_read_b64_tr_b16 a[22:23], v20 offset:6144\n\t"
  "s_waitcnt vmcnt(30) lgkmcnt(0)\n\t"
  "s_barrier\n\t"
  "s_add_u32 m0, s61, 12288\n\t"
  "s_nop 0\n\t"
  "buffer_load_dwordx4 v8, s[56:59], s62 offen lds\n\t"
  "s_add_u32 m0, s61, 13312\n\t"
  "s_nop 0\n\t"
  "buffer_load_dwordx4 v9, s[56:59], s62 offen lds\n\t"
  "s_add_u32 m0, s61, 14336\n\t"
  "s_nop 0\n\t"
  "buffer_load_dwordx4 v10, s[56:59], s62 offen lds\n\t"
  "s_add_u32 s62, s62, s60\n\t"
  "v_add_u32 v20, 24576, v14\n\t"
  "ds_read_b64_tr_b16 a[24:25], v20\n\t"
  "ds_read_b64_tr_b16 a[26:27], v20 offset:6144\n\t"
  "v_add_u32 v20, 24576, v15\n\t"
  "ds_read_b64_tr_b16 a[28:29], v20\n\t"
  "ds_read_b64_tr_b16 a[30:31], v20 offset:6144\n\t"
  "v_add_u32 v20, 24576, v16\n\t"
  "ds_read_b64_tr_b16 a[32:33], v20\n\t"
  "ds_read_b64_tr_b16 a[34:35], v20 offset:6144\n\t"
  "s_waitcnt vmcnt(30) lgkmcnt(0)\n\t"
  "s_barrier\n\t"
  "s_add_u32 m0, s61, 24576\n\t"
  "s_nop 0\n\t"
  "buffer_load_dwordx4 v8, s[56:59], s62 offen lds\n\t"
  "s_add_u32 m0, s61, 25600\n\t"
  "s_nop 0\n\t"
  "buffer_load_dwordx4 v9, s[56:59], s62 offen lds\n\t"
  "s_add_u32 m0, s61, 26624\n\t"
  "s_nop 0\n\t"
  "buffer_load_dwordx4 v10, s[56:59], s62 offen lds\n\t"
  "s_add_u32 s62, s62, s60\n\t"
  "v_add_u32 v20, 36864, v14\n\t"
  "ds_read_b64_tr_b16 a[36:37], v20\n\t"
  "ds_read_b64_tr_b16 a[38:39], v20 offset:6144\n\t"
  "v_add_u32 v20, 36864, v15\n\t"
  "ds_read_b64_tr_b16 a[40:41], v20\n\t"
  "ds_read_b64_tr_b16 a[42:43], v20 offset:6144\n\t"
  "v_add_u32 v20, 36864, v16\n\t"
  "ds_read_b64_tr_b16 a[44:45], v20\n\t"
  "ds_read_b64_tr_b16 a[46:47], v20 offset:6144\n\t"
  "s_waitcnt vmcnt(30) lgkmcnt(0)\n\t"
  "s_barrier\n\t"
  "s_add_u32 m0, s61, 36864\n\t"
  "s_nop 0\n\t"
  "buffer_load_dwordx4 v8, s[56:59], s62 offen lds\n\t"
  "s_add_u32 m0, s61, 37888\n\t"
  "s_nop 0\n\t"
  "buffer_load_dwordx4 v9, s[56:59], s62 offen lds\n\t"
  "s_add_u32 m0, s61, 38912\n\t"
  "s_nop 0\n\t"
  "buffer_load_dwordx4 v10, s[56:59], s62 offen lds\n\t"
  "s_add_u32 s62, s62, s60\n\t"
  "v_add_u32 v20, 49152, v14\n\t"
  "ds_read_b64_tr_b16 a[48:49], v20\n\t"
  "ds_read_b64_tr_b16 a[50:51], v20 offset:6144\n\t"
  "v_add_u32 v20, 49152, v15\n\t"
  "ds_read_b64_tr_b16 a[52:53], v20\n\t"
  "ds_read_b64_tr_b16 a[54:55], v20 offset:6144\n\t"
  "v_add_u32 v20, 49152, v16\n\t"
  "ds_read_b64_tr_b16 a[56:57], v20\n\t"
  "ds_read_b64_tr_b16 a[58:59], v20 offset:6144\n\t"
  "s_waitcnt vmcnt(30) lgkmcnt(0)\n\t"
  "s_barrier\n\t"
  "s_add_u32 m0, s61, 49152\n\t"
  "s_nop 0\n\t"
  "buffer_load_dwordx4 v8, s[56:59], s62 offen lds\n\t"
  "s_add_u32 m0, s61, 50176\n\t"
  "s_nop 0\n\t"
  "buffer_load_dwordx4 v9, s[56:59], s62 offen lds\n\t"
  "s_add_u32 m0, s61, 51200\n\t"
  "s_nop 0\n\t"
  "buffer_load_dwordx4 v10, s[56:59], s62 offen lds\n\t"
  "s_add_u32 s62, s62, s60\n\t"
  "v_add_u32 v20, 61440, v14\n\t"
  "ds_read_b64_tr_b16 a[60:61], v20\n\t"
  "ds_read_b64_tr_b16 a[62:63], v20 offset:6144\n\t"
  "v_add_u32 v20, 61440, v15\n\t"
  "ds_read_b64_tr_b16 a[64:65], v20\n\t"
  "ds_read_b64_tr_b16 a[66:67], v20 offset:6144\n\t"
  "v_add_u32 v20, 61440, v16\n\t"
  "ds_read_b64_tr_b16 a[68:69], v20\n\t"
  "ds_read_b64_tr_b16 a[70:71], v20 offset:6144\n\t"
  "s_waitcnt vmcnt(30) lgkmcnt(0)\n\t"
  "s_barrier\n\t"
  "s_add_u32 m0, s61, 61440\n\t"
  "s_nop 0\n\t"
  "buffer_load_dwordx4 v8, s[56:59], s62 offen lds\n\t"
  "s_add_u32 m0, s61, 62464\n\t"
  "s_nop 0\n\t"
  "buffer_load_dwordx4 v9, s[56:59], s62 offen lds\n\t"
  "s_add_u32 m0, s61, 63488\n\t"
  "s_nop 0\n\t"
  "buffer_load_dwordx4 v10, s[56:59], s62 offen lds\n\t"
  "s_add_u32 s62, s62, s60\n\t"
  "v_add_u32 v20, 73728, v14\n\t"
  "ds_read_b64_tr_b16 a[72:73], v20\n\t"
  "ds_read_b64_tr_b16 a[74:75], v20 offset:6144\n\t"
  "v_add_u32 v20, 73728, v15\n\t"
  "ds_read_b64_tr_b16 a[76:77], v20\n\t"
  "ds_read_b64_tr_b16 a[78:79], v20 offset:6144\n\t"
  "v_add_u32 v20, 73728, v16\n\t"
  "ds_read_b64_tr_b16 a[80:81], v20\n\t"
  "ds_read_b64_tr_b16 a[82:83], v20 offset:6144\n\t"
  "s_waitcnt vmcnt(30) lgkmcnt(0)\n\t"
  "s_barrier\n\t"
  "s_add_u32 m0, s61, 73728\n\t"
  "s_nop 0\n\t"
  "buffer_load_dwordx4 v8, s[56:59], s62 offen lds\n\t"
  "s_add_u32 m0, s61, 74752\n\t"
  "s_nop 0\n\t"
  "buffer_load_dwordx4 v9, s[56:59], s62 offen lds\n\t"
  "s_add_u32 m0, s61, 75776\n\t"
  "s_nop 0\n\t"
  "buffer_load_dwordx4 v10, s[56:59], s62 offen lds\n\t"
  "s_add_u32 s62, s62, s60\n\t"
  "v_add_u32 v20, 86016, v14\n\t"
  "ds_read_b64_tr_b16 a[84:85], v20\n\t"
  "ds_read_b64_tr_b16 a[86:87], v20 offset:6144\n\t"
  "v_add_u32 v20, 86016, v15\n\t"
  "ds_read_b64_tr_b16 a[88:89], v20\n\t"
  "ds_read_b64_tr_b16 a[90:91], v20 offset:6144\n\t"
  "v_add_u32 v20, 86016, v16\n\t"
  "ds_read_b64_tr_b16 a[92:93], v20\n\t"
  "ds_read_b64_tr_b16 a[94:95], v20 offset:6144\n\t"
  "s_waitcnt vmcnt(30) lgkmcnt(0)\n\t"
  "s_barrier\n\t"
  "s_add_u32 m0, s61, 86016\n\t"
  "s_nop 0\n\t"
  "buffer_load_dwordx4 v8, s[56:59], s62 offen lds\n\t"
  "s_add_u32 m0, s61, 87040\n\t"
  "s_nop 0\n\t"
  "buffer_load_dwordx4 v9, s[56:59], s62 offen lds\n\t"
  "s_add_u32 m0, s61, 88064\n\t"
  "s_nop 0\n\t"
  "buffer_load_dwordx4 v10, s[56:59], s62 offen lds\n\t"
  "s_add_u32 s62, s62, s60\n\t"
  "v_add_u32 v20, 98304, v14\n\t"
  "ds_read_b64_tr_b16 a[96:97], v20\n\t"
  "ds_read_b64_tr_b16 a[98:99], v20 offset:6144\n\t"
  "v_add_u32 v20, 98304, v15\n\t"
  "ds_read_b64_tr_b16 a[100:101], v20\n\t"
  "ds_read_b64_tr_b16 a[102:103], v20 offset:6144\n\t"
  "v_add_u32 v20, 98304, v16\n\t"
  "ds_read_b64_tr_b16 a[104:105], v20\n\t"
  "ds_read_b64_tr_b16 a[106:107], v20 offset:6144\n\t"
  "s_waitcnt vmcnt(30) lgkmcnt(0)\n\t"
  "s_barrier\n\t"
  "s_add_u32 m0, s61, 98304\n\t"
  "s_nop 0\n\t"
  "buffer_load_dwordx4 v8, s[56:59], s62 offen lds\n\t"
  "s_add_u32 m0, s61, 99328\n\t"
  "s_nop 0\n\t"
  "buffer_load_dwordx4 v9, s[56:59], s62 offen lds\n\t"
  "s_add_u32 m0, s61, 100352\n\t"
  "s_nop 0\n\t"
  "buffer_load_dwordx4 v10, s[56:59], s62 offen lds\n\t"
  "s_add_u32 s62, s62, s60\n\t"
  "v_add_u32 v20, 110592, v14\n\t"
  "ds_read_b64_tr_b16 a[108:109], v20\n\t"
  "ds_read_b64_tr_b16 a[110:111], v20 offset:6144\n\t"
  "v_add_u32 v20, 110592, v15\n\t"
  "ds_read_b64_tr_b16 a[112:113], v20\n\t"
  "ds_read_b64_tr_b16 a[114:115], v20 offset:6144\n\t"
  "v_add_u32 v20, 110592, v16\n\t"
  "ds_read_b64_tr_b16 a[116:117], v20\n\t"
  "ds_read_b64_tr_b16 a[118:119], v20 offset:6144\n\t"
  "s_waitcnt vmcnt(30) lgkmcnt(0)\n\t"
  "s_barrier\n\t"
  "s_add_u32 m0, s61, 110592\n\t"
  "s_nop 0\n\t"
  "buffer_load_dwordx4 v8, s[56:59], s62 offen lds\n\t"
  "s_add_u32 m0, s61, 111616\n\t"
  "s_nop 0\n\t"
  "buffer_load_dwordx4 v9, s[56:59], s62 offen lds\n\t"
  "s_add_u32 m0, s61, 112640\n\t"
  "s_nop 0\n\t"
  "buffer_load_dwordx4 v10, s[56:59], s62 offen lds\n\t"
  "s_add_u32 s62, s62, s60\n\t"
  "v_add_u32 v20, 122880, v14\n\t"
  "ds_read_b64_tr_b16 a[120:121], v20\n\t"
  "ds_read_b64_tr_b16 a[122:123], v20 offset:6144\n\t"
  "v_add_u32 v20, 122880, v15\n\t"
  "ds_read_b64_tr_b16 a[124:125], v20\n\t"
  "ds_read_b64_tr_b16 a[126:127], v20 offset:6144\n\t"
  "v_add_u32 v20, 122880, v16\n\t"
  "ds_read_b64_tr_b16 a[128:129], v20\n\t"
  "ds_read_b64_tr_b16 a[130:131], v20 offset:6144\n\t"
  "s_waitcnt vmcnt(30) lgkmcnt(0)\n\t"
  "s_barrier\n\t"
  "s_add_u32 m0, s61, 122880\n\t"
  "s_nop 0\n\t"
  "buffer_load_dwordx4 v8, s[56:59], s62 offen lds\n\t"
  "s_add_u32 m0, s61, 123904\n\t"
  "s_nop 0\n\t"
  "buffer_load_dwordx4 v9, s[56:59], s62 offen lds\n\t"
  "s_add_u32 m0, s61, 124928\n\t"
  "s_nop 0\n\t"
  "buffer_load_dwordx4 v10, s[56:59], s62 offen lds\n\t"
  "s_add_u32 s62, s62, s60\n\t"
  "v_add_u32 v20, 135168, v14\n\t"
  "ds_read_b64_tr_b16 a[132:133], v20\n\t"
  "ds_read_b64_tr_b16 a[134:135], v20 offset:6144\n\t"
  "v_add_u32 v20, 135168, v15\n\t"
  "ds_read_b64_tr_b16 a[136:137], v20\n\t"
  "ds_read_b64_tr_b16 a[138:139], v20 offset:6144\n\t"
  "v_add_u32 v20, 135168, v16\n\t"
  "ds_read_b64_tr_b16 a[140:141], v20\n\t"
  "ds_read_b64_tr_b16 a[142:143], v20 offset:6144\n\t"
  "s_waitcnt vmcnt(30) lgkmcnt(0)\n\t"
  "s_barrier\n\t"
  "s_add_u32 m0, s61, 135168\n\t"
  "s_nop 0\n\t"
  "buffer_load_dwordx4 v8, s[56:59], s62 offen lds\n\t"
  "s_add_u32 m0, s61, 136192\n\t"
  "s_nop 0\n\t"
  "buffer_load_dwordx4 v9, s[56:59], s62 offen lds\n\t"
  "s_add_u32 m0, s61, 137216\n\t"
  "s_nop 0\n\t"
  "buffer_load_dwordx4 v10, s[56:59], s62 offen lds\n\t"
  "s_add_u32 s62, s62, s60\n\t"
  "v_add_u32 v20, 0, v14\n\t"
  "ds_read_b64_tr_b16 a[144:145], v20\n\t"
  "ds_read_b64_tr_b16 a[146:147], v20 offset:6144\n\t"
  "v_add_u32 v20, 0, v15\n\t"
  "ds_read_b64_tr_b16 a[148:149], v20\n\t"
  "ds_read_b64_tr_b16 a[150:151], v20 offset:6144\n\t"
  "v_add_u32 v20, 0, v16\n\t"
  "ds_read_b64_tr_b16 a[152:153], v20\n\t"
  "ds_read_b64_tr_b16 a[154:155], v20 offset:6144\n\t"
  "s_waitcnt vmcnt(30) lgkmcnt(0)\n\t"
  "s_barrier\n\t"
  "s_add_u32 m0, s61, 0\n\t"
  "s_nop 0\n\t"
  "buffer_load_dwordx4 v8, s[56:59], s62 offen lds\n\t"
  "s_add_u32 m0, s61, 1024\n\t"
  "s_nop 0\n\t"
  "buffer_load_dwordx4 v9, s[56:59], s62 offen lds\n\t"
  "s_add_u32 m0, s61, 2048\n\t"
  "s_nop 0\n\t"
  "buffer_load_dwordx4 v10, s[56:59], s62 offen lds\n\t"
  "s_add_u32 s62, s62, s60\n\t"
  "v_add_u32 v20, 12288, v14\n\t"
  "ds_read_b64_tr_b16 a[156:157], v20\n\t"
  "ds_read_b64_tr_b16 a[158:159], v20 offset:6144\n\t"
  "v_add_u32 v20, 12288, v15\n\t"
  "ds_read_b64_tr_b16 a[160:161], v20\n\t"
  "ds_read_b64_tr_b16 a[162:163], v20 offset:6144\n\t"
  "v_add_u32 v20, 12288, v16\n\t"
  "ds_read_b64_tr_b16 a[164:165], v20\n\t"
  "ds_read_b64_tr_b16 a[166:167], v20 offset:6144\n\t"
  "s_waitcnt vmcnt(30) lgkmcnt(0)\n\t"
  "s_barrier\n\t"
  "s_add_u32 m0, s61, 12288\n\t"
  "s_nop 0\n\t"
  "buffer_load_dwordx4 v8, s[56:59], s62 offen lds\n\t"
  "s_add_u32 m0, s61, 13312\n\t"
  "s_nop 0\n\t"
  "buffer_load_dwordx4 v9, s[56:59], s62 offen lds\n\t"
  "s_add_u32 m0, s61, 14336\n\t"
  "s_nop 0\n\t"
  "buffer_load_dwordx4 v10, s[56:59], s62 offen lds\n\t"
  "s_add_u32 s62, s62, s60\n\t"
  "v_add_u32 v20, 24576, v14\n\t"
  "ds_read_b64_tr_b16 a[168:169], v20\n\t"
  "ds_read_b64_tr_b16 a[170:171], v20 offset:6144\n\t"
  "v_add_u32 v20, 24576, v15\n\t"
  "ds_read_b64_tr_b16 a[172:173], v20\n\t"
  "ds_read_b64_tr_b16 a[174:175], v20 offset:6144\n\t"
  "v_add_u32 v20, 24576, v16\n\t"
  "ds_read_b64_tr_b16 a[176:177], v20\n\t"
  "ds_read_b64_tr_b16 a[178:179], v20 offset:6144\n\t"
  "s_waitcnt vmcnt(30) lgkmcnt(0)\n\t"
  "s_barrier\n\t"
  "s_add_u32 m0, s61, 24576\n\t"
  "s_nop 0\n\t"
  "buffer_load_dwordx4 v8, s[56:59], s62 offen lds\n\t"
  "s_add_u32 m0, s61, 25600\n\t"
  "s_nop 0\n\t"
  "buffer_load_dwordx4 v9, s[56:59], s62 offen lds\n\t"
  "s_add_u32 m0, s61, 26624\n\t"
  "s_nop 0\n\t"
  "buffer_load_dwordx4 v10, s[56:59], s62 offen lds\n\t"
  "s_add_u32 s62, s62, s60\n\t"
  "v_add_u32 v20, 36864, v14\n\t"
  "ds_read_b64_tr_b16 a[180:181], v20\n\t"
  "ds_read_b64_tr_b16 a[182:183], v20 offset:6144\n\t"
  "v_add_u32 v20, 36864, v15\n\t"
  "ds_read_b64_tr_b16 a[184:185], v20\n\t"
  "ds_read_b64_tr_b16 a[186:187], v20 offset:6144\n\t"
  "v_add_u32 v20, 36864, v16\n\t"
  "ds_read_b64_tr_b16 a[188:189], v20\n\t"
  "ds_read_b64_tr_b16 a[190:191], v20 offset:6144\n\t"
  "s_waitcnt vmcnt(30) lgkmcnt(0)\n\t"
  "s_barrier\n\t"
  "s_add_u32 m0, s61, 36864\n\t"
  "s_nop 0\n\t"
  "buffer_load_dwordx4 v8, s[56:59], s62 offen lds\n\t"
  "s_add_u32 m0, s61, 37888\n\t"
  "s_nop 0\n\t"
  "buffer_load_dwordx4 v9, s[56:59], s62 offen lds\n\t"
  "s_add_u32 m0, s61, 38912\n\t"
  "s_nop 0\n\t"
  "buffer_load_dwordx4 v10, s[56:59], s62 offen lds\n\t"
  "s_add_u32 s62, s62, s60\n\t"
  "v_add_u32 v20, 49152, v14\n\t"
  "ds_read_b64_tr_b16 a[192:193], v20\n\t"
  "ds_read_b64_tr_b16 a[194:195], v20 offset:6144\n\t"
  "v_add_u32 v20, 49152, v15\n\t"
  "ds_read_b64_tr_b16 a[196:197], v20\n\t"
  "ds_read_b64_tr_b16 a[198:199], v20 offset:6144\n\t"
  "v_add_u32 v20, 49152, v16\n\t"
  "ds_read_b64_tr_b16 a[200:201], v20\n\t"
  "ds_read_b64_tr_b16 a[202:203], v20 offset:6144\n\t"
  "s_waitcnt vmcnt(30) lgkmcnt(0)\n\t"
  "s_barrier\n\t"
  "s_add_u32 m0, s61, 49152\n\t"
  "s_nop 0\n\t"
  "buffer_load_dwordx4 v8, s[56:59], s62 offen lds\n\t"
  "s_add_u32 m0, s61, 50176\n\t"
  "s_nop 0\n\t"
  "buffer_load_dwordx4 v9, s[56:59], s62 offen lds\n\t"
  "s_add_u32 m0, s61, 51200\n\t"
  "s_nop 0\n\t"
  "buffer_load_dwordx4 v10, s[56:59], s62 offen lds\n\t"
  "s_add_u32 s62, s62, s60\n\t"
  "v_add_u32 v20, 61440, v14\n\t"
  "ds_read_b64_tr_b16 a[204:205], v20\n\t"
  "ds_read_b64_tr_b16 a[206:207], v20 offset:6144\n\t"
  "v_add_u32 v20, 61440, v15\n\t"
  "ds_read_b64_tr_b16 a[208:209], v20\n\t"
  "ds_read_b64_tr_b16 a[210:211], v20 offset:6144\n\t"
  "v_add_u32 v20, 61440, v16\n\t"
  "ds_read_b64_tr_b16 a[212:213], v20\n\t"
  "ds_read_b64_tr_b16 a[214:215], v20 offset:6144\n\t"
  "s_waitcnt vmcnt(30) lgkmcnt(0)\n\t"
  "s_barrier\n\t"
  "s_add_u32 m0, s61, 61440\n\t"
  "s_nop 0\n\t"
  "buffer_load_dwordx4 v8, s[56:59], s62 offen lds\n\t"
  "s_add_u32 m0, s61, 62464\n\t"
  "s_nop 0\n\t"
  "buffer_load_dwordx4 v9, s[56:59], s62 offen lds\n\t"
  "s_add_u32 m0, s61, 63488\n\t"
  "s_nop 0\n\t"
  "buffer_load_dwordx4 v10, s[56:59], s62 offen lds\n\t"
  "s_add_u32 s62, s62, s60\n\t"
  "v_add_u32 v20, 73728, v14\n\t"
  "ds_read_b64_tr_b16 a[216:217], v20\n\t"
  "ds_read_b64_tr_b16 a[218:219], v20 offset:6144\n\t"
  "v_add_u32 v20, 73728, v15\n\t"
  "ds_read_b64_tr_b16 a[220:221], v20\n\t"
  "ds_read_b64_tr_b16 a[222:223], v20 offset:6144\n\t"
  "v_add_u32 v20, 73728, v16\n\t"
  "ds_read_b64_tr_b16 a[224:225], v20\n\t"
  "ds_read_b64_tr_b16 a[226:227], v20 offset:6144\n\t"
  "s_waitcnt vmcnt(30) lgkmcnt(0)\n\t"
  "s_barrier\n\t"
  "s_add_u32 m0, s61, 73728\n\t"
  "s_nop 0\n\t"
  "buffer_load_dwordx4 v8, s[56:59], s62 offen lds\n\t"
  "s_add_u32 m0, s61, 74752\n\t"
  "s_nop 0\n\t"
  "buffer_load_dwordx4 v9, s[56:59], s62 offen lds\n\t"
  "s_add_u32 m0, s61, 75776\n\t"
  "s_nop 0\n\t"
  "buffer_load_dwordx4 v10, s[56:59], s62 offen lds\n\t"
  "s_add_u32 s62, s62, s60\n\t"
  "v_add_u32 v20, 86016, v14\n\t"
  "ds_read_b64_tr_b16 a[228:229], v20\n\t"
  "ds_read_b64_tr_b16 a[230:231], v20 offset:6144\n\t"
  "v_add_u32 v20, 86016, v15\n\t"
  "ds_read_b64_tr_b16 a[232:233], v20\n\t"
  "ds_read_b64_tr_b16 a[234:235], v20 offset:6144\n\t"
  "v_add_u32 v20, 86016, v16\n\t"
  "ds_read_b64_tr_b16 a[236:237], v20\n\t"
  "ds_read_b64_tr_b16 a[238:239], v20 offset:6144\n\t"
  "s_waitcnt vmcnt(30) lgkmcnt(0)\n\t"
  "s_barrier\n\t"
  "s_add_u32 m0, s61, 86016\n\t"
  "s_nop 0\n\t"
  "buffer_load_dwordx4 v8, s[56:59], s62 offen lds\n\t"
  "s_add_u32 m0, s61, 87040\n\t"
  "s_nop 0\n\t"
  "buffer_load_dwordx4 v9, s[56:59], s62 offen lds\n\t"
  "s_add_u32 m0, s61, 88064\n\t"
  "s_nop 0\n\t"
  "buffer_load_dwordx4 v10, s[56:59], s62 offen lds\n\t"
  "s_add_u32 s62, s62, s60\n\t"
  "v_add_u32 v20, 98304, v14\n\t"
  "ds_read_b64_tr_b16 a[240:241], v20\n\t"
  "ds_read_b64_tr_b16 a[242:243], v20 offset:6144\n\t"
  "v_add_u32 v20, 98304, v15\n\t"
  "ds_read_b64_tr_b16 a[244:245], v20\n\t"
  "ds_read_b64_tr_b16 a[246:247], v20 offset:6144\n\t"
  "v_add_u32 v20, 98304, v16\n\t"
  "ds_read_b64_tr_b16 a[248:249], v20\n\t"
  "ds_read_b64_tr_b16 a[250:251], v20 offset:6144\n\t"
  "s_waitcnt vmcnt(30) lgkmcnt(0)\n\t"
  "s_barrier\n\t"
  "s_add_u32 m0, s61, 98304\n\t"
  "s_nop 0\n\t"
  "buffer_load_dwordx4 v8, s[56:59], s62 offen lds\n\t"
  "s_add_u32 m0, s61, 99328\n\t"
  "s_nop 0\n\t"
  "buffer_load_dwordx4 v9, s[56:59], s62 offen lds\n\t"
  "s_add_u32 m0, s61, 100352\n\t"
  "s_nop 0\n\t"
  "buffer_load_dwordx4 v10, s[56:59], s62 offen lds\n\t"
  "s_add_u32 s62, s62, s60\n\t"
  "v_add_u32 v20, 110592, v14\n\t"
  "ds_read_b64_tr_b16 a[252:253], v20\n\t"
  "ds_read_b64_tr_b16 a[254:255], v20 offset:6144\n\t"
  "v_add_u32 v20, 110592, v15\n\t"
  "ds_read_b64_tr_b16 v[224:225], v20\n\t"
  "ds_read_b64_tr_b16 v[226:227], v20 offset:6144\n\t"
  "v_add_u32 v20, 110592, v16\n\t"
  "ds_read_b64_tr_b16 v[228:229], v20\n\t"
  "ds_read_b64_tr_b16 v[230:231], v20 offset:6144\n\t"
  "s_waitcnt vmcnt(30) lgkmcnt(0)\n\t"
  "s_barrier\n\t"
  "s_add_u32 m0, s61, 110592\n\t"
  "s_nop 0\n\t"
  "buffer_load_dwordx4 v8, s[56:59], s62 offen lds\n\t"
  "s_add_u32 m0, s61, 111616\n\t"
  "s_nop 0\n\t"
  "buffer_load_dwordx4 v9, s[56:59], s62 offen lds\n\t"
  "s_add_u32 m0, s61, 112640\n\t"
  "s_nop 0\n\t"
  "buffer_load_dwordx4 v10, s[56:59], s62 offen lds\n\t"
  "s_add_u32 s62, s62, s60\n\t"
  "v_add_u32 v20, 122880, v14\n\t"
  "ds_read_b64_tr_b16 v[232:233], v20\n\t"
  "ds_read_b64_tr_b16 v[234:235], v20 offset:6144\n\t"
  "v_add_u32 v20, 122880, v15\n\t"
  "ds_read_b64_tr_b16 v[236:237], v20\n\t"
  "ds_read_b64_tr_b16 v[238:239], v20 offset:6144\n\t"
  "v_add_u32 v20, 122880, v16\n\t"
  "ds_read_b64_tr_b16 v[240:241], v20\n\t"
  "ds_read_b64_tr_b16 v[242:243], v20 offset:6144\n\t"
  "s_waitcnt vmcnt(30) lgkmcnt(0)\n\t"
  "s_barrier\n\t"
  "s_add_u32 m0, s61, 122880\n\t"
  "s_nop 0\n\t"
  "buffer_load_dwordx4 v8, s[56:59], s62 offen lds\n\t"
  "s_add_u32 m0, s61, 123904\n\t"
  "s_nop 0\n\t"
  "buffer_load_dwordx4 v9, s[56:59], s62 offen lds\n\t"
  "s_add_u32 m0, s61, 124928\n\t"
  "s_nop 0\n\t"
  "buffer_load_dwordx4 v10, s[56:59], s62 offen lds\n\t"
  "s_add_u32 s62, s62, s60\n\t"
  "v_add_u32 v20, 135168, v14\n\t"
  "ds_read_b64_tr_b16 v[244:245], v20\n\t"
  "ds_read_b64_tr_b16 v[246:247], v20 offset:6144\n\t"
  "v_add_u32 v20, 135168, v15\n\t"
  "ds_read_b64_tr_b16 v[248:249], v20\n\t"
  "ds_read_b64_tr_b16 v[250:251], v20 offset:6144\n\t"
  "v_add_u32 v20, 135168, v16\n\t"
  "ds_read_b64_tr_b16 v[252:253], v20\n\t"
  "ds_read_b64_tr_b16 v[254:255], v20 offset:6144\n\t"
  "s_waitcnt vmcnt(30) lgkmcnt(0)\n\t"
  "s_barrier\n\t"
  "s_add_u32 m0, s61, 135168\n\t"
  "s_nop 0\n\t"
  "buffer_load_dwordx4 v8, s[56:59], s62 offen lds\n\t"
  "s_add_u32 m0, s61, 136192\n\t"
  "s_nop 0\n\t"
  "buffer_load_dwordx4 v9, s[56:59], s62 offen lds\n\t"
  "s_add_u32 m0, s61, 137216\n\t"
  "s_nop 0\n\t"
  "buffer_load_dwordx4 v10, s[56:59], s62 offen lds\n\t"
  "s_add_u32 s62, s62, s60\n\t"
  "v_add_u32 v20, 0, v14\n\t"
  "ds_read_b64_tr_b16 v[192:193], v20\n\t"
  "ds_read_b64_tr_b16 v[194:195], v20 offset:6144\n\t"
  "v_add_u32 v20, 0, v15\n\t"
  "ds_read_b64_tr_b16 v[196:197], v20\n\t"
  "ds_read_b64_tr_b16 v[198:199], v20 offset:6144\n\t"
  "v_add_u32 v20, 0, v16\n\t"
  "ds_read_b64_tr_b16 v[200:201], v20\n\t"
  "ds_read_b64_tr_b16 v[202:203], v20 offset:6144\n\t"
  "s_waitcnt vmcnt(30) lgkmcnt(0)\n\t"
  "s_barrier\n\t"
  "v_add_u32 v20, 12288, v14\n\t"
  "ds_read_b64_tr_b16 v[204:205], v20\n\t"
  "ds_read_b64_tr_b16 v[206:207], v20 offset:6144\n\t"
  "v_add_u32 v20, 12288, v15\n\t"
  "ds_read_b64_tr_b16 v[208:209], v20\n\t"
  "ds_read_b64_tr_b16 v[210:211], v20 offset:6144\n\t"
  "v_add_u32 v20, 12288, v16\n\t"
  "ds_read_b64_tr_b16 v[212:213], v20\n\t"
  "ds_read_b64_tr_b16 v[214:215], v20 offset:6144\n\t"
  "s_waitcnt vmcnt(27) lgkmcnt(0)\n\t"
  "s_barrier\n\t"
  "v_add_u32 v20, 24576, v14\n\t"
  "ds_read_b64_tr_b16 v[216:217], v20\n\t"
  "ds_read_b64_tr_b16 v[218:219], v20 offset:6144\n\t"
  "v_add_u32 v20, 24576, v15\n\t"
  "ds_read_b64_tr_b16 v[220:221], v20\n\t"
  "ds_read_b64_tr_b16 v[222:223], v20 offset:6144\n\t"
  "v_add_u32 v20, 24576, v16\n\t"
  "ds_read_b64_tr_b16 v[160:161], v20\n\t"
  "ds_read_b64_tr_b16 v[162:163], v20 offset:6144\n\t"
  "s_waitcnt vmcnt(24) lgkmcnt(0)\n\t"
  "s_barrier\n\t"
  "v_add_u32 v20, 36864, v14\n\t"
  "ds_read_b64_tr_b16 v[164:165], v20\n\t"
  "ds_read_b64_tr_b16 v[166:167], v20 offset:6144\n\t"
  "v_add_u32 v20, 36864, v15\n\t"
  "ds_read_b64_tr_b16 v[168:169], v20\n\t"
  "ds_read_b64_tr_b16 v[170:171], v20 offset:6144\n\t"
  "v_add_u32 v20, 36864, v16\n\t"
  "ds_read_b64_tr_b16 v[172:173], v20\n\t"
  "ds_read_b64_tr_b16 v[174:175], v20 offset:6144\n\t"
  "s_waitcnt vmcnt(21) lgkmcnt(0)\n\t"
  "s_barrier\n\t"
  "v_add_u32 v20, 49152, v14\n\t"
  "ds_read_b64_tr_b16 v[176:177], v20\n\t"
  "ds_read_b64_tr_b16 v[178:179], v20 offset:6144\n\t"
  "v_add_u32 v20, 49152, v15\n\t"
  "ds_read_b64_tr_b16 v[180:181], v20\n\t"
  "ds_read_b64_tr_b16 v[182:183], v20 offset:6144\n\t"
  "v_add_u32 v20, 49152, v16\n\t"
  "ds_read_b64_tr_b16 v[184:185], v20\n\t"
  "ds_read_b64_tr_b16 v[186:187], v20 offset:6144\n\t"
  "s_waitcnt vmcnt(18) lgkmcnt(0)\n\t"
  "s_barrier\n\t"
  "v_add_u32 v20, 61440, v14\n\t"
  "ds_read_b64_tr_b16 v[188:189], v20\n\t"
  "ds_read_b64_tr_b16 v[190:191], v20 offset:6144\n\t"
  "v_add_u32 v20, 61440, v15\n\t"
  "ds_read_b64_tr_b16 v[128:129], v20\n\t"
  "ds_read_b64_tr_b16 v[130:131], v20 offset:6144\n\t"
  "v_add_u32 v20, 61440, v16\n\t"
  "ds_read_b64_tr_b16 v[132:133], v20\n\t"
  "ds_read_b64_tr_b16 v[134:135], v20 offset:6144\n\t"
  "s_waitcnt vmcnt(15) lgkmcnt(0)\n\t"
  "s_barrier\n\t"
  "v_add_u32 v20, 73728, v14\n\t"
  "ds_read_b64_tr_b16 v[136:137], v20\n\t"
  "ds_read_b64_tr_b16 v[138:139], v20 offset:6144\n\t"
  "v_add_u32 v20, 73728, v15\n\t"
  "ds_read_b64_tr_b16 v[140:141], v20\n\t"
  "ds_read_b64_tr_b16 v[142:143], v20 offset:6144\n\t"
  "v_add_u32 v20, 73728, v16\n\t"
  "ds_read_b64_tr_b16 v[144:145], v20\n\t"
  "ds_read_b64_tr_b16 v[146:147], v20 offset:6144\n\t"
  "s_waitcnt vmcnt(12) lgkmcnt(0)\n\t"
  "s_barrier\n\t"
  "v_add_u32 v20, 86016, v14\n\t"
  "ds_read_b64_tr_b16 v[148:149], v20\n\t"
  "ds_read_b64_tr_b16 v[150:151], v20 offset:6144\n\t"
  "v_add_u32 v20, 86016, v15\n\t"
  "ds_read_b64_tr_b16 v[152:153], v20\n\t"
  "ds_read_b64_tr_b16 v[154:155], v20 offset:6144\n\t"
  "v_add_u32 v20, 86016, v16\n\t"
  "ds_read_b64_tr_b16 v[156:157], v20\n\t"
  "ds_read_b64_tr_b16 v[158:159], v20 offset:6144\n\t"
  "s_waitcnt vmcnt(9) lgkmcnt(0)\n\t"
  "s_barrier\n\t"
  "v_add_u32 v20, 98304, v14\n\t"
  "ds_read_b64_tr_b16 v[96:97], v20\n\t"
  "ds_read_b64_tr_b16 v[98:99], v20 offset:6144\n\t"
  "v_add_u32 v20, 98304, v15\n\t"
  "ds_read_b64_tr_b16 v[100:101], v20\n\t"
  "ds_read_b64_tr_b16 v[102:103], v20 offset:6144\n\t"
  "v_add_u32 v20, 98304, v16\n\t"
  "ds_read_b64_tr_b16 v[104:105], v20\n\t"
  "ds_read_b64_tr_b16 v[106:107], v20 offset:6144\n\t"
  "s_waitcnt vmcnt(6) lgkmcnt(0)\n\t"
  "s_barrier\n\t"
  "v_add_u32 v20, 110592, v14\n\t"
  "ds_read_b64_tr_b16 v[108:109], v20\n\t"
  "ds_read_b64_tr_b16 v[110:111], v20 offset:6144\n\t"
  "v_add_u32 v20, 110592, v15\n\t"
  "ds_read_b64_tr_b16 v[112:113], v20\n\t"
  "ds_read_b64_tr_b16 v[114:115], v20 offset:6144\n\t"
  "v_add_u32 v20, 110592, v16\n\t"
  "ds_read_b64_tr_b16 v[116:117], v20\n\t"
  "ds_read_b64_tr_b16 v[118:119], v20 offset:6144\n\t"
  "s_waitcnt vmcnt(3) lgkmcnt(0)\n\t"
  "s_barrier\n\t"
  "v_add_u32 v20, 122880, v14\n\t"
  "ds_read_b64_tr_b16 v[120:121], v20\n\t"
  "ds_read_b64_tr_b16 v[122:123], v20 offset:6144\n\t"
  "v_add_u32 v20, 122880, v15\n\t"
  "ds_read_b64_tr_b16 v[124:125], v20\n\t"
  "ds_read_b64_tr_b16 v[126:127], v20 offset:6144\n\t"
  "v_add_u32 v20, 122880, v16\n\t"
  "ds_read_b64_tr_b16 v[80:81], v20\n\t"
  "ds_read_b64_tr_b16 v[82:83], v20 offset:6144\n\t"
  "s_waitcnt vmcnt(0) lgkmcnt(0)\n\t"
  "s_barrier\n\t"
  "v_add_u32 v20, 135168, v14\n\t"
  "ds_read_b64_tr_b16 v[84:85], v20\n\t"
  "ds_read_b64_tr_b16 v[86:87], v20 offset:6144\n\t"
  "v_add_u32 v20, 135168, v15\n\t"
  "ds_read_b64_tr_b16 v[88:89], v20\n\t"
  "ds_read_b64_tr_b16 v[90:91], v20 offset:6144\n\t"
  "v_add_u32 v20, 135168, v16\n\t"
  "ds_read_b64_tr_b16 v[92:93], v20\n\t"
  "ds_read_b64_tr_b16 v[94:95], v20 offset:6144\n\t"
  "s_waitcnt lgkmcnt(0)\n\t"
  "s_barrier\n\t"
  "s_mov_b64 s[60:61], s[36:37]\n\t"
  "s_mov_b32 s63, s39\n\t"
  "s_mov_b64 s[64:65], s[36:37]\n\t"
  "s_mov_b32 s67, s39\n\t"
  "s_mov_b64 s[84:85], s[36:37]\n\t"
  "s_mov_b32 s87, s39\n\t"
  "s_mov_b32 s86, 0\n\t"
  "s_mov_b32 s57, 0\n\t"
  "s_mov_b32 s56, -1\n\t"
  "s_mov_b32 s80, 0x20000\n\t"
  "s_lshr_b32 s78, s41, 9\n\t"
  "s_mov_b32 s71, s41\n\t"
  "s_mov_b32 s74, -73728\n\t"
  "v_readlane_b32 s68, v4, 0\n\t"
  "v_mov_b32 v3, -1\n\t"
  "s_branch 3f\n\t"
  "1:\n\t"
  "ds_read_b128 v[20:23], v1 offset:3072\n\t"
  "s_waitcnt lgkmcnt(3)\n\t"
  "v_mfma_f32_16x16x32_bf16 v[30:33], a[0:3], v[8:11], 0\n\t"
  "s_add_u32 m0, s71, 32768\n\t"
  "s_add_u32 s76, s68, 65536\n\t"
  "buffer_load_dwordx4 v0, s[36:39], s76 offen sc1 lds\n\t"
  "v_mfma_f32_16x16x32_bf16 v[34:37], a[4:7], v[8:11], 0\n\t"
  "s_mov_b32 s70, s69\n\t"
  "s_mov_b32 s56, s57\n\t"
  "s_add_u32 s57, s57, 1\n\t"
  "v_mfma_f32_16x16x32_bf16 v[38:41], a[8:11], v[8:11], 0\n\t"
  "ds_read_b128 v[8:11], v1 offset:4096\n\t"
  "s_waitcnt lgkmcnt(3)\n\t"
  "v_mfma_f32_16x16x32_bf16 v[30:33], a[12:15], v[12:15], v[30:33]\n\t"
  "v_mfma_f32_16x16x32_bf16 v[34:37], a[16:19], v[12:15], v[34:37]\n\t"
  "buffer_load_dwordx4 v0, s[36:39], s76 offen offset:1024 sc1 lds\n\t"
  "v_mfma_f32_16x16x32_bf16 v[38:41], a[20:23], v[12:15], v[38:41]\n\t"
  "ds_read_b128 v[12:15], v1 offset:5120\n\t"
  "s_waitcnt lgkmcnt(3)\n\t"
  "v_mfma_f32_16x16x32_bf16 v[30:33], a[24:27], v[16:19], v[30:33]\n\t"
  "v_mfma_f32_16x16x32_bf16 v[34:37], a[28:31], v[16:19], v[34:37]\n\t"
  "v_mfma_f32_16x16x32_bf16 v[38:41], a[32:35], v[16:19], v[38:41]\n\t"
  "ds_read_b128 v[16:19], v1 offset:6144\n\t"
  "s_waitcnt lgkmcnt(3)\n\t"
  "v_mfma_f32_16x16x32_bf16 v[30:33], a[36:39], v[20:23], v[30:33]\n\t"
  "s_add_u32 m0, s71, 40960\n\t"
  "s_add_u32 s76, s68, 81920\n\t"
  "buffer_load_dwordx4 v0, s[36:39], s76 offen sc1 lds\n\t"
  "v_mfma_f32_16x16x32_bf16 v[34:37], a[40:43], v[20:23], v[34:37]\n\t"
  "v_mfma_f32_16x16x32_bf16 v[38:41], a[44:47], v[20:23], v[38:41]\n\t"
  "ds_read_b128 v[20:23], v1 offset:7168\n\t"
  "s_waitcnt lgkmcnt(3)\n\t"
  "v_mfma_f32_16x16x32_bf16 v[30:33], a[48:51], v[8:11], v[30:33]\n\t"
  "v_mfma_f32_16x16x32_bf16 v[34:37], a[52:55], v[8:11], v[34:37]\n\t"
  "buffer_load_dwordx4 v0, s[36:39], s76 offen offset:1024 sc1 lds\n\t"
  "v_mfma_f32_16x16x32_bf16 v[38:41], a[56:59], v[8:11], v[38:41]\n\t"
  "ds_read_b128 v[8:11], v1 offset:8192\n\t"
  "s_waitcnt lgkmcnt(3)\n\t"
  "v_mfma_f32_16x16x32_bf16 v[30:33], a[60:63], v[12:15], v[30:33]\n\t"
  "v_mov_b32 v76, s56\n\t"
  "v_mfma_f32_16x16x32_bf16 v[34:37], a[64:67], v[12:15], v[34:37]\n\t"
  "v_mfma_f32_16x16x32_bf16 v[38:41], a[68:71], v[12:15], v[38:41]\n\t"
  "ds_read_b128 v[12:15], v1 offset:9216\n\t"
  "s_waitcnt lgkmcnt(3)\n\t"
  "v_mfma_f32_16x16x32_bf16 v[30:33], a[72:75], v[16:19], v[30:33]\n\t"
  "s_add_u32 m0, s71, 49152\n\t"
  "s_add_u32 s76, s68, 98304\n\t"
  "buffer_load_dwordx4 v0, s[36:39], s76 offen sc1 lds\n\t"
  "v_mfma_f32_16x16x32_bf16 v[34:37], a[76:79], v[16:19], v[34:37]\n\t"
  "v_mfma_f32_16x16x32_bf16 v[38:41], a[80:83], v[16:19], v[38:41]\n\t"
  "ds_read_b128 v[16:19], v1 offset:10240\n\t"
  "s_waitcnt lgkmcnt(3)\n\t"
  "v_mfma_f32_16x16x32_bf16 v[30:33], a[84:87], v[20:23], v[30:33]\n\t"
  "v_mfma_f32_16x16x32_bf16 v[34:37], a[88:91], v[20:23], v[34:37]\n\t"
  "buffer_load_dwordx4 v0, s[36:39], s76 offen offset:1024 sc1 lds\n\t"
  "v_mfma_f32_16x16x32_bf16 v[38:41], a[92:95], v[20:23], v[38:41]\n\t"
  "ds_read_b128 v[20:23], v1 offset:11264\n\t"
  "s_waitcnt lgkmcnt(3)\n\t"
  "v_mfma_f32_16x16x32_bf16 v[30:33], a[96:99], v[8:11], v[30:33]\n\t"
  "v_mfma_f32_16x16x32_bf16 v[34:37], a[100:103], v[8:11], v[34:37]\n\t"
  "v_mfma_f32_16x16x32_bf16 v[38:41], a[104:107], v[8:11], v[38:41]\n\t"
  "ds_read_b128 v[8:11], v1 offset:12288\n\t"
  "s_waitcnt lgkmcnt(3)\n\t"
  "v_mfma_f32_16x16x32_bf16 v[30:33], a[108:111], v[12:15], v[30:33]\n\t"
  "s_add_u32 m0, s71, 57344\n\t"
  "s_add_u32 s76, s68, 114688\n\t"
  "buffer_load_dwordx4 v0, s[36:39], s76 offen sc1 lds\n\t"
  "v_mfma_f32_16x16x32_bf16 v[34:37], a[112:115], v[12:15], v[34:37]\n\t"
  "v_mfma_f32_16x16x32_bf16 v[38:41], a[116:119], v[12:15], v[38:41]\n\t"
  "ds_read_b128 v[12:15], v1 offset:13312\n\t"
  "s_waitcnt lgkmcnt(3)\n\t"
  "v_mfma_f32_16x16x32_bf16 v[30:33], a[120:123], v[16:19], v[30:33]\n\t"
  "v_mfma_f32_16x16x32_bf16 v[34:37], a[124:127], v[16:19], v[34:37]\n\t"
  "buffer_load_dwordx4 v0, s[36:39], s76 offen offset:1024 sc1 lds\n\t"
  "v_mfma_f32_16x16x32_bf16 v[38:41], a[128:131], v[16:19], v[38:41]\n\t"
  "ds_read_b128 v[16:19], v1 offset:14336\n\t"
  "s_waitcnt lgkmcnt(3)\n\t"
  "v_mfma_f32_16x16x32_bf16 v[30:33], a[132:135], v[20:23], v[30:33]\n\t"
  "v_mfma_f32_16x16x32_bf16 v[34:37], a[136:139], v[20:23], v[34:37]\n\t"
  "v_mfma_f32_16x16x32_bf16 v[38:41], a[140:143], v[20:23], v[38:41]\n\t"
  "ds_read_b128 v[20:23], v1 offset:15360\n\t"
  "s_waitcnt lgkmcnt(3)\n\t"
  "v_mfma_f32_16x16x32_bf16 v[30:33], a[144:147], v[8:11], v[30:33]\n\t"
  "s_add_u32 m0, s71, 65536\n\t"
  "s_add_u32 s76, s68, 131072\n\t"
  "buffer_load_dwordx4 v0, s[36:39], s76 offen sc1 lds\n\t"
  "v_mfma_f32_16x16x32_bf16 v[34:37], a[148:151], v[8:11], v[34:37]\n\t"
  "s_waitcnt vmcnt(12)\n\t"
  "s_barrier\n\t"
  "v_mfma_f32_16x16x32_bf16 v[38:41], a[152:155], v[8:11], v[38:41]\n\t"
  "ds_read_b128 v[8:11], v1 offset:16384\n\t"
  "s_waitcnt lgkmcnt(3)\n\t"
  "v_mfma_f32_16x16x32_bf16 v[30:33], a[156:159], v[12:15], v[30:33]\n\t"
  "v_mfma_f32_16x16x32_bf16 v[34:37], a[160:163], v[12:15], v[34:37]\n\t"
  "buffer_load_dwordx4 v0, s[36:39], s76 offen offset:1024 sc1 lds\n\t"
  "v_mfma_f32_16x16x32_bf16 v[38:41], a[164:167], v[12:15], v[38:41]\n\t"
  "ds_read_b128 v[12:15], v1 offset:17408\n\t"
  "s_waitcnt lgkmcnt(3)\n\t"
  "v_mfma_f32_16x16x32_bf16 v[30:33], a[168:171], v[16:19], v[30:33]\n\t"
  "v_mfma_f32_16x16x32_bf16 v[34:37], a[172:175], v[16:19], v[34:37]\n\t"
  "v_mfma_f32_16x16x32_bf16 v[38:41], a[176:179], v[16:19], v[38:41]\n\t"
  "ds_read_b128 v[16:19], v1 offset:18432\n\t"
  "s_waitcnt lgkmcnt(3)\n\t"
  "v_mfma_f32_16x16x32_bf16 v[30:33], a[180:183], v[20:23], v[30:33]\n\t"
  "v_subrev_u32 v76, 1, v76\n\t"
  "s_waitcnt vmcnt(10)\n\t"
  "buffer_store_dword v76, off, s[84:87], s40 sc1\n\t"
  "v_mfma_f32_16x16x32_bf16 v[34:37], a[184:187], v[20:23], v[34:37]\n\t"
  "v_mfma_f32_16x16x32_bf16 v[38:41], a[188:191], v[20:23], v[38:41]\n\t"
  "ds_read_b128 v[20:23], v1 offset:19456\n\t"
  "s_waitcnt lgkmcnt(3)\n\t"
  "v_mfma_f32_16x16x32_bf16 v[30:33], a[192:195], v[8:11], v[30:33]\n\t"
  "v_mfma_f32_16x16x32_bf16 v[34:37], a[196:199], v[8:11], v[34:37]\n\t"
  "v_mfma_f32_16x16x32_bf16 v[38:41], a[200:203], v[8:11], v[38:41]\n\t"
  "ds_read_b128 v[8:11], v1 offset:20480\n\t"
  "s_waitcnt lgkmcnt(3)\n\t"
  "v_mfma_f32_16x16x32_bf16 v[30:33], a[204:207], v[12:15], v[30:33]\n\t"
  "v_mfma_f32_16x16x32_bf16 v[34:37], a[208:211], v[12:15], v[34:37]\n\t"
  "v_mfma_f32_16x16x32_bf16 v[38:41], a[212:215], v[12:15], v[38:41]\n\t"
  "ds_read_b128 v[12:15], v1 offset:21504\n\t"
  "s_waitcnt lgkmcnt(3)\n\t"
  "v_mfma_f32_16x16x32_bf16 v[30:33], a[216:219], v[16:19], v[30:33]\n\t"
  "v_mfma_f32_16x16x32_bf16 v[34:37], a[220:223], v[16:19], v[34:37]\n\t"
  "v_mfma_f32_16x16x32_bf16 v[38:41], a[224:227], v[16:19], v[38:41]\n\t"
  "ds_read_b128 v[16:19], v1 offset:22528\n\t"
  "s_waitcnt lgkmcnt(3)\n\t"
  "v_mfma_f32_16x16x32_bf16 v[30:33], a[228:231], v[20:23], v[30:33]\n\t"
  "v_mfma_f32_16x16x32_bf16 v[34:37], a[232:235], v[20:23], v[34:37]\n\t"
  "v_mfma_f32_16x16x32_bf16 v[38:41], a[236:239], v[20:23], v[38:41]\n\t"
  "ds_read_b128 v[20:23], v1 offset:23552\n\t"
  "s_waitcnt lgkmcnt(3)\n\t"
  "v_mfma_f32_16x16x32_bf16 v[30:33], a[240:243], v[8:11], v[30:33]\n\t"
  "v_mfma_f32_16x16x32_bf16 v[34:37], a[244:247], v[8:11], v[34:37]\n\t"
  "v_mfma_f32_16x16x32_bf16 v[38:41], a[248:251], v[8:11], v[38:41]\n\t"
  "ds_read_b128 v[8:11], v1 offset:24576\n\t"
  "s_waitcnt lgkmcnt(3)\n\t"
  "v_mfma_f32_16x16x32_bf16 v[30:33], a[252:255], v[12:15], v[30:33]\n\t"
  "v_mfma_f32_16x16x32_bf16 v[34:37], v[224:227], v[12:15], v[34:37]\n\t"
  "v_mfma_f32_16x16x32_bf16 v[38:41], v[228:231], v[12:15], v[38:41]\n\t"
  "ds_read_b128 v[12:15], v1 offset:25600\n\t"
  "s_waitcnt lgkmcnt(3)\n\t"
  "v_mfma_f32_16x16x32_bf16 v[30:33], v[232:235], v[16:19], v[30:33]\n\t"
  "v_mfma_f32_16x16x32_bf16 v[34:37], v[236:239], v[16:19], v[34:37]\n\t"
  "v_mfma_f32_16x16x32_bf16 v[38:41], v[240:243], v[16:19], v[38:41]\n\t"
  "ds_read_b128 v[16:19], v1 offset:26624\n\t"
  "s_waitcnt lgkmcnt(3)\n\t"
  "v_mfma_f32_16x16x32_bf16 v[30:33], v[244:247], v[20:23], v[30:33]\n\t"
  "v_mfma_f32_16x16x32_bf16 v[34:37], v[248:251], v[20:23], v[34:37]\n\t"
  "v_mfma_f32_16x16x32_bf16 v[38:41], v[252:255], v[20:23], v[38:41]\n\t"
  "ds_read_b128 v[20:23], v1 offset:27648\n\t"
  "s_waitcnt lgkmcnt(3)\n\t"
  "v_mfma_f32_16x16x32_bf16 v[30:33], v[192:195], v[8:11], v[30:33]\n\t"
  "v_mfma_f32_16x16x32_bf16 v[34:37], v[196:199], v[8:11], v[34:37]\n\t"
  "v_mfma_f32_16x16x32_bf16 v[38:41], v[200:203], v[8:11], v[38:41]\n\t"
  "ds_read_b128 v[8:11], v1 offset:28672\n\t"
  "s_waitcnt lgkmcnt(3)\n\t"
  "v_mfma_f32_16x16x32_bf16 v[30:33], v[204:207], v[12:15], v[30:33]\n\t"
  "v_mfma_f32_16x16x32_bf16 v[34:37], v[208:211], v[12:15], v[34:37]\n\t"
  "v_mfma_f32_16x16x32_bf16 v[38:41], v[212:215], v[12:15], v[38:41]\n\t"
  "ds_read_b128 v[12:15], v1 offset:29696\n\t"
  "s_waitcnt lgkmcnt(3)\n\t"
  "v_mfma_f32_16x16x32_bf16 v[30:33], v[216:219], v[16:19], v[30:33]\n\t"
  "v_mfma_f32_16x16x32_bf16 v[34:37], v[220:223], v[16:19], v[34:37]\n\t"
  "v_mfma_f32_16x16x32_bf16 v[38:41], v[160:163], v[16:19], v[38:41]\n\t"
  "ds_read_b128 v[16:19], v1 offset:30720\n\t"
  "s_waitcnt lgkmcnt(3)\n\t"
  "v_mfma_f32_16x16x32_bf16 v[30:33], v[164:167], v[20:23], v[30:33]\n\t"
  "v_mfma_f32_16x16x32_bf16 v[34:37], v[168:171], v[20:23], v[34:37]\n\t"
  "v_mfma_f32_16x16x32_bf16 v[38:41], v[172:175], v[20:23], v[38:41]\n\t"
  "ds_read_b128 v[20:23], v1 offset:31744\n\t"
  "s_waitcnt lgkmcnt(3)\n\t"
  "v_mfma_f32_16x16x32_bf16 v[30:33], v[176:179], v[8:11], v[30:33]\n\t"
  "v_mfma_f32_16x16x32_bf16 v[34:37], v[180:183], v[8:11], v[34:37]\n\t"
  "s_waitcnt vmcnt(1)\n\t"
  "s_barrier\n\t"
  "v_mfma_f32_16x16x32_bf16 v[38:41], v[184:187], v[8:11], v[38:41]\n\t"
  "ds_read_b128 v[8:11], v1 offset:32768\n\t"
  "s_waitcnt lgkmcnt(3)\n\t"
  "v_mfma_f32_16x16x32_bf16 v[30:33], v[188:191], v[12:15], v[30:33]\n\t"
  "buffer_load_dword v3, v2, s[36:39], 0 offen sc1\n\t"
  "v_mfma_f32_16x16x32_bf16 v[34:37], v[128:131], v[12:15], v[34:37]\n\t"
  "v_mfma_f32_16x16x32_bf16 v[38:41], v[132:135], v[12:15], v[38:41]\n\t"
  "ds_read_b128 v[12:15], v1 offset:33792\n\t"
  "s_waitcnt lgkmcnt(3)\n\t"
  "v_mfma_f32_16x16x32_bf16 v[30:33], v[136:139], v[16:19], v[30:33]\n\t"
  "v_mfma_f32_16x16x32_bf16 v[34:37], v[140:143], v[16:19], v[34:37]\n\t"
  "v_mfma_f32_16x16x32_bf16 v[38:41], v[144:147], v[16:19], v[38:41]\n\t"
  "ds_read_b128 v[16:19], v1 offset:34816\n\t"
  "s_waitcnt lgkmcnt(3)\n\t"
  "v_mfma_f32_16x16x32_bf16 v[30:33], v[148:151], v[20:23], v[30:33]\n\t"
  "v_mfma_f32_16x16x32_bf16 v[34:37], v[152:155], v[20:23], v[34:37]\n\t"
  "v_mfma_f32_16x16x32_bf16 v[38:41], v[156:159], v[20:23], v[38:41]\n\t"
  "ds_read_b128 v[20:23], v1 offset:35840\n\t"
  "s_waitcnt lgkmcnt(3)\n\t"
  "v_mfma_f32_16x16x32_bf16 v[30:33], v[96:99], v[8:11], v[30:33]\n\t"
  "v_mfma_f32_16x16x32_bf16 v[34:37], v[100:103], v[8:11], v[34:37]\n\t"
  "v_mfma_f32_16x16x32_bf16 v[38:41], v[104:107], v[8:11], v[38:41]\n\t"
  "ds_read_b128 v[8:11], v7 offset:0\n\t"
  "s_waitcnt lgkmcnt(3)\n\t"
  "v_mfma_f32_16x16x32_bf16 v[30:33], v[108:111], v[12:15], v[30:33]\n\t"
  "s_sub_u32 s71, s71, s74\n\t"
  "s_sub_u32 s74, 0, s74\n\t"
  "v_readlane_b32 s68, v4, s57\n\t"
  "v_mfma_f32_16x16x32_bf16 v[34:37], v[112:115], v[12:15], v[34:37]\n\t"
  "s_mov_b32 s58, 0\n\t"
  "v_readlane_b32 s69, v5, s56\n\t"
  "v_mfma_f32_16x16x32_bf16 v[38:41], v[116:119], v[12:15], v[38:41]\n\t"
  "ds_read_b128 v[12:15], v7 offset:1024\n\t"
  "s_waitcnt lgkmcnt(3)\n\t"
  "v_mfma_f32_16x16x32_bf16 v[30:33], v[120:123], v[16:19], v[30:33]\n\t"
  "v_mfma_f32_16x16x32_bf16 v[34:37], v[124:127], v[16:19], v[34:37]\n\t"
  "v_mfma_f32_16x16x32_bf16 v[38:41], v[80:83], v[16:19], v[38:41]\n\t"
  "ds_read_b128 v[16:19], v7 offset:2048\n\t"
  "s_waitcnt lgkmcnt(3)\n\t"
  "v_mfma_f32_16x16x32_bf16 v[30:33], v[84:87], v[20:23], v[30:33]\n\t"
  "v_cmp_le_i32 vcc, s57, v3\n\t"
  "v_mfma_f32_16x16x32_bf16 v[34:37], v[88:91], v[20:23], v[34:37]\n\t"
  "s_cmp_eq_u64 vcc, -1\n\t"
  "s_cselect_b32 s75, 1, 0\n\t"
  "s_mov_b32 s59, s75\n\t"
  "v_mfma_f32_16x16x32_bf16 v[38:41], v[92:95], v[20:23], v[38:41]\n\t"
  "ds_read_b128 v[20:23], v7 offset:3072\n\t"
  "s_waitcnt lgkmcnt(3)\n\t"
  "v_mfma_f32_16x16x32_bf16 v[42:45], a[0:3], v[8:11], 0\n\t"
  "s_andn2_b32 s75, s75, s58\n\t"
  "s_cselect_b32 s62, s51, 0\n\t"
  "s_cselect_b32 s72, s71, 155648\n\t"
  "v_mfma_f32_16x16x32_bf16 v[46:49], a[4:7], v[8:11], 0\n\t"
  "s_lshl_b32 s73, s75, 13\n\t"
  "s_or_b32 s58, s58, s75\n\t"
  "v_cvt_pk_bf16_f32 v24, v30, v31\n\t"
  "v_mfma_f32_16x16x32_bf16 v[50:53], a[8:11], v[8:11], 0\n\t"
  "ds_read_b128 v[8:11], v7 offset:4096\n\t"
  "s_waitcnt lgkmcnt(3)\n\t"
  "v_mfma_f32_16x16x32_bf16 v[42:45], a[12:15], v[12:15], v[42:45]\n\t"
  "s_mov_b32 m0, s72\n\t"
  "s_add_u32 s76, s68, 0\n\t"
  "buffer_load_dwordx4 v0, s[60:63], s76 offen sc1 lds\n\t"
  "v_mfma_f32_16x16x32_bf16 v[46:49], a[16:19], v[12:15], v[46:49]\n\t"
  "v_cvt_pk_bf16_f32 v25, v32, v33\n\t"
  "v_cvt_pk_bf16_f32 v26, v34, v35\n\t"
  "v_mfma_f32_16x16x32_bf16 v[50:53], a[20:23], v[12:15], v[50:53]\n\t"
  "ds_read_b128 v[12:15], v7 offset:5120\n\t"
  "s_waitcnt lgkmcnt(3)\n\t"
  "v_mfma_f32_16x16x32_bf16 v[42:45], a[24:27], v[16:19], v[42:45]\n\t"
  "v_cvt_pk_bf16_f32 v27, v36, v37\n\t"
  "v_cvt_pk_bf16_f32 v28, v38, v39\n\t"
  "v_mfma_f32_16x16x32_bf16 v[46:49], a[28:31], v[16:19], v[46:49]\n\t"
  "s_add_u32 s72, s72, s73\n\t"
  "buffer_load_dwordx4 v0, s[60:63], s76 offen offset:1024 sc1 lds\n\t"
  "v_cvt_pk_bf16_f32 v29, v40, v41\n\t"
  "v_mfma_f32_16x16x32_bf16 v[50:53], a[32:35], v[16:19], v[50:53]\n\t"
  "ds_read_b128 v[16:19], v7 offset:6144\n\t"
  "s_waitcnt lgkmcnt(3)\n\t"
  "v_mfma_f32_16x16x32_bf16 v[42:45], a[36:39], v[20:23], v[42:45]\n\t"
  "v_mfma_f32_16x16x32_bf16 v[46:49], a[40:43], v[20:23], v[46:49]\n\t"
  "v_mfma_f32_16x16x32_bf16 v[50:53], a[44:47], v[20:23], v[50:53]\n\t"
  "ds_read_b128 v[20:23], v7 offset:7168\n\t"
  "s_waitcnt lgkmcnt(3)\n\t"
  "v_mfma_f32_16x16x32_bf16 v[42:45], a[48:51], v[8:11], v[42:45]\n\t"
  "s_mov_b32 m0, s72\n\t"
  "s_add_u32 s76, s68, 16384\n\t"
  "buffer_load_dwordx4 v0, s[60:63], s76 offen sc1 lds\n\t"
  "v_mfma_f32_16x16x32_bf16 v[46:49], a[52:55], v[8:11], v[46:49]\n\t"
  "v_mfma_f32_16x16x32_bf16 v[50:53], a[56:59], v[8:11], v[50:53]\n\t"
  "ds_read_b128 v[8:11], v7 offset:8192\n\t"
  "s_waitcnt lgkmcnt(3)\n\t"
  "v_mfma_f32_16x16x32_bf16 v[42:45], a[60:63], v[12:15], v[42:45]\n\t"
  "v_mfma_f32_16x16x32_bf16 v[46:49], a[64:67], v[12:15], v[46:49]\n\t"
  "s_add_u32 s72, s72, s73\n\t"
  "buffer_load_dwordx4 v0, s[60:63], s76 offen offset:1024 sc1 lds\n\t"
  "v_mfma_f32_16x16x32_bf16 v[50:53], a[68:71], v[12:15], v[50:53]\n\t"
  "ds_read_b128 v[12:15], v7 offset:9216\n\t"
  "s_waitcnt lgkmcnt(3)\n\t"
  "v_mfma_f32_16x16x32_bf16 v[42:45], a[72:75], v[16:19], v[42:45]\n\t"
  "v_mfma_f32_16x16x32_bf16 v[46:49], a[76:79], v[16:19], v[46:49]\n\t"
  "v_mfma_f32_16x16x32_bf16 v[50:53], a[80:83], v[16:19], v[50:53]\n\t"
  "ds_read_b128 v[16:19], v7 offset:10240\n\t"
  "s_waitcnt lgkmcnt(3)\n\t"
  "v_mfma_f32_16x16x32_bf16 v[42:45], a[84:87], v[20:23], v[42:45]\n\t"
  "s_mov_b32 m0, s72\n\t"
  "s_add_u32 s76, s68, 32768\n\t"
  "buffer_load_dwordx4 v0, s[60:63], s76 offen sc1 lds\n\t"
  "v_mfma_f32_16x16x32_bf16 v[46:49], a[88:91], v[20:23], v[46:49]\n\t"
  "v_mfma_f32_16x16x32_bf16 v[50:53], a[92:95], v[20:23], v[50:53]\n\t"
  "ds_read_b128 v[20:23], v7 offset:11264\n\t"
  "s_waitcnt lgkmcnt(3)\n\t"
  "v_mfma_f32_16x16x32_bf16 v[42:45], a[96:99], v[8:11], v[42:45]\n\t"
  "v_mfma_f32_16x16x32_bf16 v[46:49], a[100:103], v[8:11], v[46:49]\n\t"
  "s_add_u32 s72, s72, s73\n\t"
  "buffer_load_dwordx4 v0, s[60:63], s76 offen offset:1024 sc1 lds\n\t"
  "v_mfma_f32_16x16x32_bf16 v[50:53], a[104:107], v[8:11], v[50:53]\n\t"
  "ds_read_b128 v[8:11], v7 offset:12288\n\t"
  "s_waitcnt lgkmcnt(3)\n\t"
  "v_mfma_f32_16x16x32_bf16 v[42:45], a[108:111], v[12:15], v[42:45]\n\t"
  "v_mfma_f32_16x16x32_bf16 v[46:49], a[112:115], v[12:15], v[46:49]\n\t"
  "v_mfma_f32_16x16x32_bf16 v[50:53], a[116:119], v[12:15], v[50:53]\n\t"
  "ds_read_b128 v[12:15], v7 offset:13312\n\t"
  "s_waitcnt lgkmcnt(3)\n\t"
  "v_mfma_f32_16x16x32_bf16 v[42:45], a[120:123], v[16:19], v[42:45]\n\t"
  "s_mov_b32 m0, s72\n\t"
  "s_add_u32 s76, s68, 49152\n\t"
  "buffer_load_dwordx4 v0, s[60:63], s76 offen sc1 lds\n\t"
  "v_mfma_f32_16x16x32_bf16 v[46:49], a[124:127], v[16:19], v[46:49]\n\t"
  "v_mfma_f32_16x16x32_bf16 v[50:53], a[128:131], v[16:19], v[50:53]\n\t"
  "ds_read_b128 v[16:19], v7 offset:14336\n\t"
  "s_waitcnt lgkmcnt(3)\n\t"
  "v_mfma_f32_16x16x32_bf16 v[42:45], a[132:135], v[20:23], v[42:45]\n\t"
  "v_mfma_f32_16x16x32_bf16 v[46:49], a[136:139], v[20:23], v[46:49]\n\t"
  "s_add_u32 s72, s72, s73\n\t"
  "buffer_load_dwordx4 v0, s[60:63], s76 offen offset:1024 sc1 lds\n\t"
  "v_mfma_f32_16x16x32_bf16 v[50:53], a[140:143], v[20:23], v[50:53]\n\t"
  "ds_read_b128 v[20:23], v7 offset:15360\n\t"
  "s_waitcnt lgkmcnt(3)\n\t"
  "v_mfma_f32_16x16x32_bf16 v[42:45], a[144:147], v[8:11], v[42:45]\n\t"
  "v_mfma_f32_16x16x32_bf16 v[46:49], a[148:151], v[8:11], v[46:49]\n\t"
  "v_mfma_f32_16x16x32_bf16 v[50:53], a[152:155], v[8:11], v[50:53]\n\t"
  "ds_read_b128 v[8:11], v7 offset:16384\n\t"
  "s_waitcnt lgkmcnt(3)\n\t"
  "v_mfma_f32_16x16x32_bf16 v[42:45], a[156:159], v[12:15], v[42:45]\n\t"
  "buffer_load_dword v3, v2, s[36:39], 0 offen sc1\n\t"
  "v_mfma_f32_16x16x32_bf16 v[46:49], a[160:163], v[12:15], v[46:49]\n\t"
  "v_mfma_f32_16x16x32_bf16 v[50:53], a[164:167], v[12:15], v[50:53]\n\t"
  "ds_read_b128 v[12:15], v7 offset:17408\n\t"
  "s_waitcnt lgkmcnt(3)\n\t"
  "v_mfma_f32_16x16x32_bf16 v[42:45], a[168:171], v[16:19], v[42:45]\n\t"
  "v_mfma_f32_16x16x32_bf16 v[46:49], a[172:175], v[16:19], v[46:49]\n\t"
  "v_mfma_f32_16x16x32_bf16 v[50:53], a[176:179], v[16:19], v[50:53]\n\t"
  "ds_read_b128 v[16:19], v7 offset:18432\n\t"
  "s_waitcnt lgkmcnt(3)\n\t"
  "v_mfma_f32_16x16x32_bf16 v[42:45], a[180:183], v[20:23], v[42:45]\n\t"
  "v_mfma_f32_16x16x32_bf16 v[46:49], a[184:187], v[20:23], v[46:49]\n\t"
  "v_mfma_f32_16x16x32_bf16 v[50:53], a[188:191], v[20:23], v[50:53]\n\t"
  "ds_read_b128 v[20:23], v7 offset:19456\n\t"
  "s_waitcnt lgkmcnt(3)\n\t"
  "v_mfma_f32_16x16x32_bf16 v[42:45], a[192:195], v[8:11], v[42:45]\n\t"
  "v_mfma_f32_16x16x32_bf16 v[46:49], a[196:199], v[8:11], v[46:49]\n\t"
  "v_mfma_f32_16x16x32_bf16 v[50:53], a[200:203], v[8:11], v[50:53]\n\t"
  "ds_read_b128 v[8:11], v7 offset:20480\n\t"
  "s_waitcnt lgkmcnt(3)\n\t"
  "v_mfma_f32_16x16x32_bf16 v[42:45], a[204:207], v[12:15], v[42:45]\n\t"
  "v_mfma_f32_16x16x32_bf16 v[46:49], a[208:211], v[12:15], v[46:49]\n\t"
  "v_mfma_f32_16x16x32_bf16 v[50:53], a[212:215], v[12:15], v[50:53]\n\t"
  "ds_read_b128 v[12:15], v7 offset:21504\n\t"
  "s_waitcnt lgkmcnt(3)\n\t"
  "v_mfma_f32_16x16x32_bf16 v[42:45], a[216:219], v[16:19], v[42:45]\n\t"
  "v_mfma_f32_16x16x32_bf16 v[46:49], a[220:223], v[16:19], v[46:49]\n\t"
  "v_mfma_f32_16x16x32_bf16 v[50:53], a[224:227], v[16:19], v[50:53]\n\t"
  "ds_read_b128 v[16:19], v7 offset:22528\n\t"
  "s_waitcnt lgkmcnt(3)\n\t"
  "v_mfma_f32_16x16x32_bf16 v[42:45], a[228:231], v[20:23], v[42:45]\n\t"
  "v_cmp_le_i32 vcc, s57, v3\n\t"
  "v_mfma_f32_16x16x32_bf16 v[46:49], a[232:235], v[20:23], v[46:49]\n\t"
  "s_cmp_eq_u64 vcc, -1\n\t"
  "s_cselect_b32 s75, 1, 0\n\t"
  "v_mfma_f32_16x16x32_bf16 v[50:53], a[236:239], v[20:23], v[50:53]\n\t"
  "ds_read_b128 v[20:23], v7 offset:23552\n\t"
  "s_waitcnt lgkmcnt(3)\n\t"
  "v_mfma_f32_16x16x32_bf16 v[42:45], a[240:243], v[8:11], v[42:45]\n\t"
  "s_andn2_b32 s75, s75, s58\n\t"
  "s_cselect_b32 s62, s51, 0\n\t"
  "s_cselect_b32 s72, s71, 155648\n\t"
  "v_mfma_f32_16x16x32_bf16 v[46:49], a[244:247], v[8:11], v[46:49]\n\t"
  "s_lshl_b32 s73, s75, 13\n\t"
  "s_or_b32 s58, s58, s75\n\t"
  "v_mfma_f32_16x16x32_bf16 v[50:53], a[248:251], v[8:11], v[50:53]\n\t"
  "ds_read_b128 v[8:11], v7 offset:24576\n\t"
  "s_waitcnt lgkmcnt(3)\n\t"
  "v_mfma_f32_16x16x32_bf16 v[42:45], a[252:255], v[12:15], v[42:45]\n\t"
  "s_mov_b32 m0, s72\n\t"
  "s_add_u32 s76, s68, 0\n\t"
  "buffer_load_dwordx4 v0, s[60:63], s76 offen sc1 lds\n\t"
  "v_mfma_f32_16x16x32_bf16 v[46:49], v[224:227], v[12:15], v[46:49]\n\t"
  "v_mfma_f32_16x16x32_bf16 v[50:53], v[228:231], v[12:15], v[50:53]\n\t"
  "ds_read_b128 v[12:15], v7 offset:25600\n\t"
  "s_waitcnt lgkmcnt(3)\n\t"
  "v_mfma_f32_16x16x32_bf16 v[42:45], v[232:235], v[16:19], v[42:45]\n\t"
  "v_mfma_f32_16x16x32_bf16 v[46:49], v[236:239], v[16:19], v[46:49]\n\t"
  "s_add_u32 s72, s72, s73\n\t"
  "buffer_load_dwordx4 v0, s[60:63], s76 offen offset:1024 sc1 lds\n\t"
  "v_mfma_f32_16x16x32_bf16 v[50:53], v[240:243], v[16:19], v[50:53]\n\t"
  "ds_read_b128 v[16:19], v7 offset:26624\n\t"
  "s_waitcnt lgkmcnt(3)\n\t"
  "v_mfma_f32_16x16x32_bf16 v[42:45], v[244:247], v[20:23], v[42:45]\n\t"
  "v_mfma_f32_16x16x32_bf16 v[46:49], v[248:251], v[20:23], v[46:49]\n\t"
  "v_mfma_f32_16x16x32_bf16 v[50:53], v[252:255], v[20:23], v[50:53]\n\t"
  "ds_read_b128 v[20:23], v7 offset:27648\n\t"
  "s_waitcnt lgkmcnt(3)\n\t"
  "v_mfma_f32_16x16x32_bf16 v[42:45], v[192:195], v[8:11], v[42:45]\n\t"
  "s_mov_b32 m0, s72\n\t"
  "s_add_u32 s76, s68, 16384\n\t"
  "buffer_load_dwordx4 v0, s[60:63], s76 offen sc1 lds\n\t"
  "v_mfma_f32_16x16x32_bf16 v[46:49], v[196:199], v[8:11], v[46:49]\n\t"
  "v_mfma_f32_16x16x32_bf16 v[50:53], v[200:203], v[8:11], v[50:53]\n\t"
  "ds_read_b128 v[8:11], v7 offset:28672\n\t"
  "s_waitcnt lgkmcnt(3)\n\t"
  "v_mfma_f32_16x16x32_bf16 v[42:45], v[204:207], v[12:15], v[42:45]\n\t"
  "v_mfma_f32_16x16x32_bf16 v[46:49], v[208:211], v[12:15], v[46:49]\n\t"
  "s_add_u32 s72, s72, s73\n\t"
  "buffer_load_dwordx4 v0, s[60:63], s76 offen offset:1024 sc1 lds\n\t"
  "v_mfma_f32_16x16x32_bf16 v[50:53], v[212:215], v[12:15], v[50:53]\n\t"
  "ds_read_b128 v[12:15], v7 offset:29696\n\t"
  "s_waitcnt lgkmcnt(3)\n\t"
  "v_mfma_f32_16x16x32_bf16 v[42:45], v[216:219], v[16:19], v[42:45]\n\t"
  "v_mfma_f32_16x16x32_bf16 v[46:49], v[220:223], v[16:19], v[46:49]\n\t"
  "v_mfma_f32_16x16x32_bf16 v[50:53], v[160:163], v[16:19], v[50:53]\n\t"
  "ds_read_b128 v[16:19], v7 offset:30720\n\t"
  "s_waitcnt lgkmcnt(3)\n\t"
  "v_mfma_f32_16x16x32_bf16 v[42:45], v[164:167], v[20:23], v[42:45]\n\t"
  "s_mov_b32 m0, s72\n\t"
  "s_add_u32 s76, s68, 32768\n\t"
  "buffer_load_dwordx4 v0, s[60:63], s76 offen sc1 lds\n\t"
  "v_mfma_f32_16x16x32_bf16 v[46:49], v[168:171], v[20:23], v[46:49]\n\t"
  "v_mfma_f32_16x16x32_bf16 v[50:53], v[172:175], v[20:23], v[50:53]\n\t"
  "ds_read_b128 v[20:23], v7 offset:31744\n\t"
  "s_waitcnt lgkmcnt(3)\n\t"
  "v_mfma_f32_16x16x32_bf16 v[42:45], v[176:179], v[8:11], v[42:45]\n\t"
  "v_mfma_f32_16x16x32_bf16 v[46:49], v[180:183], v[8:11], v[46:49]\n\t"
  "buffer_load_dword v3, v2, s[36:39], 0 offen sc1\n\t"
  "v_mfma_f32_16x16x32_bf16 v[50:53], v[184:187], v[8:11], v[50:53]\n\t"
  "ds_read_b128 v[8:11], v7 offset:32768\n\t"
  "s_waitcnt lgkmcnt(3)\n\t"
  "v_mfma_f32_16x16x32_bf16 v[42:45], v[188:191], v[12:15], v[42:45]\n\t"
  "v_mfma_f32_16x16x32_bf16 v[46:49], v[128:131], v[12:15], v[46:49]\n\t"
  "v_mfma_f32_16x16x32_bf16 v[50:53], v[132:135], v[12:15], v[50:53]\n\t"
  "ds_read_b128 v[12:15], v7 offset:33792\n\t"
  "s_waitcnt lgkmcnt(3)\n\t"
  "v_mfma_f32_16x16x32_bf16 v[42:45], v[136:139], v[16:19], v[42:45]\n\t"
  "s_add_u32 s72, s72, s73\n\t"
  "buffer_load_dwordx4 v0, s[60:63], s76 offen offset:1024 sc1 lds\n\t"
  "v_mfma_f32_16x16x32_bf16 v[46:49], v[140:143], v[16:19], v[46:49]\n\t"
  "v_mfma_f32_16x16x32_bf16 v[50:53], v[144:147], v[16:19], v[50:53]\n\t"
  "ds_read_b128 v[16:19], v7 offset:34816\n\t"
  "s_waitcnt lgkmcnt(3)\n\t"
  "v_mfma_f32_16x16x32_bf16 v[42:45], v[148:151], v[20:23], v[42:45]\n\t"
  "v_mfma_f32_16x16x32_bf16 v[46:49], v[152:155], v[20:23], v[46:49]\n\t"
  "s_mov_b32 m0, s72\n\t"
  "s_add_u32 s76, s68, 49152\n\t"
  "buffer_load_dwordx4 v0, s[60:63], s76 offen sc1 lds\n\t"
  "v_mfma_f32_16x16x32_bf16 v[50:53], v[156:159], v[20:23], v[50:53]\n\t"
  "ds_read_b128 v[20:23], v7 offset:35840\n\t"
  "s_waitcnt lgkmcnt(3)\n\t"
  "v_mfma_f32_16x16x32_bf16 v[42:45], v[96:99], v[8:11], v[42:45]\n\t"
  "v_add_u32 v1, s74, v1\n\t"
  "v_add_u32 v7, s74, v7\n\t"
  "v_mfma_f32_16x16x32_bf16 v[46:49], v[100:103], v[8:11], v[46:49]\n\t"
  "v_mfma_f32_16x16x32_bf16 v[50:53], v[104:107], v[8:11], v[50:53]\n\t"
  "s_waitcnt lgkmcnt(2)\n\t"
  "v_mfma_f32_16x16x32_bf16 v[42:45], v[108:111], v[12:15], v[42:45]\n\t"
  "s_add_u32 s72, s72, s73\n\t"
  "buffer_load_dwordx4 v0, s[60:63], s76 offen offset:1024 sc1 lds\n\t"
  "v_mfma_f32_16x16x32_bf16 v[46:49], v[112:115], v[12:15], v[46:49]\n\t"
  "v_mfma_f32_16x16x32_bf16 v[50:53], v[116:119], v[12:15], v[50:53]\n\t"
  "s_waitcnt lgkmcnt(1)\n\t"
  "v_mfma_f32_16x16x32_bf16 v[42:45], v[120:123], v[16:19], v[42:45]\n\t"
  "v_mfma_f32_16x16x32_bf16 v[46:49], v[124:127], v[16:19], v[46:49]\n\t"
  "s_lshl_b32 s52, s58, 7\n\t"
  "s_add_u32 s52, s52, s57\n\t"
  "v_mfma_f32_16x16x32_bf16 v[50:53], v[80:83], v[16:19], v[50:53]\n\t"
  "s_waitcnt lgkmcnt(0)\n\t"
  "v_mfma_f32_16x16x32_bf16 v[42:45], v[84:87], v[20:23], v[42:45]\n\t"
  "v_mfma_f32_16x16x32_bf16 v[46:49], v[88:91], v[20:23], v[46:49]\n\t"
  "v_mfma_f32_16x16x32_bf16 v[50:53], v[92:95], v[20:23], v[50:53]\n\t"
  "s_nop 0\n\t"
  "v_cmp_le_i32 vcc, s52, v3\n\t"
  "s_cmp_eq_u64 vcc, -1\n\t"
  "s_cbranch_scc1 80f\n\t"
  "70:\n\t"
  "s_nop 3\n\t"
  "v_cvt_pk_bf16_f32 v45, v44, v45\n\t"
  "v_cvt_pk_bf16_f32 v44, v42, v43\n\t"
  "v_mov_b64 v[42:43], v[24:25]\n\t"
  "s_add_u32 s77, s69, s44\n\t"
  "buffer_store_dwordx4 v[42:45], v6, s[36:39], s77 offen sc1\n\t"
  "v_cvt_pk_bf16_f32 v49, v48, v49\n\t"
  "v_cvt_pk_bf16_f32 v48, v46, v47\n\t"
  "v_mov_b64 v[46:47], v[26:27]\n\t"
  "s_add_u32 s77, s69, s45\n\t"
  "buffer_store_dwordx4 v[46:49], v6, s[36:39], s77 offen sc1\n\t"
  "v_cvt_pk_bf16_f32 v53, v52, v53\n\t"
  "v_cvt_pk_bf16_f32 v52, v50, v51\n\t"
  "v_mov_b64 v[50:51], v[28:29]\n\t"
  "s_add_u32 s77, s69, s46\n\t"
  "buffer_store_dwordx4 v[50:53], v6, s[36:39], s77 offen sc1\n\t"
  "s_mov_b32 s86, s51\n\t"
  "s_mov_b32 s66, 0\n\t"
  "s_cmp_lg_u32 s58, 0\n\t"
  "s_cbranch_scc1 5f\n\t"
  "v_cmp_le_i32 vcc, s57, v3\n\t"
  "s_cmp_eq_u64 vcc, -1\n\t"
  "s_cbranch_scc1 41f\n\t"
  "buffer_load_dword v3, v2, s[36:39], 0 offen sc1\n\t"
  "s_sleep 2\n\t"
  "v_cmp_le_i32 vcc, s57, v3\n\t"
  "s_cmp_eq_u64 vcc, -1\n\t"
  "s_cbranch_scc1 41f\n\t"
  "buffer_load_dword v3, v2, s[36:39], 0 offen sc1\n\t"
  "s_sleep 2\n\t"
  "v_cmp_le_i32 vcc, s57, v3\n\t"
  "s_cmp_eq_u64 vcc, -1\n\t"
  "s_cbranch_scc1 41f\n\t"
  "buffer_load_dword v3, v2, s[36:39], 0 offen sc1\n\t"
  "s_sleep 2\n\t"
  "v_cmp_le_i32 vcc, s57, v3\n\t"
  "s_cmp_eq_u64 vcc, -1\n\t"
  "s_cbranch_scc1 41f\n\t"
  "buffer_load_dword v3, v2, s[36:39], 0 offen sc1\n\t"
  "s_sleep 2\n\t"
  "s_mov_b32 s86, 0\n\t"
  "v_mov_b32 v30, s56\n\t"
  "s_waitcnt vmcnt(4)\n\t"
  "buffer_store_dword v30, off, s[36:39], s40 sc1\n\t"
  "s_cmp_eq_u32 s56, 63\n\t"
  "s_cbranch_scc1 9f\n\t"
  "3:\n\t"
  "s_mov_b32 s79, 0\n\t"
  "s_mov_b32 s66, 0\n\t"
  "4:\n\t"
  "v_cmp_le_i32 vcc, s57, v3\n\t"
  "s_cmp_eq_u64 vcc, -1\n\t"
  "s_cbranch_scc1 41f\n\t"
  "s_waitcnt vmcnt(8)\n\t"
  "buffer_load_dword v3, v2, s[36:39], 0 offen sc1\n\t"
  "s_sleep 4\n\t"
  "s_add_u32 s79, s79, 1\n\t"
  "s_cmp_lt_u32 s79, s80\n\t"
  "s_cbranch_scc1 4b\n\t"
  "s_mov_b32 s80, 0\n\t"
  "41:\n\t"
  "s_add_u32 m0, s71, 0\n\t"
  "s_add_u32 s76, s68, 0\n\t"
  "buffer_load_dwordx4 v0, s[36:39], s76 offen sc1 lds\n\t"
  "buffer_load_dwordx4 v0, s[36:39], s76 offen offset:1024 sc1 lds\n\t"
  "s_add_u32 m0, s71, 8192\n\t"
  "s_add_u32 s76, s68, 16384\n\t"
  "buffer_load_dwordx4 v0, s[36:39], s76 offen sc1 lds\n\t"
  "buffer_load_dwordx4 v0, s[36:39], s76 offen offset:1024 sc1 lds\n\t"
  "s_add_u32 m0, s71, 16384\n\t"
  "s_add_u32 s76, s68, 32768\n\t"
  "buffer_load_dwordx4 v0, s[36:39], s76 offen sc1 lds\n\t"
  "buffer_load_dwordx4 v0, s[36:39], s76 offen offset:1024 sc1 lds\n\t"
  "s_add_u32 m0, s71, 24576\n\t"
  "s_add_u32 s76, s68, 49152\n\t"
  "buffer_load_dwordx4 v0, s[36:39], s76 offen sc1 lds\n\t"
  "buffer_load_dwordx4 v0, s[36:39], s76 offen offset:1024 sc1 lds\n\t"
  "v_mov_b32 v30, s56\n\t"
  "buffer_store_dword v30, off, s[64:67], s40 sc1\n\t"
  "buffer_store_dword v30, off, s[64:67], s40 sc1\n\t"
  "s_waitcnt vmcnt(10)\n\t"
  "buffer_store_dword v30, off, s[84:87], s40 sc1\n\t"
  "s_mov_b32 s86, 0\n\t"
  "s_waitcnt vmcnt(7)\n\t"
  "s_branch 6f\n\t"
  "5:\n\t"
  "s_waitcnt vmcnt(7)\n\t"
  "6:\n\t"
  "s_waitcnt lgkmcnt(0)\n\t"
  "s_barrier\n\t"
  "ds_read_b128 v[8:11], v1 offset:0\n\t"
  "ds_read_b128 v[12:15], v1 offset:1024\n\t"
  "ds_read_b128 v[16:19], v1 offset:2048\n\t"
  "s_branch 1b\n\t"
  "80:\n\t"
  "s_add_u32 m0, s71, 0\n\t"
  "s_add_u32 s76, s68, 0\n\t"
  "buffer_load_dwordx4 v0, s[36:39], s76 offen sc1 lds\n\t"
  "buffer_load_dwordx4 v0, s[36:39], s76 offen offset:1024 sc1 lds\n\t"
  "s_add_u32 m0, s71, 8192\n\t"
  "s_add_u32 s76, s68, 16384\n\t"
  "buffer_load_dwordx4 v0, s[36:39], s76 offen sc1 lds\n\t"
  "buffer_load_dwordx4 v0, s[36:39], s76 offen offset:1024 sc1 lds\n\t"
  "s_add_u32 m0, s71, 16384\n\t"
  "s_add_u32 s76, s68, 32768\n\t"
  "buffer_load_dwordx4 v0, s[36:39], s76 offen sc1 lds\n\t"
  "buffer_load_dwordx4 v0, s[36:39], s76 offen offset:1024 sc1 lds\n\t"
  "s_add_u32 m0, s71, 24576\n\t"
  "s_add_u32 s76, s68, 49152\n\t"
  "buffer_load_dwordx4 v0, s[36:39], s76 offen sc1 lds\n\t"
  "buffer_load_dwordx4 v0, s[36:39], s76 offen offset:1024 sc1 lds\n\t"
  "s_mov_b32 s58, 1\n\t"
  "s_mov_b32 s52, 127\n\t"
  "s_branch 70b\n\t"
  "9:\n\t"
  ".Lws_end_D:\n\t"
  "s_waitcnt vmcnt(0)\n\t"
  "s_waitcnt vmcnt(0) lgkmcnt(0)\n\t"
    : "+{v[0:7]}"(lc), "+{v[8:23]}"(so) :: CLOB_ALL);
  }
}

__device__ __forceinline__ unsigned pko(float a, float b) { return (unsigned)opaque((int)pk(a, b)); }
__device__ __forceinline__ float fo(float x) { asm volatile("" : "+v"(x)); return x; }

__device__ __forceinline__ void role_c(Ctx& c, const Params& p) {
  const int j = c.idx - R_C;
  v16u so = {};
  #pragma unroll
  for (int q = 0; q < 5; ++q) { int row, byte; slab_src<10>((c.w & 1) * 5 + q, c.lane, row, byte); so[q] = j * 320 + byte < DF * 2 ? row * (DF * 2) + j * 320 + byte : OOB; }
  so[6] = slab_rd<10>(c.lane, 2 * c.w); so[7] = slab_rd<10>(c.lane, 2 * c.w + 1); so[8] = slab_rd<10>(c.lane, 8 + (c.w >> 1));
  const int ka = 5 * j + c.w, kb = 5 * j + 4, junk = sp(496 << 10);
  v8u lc = {(unsigned)(c.lane * 16), (unsigned)xn_off((c.lane >> 2) & 3, c.lane >> 4, c.lane & 3), (unsigned)flag_at(c.layer, F_XN1 + (c.lane >> 3), 0), 0u,
            (unsigned)(base_b(c, c.lane) + B_XN1 + c.w * 2048), (unsigned)(base_b(c, c.lane) + B_HF), (unsigned)(c.lane * 16 + c.w * 4), c.lane < 32 ? c.lane * 16u : OOB};
  lc[3] = stage_scalars(c, c.w < 2 ? p.wg : p.wu, DM * DF * 2, DF * 2, 0, 0, spd(((ka / 36) * 72 + ka % 36) * 1024), spd(((ka / 36) * 72 + 36 + ka % 36) * 1024),
                        kb < 144 ? spd(((kb / 36) * 72 + (c.w >> 1) * 36 + kb % 36) * 1024) + (c.w & 1) * 512 : junk, 0, 0, 0);
  asm volatile(
  "s_nop 4\n\t"
  "v_readlane_b32 s36, v3, 0\n\t"
  "v_readlane_b32 s37, v3, 1\n\t"
  "v_readlane_b32 s38, v3, 2\n\t"
  "v_readlane_b32 s39, v3, 3\n\t"
  "v_readlane_b32 s40, v3, 4\n\t"
  "v_readlane_b32 s41, v3, 5\n\t"
  "v_readlane_b32 s42, v3, 6\n\t"
  "v_readlane_b32 s43, v3, 7\n\t"
  "v_readlane_b32 s44, v3, 8\n\t"
  "v_readlane_b32 s45, v3, 9\n\t"
  "v_readlane_b32 s46, v3, 10\n\t"
  "v_readlane_b32 s47, v3, 11\n\t"
  "v_readlane_b32 s48, v3, 12\n\t"
  "v_readlane_b32 s49, v3, 13\n\t"
  "v_readlane_b32 s50, v3, 14\n\t"
  "v_readlane_b32 s51, v3, 15\n\t"
  "s_nop 4\n\t"
  "v_readlane_b32 s56, v3, 16\n\t"
  "v_readlane_b32 s57, v3, 17\n\t"
  "v_readlane_b32 s58, v3, 18\n\t"
  "v_readlane_b32 s60, v3, 19\n\t"
  "s_mov_b32 s59, s39\n\t"
  "s_mul_i32 s61, s41, 5\n\t"
  "s_lshr_b32 s61, s61, 1\n\t"
  "s_mov_b32 s62, 0\n\t"
  "s_nop 2\n\t"
  "v_and_b32 v20, 32, v0\n\t"
  "s_lshr_b32 s63, s41, 6\n\t"
  "s_and_b32 s63, s63, 32\n\t"
  "v_cmp_eq_u32 vcc, s63, v20\n\t"
  "s_add_u32 m0, s61, 0\n\t"
  "s_nop 0\n\t"
  "buffer_load_dwordx4 v8, s[56:59], s62 offen lds\n\t"
  "s_add_u32 m0, s61, 1024\n\t"
  "s_nop 0\n\t"
  "buffer_load_dwordx4 v9, s[56:59], s62 offen lds\n\t"
  "s_add_u32 m0, s61, 2048\n\t"
  "s_nop 0\n\t"
  "buffer_load_dwordx4 v10, s[56:59], s62 offen lds\n\t"
  "s_add_u32 m0, s61, 3072\n\t"
  "s_nop 0\n\t"
  "buffer_load_dwordx4 v11, s[56:59], s62 offen lds\n\t"
  "s_add_u32 m0, s61, 4096\n\t"
  "s_nop 0\n\t"
  "buffer_load_dwordx4 v12, s[56:59], s62 offen lds\n\t"
  "s_add_u32 s62, s62, s60\n\t"
  "s_add_u32 m0, s61, 20480\n\t"
  "s_nop 0\n\t"
  "buffer_load_dwordx4 v8, s[56:59], s62 offen lds\n\t"
  "s_add_u32 m0, s61, 21504\n\t"
  "s_nop 0\n\t"
  "buffer_load_dwordx4 v9, s[56:59], s62 offen lds\n\t"
  "s_add_u32 m0, s61, 22528\n\t"
  "s_nop 0\n\t"
  "buffer_load_dwordx4 v10, s[56:59], s62 offen lds\n\t"
  "s_add_u32 m0, s61, 23552\n\t"
  "s_nop 0\n\t"
  "buffer_load_dwordx4 v11, s[56:59], s62 offen lds\n\t"
  "s_add_u32 m0, s61, 24576\n\t"
  "s_nop 0\n\t"
  "buffer_load_dwordx4 v12, s[56:59], s62 offen lds\n\t"
  "s_add_u32 s62, s62, s60\n\t"
  "s_add_u32 m0, s61, 40960\n\t"
  "s_nop 0\n\t"
  "buffer_load_dwordx4 v8, s[56:59], s62 offen lds\n\t"
  "s_add_u32 m0, s61, 41984\n\t"
  "s_nop 0\n\t"
  "buffer_load_dwordx4 v9, s[56:59], s62 offen lds\n\t"
  "s_add_u32 m0, s61, 43008\n\t"
  "s_nop 0\n\t"
  "buffer_load_dwordx4 v10, s[56:59], s62 offen lds\n\t"
  "s_add_u32 m0, s61, 44032\n\t"
  "s_nop 0\n\t"
  "buffer_load_dwordx4 v11, s[56:59], s62 offen lds\n\t"
  "s_add_u32 m0, s61, 45056\n\t"
  "s_nop 0\n\t"
  "buffer_load_dwordx4 v12, s[56:59], s62 offen lds\n\t"
  "s_add_u32 s62, s62, s60\n\t"
  "s_add_u32 m0, s61, 61440\n\t"
  "s_nop 0\n\t"
  "buffer_load_dwordx4 v8, s[56:59], s62 offen lds\n\t"
  "s_add_u32 m0, s61, 62464\n\t"
  "s_nop 0\n\t"
  "buffer_load_dwordx4 v9, s[56:59], s62 offen lds\n\t"
  "s_add_u32 m0, s61, 63488\n\t"
  "s_nop 0\n\t"
  "buffer_load_dwordx4 v10, s[56:59], s62 offen lds\n\t"
  "s_add_u32 m0, s61, 64512\n\t"
  "s_nop 0\n\t"
  "buffer_load_dwordx4 v11, s[56:59], s62 offen lds\n\t"
  "s_add_u32 m0, s61, 65536\n\t"
  "s_nop 0\n\t"
  "buffer_load_dwordx4 v12, s[56:59], s62 offen lds\n\t"
  "s_add_u32 s62, s62, s60\n\t"
  "s_add_u32 m0, s61, 81920\n\t"
  "s_nop 0\n\t"
  "buffer_load_dwordx4 v8, s[56:59], s62 offen lds\n\t"
  "s_add_u32 m0, s61, 82944\n\t"
  "s_nop 0\n\t"
  "buffer_load_dwordx4 v9, s[56:59], s62 offen lds\n\t"
  "s_add_u32 m0, s61, 83968\n\t"
  "s_nop 0\n\t"
  "buffer_load_dwordx4 v10, s[56:59], s62 offen lds\n\t"
  "s_add_u32 m0, s61, 84992\n\t"
  "s_nop 0\n\t"
  "buffer_load_dwordx4 v11, s[56:59], s62 offen lds\n\t"
  "s_add_u32 m0, s61, 86016\n\t"
  "s_nop 0\n\t"
  "buffer_load_dwordx4 v12, s[56:59], s62 offen lds\n\t"
  "s_add_u32 s62, s62, s60\n\t"
  "s_add_u32 m0, s61, 102400\n\t"
  "s_nop 0\n\t"
  "buffer_load_dwordx4 v8, s[56:59], s62 offen lds\n\t"
  "s_add_u32 m0, s61, 103424\n\t"
  "s_nop 0\n\t"
  "buffer_load_dwordx4 v9, s[56:59], s62 offen lds\n\t"
  "s_add_u32 m0, s61, 104448\n\t"
  "s_nop 0\n\t"
  "buffer_load_dwordx4 v10, s[56:59], s62 offen lds\n\t"
  "s_add_u32 m0, s61, 105472\n\t"
  "s_nop 0\n\t"
  "buffer_load_dwordx4 v11, s[56:59], s62 offen lds\n\t"
  "s_add_u32 m0, s61, 106496\n\t"
  "s_nop 0\n\t"
  "buffer_load_dwordx4 v12, s[56:59], s62 offen lds\n\t"
  "s_add_u32 s62, s62, s60\n\t"
  "s_add_u32 m0, s61, 122880\n\t"
  "s_nop 0\n\t"
  "buffer_load_dwordx4 v8, s[56:59], s62 offen lds\n\t"
  "s_add_u32 m0, s61, 123904\n\t"
  "s_nop 0\n\t"
  "buffer_load_dwordx4 v9, s[56:59], s62 offen lds\n\t"
  "s_add_u32 m0, s61, 124928\n\t"
  "s_nop 0\n\t"
  "buffer_load_dwordx4 v10, s[56:59], s62 offen lds\n\t"
  "s_add_u32 m0, s61, 125952\n\t"
  "s_nop 0\n\t"
  "buffer_load_dwordx4 v11, s[56:59], s62 offen lds\n\t"
  "s_add_u32 m0, s61, 126976\n\t"
  "s_nop 0\n\t"
  "buffer_load_dwordx4 v12, s[56:59], s62 offen lds\n\t"
  "s_add_u32 s62, s62, s60\n\t"
  "s_waitcnt vmcnt(30) lgkmcnt(0)\n\t"
  "s_barrier\n\t"
  "s_add_u32 m0, s61, 143360\n\t"
  "s_nop 0\n\t"
  "buffer_load_dwordx4 v8, s[56:59], s62 offen lds\n\t"
  "s_add_u32 m0, s61, 144384\n\t"
  "s_nop 0\n\t"
  "buffer_load_dwordx4 v9, s[56:59], s62 offen lds\n\t"
  "s_add_u32 m0, s61, 145408\n\t"
  "s_nop 0\n\t"
  "buffer_load_dwordx4 v10, s[56:59], s62 offen lds\n\t"
  "s_add_u32 m0, s61, 146432\n\t"
  "s_nop 0\n\t"
  "buffer_load_dwordx4 v11, s[56:59], s62 offen lds\n\t"
  "s_add_u32 m0, s61, 147456\n\t"
  "s_nop 0\n\t"
  "buffer_load_dwordx4 v12, s[56:59], s62 offen lds\n\t"
  "s_add_u32 s62, s62, s60\n\t"
  "v_add_u32 v20, 0, v16\n\t"
  "ds_read_b64_tr_b16 v[24:25], v20\n\t"
  "ds_read_b64_tr_b16 v[26:27], v20 offset:5120\n\t"
  "v_add_u32 v20, 10240, v16\n\t"
  "ds_read_b64_tr_b16 v[28:29], v20\n\t"
  "ds_read_b64_tr_b16 v[30:31], v20 offset:5120\n\t"
  "v_add_u32 v20, 0, v14\n\t"
  "ds_read_b64_tr_b16 a[0:1], v20\n\t"
  "ds_read_b64_tr_b16 a[2:3], v20 offset:5120\n\t"
  "v_add_u32 v20, 10240, v14\n\t"
  "ds_read_b64_tr_b16 a[4:5], v20\n\t"
  "ds_read_b64_tr_b16 a[6:7], v20 offset:5120\n\t"
  "v_add_u32 v20, 0, v15\n\t"
  "ds_read_b64_tr_b16 a[8:9], v20\n\t"
  "ds_read_b64_tr_b16 a[10:11], v20 offset:5120\n\t"
  "v_add_u32 v20, 10240, v15\n\t"
  "ds_read_b64_tr_b16 a[12:13], v20\n\t"
  "ds_read_b64_tr_b16 a[14:15], v20 offset:5120\n\t"
  "s_waitcnt lgkmcnt(8)\n\t"
  "s_cmp_lg_u32 s63, 0\n\t"
  "s_cbranch_scc1 71f\n\t"
  "v_cndmask_b32_dpp v32, v28, v24, vcc quad_perm:[0,1,0,1] row_mask:0xf bank_mask:0xf\n\t"
  "v_cndmask_b32_dpp v33, v29, v25, vcc quad_perm:[0,1,0,1] row_mask:0xf bank_mask:0xf\n\t"
  "v_cndmask_b32_dpp v34, v30, v26, vcc quad_perm:[0,1,0,1] row_mask:0xf bank_mask:0xf\n\t"
  "v_cndmask_b32_dpp v35, v31, v27, vcc quad_perm:[0,1,0,1] row_mask:0xf bank_mask:0xf\n\t"
  "s_branch 72f\n\t"
  "71:\n\t"
  "v_cndmask_b32_dpp v32, v24, v28, vcc quad_perm:[2,3,2,3] row_mask:0xf bank_mask:0xf\n\t"
  "v_cndmask_b32_dpp v33, v25, v29, vcc quad_perm:[2,3,2,3] row_mask:0xf bank_mask:0xf\n\t"
  "v_cndmask_b32_dpp v34, v26, v30, vcc quad_perm:[2,3,2,3] row_mask:0xf bank_mask:0xf\n\t"
  "v_cndmask_b32_dpp v35, v27, v31, vcc quad_perm:[2,3,2,3] row_mask:0xf bank_mask:0xf\n\t"
  "72:\n\t"
  "v_accvgpr_write_b32 a16, v32\n\t"
  "v_accvgpr_write_b32 a17, v33\n\t"
  "v_accvgpr_write_b32 a18, v34\n\t"
  "v_accvgpr_write_b32 a19, v35\n\t"
  "s_waitcnt vmcnt(30) lgkmcnt(0)\n\t"
  "s_barrier\n\t"
  "s_add_u32 m0, s61, 0\n\t"
  "s_nop 0\n\t"
  "buffer_load_dwordx4 v8, s[56:59], s62 offen lds\n\t"
  "s_add_u32 m0, s61, 1024\n\t"
  "s_nop 0\n\t"
  "buffer_load_dwordx4 v9, s[56:59], s62 offen lds\n\t"
  "s_add_u32 m0, s61, 2048\n\t"
  "s_nop 0\n\t"
  "buffer_load_dwordx4 v10, s[56:59], s62 offen lds\n\t"
  "s_add_u32 m0, s61, 3072\n\t"
  "s_nop 0\n\t"
  "buffer_load_dwordx4 v11, s[56:59], s62 offen lds\n\t"
  "s_add_u32 m0, s61, 4096\n\t"
  "s_nop 0\n\t"
  "buffer_load_dwordx4 v12, s[56:59], s62 offen lds\n\t"
  "s_add_u32 s62, s62, s60\n\t"
  "v_add_u32 v20, 20480, v16\n\t"
  "ds_read_b64_tr_b16 v[24:25], v20\n\t"
  "ds_read_b64_tr_b16 v[26:27], v20 offset:5120\n\t"
  "v_add_u32 v20, 30720, v16\n\t"
  "ds_read_b64_tr_b16 v[28:29], v20\n\t"
  "ds_read_b64_tr_b16 v[30:31], v20 offset:5120\n\t"
  "v_add_u32 v20, 20480, v14\n\t"
  "ds_read_b64_tr_b16 a[20:21], v20\n\t"
  "ds_read_b64_tr_b16 a[22:23], v20 offset:5120\n\t"
  "v_add_u32 v20, 30720, v14\n\t"
  "ds_read_b64_tr_b16 a[24:25], v20\n\t"
  "ds_read_b64_tr_b16 a[26:27], v20 offset:5120\n\t"
  "v_add_u32 v20, 20480, v15\n\t"
  "ds_read_b64_tr_b16 a[28:29], v20\n\t"
  "ds_read_b64_tr_b16 a[30:31], v20 offset:5120\n\t"
  "v_add_u32 v20, 30720, v15\n\t"
  "ds_read_b64_tr_b16 a[32:33], v20\n\t"
  "ds_read_b64_tr_b16 a[34:35], v20 offset:5120\n\t"
  "s_waitcnt lgkmcnt(8)\n\t"
  "s_cmp_lg_u32 s63, 0\n\t"
  "s_cbranch_scc1 71f\n\t"
  "v_cndmask_b32_dpp v32, v28, v24, vcc quad_perm:[0,1,0,1] row_mask:0xf bank_mask:0xf\n\t"
  "v_cndmask_b32_dpp v33, v29, v25, vcc quad_perm:[0,1,0,1] row_mask:0xf bank_mask:0xf\n\t"
  "v_cndmask_b32_dpp v34, v30, v26, vcc quad_perm:[0,1,0,1] row_mask:0xf bank_mask:0xf\n\t"
  "v_cndmask_b32_dpp v35, v31, v27, vcc quad_perm:[0,1,0,1] row_mask:0xf bank_mask:0xf\n\t"
  "s_branch 72f\n\t"
  "71:\n\t"
  "v_cndmask_b32_dpp v32, v24, v28, vcc quad_perm:[2,3,2,3] row_mask:0xf bank_mask:0xf\n\t"
  "v_cndmask_b32_dpp v33, v25, v29, vcc quad_perm:[2,3,2,3] row_mask:0xf bank_mask:0xf\n\t"
  "v_cndmask_b32_dpp v34, v26, v30, vcc quad_perm:[2,3,2,3] row_mask:0xf bank_mask:0xf\n\t"
  "v_cndmask_b32_dpp v35, v27, v31, vcc quad_perm:[2,3,2,3] row_mask:0xf bank_mask:0xf\n\t"
  "72:\n\t"
  "v_accvgpr_write_b32 a36, v32\n\t"
  "v_accvgpr_write_b32 a37, v33\n\t"
  "v_accvgpr_write_b32 a38, v34\n\t"
  "v_accvgpr_write_b32 a39, v35\n\t"
  "s_waitcnt vmcnt(30) lgkmcnt(0)\n\t"
  "s_barrier\n\t"
  "s_add_u32 m0, s61, 20480\n\t"
  "s_nop 0\n\t"
  "buffer_load_dwordx4 v8, s[56:59], s62 offen lds\n\t"
  "s_add_u32 m0, s61, 21504\n\t"
  "s_nop 0\n\t"
  "buffer_load_dwordx4 v9, s[56:59], s62 offen lds\n\t"
  "s_add_u32 m0, s61, 22528\n\t"
  "s_nop 0\n\t"
  "buffer_load_dwordx4 v10, s[56:59], s62 offen lds\n\t"
  "s_add_u32 m0, s61, 23552\n\t"
  "s_nop 0\n\t"
  "buffer_load_dwordx4 v11, s[56:59], s62 offen lds\n\t"
  "s_add_u32 m0, s61, 24576\n\t"
  "s_nop 0\n\t"
  "buffer_load_dwordx4 v12, s[56:59], s62 offen lds\n\t"
  "s_add_u32 s62, s62, s60\n\t"
  "v_add_u32 v20, 40960, v16\n\t"
  "ds_read_b64_tr_b16 v[24:25], v20\n\t"
  "ds_read_b64_tr_b16 v[26:27], v20 offset:5120\n\t"
  "v_add_u32 v20, 51200, v16\n\t"
  "ds_read_b64_tr_b16 v[28:29], v20\n\t"
  "ds_read_b64_tr_b16 v[30:31], v20 offset:5120\n\t"
  "v_add_u32 v20, 40960, v14\n\t"
  "ds_read_b64_tr_b16 a[40:41], v20\n\t"
  "ds_read_b64_tr_b16 a[42:43], v20 offset:5120\n\t"
  "v_add_u32 v20, 51200, v14\n\t"
  "ds_read_b64_tr_b16 a[44:45], v20\n\t"
  "ds_read_b64_tr_b16 a[46:47], v20 offset:5120\n\t"
  "v_add_u32 v20, 40960, v15\n\t"
  "ds_read_b64_tr_b16 a[48:49], v20\n\t"
  "ds_read_b64_tr_b16 a[50:51], v20 offset:5120\n\t"
  "v_add_u32 v20, 51200, v15\n\t"
  "ds_read_b64_tr_b16 a[52:53], v20\n\t"
  "ds_read_b64_tr_b16 a[54:55], v20 offset:5120\n\t"
  "s_waitcnt lgkmcnt(8)\n\t"
  "s_cmp_lg_u32 s63, 0\n\t"
  "s_cbranch_scc1 71f\n\t"
  "v_cndmask_b32_dpp v32, v28, v24, vcc quad_perm:[0,1,0,1] row_mask:0xf bank_mask:0xf\n\t"
  "v_cndmask_b32_dpp v33, v29, v25, vcc quad_perm:[0,1,0,1] row_mask:0xf bank_mask:0xf\n\t"
  "v_cndmask_b32_dpp v34, v30, v26, vcc quad_perm:[0,1,0,1] row_mask:0xf bank_mask:0xf\n\t"
  "v_cndmask_b32_dpp v35, v31, v27, vcc quad_perm:[0,1,0,1] row_mask:0xf bank_mask:0xf\n\t"
  "s_branch 72f\n\t"
  "71:\n\t"
  "v_cndmask_b32_dpp v32, v24, v28, vcc quad_perm:[2,3,2,3] row_mask:0xf bank_mask:0xf\n\t"
  "v_cndmask_b32_dpp v33, v25, v29, vcc quad_perm:[2,3,2,3] row_mask:0xf bank_mask:0xf\n\t"
  "v_cndmask_b32_dpp v34, v26, v30, vcc quad_perm:[2,3,2,3] row_mask:0xf bank_mask:0xf\n\t"
  "v_cndmask_b32_dpp v35, v27, v31, vcc quad_perm:[2,3,2,3] row_mask:0xf bank_mask:0xf\n\t"
  "72:\n\t"
  "v_accvgpr_write_b32 a56, v32\n\t"
  "v_accvgpr_write_b32 a57, v33\n\t"
  "v_accvgpr_write_b32 a58, v34\n\t"
  "v_accvgpr_write_b32 a59, v35\n\t"
  "s_waitcnt vmcnt(30) lgkmcnt(0)\n\t"
  "s_barrier\n\t"
  "s_add_u32 m0, s61, 40960\n\t"
  "s_nop 0\n\t"
  "buffer_load_dwordx4 v8, s[56:59], s62 offen lds\n\t"
  "s_add_u32 m0, s61, 41984\n\t"
  "s_nop 0\n\t"
  "buffer_load_dwordx4 v9, s[56:59], s62 offen lds\n\t"
  "s_add_u32 m0, s61, 43008\n\t"
  "s_nop 0\n\t"
  "buffer_load_dwordx4 v10, s[56:59], s62 offen lds\n\t"
  "s_add_u32 m0, s61, 44032\n\t"
  "s_nop 0\n\t"
  "buffer_load_dwordx4 v11, s[56:59], s62 offen lds\n\t"
  "s_add_u32 m0, s61, 45056\n\t"
  "s_nop 0\n\t"
  "buffer_load_dwordx4 v12, s[56:59], s62 offen lds\n\t"
  "s_add_u32 s62, s62, s60\n\t"
  "v_add_u32 v20, 61440, v16\n\t"
  "ds_read_b64_tr_b16 v[24:25], v20\n\t"
  "ds_read_b64_tr_b16 v[26:27], v20 offset:5120\n\t"
  "v_add_u32 v20, 71680, v16\n\t"
  "ds_read_b64_tr_b16 v[28:29], v20\n\t"
  "ds_read_b64_tr_b16 v[30:31], v20 offset:5120\n\t"
  "v_add_u32 v20, 61440, v14\n\t"
  "ds_read_b64_tr_b16 a[60:61], v20\n\t"
  "ds_read_b64_tr_b16 a[62:63], v20 offset:5120\n\t"
  "v_add_u32 v20, 71680, v14\n\t"
  "ds_read_b64_tr_b16 a[64:65], v20\n\t"
  "ds_read_b64_tr_b16 a[66:67], v20 offset:5120\n\t"
  "v_add_u32 v20, 61440, v15\n\t"
  "ds_read_b64_tr_b16 a[68:69], v20\n\t"
  "ds_read_b64_tr_b16 a[70:71], v20 offset:5120\n\t"
  "v_add_u32 v20, 71680, v15\n\t"
  "ds_read_b64_tr_b16 a[72:73], v20\n\t"
  "ds_read_b64_tr_b16 a[74:75], v20 offset:5120\n\t"
  "s_waitcnt lgkmcnt(8)\n\t"
  "s_cmp_lg_u32 s63, 0\n\t"
  "s_cbranch_scc1 71f\n\t"
  "v_cndmask_b32_dpp v32, v28, v24, vcc quad_perm:[0,1,0,1] row_mask:0xf bank_mask:0xf\n\t"
  "v_cndmask_b32_dpp v33, v29, v25, vcc quad_perm:[0,1,0,1] row_mask:0xf bank_mask:0xf\n\t"
  "v_cndmask_b32_dpp v34, v30, v26, vcc quad_perm:[0,1,0,1] row_mask:0xf bank_mask:0xf\n\t"
  "v_cndmask_b32_dpp v35, v31, v27, vcc quad_perm:[0,1,0,1] row_mask:0xf bank_mask:0xf\n\t"
  "s_branch 72f\n\t"
  "71:\n\t"
  "v_cndmask_b32_dpp v32, v24, v28, vcc quad_perm:[2,3,2,3] row_mask:0xf bank_mask:0xf\n\t"
  "v_cndmask_b32_dpp v33, v25, v29, vcc quad_perm:[2,3,2,3] row_mask:0xf bank_mask:0xf\n\t"
  "v_cndmask_b32_dpp v34, v26, v30, vcc quad_perm:[2,3,2,3] row_mask:0xf bank_mask:0xf\n\t"
  "v_cndmask_b32_dpp v35, v27, v31, vcc quad_perm:[2,3,2,3] row_mask:0xf bank_mask:0xf\n\t"
  "72:\n\t"
  "v_accvgpr_write_b32 a76, v32\n\t"
  "v_accvgpr_write_b32 a77, v33\n\t"
  "v_accvgpr_write_b32 a78, v34\n\t"
  "v_accvgpr_write_b32 a79, v35\n\t"
  "s_waitcnt vmcnt(30) lgkmcnt(0)\n\t"
  "s_barrier\n\t"
  "s_add_u32 m0, s61, 61440\n\t"
  "s_nop 0\n\t"
  "buffer_load_dwordx4 v8, s[56:59], s62 offen lds\n\t"
  "s_add_u32 m0, s61, 62464\n\t"
  "s_nop 0\n\t"
  "buffer_load_dwordx4 v9, s[56:59], s62 offen lds\n\t"
  "s_add_u32 m0, s61, 63488\n\t"
  "s_nop 0\n\t"
  "buffer_load_dwordx4 v10, s[56:59], s62 offen lds\n\t"
  "s_add_u32 m0, s61, 64512\n\t"
  "s_nop 0\n\t"
  "buffer_load_dwordx4 v11, s[56:59], s62 offen lds\n\t"
  "s_add_u32 m0, s61, 65536\n\t"
  "s_nop 0\n\t"
  "buffer_load_dwordx4 v12, s[56:59], s62 offen lds\n\t"
  "s_add_u32 s62, s62, s60\n\t"
  "v_add_u32 v20, 81920, v16\n\t"
  "ds_read_b64_tr_b16 v[24:25], v20\n\t"
  "ds_read_b64_tr_b16 v[26:27], v20 offset:5120\n\t"
  "v_add_u32 v20, 92160, v16\n\t"
  "ds_read_b64_tr_b16 v[28:29], v20\n\t"
  "ds_read_b64_tr_b16 v[30:31], v20 offset:5120\n\t"
  "v_add_u32 v20, 81920, v14\n\t"
  "ds_read_b64_tr_b16 a[80:81], v20\n\t"
  "ds_read_b64_tr_b16 a[82:83], v20 offset:5120\n\t"
  "v_add_u32 v20, 92160, v14\n\t"
  "ds_read_b64_tr_b16 a[84:85], v20\n\t"
  "ds_read_b64_tr_b16 a[86:87], v20 offset:5120\n\t"
  "v_add_u32 v20, 81920, v15\n\t"
  "ds_read_b64_tr_b16 a[88:89], v20\n\t"
  "ds_read_b64_tr_b16 a[90:91], v20 offset:5120\n\t"
  "v_add_u32 v20, 92160, v15\n\t"
  "ds_read_b64_tr_b16 a[92:93], v20\n\t"
  "ds_read_b64_tr_b16 a[94:95], v20 offset:5120\n\t"
  "s_waitcnt lgkmcnt(8)\n\t"
  "s_cmp_lg_u32 s63, 0\n\t"
  "s_cbranch_scc1 71f\n\t"
  "v_cndmask_b32_dpp v32, v28, v24, vcc quad_perm:[0,1,0,1] row_mask:0xf bank_mask:0xf\n\t"
  "v_cndmask_b32_dpp v33, v29, v25, vcc quad_perm:[0,1,0,1] row_mask:0xf bank_mask:0xf\n\t"
  "v_cndmask_b32_dpp v34, v30, v26, vcc quad_perm:[0,1,0,1] row_mask:0xf bank_mask:0xf\n\t"
  "v_cndmask_b32_dpp v35, v31, v27, vcc quad_perm:[0,1,0,1] row_mask:0xf bank_mask:0xf\n\t"
  "s_branch 72f\n\t"
  "71:\n\t"
  "v_cndmask_b32_dpp v32, v24, v28, vcc quad_perm:[2,3,2,3] row_mask:0xf bank_mask:0xf\n\t"
  "v_cndmask_b32_dpp v33, v25, v29, vcc quad_perm:[2,3,2,3] row_mask:0xf bank_mask:0xf\n\t"
  "v_cndmask_b32_dpp v34, v26, v30, vcc quad_perm:[2,3,2,3] row_mask:0xf bank_mask:0xf\n\t"
  "v_cndmask_b32_dpp v35, v27, v31, vcc quad_perm:[2,3,2,3] row_mask:0xf bank_mask:0xf\n\t"
  "72:\n\t"
  "v_accvgpr_write_b32 a96, v32\n\t"
  "v_accvgpr_write_b32 a97, v33\n\t"
  "v_accvgpr_write_b32 a98, v34\n\t"
  "v_accvgpr_write_b32 a99, v35\n\t"
  "s_waitcnt vmcnt(30) lgkmcnt(0)\n\t"
  "s_barrier\n\t"
  "s_add_u32 m0, s61, 81920\n\t"
  "s_nop 0\n\t"
  "buffer_load_dwordx4 v8, s[56:59], s62 offen lds\n\t"
  "s_add_u32 m0, s61, 82944\n\t"
  "s_nop 0\n\t"
  "buffer_load_dwordx4 v9, s[56:59], s62 offen lds\n\t"
  "s_add_u32 m0, s61, 83968\n\t"
  "s_nop 0\n\t"
  "buffer_load_dwordx4 v10, s[56:59], s62 offen lds\n\t"
  "s_add_u32 m0, s61, 84992\n\t"
  "s_nop 0\n\t"
  "buffer_load_dwordx4 v11, s[56:59], s62 offen lds\n\t"
  "s_add_u32 m0, s61, 86016\n\t"
  "s_nop 0\n\t"
  "buffer_load_dwordx4 v12, s[56:59], s62 offen lds\n\t"
  "s_add_u32 s62, s62, s60\n\t"
  "v_add_u32 v20, 102400, v16\n\t"
  "ds_read_b64_tr_b16 v[24:25], v20\n\t"
  "ds_read_b64_tr_b16 v[26:27], v20 offset:5120\n\t"
  "v_add_u32 v20, 112640, v16\n\t"
  "ds_read_b64_tr_b16 v[28:29], v20\n\t"
  "ds_read_b64_tr_b16 v[30:31], v20 offset:5120\n\t"
  "v_add_u32 v20, 102400, v14\n\t"
  "ds_read_b64_tr_b16 a[100:101], v20\n\t"
  "ds_read_b64_tr_b16 a[102:103], v20 offset:5120\n\t"
  "v_add_u32 v20, 112640, v14\n\t"
  "ds_read_b64_tr_b16 a[104:105], v20\n\t"
  "ds_read_b64_tr_b16 a[106:107], v20 offset:5120\n\t"
  "v_add_u32 v20, 102400, v15\n\t"
  "ds_read_b64_tr_b16 a[108:109], v20\n\t"
  "ds_read_b64_tr_b16 a[110:111], v20 offset:5120\n\t"
  "v_add_u32 v20, 112640, v15\n\t"
  "ds_read_b64_tr_b16 a[112:113], v20\n\t"
  "ds_read_b64_tr_b16 a[114:115], v20 offset:5120\n\t"
  "s_waitcnt lgkmcnt(8)\n\t"
  "s_cmp_lg_u32 s63, 0\n\t"
  "s_cbranch_scc1 71f\n\t"
  "v_cndmask_b32_dpp v32, v28, v24, vcc quad_perm:[0,1,0,1] row_mask:0xf bank_mask:0xf\n\t"
  "v_cndmask_b32_dpp v33, v29, v25, vcc quad_perm:[0,1,0,1] row_mask:0xf bank_mask:0xf\n\t"
  "v_cndmask_b32_dpp v34, v30, v26, vcc quad_perm:[0,1,0,1] row_mask:0xf bank_mask:0xf\n\t"
  "v_cndmask_b32_dpp v35, v31, v27, vcc quad_perm:[0,1,0,1] row_mask:0xf bank_mask:0xf\n\t"
  "s_branch 72f\n\t"
  "71:\n\t"
  "v_cndmask_b32_dpp v32, v24, v28, vcc quad_perm:[2,3,2,3] row_mask:0xf bank_mask:0xf\n\t"
  "v_cndmask_b32_dpp v33, v25, v29, vcc quad_perm:[2,3,2,3] row_mask:0xf bank_mask:0xf\n\t"
  "v_cndmask_b32_dpp v34, v26, v30, vcc quad_perm:[2,3,2,3] row_mask:0xf bank_mask:0xf\n\t"
  "v_cndmask_b32_dpp v35, v27, v31, vcc quad_perm:[2,3,2,3] row_mask:0xf bank_mask:0xf\n\t"
  "72:\n\t"
  "v_accvgpr_write_b32 a116, v32\n\t"
  "v_accvgpr_write_b32 a117, v33\n\t"
  "v_accvgpr_write_b32 a118, v34\n\t"
  "v_accvgpr_write_b32 a119, v35\n\t"
  "s_waitcnt vmcnt(30) lgkmcnt(0)\n\t"
  "s_barrier\n\t"
  "s_add_u32 m0, s61, 102400\n\t"
  "s_nop 0\n\t"
  "buffer_load_dwordx4 v8, s[56:59], s62 offen lds\n\t"
  "s_add_u32 m0, s61, 103424\n\t"
  "s_nop 0\n\t"
  "buffer_load_dwordx4 v9, s[56:59], s62 offen lds\n\t"
  "s_add_u32 m0, s61, 104448\n\t"
  "s_nop 0\n\t"
  "buffer_load_dwordx4 v10, s[56:59], s62 offen lds\n\t"
  "s_add_u32 m0, s61, 105472\n\t"
  "s_nop 0\n\t"
  "buffer_load_dwordx4 v11, s[56:59], s62 offen lds\n\t"
  "s_add_u32 m0, s61, 106496\n\t"
  "s_nop 0\n\t"
  "buffer_load_dwordx4 v12, s[56:59], s62 offen lds\n\t"
  "s_add_u32 s62, s62, s60\n\t"
  "v_add_u32 v20, 122880, v16\n\t"
  "ds_read_b64_tr_b16 v[24:25], v20\n\t"
  "ds_read_b64_tr_b16 v[26:27], v20 offset:5120\n\t"
  "v_add_u32 v20, 133120, v16\n\t"
  "ds_read_b64_tr_b16 v[28:29], v20\n\t"
  "ds_read_b64_tr_b16 v[30:31], v20 offset:5120\n\t"
  "v_add_u32 v20, 122880, v14\n\t"
  "ds_read_b64_tr_b16 a[120:121], v20\n\t"
  "ds_read_b64_tr_b16 a[122:123], v20 offset:5120\n\t"
  "v_add_u32 v20, 133120, v14\n\t"
  "ds_read_b64_tr_b16 a[124:125], v20\n\t"
  "ds_read_b64_tr_b16 a[126:127], v20 offset:5120\n\t"
  "v_add_u32 v20, 122880, v15\n\t"
  "ds_read_b64_tr_b16 a[128:129], v20\n\t"
  "ds_read_b64_tr_b16 a[130:131], v20 offset:5120\n\t"
  "v_add_u32 v20, 133120, v15\n\t"
  "ds_read_b64_tr_b16 a[132:133], v20\n\t"
  "ds_read_b64_tr_b16 a[134:135], v20 offset:5120\n\t"
  "s_waitcnt lgkmcnt(8)\n\t"
  "s_cmp_lg_u32 s63, 0\n\t"
  "s_cbranch_scc1 71f\n\t"
  "v_cndmask_b32_dpp v32, v28, v24, vcc quad_perm:[0,1,0,1] row_mask:0xf bank_mask:0xf\n\t"
  "v_cndmask_b32_dpp v33, v29, v25, vcc quad_perm:[0,1,0,1] row_mask:0xf bank_mask:0xf\n\t"
  "v_cndmask_b32_dpp v34, v30, v26, vcc quad_perm:[0,1,0,1] row_mask:0xf bank_mask:0xf\n\t"
  "v_cndmask_b32_dpp v35, v31, v27, vcc quad_perm:[0,1,0,1] row_mask:0xf bank_mask:0xf\n\t"
  "s_branch 72f\n\t"
  "71:\n\t"
  "v_cndmask_b32_dpp v32, v24, v28, vcc quad_perm:[2,3,2,3] row_mask:0xf bank_mask:0xf\n\t"
  "v_cndmask_b32_dpp v33, v25, v29, vcc quad_perm:[2,3,2,3] row_mask:0xf bank_mask:0xf\n\t"
  "v_cndmask_b32_dpp v34, v26, v30, vcc quad_perm:[2,3,2,3] row_mask:0xf bank_mask:0xf\n\t"
  "v_cndmask_b32_dpp v35, v27, v31, vcc quad_perm:[2,3,2,3] row_mask:0xf bank_mask:0xf\n\t"
  "72:\n\t"
  "v_accvgpr_write_b32 a136, v32\n\t"
  "v_accvgpr_write_b32 a137, v33\n\t"
  "v_accvgpr_write_b32 a138, v34\n\t"
  "v_accvgpr_write_b32 a139, v35\n\t"
  "s_waitcnt vmcnt(30) lgkmcnt(0)\n\t"
  "s_barrier\n\t"
  "s_add_u32 m0, s61, 122880\n\t"
  "s_nop 0\n\t"
  "buffer_load_dwordx4 v8, s[56:59], s62 offen lds\n\t"
  "s_add_u32 m0, s61, 123904\n\t"
  "s_nop 0\n\t"
  "buffer_load_dwordx4 v9, s[56:59], s62 offen lds\n\t"
  "s_add_u32 m0, s61, 124928\n\t"
  "s_nop 0\n\t"
  "buffer_load_dwordx4 v10, s[56:59], s62 offen lds\n\t"
  "s_add_u32 m0, s61, 125952\n\t"
  "s_nop 0\n\t"
  "buffer_load_dwordx4 v11, s[56:59], s62 offen lds\n\t"
  "s_add_u32 m0, s61, 126976\n\t"
  "s_nop 0\n\t"
  "buffer_load_dwordx4 v12, s[56:59], s62 offen lds\n\t"
  "s_add_u32 s62, s62, s60\n\t"
  "v_add_u32 v20, 143360, v16\n\t"
  "ds_read_b64_tr_b16 v[24:25], v20\n\t"
  "ds_read_b64_tr_b16 v[26:27], v20 offset:5120\n\t"
  "v_add_u32 v20, 153600, v16\n\t"
  "ds_read_b64_tr_b16 v[28:29], v20\n\t"
  "ds_read_b64_tr_b16 v[30:31], v20 offset:5120\n\t"
  "v_add_u32 v20, 143360, v14\n\t"
  "ds_read_b64_tr_b16 a[140:141], v20\n\t"
  "ds_read_b64_tr_b16 a[142:143], v20 offset:5120\n\t"
  "v_add_u32 v20, 153600, v14\n\t"
  "ds_read_b64_tr_b16 a[144:145], v20\n\t"
  "ds_read_b64_tr_b16 a[146:147], v20 offset:5120\n\t"
  "v_add_u32 v20, 143360, v15\n\t"
  "ds_read_b64_tr_b16 a[148:149], v20\n\t"
  "ds_read_b64_tr_b16 a[150:151], v20 offset:5120\n\t"
  "v_add_u32 v20, 153600, v15\n\t"
  "ds_read_b64_tr_b16 a[152:153], v20\n\t"
  "ds_read_b64_tr_b16 a[154:155], v20 offset:5120\n\t"
  "s_waitcnt lgkmcnt(8)\n\t"
  "s_cmp_lg_u32 s63, 0\n\t"
  "s_cbranch_scc1 71f\n\t"
  "v_cndmask_b32_dpp v32, v28, v24, vcc quad_perm:[0,1,0,1] row_mask:0xf bank_mask:0xf\n\t"
  "v_cndmask_b32_dpp v33, v29, v25, vcc quad_perm:[0,1,0,1] row_mask:0xf bank_mask:0xf\n\t"
  "v_cndmask_b32_dpp v34, v30, v26, vcc quad_perm:[0,1,0,1] row_mask:0xf bank_mask:0xf\n\t"
  "v_cndmask_b32_dpp v35, v31, v27, vcc quad_perm:[0,1,0,1] row_mask:0xf bank_mask:0xf\n\t"
  "s_branch 72f\n\t"
  "71:\n\t"
  "v_cndmask_b32_dpp v32, v24, v28, vcc quad_perm:[2,3,2,3] row_mask:0xf bank_mask:0xf\n\t"
  "v_cndmask_b32_dpp v33, v25, v29, vcc quad_perm:[2,3,2,3] row_mask:0xf bank_mask:0xf\n\t"
  "v_cndmask_b32_dpp v34, v26, v30, vcc quad_perm:[2,3,2,3] row_mask:0xf bank_mask:0xf\n\t"
  "v_cndmask_b32_dpp v35, v27, v31, vcc quad_perm:[2,3,2,3] row_mask:0xf bank_mask:0xf\n\t"
  "72:\n\t"
  "v_accvgpr_write_b32 a156, v32\n\t"
  "v_accvgpr_write_b32 a157, v33\n\t"
  "v_accvgpr_write_b32 a158, v34\n\t"
  "v_accvgpr_write_b32 a159, v35\n\t"
  "s_waitcnt vmcnt(30) lgkmcnt(0)\n\t"
  "s_barrier\n\t"
  "s_add_u32 m0, s61, 143360\n\t"
  "s_nop 0\n\t"
  "buffer_load_dwordx4 v8, s[56:59], s62 offen lds\n\t"
  "s_add_u32 m0, s61, 144384\n\t"
  "s_nop 0\n\t"
  "buffer_load_dwordx4 v9, s[56:59], s62 offen lds\n\t"
  "s_add_u32 m0, s61, 145408\n\t"
  "s_nop 0\n\t"
  "buffer_load_dwordx4 v10, s[56:59], s62 offen lds\n\t"
  "s_add_u32 m0, s61, 146432\n\t"
  "s_nop 0\n\t"
  "buffer_load_dwordx4 v11, s[56:59], s62 offen lds\n\t"
  "s_add_u32 m0, s61, 147456\n\t"
  "s_nop 0\n\t"
  "buffer_load_dwordx4 v12, s[56:59], s62 offen lds\n\t"
  "s_add_u32 s62, s62, s60\n\t"
  "v_add_u32 v20, 0, v16\n\t"
  "ds_read_b64_tr_b16 v[24:25], v20\n\t"
  "ds_read_b64_tr_b16 v[26:27], v20 offset:5120\n\t"
  "v_add_u32 v20, 10240, v16\n\t"
  "ds_read_b64_tr_b16 v[28:29], v20\n\t"
  "ds_read_b64_tr_b16 v[30:31], v20 offset:5120\n\t"
  "v_add_u32 v20, 0, v14\n\t"
  "ds_read_b64_tr_b16 a[160:161], v20\n\t"
  "ds_read_b64_tr_b16 a[162:163], v20 offset:5120\n\t"
  "v_add_u32 v20, 10240, v14\n\t"
  "ds_read_b64_tr_b16 a[164:165], v20\n\t"
  "ds_read_b64_tr_b16 a[166:167], v20 offset:5120\n\t"
  "v_add_u32 v20, 0, v15\n\t"
  "ds_read_b64_tr_b16 a[168:169], v20\n\t"
  "ds_read_b64_tr_b16 a[170:171], v20 offset:5120\n\t"
  "v_add_u32 v20, 10240, v15\n\t"
  "ds_read_b64_tr_b16 a[172:173], v20\n\t"
  "ds_read_b64_tr_b16 a[174:175], v20 offset:5120\n\t"
  "s_waitcnt lgkmcnt(8)\n\t"
  "s_cmp_lg_u32 s63, 0\n\t"
  "s_cbranch_scc1 71f\n\t"
  "v_cndmask_b32_dpp v32, v28, v24, vcc quad_perm:[0,1,0,1] row_mask:0xf bank_mask:0xf\n\t"
  "v_cndmask_b32_dpp v33, v29, v25, vcc quad_perm:[0,1,0,1] row_mask:0xf bank_mask:0xf\n\t"
  "v_cndmask_b32_dpp v34, v30, v26, vcc quad_perm:[0,1,0,1] row_mask:0xf bank_mask:0xf\n\t"
  "v_cndmask_b32_dpp v35, v31, v27, vcc quad_perm:[0,1,0,1] row_mask:0xf bank_mask:0xf\n\t"
  "s_branch 72f\n\t"
  "71:\n\t"
  "v_cndmask_b32_dpp v32, v24, v28, vcc quad_perm:[2,3,2,3] row_mask:0xf bank_mask:0xf\n\t"
  "v_cndmask_b32_dpp v33, v25, v29, vcc quad_perm:[2,3,2,3] row_mask:0xf bank_mask:0xf\n\t"
  "v_cndmask_b32_dpp v34, v26, v30, vcc quad_perm:[2,3,2,3] row_mask:0xf bank_mask:0xf\n\t"
  "v_cndmask_b32_dpp v35, v27, v31, vcc quad_perm:[2,3,2,3] row_mask:0xf bank_mask:0xf\n\t"
  "72:\n\t"
  "v_accvgpr_write_b32 a176, v32\n\t"
  "v_accvgpr_write_b32 a177, v33\n\t"
  "v_accvgpr_write_b32 a178, v34\n\t"
  "v_accvgpr_write_b32 a179, v35\n\t"
  "s_waitcnt vmcnt(30) lgkmcnt(0)\n\t"
  "s_barrier\n\t"
  "s_add_u32 m0, s61, 0\n\t"
  "s_nop 0\n\t"
  "buffer_load_dwordx4 v8, s[56:59], s62 offen lds\n\t"
  "s_add_u32 m0, s61, 1024\n\t"
  "s_nop 0\n\t"
  "buffer_load_dwordx4 v9, s[56:59], s62 offen lds\n\t"
  "s_add_u32 m0, s61, 2048\n\t"
  "s_nop 0\n\t"
  "buffer_load_dwordx4 v10, s[56:59], s62 offen lds\n\t"
  "s_add_u32 m0, s61, 3072\n\t"
  "s_nop 0\n\t"
  "buffer_load_dwordx4 v11, s[56:59], s62 offen lds\n\t"
  "s_add_u32 m0, s61, 4096\n\t"
  "s_nop 0\n\t"
  "buffer_load_dwordx4 v12, s[56:59], s62 offen lds\n\t"
  "s_add_u32 s62, s62, s60\n\t"
  "v_add_u32 v20, 20480, v16\n\t"
  "ds_read_b64_tr_b16 v[24:25], v20\n\t"
  "ds_read_b64_tr_b16 v[26:27], v20 offset:5120\n\t"
  "v_add_u32 v20, 30720, v16\n\t"
  "ds_read_b64_tr_b16 v[28:29], v20\n\t"
  "ds_read_b64_tr_b16 v[30:31], v20 offset:5120\n\t"
  "v_add_u32 v20, 20480, v14\n\t"
  "ds_read_b64_tr_b16 a[180:181], v20\n\t"
  "ds_read_b64_tr_b16 a[182:183], v20 offset:5120\n\t"
  "v_add_u32 v20, 30720, v14\n\t"
  "ds_read_b64_tr_b16 a[184:185], v20\n\t"
  "ds_read_b64_tr_b16 a[186:187], v20 offset:5120\n\t"
  "v_add_u32 v20, 20480, v15\n\t"
  "ds_read_b64_tr_b16 a[188:189], v20\n\t"
  "ds_read_b64_tr_b16 a[190:191], v20 offset:5120\n\t"
  "v_add_u32 v20, 30720, v15\n\t"
  "ds_read_b64_tr_b16 a[192:193], v20\n\t"
  "ds_read_b64_tr_b16 a[194:195], v20 offset:5120\n\t"
  "s_waitcnt lgkmcnt(8)\n\t"
  "s_cmp_lg_u32 s63, 0\n\t"
  "s_cbranch_scc1 71f\n\t"
  "v_cndmask_b32_dpp v32, v28, v24, vcc quad_perm:[0,1,0,1] row_mask:0xf bank_mask:0xf\n\t"
  "v_cndmask_b32_dpp v33, v29, v25, vcc quad_perm:[0,1,0,1] row_mask:0xf bank_mask:0xf\n\t"
  "v_cndmask_b32_dpp v34, v30, v26, vcc quad_perm:[0,1,0,1] row_mask:0xf bank_mask:0xf\n\t"
  "v_cndmask_b32_dpp v35, v31, v27, vcc quad_perm:[0,1,0,1] row_mask:0xf bank_mask:0xf\n\t"
  "s_branch 72f\n\t"
  "71:\n\t"
  "v_cndmask_b32_dpp v32, v24, v28, vcc quad_perm:[2,3,2,3] row_mask:0xf bank_mask:0xf\n\t"
  "v_cndmask_b32_dpp v33, v25, v29, vcc quad_perm:[2,3,2,3] row_mask:0xf bank_mask:0xf\n\t"
  "v_cndmask_b32_dpp v34, v26, v30, vcc quad_perm:[2,3,2,3] row_mask:0xf bank_mask:0xf\n\t"
  "v_cndmask_b32_dpp v35, v27, v31, vcc quad_perm:[2,3,2,3] row_mask:0xf bank_mask:0xf\n\t"
  "72:\n\t"
  "v_accvgpr_write_b32 a196, v32\n\t"
  "v_accvgpr_write_b32 a197, v33\n\t"
  "v_accvgpr_write_b32 a198, v34\n\t"
  "v_accvgpr_write_b32 a199, v35\n\t"
  "s_waitcnt vmcnt(30) lgkmcnt(0)\n\t"
  "s_barrier\n\t"
  "s_add_u32 m0, s61, 20480\n\t"
  "s_nop 0\n\t"
  "buffer_load_dwordx4 v8, s[56:59], s62 offen lds\n\t"
  "s_add_u32 m0, s61, 21504\n\t"
  "s_nop 0\n\t"
  "buffer_load_dwordx4 v9, s[56:59], s62 offen lds\n\t"
  "s_add_u32 m0, s61, 22528\n\t"
  "s_nop 0\n\t"
  "buffer_load_dwordx4 v10, s[56:59], s62 offen lds\n\t"
  "s_add_u32 m0, s61, 23552\n\t"
  "s_nop 0\n\t"
  "buffer_load_dwordx4 v11, s[56:59], s62 offen lds\n\t"
  "s_add_u32 m0, s61, 24576\n\t"
  "s_nop 0\n\t"
  "buffer_load_dwordx4 v12, s[56:59], s62 offen lds\n\t"
  "s_add_u32 s62, s62, s60\n\t"
  "v_add_u32 v20, 40960, v16\n\t"
  "ds_read_b64_tr_b16 v[24:25], v20\n\t"
  "ds_read_b64_tr_b16 v[26:27], v20 offset:5120\n\t"
  "v_add_u32 v20, 51200, v16\n\t"
  "ds_read_b64_tr_b16 v[28:29], v20\n\t"
  "ds_read_b64_tr_b16 v[30:31], v20 offset:5120\n\t"
  "v_add_u32 v20, 40960, v14\n\t"
  "ds_read_b64_tr_b16 a[200:201], v20\n\t"
  "ds_read_b64_tr_b16 a[202:203], v20 offset:5120\n\t"
  "v_add_u32 v20, 51200, v14\n\t"
  "ds_read_b64_tr_b16 a[204:205], v20\n\t"
  "ds_read_b64_tr_b16 a[206:207], v20 offset:5120\n\t"
  "v_add_u32 v20, 40960, v15\n\t"
  "ds_read_b64_tr_b16 a[208:209], v20\n\t"
  "ds_read_b64_tr_b16 a[210:211], v20 offset:5120\n\t"
  "v_add_u32 v20, 51200, v15\n\t"
  "ds_read_b64_tr_b16 a[212:213], v20\n\t"
  "ds_read_b64_tr_b16 a[214:215], v20 offset:5120\n\t"
  "s_waitcnt lgkmcnt(8)\n\t"
  "s_cmp_lg_u32 s63, 0\n\t"
  "s_cbranch_scc1 71f\n\t"
  "v_cndmask_b32_dpp v32, v28, v24, vcc quad_perm:[0,1,0,1] row_mask:0xf bank_mask:0xf\n\t"
  "v_cndmask_b32_dpp v33, v29, v25, vcc quad_perm:[0,1,0,1] row_mask:0xf bank_mask:0xf\n\t"
  "v_cndmask_b32_dpp v34, v30, v26, vcc quad_perm:[0,1,0,1] row_mask:0xf bank_mask:0xf\n\t"
  "v_cndmask_b32_dpp v35, v31, v27, vcc quad_perm:[0,1,0,1] row_mask:0xf bank_mask:0xf\n\t"
  "s_branch 72f\n\t"
  "71:\n\t"
  "v_cndmask_b32_dpp v32, v24, v28, vcc quad_perm:[2,3,2,3] row_mask:0xf bank_mask:0xf\n\t"
  "v_cndmask_b32_dpp v33, v25, v29, vcc quad_perm:[2,3,2,3] row_mask:0xf bank_mask:0xf\n\t"
  "v_cndmask_b32_dpp v34, v26, v30, vcc quad_perm:[2,3,2,3] row_mask:0xf bank_mask:0xf\n\t"
  "v_cndmask_b32_dpp v35, v27, v31, vcc quad_perm:[2,3,2,3] row_mask:0xf bank_mask:0xf\n\t"
  "72:\n\t"
  "v_accvgpr_write_b32 a216, v32\n\t"
  "v_accvgpr_write_b32 a217, v33\n\t"
  "v_accvgpr_write_b32 a218, v34\n\t"
  "v_accvgpr_write_b32 a219, v35\n\t"
  "s_waitcnt vmcnt(30) lgkmcnt(0)\n\t"
  "s_barrier\n\t"
  "v_add_u32 v20, 61440, v16\n\t"
  "ds_read_b64_tr_b16 v[24:25], v20\n\t"
  "ds_read_b64_tr_b16 v[26:27], v20 offset:5120\n\t"
  "v_add_u32 v20, 71680, v16\n\t"
  "ds_read_b64_tr_b16 v[28:29], v20\n\t"
  "ds_read_b64_tr_b16 v[30:31], v20 offset:5120\n\t"
  "v_add_u32 v20, 61440, v14\n\t"
  "ds_read_b64_tr_b16 a[220:221], v20\n\t"
  "ds_read_b64_tr_b16 a[222:223], v20 offset:5120\n\t"
  "v_add_u32 v20, 71680, v14\n\t"
  "ds_read_b64_tr_b16 a[224:225], v20\n\t"
  "ds_read_b64_tr_b16 a[226:227], v20 offset:5120\n\t"
  "v_add_u32 v20, 61440, v15\n\t"
  "ds_read_b64_tr_b16 a[228:229], v20\n\t"
  "ds_read_b64_tr_b16 a[230:231], v20 offset:5120\n\t"
  "v_add_u32 v20, 71680, v15\n\t"
  "ds_read_b64_tr_b16 a[232:233], v20\n\t"
  "ds_read_b64_tr_b16 a[234:235], v20 offset:5120\n\t"
  "s_waitcnt lgkmcnt(8)\n\t"
  "s_cmp_lg_u32 s63, 0\n\t"
  "s_cbranch_scc1 71f\n\t"
  "v_cndmask_b32_dpp v32, v28, v24, vcc quad_perm:[0,1,0,1] row_mask:0xf bank_mask:0xf\n\t"
  "v_cndmask_b32_dpp v33, v29, v25, vcc quad_perm:[0,1,0,1] row_mask:0xf bank_mask:0xf\n\t"
  "v_cndmask_b32_dpp v34, v30, v26, vcc quad_perm:[0,1,0,1] row_mask:0xf bank_mask:0xf\n\t"
  "v_cndmask_b32_dpp v35, v31, v27, vcc quad_perm:[0,1,0,1] row_mask:0xf bank_mask:0xf\n\t"
  "s_branch 72f\n\t"
  "71:\n\t"
  "v_cndmask_b32_dpp v32, v24, v28, vcc quad_perm:[2,3,2,3] row_mask:0xf bank_mask:0xf\n\t"
  "v_cndmask_b32_dpp v33, v25, v29, vcc quad_perm:[2,3,2,3] row_mask:0xf bank_mask:0xf\n\t"
  "v_cndmask_b32_dpp v34, v26, v30, vcc quad_perm:[2,3,2,3] row_mask:0xf bank_mask:0xf\n\t"
  "v_cndmask_b32_dpp v35, v27, v31, vcc quad_perm:[2,3,2,3] row_mask:0xf bank_mask:0xf\n\t"
  "72:\n\t"
  "v_accvgpr_write_b32 a236, v32\n\t"
  "v_accvgpr_write_b32 a237, v33\n\t"
  "v_accvgpr_write_b32 a238, v34\n\t"
  "v_accvgpr_write_b32 a239, v35\n\t"
  "s_waitcnt vmcnt(25) lgkmcnt(0)\n\t"
  "s_barrier\n\t"
  "v_add_u32 v20, 81920, v16\n\t"
  "ds_read_b64_tr_b16 v[24:25], v20\n\t"
  "ds_read_b64_tr_b16 v[26:27], v20 offset:5120\n\t"
  "v_add_u32 v20, 92160, v16\n\t"
  "ds_read_b64_tr_b16 v[28:29], v20\n\t"
  "ds_read_b64_tr_b16 v[30:31], v20 offset:5120\n\t"
  "v_add_u32 v20, 81920, v14\n\t"
  "ds_read_b64_tr_b16 a[240:241], v20\n\t"
  "ds_read_b64_tr_b16 a[242:243], v20 offset:5120\n\t"
  "v_add_u32 v20, 92160, v14\n\t"
  "ds_read_b64_tr_b16 a[244:245], v20\n\t"
  "ds_read_b64_tr_b16 a[246:247], v20 offset:5120\n\t"
  "v_add_u32 v20, 81920, v15\n\t"
  "ds_read_b64_tr_b16 a[248:249], v20\n\t"
  "ds_read_b64_tr_b16 a[250:251], v20 offset:5120\n\t"
  "v_add_u32 v20, 92160, v15\n\t"
  "ds_read_b64_tr_b16 a[252:253], v20\n\t"
  "ds_read_b64_tr_b16 a[254:255], v20 offset:5120\n\t"
  "s_waitcnt lgkmcnt(8)\n\t"
  "s_cmp_lg_u32 s63, 0\n\t"
  "s_cbranch_scc1 71f\n\t"
  "v_cndmask_b32_dpp v32, v28, v24, vcc quad_perm:[0,1,0,1] row_mask:0xf bank_mask:0xf\n\t"
  "v_cndmask_b32_dpp v33, v29, v25, vcc quad_perm:[0,1,0,1] row_mask:0xf bank_mask:0xf\n\t"
  "v_cndmask_b32_dpp v34, v30, v26, vcc quad_perm:[0,1,0,1] row_mask:0xf bank_mask:0xf\n\t"
  "v_cndmask_b32_dpp v35, v31, v27, vcc quad_perm:[0,1,0,1] row_mask:0xf bank_mask:0xf\n\t"
  "s_branch 72f\n\t"
  "71:\n\t"
  "v_cndmask_b32_dpp v32, v24, v28, vcc quad_perm:[2,3,2,3] row_mask:0xf bank_mask:0xf\n\t"
  "v_cndmask_b32_dpp v33, v25, v29, vcc quad_perm:[2,3,2,3] row_mask:0xf bank_mask:0xf\n\t"
  "v_cndmask_b32_dpp v34, v26, v30, vcc quad_perm:[2,3,2,3] row_mask:0xf bank_mask:0xf\n\t"
  "v_cndmask_b32_dpp v35, v27, v31, vcc quad_perm:[2,3,2,3] row_mask:0xf bank_mask:0xf\n\t"
  "72:\n\t"
  "v_mov_b32 v224, v32\n\t"
  "v_mov_b32 v225, v33\n\t"
  "v_mov_b32 v226, v34\n\t"
  "v_mov_b32 v227, v35\n\t"
  "s_waitcnt vmcnt(20) lgkmcnt(0)\n\t"
  "s_barrier\n\t"
  "v_add_u32 v20, 102400, v16\n\t"
  "ds_read_b64_tr_b16 v[24:25], v20\n\t"
  "ds_read_b64_tr_b16 v[26:27], v20 offset:5120\n\t"
  "v_add_u32 v20, 112640, v16\n\t"
  "ds_read_b64_tr_b16 v[28:29], v20\n\t"
  "ds_read_b64_tr_b16 v[30:31], v20 offset:5120\n\t"
  "v_add_u32 v20, 102400, v14\n\t"
  "ds_read_b64_tr_b16 v[228:229], v20\n\t"
  "ds_read_b64_tr_b16 v[230:231], v20 offset:5120\n\t"
  "v_add_u32 v20, 112640, v14\n\t"
  "ds_read_b64_tr_b16 v[232:233], v20\n\t"
  "ds_read_b64_tr_b16 v[234:235], v20 offset:5120\n\t"
  "v_add_u32 v20, 102400, v15\n\t"
  "ds_read_b64_tr_b16 v[236:237], v20\n\t"
  "ds_read_b64_tr_b16 v[238:239], v20 offset:5120\n\t"
  "v_add_u32 v20, 112640, v15\n\t"
  "ds_read_b64_tr_b16 v[240:241], v20\n\t"
  "ds_read_b64_tr_b16 v[242:243], v20 offset:5120\n\t"
  "s_waitcnt lgkmcnt(8)\n\t"
  "s_cmp_lg_u32 s63, 0\n\t"
  "s_cbranch_scc1 71f\n\t"
  "v_cndmask_b32_dpp v32, v28, v24, vcc quad_perm:[0,1,0,1] row_mask:0xf bank_mask:0xf\n\t"
  "v_cndmask_b32_dpp v33, v29, v25, vcc quad_perm:[0,1,0,1] row_mask:0xf bank_mask:0xf\n\t"
  "v_cndmask_b32_dpp v34, v30, v26, vcc quad_perm:[0,1,0,1] row_mask:0xf bank_mask:0xf\n\t"
  "v_cndmask_b32_dpp v35, v31, v27, vcc quad_perm:[0,1,0,1] row_mask:0xf bank_mask:0xf\n\t"
  "s_branch 72f\n\t"
  "71:\n\t"
  "v_cndmask_b32_dpp v32, v24, v28, vcc quad_perm:[2,3,2,3] row_mask:0xf bank_mask:0xf\n\t"
  "v_cndmask_b32_dpp v33, v25, v29, vcc quad_perm:[2,3,2,3] row_mask:0xf bank_mask:0xf\n\t"
  "v_cndmask_b32_dpp v34, v26, v30, vcc quad_perm:[2,3,2,3] row_mask:0xf bank_mask:0xf\n\t"
  "v_cndmask_b32_dpp v35, v27, v31, vcc quad_perm:[2,3,2,3] row_mask:0xf bank_mask:0xf\n\t"
  "72:\n\t"
  "v_mov_b32 v244, v32\n\t"
  "v_mov_b32 v245, v33\n\t"
  "v_mov_b32 v246, v34\n\t"
  "v_mov_b32 v247, v35\n\t"
  "s_waitcnt vmcnt(15) lgkmcnt(0)\n\t"
  "s_barrier\n\t"
  "v_add_u32 v20, 122880, v16\n\t"
  "ds_read_b64_tr_b16 v[24:25], v20\n\t"
  "ds_read_b64_tr_b16 v[26:27], v20 offset:5120\n\t"
  "v_add_u32 v20, 133120, v16\n\t"
  "ds_read_b64_tr_b16 v[28:29], v20\n\t"
  "ds_read_b64_tr_b16 v[30:31], v20 offset:5120\n\t"
  "v_add_u32 v20, 122880, v14\n\t"
  "ds_read_b64_tr_b16 v[248:249], v20\n\t"
  "ds_read_b64_tr_b16 v[250:251], v20 offset:5120\n\t"
  "v_add_u32 v20, 133120, v14\n\t"
  "ds_read_b64_tr_b16 v[252:253], v20\n\t"
  "ds_read_b64_tr_b16 v[254:255], v20 offset:5120\n\t"
  "v_add_u32 v20, 122880, v15\n\t"
  "ds_read_b64_tr_b16 v[192:193], v20\n\t"
  "ds_read_b64_tr_b16 v[194:195], v20 offset:5120\n\t"
  "v_add_u32 v20, 133120, v15\n\t"
  "ds_read_b64_tr_b16 v[196:197], v20\n\t"
  "ds_read_b64_tr_b16 v[198:199], v20 offset:5120\n\t"
  "s_waitcnt lgkmcnt(8)\n\t"
  "s_cmp_lg_u32 s63, 0\n\t"
  "s_cbranch_scc1 71f\n\t"
  "v_cndmask_b32_dpp v32, v28, v24, vcc quad_perm:[0,1,0,1] row_mask:0xf bank_mask:0xf\n\t"
  "v_cndmask_b32_dpp v33, v29, v25, vcc quad_perm:[0,1,0,1] row_mask:0xf bank_mask:0xf\n\t"
  "v_cndmask_b32_dpp v34, v30, v26, vcc quad_perm:[0,1,0,1] row_mask:0xf bank_mask:0xf\n\t"
  "v_cndmask_b32_dpp v35, v31, v27, vcc quad_perm:[0,1,0,1] row_mask:0xf bank_mask:0xf\n\t"
  "s_branch 72f\n\t"
  "71:\n\t"
  "v_cndmask_b32_dpp v32, v24, v28, vcc quad_perm:[2,3,2,3] row_mask:0xf bank_mask:0xf\n\t"
  "v_cndmask_b32_dpp v33, v25, v29, vcc quad_perm:[2,3,2,3] row_mask:0xf bank_mask:0xf\n\t"
  "v_cndmask_b32_dpp v34, v26, v30, vcc quad_perm:[2,3,2,3] row_mask:0xf bank_mask:0xf\n\t"
  "v_cndmask_b32_dpp v35, v27, v31, vcc quad_perm:[2,3,2,3] row_mask:0xf bank_mask:0xf\n\t"
  "72:\n\t"
  "v_mov_b32 v200, v32\n\t"
  "v_mov_b32 v201, v33\n\t"
  "v_mov_b32 v202, v34\n\t"
  "v_mov_b32 v203, v35\n\t"
  "s_waitcnt vmcnt(10) lgkmcnt(0)\n\t"
  "s_barrier\n\t"
  "v_add_u32 v20, 143360, v16\n\t"
  "ds_read_b64_tr_b16 v[24:25], v20\n\t"
  "ds_read_b64_tr_b16 v[26:27], v20 offset:5120\n\t"
  "v_add_u32 v20, 153600, v16\n\t"
  "ds_read_b64_tr_b16 v[28:29], v20\n\t"
  "ds_read_b64_tr_b16 v[30:31], v20 offset:5120\n\t"
  "v_add_u32 v20, 143360, v14\n\t"
  "ds_read_b64_tr_b16 v[204:205], v20\n\t"
  "ds_read_b64_tr_b16 v[206:207], v20 offset:5120\n\t"
  "v_add_u32 v20, 153600, v14\n\t"
  "ds_read_b64_tr_b16 v[208:209], v20\n\t"
  "ds_read_b64_tr_b16 v[210:211], v20 offset:5120\n\t"
  "v_add_u32 v20, 143360, v15\n\t"
  "ds_read_b64_tr_b16 v[212:213], v20\n\t"
  "ds_read_b64_tr_b16 v[214:215], v20 offset:5120\n\t"
  "v_add_u32 v20, 153600, v15\n\t"
  "ds_read_b64_tr_b16 v[216:217], v20\n\t"
  "ds_read_b64_tr_b16 v[218:219], v20 offset:5120\n\t"
  "s_waitcnt lgkmcnt(8)\n\t"
  "s_cmp_lg_u32 s63, 0\n\t"
  "s_cbranch_scc1 71f\n\t"
  "v_cndmask_b32_dpp v32, v28, v24, vcc quad_perm:[0,1,0,1] row_mask:0xf bank_mask:0xf\n\t"
  "v_cndmask_b32_dpp v33, v29, v25, vcc quad_perm:[0,1,0,1] row_mask:0xf bank_mask:0xf\n\t"
  "v_cndmask_b32_dpp v34, v30, v26, vcc quad_perm:[0,1,0,1] row_mask:0xf bank_mask:0xf\n\t"
  "v_cndmask_b32_dpp v35, v31, v27, vcc quad_perm:[0,1,0,1] row_mask:0xf bank_mask:0xf\n\t"
  "s_branch 72f\n\t"
  "71:\n\t"
  "v_cndmask_b32_dpp v32, v24, v28, vcc quad_perm:[2,3,2,3] row_mask:0xf bank_mask:0xf\n\t"
  "v_cndmask_b32_dpp v33, v25, v29, vcc quad_perm:[2,3,2,3] row_mask:0xf bank_mask:0xf\n\t"
  "v_cndmask_b32_dpp v34, v26, v30, vcc quad_perm:[2,3,2,3] row_mask:0xf bank_mask:0xf\n\t"
  "v_cndmask_b32_dpp v35, v27, v31, vcc quad_perm:[2,3,2,3] row_mask:0xf bank_mask:0xf\n\t"
  "72:\n\t"
  "v_mov_b32 v220, v32\n\t"
  "v_mov_b32 v221, v33\n\t"
  "v_mov_b32 v222, v34\n\t"
  "v_mov_b32 v223, v35\n\t"
  "s_waitcnt vmcnt(5) lgkmcnt(0)\n\t"
  "s_barrier\n\t"
  "v_add_u32 v20, 0, v16\n\t"
  "ds_read_b64_tr_b16 v[24:25], v20\n\t"
  "ds_read_b64_tr_b16 v[26:27], v20 offset:5120\n\t"
  "v_add_u32 v20, 10240, v16\n\t"
  "ds_read_b64_tr_b16 v[28:29], v20\n\t"
  "ds_read_b64_tr_b16 v[30:31], v20 offset:5120\n\t"
  "v_add_u32 v20, 0, v14\n\t"
  "ds_read_b64_tr_b16 v[160:161], v20\n\t"
  "ds_read_b64_tr_b16 v[162:163], v20 offset:5120\n\t"
  "v_add_u32 v20, 10240, v14\n\t"
  "ds_read_b64_tr_b16 v[164:165], v20\n\t"
  "ds_read_b64_tr_b16 v[166:167], v20 offset:5120\n\t"
  "v_add_u32 v20, 0, v15\n\t"
  "ds_read_b64_tr_b16 v[168:169], v20\n\t"
  "ds_read_b64_tr_b16 v[170:171], v20 offset:5120\n\t"
  "v_add_u32 v20, 10240, v15\n\t"
  "ds_read_b64_tr_b16 v[172:173], v20\n\t"
  "ds_read_b64_tr_b16 v[174:175], v20 offset:5120\n\t"
  "s_waitcnt lgkmcnt(8)\n\t"
  "s_cmp_lg_u32 s63, 0\n\t"
  "s_cbranch_scc1 71f\n\t"
  "v_cndmask_b32_dpp v32, v28, v24, vcc quad_perm:[0,1,0,1] row_mask:0xf bank_mask:0xf\n\t"
  "v_cndmask_b32_dpp v33, v29, v25, vcc quad_perm:[0,1,0,1] row_mask:0xf bank_mask:0xf\n\t"
  "v_cndmask_b32_dpp v34, v30, v26, vcc quad_perm:[0,1,0,1] row_mask:0xf bank_mask:0xf\n\t"
  "v_cndmask_b32_dpp v35, v31, v27, vcc quad_perm:[0,1,0,1] row_mask:0xf bank_mask:0xf\n\t"
  "s_branch 72f\n\t"
  "71:\n\t"
  "v_cndmask_b32_dpp v32, v24, v28, vcc quad_perm:[2,3,2,3] row_mask:0xf bank_mask:0xf\n\t"
  "v_cndmask_b32_dpp v33, v25, v29, vcc quad_perm:[2,3,2,3] row_mask:0xf bank_mask:0xf\n\t"
  "v_cndmask_b32_dpp v34, v26, v30, vcc quad_perm:[2,3,2,3] row_mask:0xf bank_mask:0xf\n\t"
  "v_cndmask_b32_dpp v35, v27, v31, vcc quad_perm:[2,3,2,3] row_mask:0xf bank_mask:0xf\n\t"
  "72:\n\t"
  "v_mov_b32 v176, v32\n\t"
  "v_mov_b32 v177, v33\n\t"
  "v_mov_b32 v178, v34\n\t"
  "v_mov_b32 v179, v35\n\t"
  "s_waitcnt vmcnt(0) lgkmcnt(0)\n\t"
  "s_barrier\n\t"
  "v_add_u32 v20, 20480, v16\n\t"
  "ds_read_b64_tr_b16 v[24:25], v20\n\t"
  "ds_read_b64_tr_b16 v[26:27], v20 offset:5120\n\t"
  "v_add_u32 v20, 30720, v16\n\t"
  "ds_read_b64_tr_b16 v[28:29], v20\n\t"
  "ds_read_b64_tr_b16 v[30:31], v20 offset:5120\n\t"
  "v_add_u32 v20, 20480, v14\n\t"
  "ds_read_b64_tr_b16 v[180:181], v20\n\t"
  "ds_read_b64_tr_b16 v[182:183], v20 offset:5120\n\t"
  "v_add_u32 v20, 30720, v14\n\t"
  "ds_read_b64_tr_b16 v[184:185], v20\n\t"
  "ds_read_b64_tr_b16 v[186:187], v20 offset:5120\n\t"
  "v_add_u32 v20, 20480, v15\n\t"
  "ds_read_b64_tr_b16 v[188:189], v20\n\t"
  "ds_read_b64_tr_b16 v[190:191], v20 offset:5120\n\t"
  "v_add_u32 v20, 30720, v15\n\t"
  "ds_read_b64_tr_b16 v[128:129], v20\n\t"
  "ds_read_b64_tr_b16 v[130:131], v20 offset:5120\n\t"
  "s_waitcnt lgkmcnt(8)\n\t"
  "s_cmp_lg_u32 s63, 0\n\t"
  "s_cbranch_scc1 71f\n\t"
  "v_cndmask_b32_dpp v32, v28, v24, vcc quad_perm:[0,1,0,1] row_mask:0xf bank_mask:0xf\n\t"
  "v_cndmask_b32_dpp v33, v29, v25, vcc quad_perm:[0,1,0,1] row_mask:0xf bank_mask:0xf\n\t"
  "v_cndmask_b32_dpp v34, v30, v26, vcc quad_perm:[0,1,0,1] row_mask:0xf bank_mask:0xf\n\t"
  "v_cndmask_b32_dpp v35, v31, v27, vcc quad_perm:[0,1,0,1] row_mask:0xf bank_mask:0xf\n\t"
  "s_branch 72f\n\t"
  "71:\n\t"
  "v_cndmask_b32_dpp v32, v24, v28, vcc quad_perm:[2,3,2,3] row_mask:0xf bank_mask:0xf\n\t"
  "v_cndmask_b32_dpp v33, v25, v29, vcc quad_perm:[2,3,2,3] row_mask:0xf bank_mask:0xf\n\t"
  "v_cndmask_b32_dpp v34, v26, v30, vcc quad_perm:[2,3,2,3] row_mask:0xf bank_mask:0xf\n\t"
  "v_cndmask_b32_dpp v35, v27, v31, vcc quad_perm:[2,3,2,3] row_mask:0xf bank_mask:0xf\n\t"
  "72:\n\t"
  "v_mov_b32 v132, v32\n\t"
  "v_mov_b32 v133, v33\n\t"
  "v_mov_b32 v134, v34\n\t"
  "v_mov_b32 v135, v35\n\t"
  "s_waitcnt lgkmcnt(0)\n\t"
  "s_barrier\n\t"
  "s_mov_b64 s[60:61], s[36:37]\n\t"
  "s_mov_b32 s63, s39\n\t"
  "s_mov_b64 s[64:65], s[36:37]\n\t"
  "s_mov_b32 s67, s39\n\t"
  "s_mov_b64 s[84:85], s[36:37]\n\t"
  "s_mov_b32 s87, s39\n\t"
  "s_mov_b32 s86, 0\n\t"
  "s_mov_b32 s57, 0\n\t"
  "s_mov_b32 s56, -1\n\t"
  "s_mov_b32 s80, 0x20000\n\t"
  "s_lshr_b32 s78, s41, 9\n\t"
  "s_mov_b32 s71, s41\n\t"
  "s_mov_b32 s74, -40960\n\t"
  "v_readlane_b32 s68, v4, 0\n\t"
  "v_mov_b32 v3, -1\n\t"
  "v_add_u32 v6, 159744, v6\n\t"
  "s_lshr_b32 s76, s41, 2\n\t"
  "s_add_u32 s76, s76, 159744\n\t"
  "v_add_u32 v94, s76, v0\n\t"
  "v_mov_b32 v76, 0xbdd2d3e8\n\t"
  "v_mov_b32 v77, 0xbdd2d3e8\n\t"
  "v_mov_b32 v78, 0xc0135761\n\t"
  "v_mov_b32 v79, 0xc0135761\n\t"
  "v_mov_b32 v80, 1.0\n\t"
  "v_mov_b32 v81, 1.0\n\t"
  "s_branch 3f\n\t"
  "1:\n\t"
  "ds_read_b128 v[16:19], v1 offset:2048\n\t"
  "s_waitcnt lgkmcnt(2)\n\t"
  "v_mfma_f32_16x16x32_bf16 v[20:23], a[0:3], v[8:11], 0\n\t"
  "s_add_u32 m0, s71, 16384\n\t"
  "s_add_u32 s76, s68, 32768\n\t"
  "buffer_load_dwordx4 v0, s[36:39], s76 offen sc1 lds\n\t"
  "v_mfma_f32_16x16x32_bf16 v[24:27], a[4:7], v[8:11], 0\n\t"
  "s_mov_b32 s70, s69\n\t"
  "s_mov_b32 s56, s57\n\t"
  "s_add_u32 s57, s57, 1\n\t"
  "v_mfma_f32_16x16x32_bf16 v[28:31], a[8:11], v[8:11], 0\n\t"
  "v_mfma_f32_16x16x32_bf16 v[32:35], a[12:15], v[8:11], 0\n\t"
  "v_mfma_f32_16x16x32_bf16 v[36:39], a[16:19], v[8:11], 0\n\t"
  "ds_read_b128 v[8:11], v1 offset:3072\n\t"
  "s_waitcnt lgkmcnt(2)\n\t"
  "v_mfma_f32_16x16x32_bf16 v[20:23], a[20:23], v[12:15], v[20:23]\n\t"
  "buffer_load_dwordx4 v0, s[36:39], s76 offen offset:1024 sc1 lds\n\t"
  "v_mfma_f32_16x16x32_bf16 v[24:27], a[24:27], v[12:15], v[24:27]\n\t"
  "v_mfma_f32_16x16x32_bf16 v[28:31], a[28:31], v[12:15], v[28:31]\n\t"
  "v_mfma_f32_16x16x32_bf16 v[32:35], a[32:35], v[12:15], v[32:35]\n\t"
  "v_mfma_f32_16x16x32_bf16 v[36:39], a[36:39], v[12:15], v[36:39]\n\t"
  "ds_read_b128 v[12:15], v1 offset:4096\n\t"
  "s_waitcnt lgkmcnt(2)\n\t"
  "v_mfma_f32_16x16x32_bf16 v[20:23], a[40:43], v[16:19], v[20:23]\n\t"
  "s_add_u32 m0, s71, 24576\n\t"
  "s_add_u32 s76, s68, 49152\n\t"
  "buffer_load_dwordx4 v0, s[36:39], s76 offen sc1 lds\n\t"
  "v_mfma_f32_16x16x32_bf16 v[24:27], a[44:47], v[16:19], v[24:27]\n\t"
  "v_mfma_f32_16x16x32_bf16 v[28:31], a[48:51], v[16:19], v[28:31]\n\t"
  "v_mfma_f32_16x16x32_bf16 v[32:35], a[52:55], v[16:19], v[32:35]\n\t"
  "v_mfma_f32_16x16x32_bf16 v[36:39], a[56:59], v[16:19], v[36:39]\n\t"
  "ds_read_b128 v[16:19], v1 offset:5120\n\t"
  "s_waitcnt lgkmcnt(2)\n\t"
  "v_mfma_f32_16x16x32_bf16 v[20:23], a[60:63], v[8:11], v[20:23]\n\t"
  "buffer_load_dwordx4 v0, s[36:39], s76 offen offset:1024 sc1 lds\n\t"
  "v_mfma_f32_16x16x32_bf16 v[24:27], a[64:67], v[8:11], v[24:27]\n\t"
  "v_mfma_f32_16x16x32_bf16 v[28:31], a[68:71], v[8:11], v[28:31]\n\t"
  "v_mfma_f32_16x16x32_bf16 v[32:35], a[72:75], v[8:11], v[32:35]\n\t"
  "v_mfma_f32_16x16x32_bf16 v[36:39], a[76:79], v[8:11], v[36:39]\n\t"
  "ds_read_b128 v[8:11], v1 offset:6144\n\t"
  "s_waitcnt lgkmcnt(2)\n\t"
  "v_mfma_f32_16x16x32_bf16 v[20:23], a[80:83], v[12:15], v[20:23]\n\t"
  "v_mov_b32 v82, s56\n\t"
  "v_mfma_f32_16x16x32_bf16 v[24:27], a[84:87], v[12:15], v[24:27]\n\t"
  "s_add_u32 m0, s71, 32768\n\t"
  "s_add_u32 s76, s68, 65536\n\t"
  "buffer_load_dwordx4 v0, s[36:39], s76 offen sc1 lds\n\t"
  "v_mfma_f32_16x16x32_bf16 v[28:31], a[88:91], v[12:15], v[28:31]\n\t"
  "v_mfma_f32_16x16x32_bf16 v[32:35], a[92:95], v[12:15], v[32:35]\n\t"
  "v_mfma_f32_16x16x32_bf16 v[36:39], a[96:99], v[12:15], v[36:39]\n\t"
  "ds_read_b128 v[12:15], v1 offset:7168\n\t"
  "s_waitcnt lgkmcnt(2)\n\t"
  "v_mfma_f32_16x16x32_bf16 v[20:23], a[100:103], v[16:19], v[20:23]\n\t"
  "v_mfma_f32_16x16x32_bf16 v[24:27], a[104:107], v[16:19], v[24:27]\n\t"
  "buffer_load_dwordx4 v0, s[36:39], s76 offen offset:1024 sc1 lds\n\t"
  "v_mfma_f32_16x16x32_bf16 v[28:31], a[108:111], v[16:19], v[28:31]\n\t"
  "v_mfma_f32_16x16x32_bf16 v[32:35], a[112:115], v[16:19], v[32:35]\n\t"
  "v_mfma_f32_16x16x32_bf16 v[36:39], a[116:119], v[16:19], v[36:39]\n\t"
  "ds_read_b128 v[16:19], v1 offset:8192\n\t"
  "s_waitcnt lgkmcnt(2)\n\t"
  "v_mfma_f32_16x16x32_bf16 v[20:23], a[120:123], v[8:11], v[20:23]\n\t"
  "v_mfma_f32_16x16x32_bf16 v[24:27], a[124:127], v[8:11], v[24:27]\n\t"
  "v_mfma_f32_16x16x32_bf16 v[28:31], a[128:131], v[8:11], v[28:31]\n\t"
  "v_mfma_f32_16x16x32_bf16 v[32:35], a[132:135], v[8:11], v[32:35]\n\t"
  "v_mfma_f32_16x16x32_bf16 v[36:39], a[136:139], v[8:11], v[36:39]\n\t"
  "ds_read_b128 v[8:11], v1 offset:9216\n\t"
  "s_waitcnt lgkmcnt(2)\n\t"
  "v_mfma_f32_16x16x32_bf16 v[20:23], a[140:143], v[12:15], v[20:23]\n\t"
  "v_mfma_f32_16x16x32_bf16 v[24:27], a[144:147], v[12:15], v[24:27]\n\t"
  "v_mfma_f32_16x16x32_bf16 v[28:31], a[148:151], v[12:15], v[28:31]\n\t"
  "v_mfma_f32_16x16x32_bf16 v[32:35], a[152:155], v[12:15], v[32:35]\n\t"
  "v_mfma_f32_16x16x32_bf16 v[36:39], a[156:159], v[12:15], v[36:39]\n\t"
  "ds_read_b128 v[12:15], v1 offset:10240\n\t"
  "s_waitcnt lgkmcnt(2)\n\t"
  "v_mfma_f32_16x16x32_bf16 v[20:23], a[160:163], v[16:19], v[20:23]\n\t"
  "v_subrev_u32 v82, 1, v82\n\t"
  "s_waitcnt vmcnt(6)\n\t"
  "buffer_store_dword v82, off, s[84:87], s40 sc1\n\t"
  "v_mfma_f32_16x16x32_bf16 v[24:27], a[164:167], v[16:19], v[24:27]\n\t"
  "v_mfma_f32_16x16x32_bf16 v[28:31], a[168:171], v[16:19], v[28:31]\n\t"
  "v_mfma_f32_16x16x32_bf16 v[32:35], a[172:175], v[16:19], v[32:35]\n\t"
  "v_mfma_f32_16x16x32_bf16 v[36:39], a[176:179], v[16:19], v[36:39]\n\t"
  "ds_read_b128 v[16:19], v1 offset:11264\n\t"
  "s_waitcnt lgkmcnt(2)\n\t"
  "v_mfma_f32_16x16x32_bf16 v[20:23], a[180:183], v[8:11], v[20:23]\n\t"
  "v_mfma_f32_16x16x32_bf16 v[24:27], a[184:187], v[8:11], v[24:27]\n\t"
  "v_mfma_f32_16x16x32_bf16 v[28:31], a[188:191], v[8:11], v[28:31]\n\t"
  "v_mfma_f32_16x16x32_bf16 v[32:35], a[192:195], v[8:11], v[32:35]\n\t"
  "v_mfma_f32_16x16x32_bf16 v[36:39], a[196:199], v[8:11], v[36:39]\n\t"
  "ds_read_b128 v[8:11], v1 offset:12288\n\t"
  "s_waitcnt lgkmcnt(2)\n\t"
  "v_mfma_f32_16x16x32_bf16 v[20:23], a[200:203], v[12:15], v[20:23]\n\t"
  "v_mfma_f32_16x16x32_bf16 v[24:27], a[204:207], v[12:15], v[24:27]\n\t"
  "v_mfma_f32_16x16x32_bf16 v[28:31], a[208:211], v[12:15], v[28:31]\n\t"
  "v_mfma_f32_16x16x32_bf16 v[32:35], a[212:215], v[12:15], v[32:35]\n\t"
  "v_mfma_f32_16x16x32_bf16 v[36:39], a[216:219], v[12:15], v[36:39]\n\t"
  "ds_read_b128 v[12:15], v1 offset:13312\n\t"
  "s_waitcnt lgkmcnt(2)\n\t"
  "v_mfma_f32_16x16x32_bf16 v[20:23], a[220:223], v[16:19], v[20:23]\n\t"
  "v_mfma_f32_16x16x32_bf16 v[24:27], a[224:227], v[16:19], v[24:27]\n\t"
  "v_mfma_f32_16x16x32_bf16 v[28:31], a[228:231], v[16:19], v[28:31]\n\t"
  "v_mfma_f32_16x16x32_bf16 v[32:35], a[232:235], v[16:19], v[32:35]\n\t"
  "v_mfma_f32_16x16x32_bf16 v[36:39], a[236:239], v[16:19], v[36:39]\n\t"
  "ds_read_b128 v[16:19], v1 offset:14336\n\t"
  "s_waitcnt lgkmcnt(2)\n\t"
  "v_mfma_f32_16x16x32_bf16 v[20:23], a[240:243], v[8:11], v[20:23]\n\t"
  "v_mfma_f32_16x16x32_bf16 v[24:27], a[244:247], v[8:11], v[24:27]\n\t"
  "v_mfma_f32_16x16x32_bf16 v[28:31], a[248:251], v[8:11], v[28:31]\n\t"
  "v_mfma_f32_16x16x32_bf16 v[32:35], a[252:255], v[8:11], v[32:35]\n\t"
  "v_mfma_f32_16x16x32_bf16 v[36:39], v[224:227], v[8:11], v[36:39]\n\t"
  "ds_read_b128 v[8:11], v1 offset:15360\n\t"
  "s_waitcnt lgkmcnt(2)\n\t"
  "v_mfma_f32_16x16x32_bf16 v[20:23], v[228:231], v[12:15], v[20:23]\n\t"
  "v_mfma_f32_16x16x32_bf16 v[24:27], v[232:235], v[12:15], v[24:27]\n\t"
  "v_mfma_f32_16x16x32_bf16 v[28:31], v[236:239], v[12:15], v[28:31]\n\t"
  "v_mfma_f32_16x16x32_bf16 v[32:35], v[240:243], v[12:15], v[32:35]\n\t"
  "s_waitcnt vmcnt(1)\n\t"
  "s_barrier\n\t"
  "v_mfma_f32_16x16x32_bf16 v[36:39], v[244:247], v[12:15], v[36:39]\n\t"
  "ds_read_b128 v[12:15], v1 offset:16384\n\t"
  "s_waitcnt lgkmcnt(2)\n\t"
  "v_mfma_f32_16x16x32_bf16 v[20:23], v[248:251], v[16:19], v[20:23]\n\t"
  "v_mfma_f32_16x16x32_bf16 v[24:27], v[252:255], v[16:19], v[24:27]\n\t"
  "v_mfma_f32_16x16x32_bf16 v[28:31], v[192:195], v[16:19], v[28:31]\n\t"
  "v_mfma_f32_16x16x32_bf16 v[32:35], v[196:199], v[16:19], v[32:35]\n\t"
  "v_mfma_f32_16x16x32_bf16 v[36:39], v[200:203], v[16:19], v[36:39]\n\t"
  "ds_read_b128 v[16:19], v1 offset:17408\n\t"
  "s_waitcnt lgkmcnt(2)\n\t"
  "v_mfma_f32_16x16x32_bf16 v[20:23], v[204:207], v[8:11], v[20:23]\n\t"
  "v_mfma_f32_16x16x32_bf16 v[24:27], v[208:211], v[8:11], v[24:27]\n\t"
  "v_mfma_f32_16x16x32_bf16 v[28:31], v[212:215], v[8:11], v[28:31]\n\t"
  "v_mfma_f32_16x16x32_bf16 v[32:35], v[216:219], v[8:11], v[32:35]\n\t"
  "v_mfma_f32_16x16x32_bf16 v[36:39], v[220:223], v[8:11], v[36:39]\n\t"
  "ds_read_b128 v[8:11], v1 offset:18432\n\t"
  "s_waitcnt lgkmcnt(2)\n\t"
  "v_mfma_f32_16x16x32_bf16 v[20:23], v[160:163], v[12:15], v[20:23]\n\t"
  "buffer_load_dword v3, v2, s[36:39], 0 offen sc1\n\t"
  "v_mfma_f32_16x16x32_bf16 v[24:27], v[164:167], v[12:15], v[24:27]\n\t"
  "s_mov_b32 s58, 0\n\t"
  "v_readlane_b32 s69, v5, s56\n\t"
  "v_mfma_f32_16x16x32_bf16 v[28:31], v[168:171], v[12:15], v[28:31]\n\t"
  "v_mfma_f32_16x16x32_bf16 v[32:35], v[172:175], v[12:15], v[32:35]\n\t"
  "v_mfma_f32_16x16x32_bf16 v[36:39], v[176:179], v[12:15], v[36:39]\n\t"
  "ds_read_b128 v[12:15], v1 offset:19456\n\t"
  "s_waitcnt lgkmcnt(2)\n\t"
  "v_mfma_f32_16x16x32_bf16 v[20:23], v[180:183], v[16:19], v[20:23]\n\t"
  "s_sub_u32 s71, s71, s74\n\t"
  "s_sub_u32 s74, 0, s74\n\t"
  "v_readlane_b32 s68, v4, s57\n\t"
  "v_mfma_f32_16x16x32_bf16 v[24:27], v[184:187], v[16:19], v[24:27]\n\t"
  "v_mfma_f32_16x16x32_bf16 v[28:31], v[188:191], v[16:19], v[28:31]\n\t"
  "v_mfma_f32_16x16x32_bf16 v[32:35], v[128:131], v[16:19], v[32:35]\n\t"
  "v_mfma_f32_16x16x32_bf16 v[36:39], v[132:135], v[16:19], v[36:39]\n\t"
  "ds_read_b128 v[16:19], v1 offset:20480\n\t"
  "s_waitcnt lgkmcnt(2)\n\t"
  "v_mfma_f32_16x16x32_bf16 v[40:43], a[0:3], v[8:11], 0\n\t"
  "v_mfma_f32_16x16x32_bf16 v[44:47], a[4:7], v[8:11], 0\n\t"
  "v_mul_f32 v60, v20, v20\n\t"
  "v_mul_f32 v61, v21, v21\n\t"
  "v_mfma_f32_16x16x32_bf16 v[48:51], a[8:11], v[8:11], 0\n\t"
  "v_fma_f32 v60, v60, v76, v78\n\t"
  "v_fma_f32 v61, v61, v76, v78\n\t"
  "v_mul_f32 v60, v60, v20\n\t"
  "v_mfma_f32_16x16x32_bf16 v[52:55], a[12:15], v[8:11], 0\n\t"
  "v_mul_f32 v61, v61, v21\n\t"
  "v_exp_f32 v60, v60\n\t"
  "v_mfma_f32_16x16x32_bf16 v[56:59], a[16:19], v[8:11], 0\n\t"
  "ds_read_b128 v[8:11], v1 offset:21504\n\t"
  "s_waitcnt lgkmcnt(2)\n\t"
  "v_mfma_f32_16x16x32_bf16 v[40:43], a[20:23], v[12:15], v[40:43]\n\t"
  "v_exp_f32 v61, v61\n\t"
  "v_mfma_f32_16x16x32_bf16 v[44:47], a[24:27], v[12:15], v[44:47]\n\t"
  "v_mul_f32 v24, v20, v24\n\t"
  "v_mul_f32 v25, v21, v25\n\t"
  "v_mfma_f32_16x16x32_bf16 v[48:51], a[28:31], v[12:15], v[48:51]\n\t"
  "v_add_f32 v60, 1.0, v60\n\t"
  "v_add_f32 v61, 1.0, v61\n\t"
  "v_mfma_f32_16x16x32_bf16 v[52:55], a[32:35], v[12:15], v[52:55]\n\t"
  "v_rcp_f32 v60, v60\n\t"
  "v_mfma_f32_16x16x32_bf16 v[56:59], a[36:39], v[12:15], v[56:59]\n\t"
  "ds_read_b128 v[12:15], v1 offset:22528\n\t"
  "s_waitcnt lgkmcnt(2)\n\t"
  "v_mfma_f32_16x16x32_bf16 v[40:43], a[40:43], v[16:19], v[40:43]\n\t"
  "v_cmp_le_i32 vcc, s57, v3\n\t"
  "v_rcp_f32 v61, v61\n\t"
  "v_mfma_f32_16x16x32_bf16 v[44:47], a[44:47], v[16:19], v[44:47]\n\t"
  "s_cmp_eq_u64 vcc, -1\n\t"
  "s_cselect_b32 s75, 1, 0\n\t"
  "s_mov_b32 s59, s75\n\t"
  "v_mul_f32 v24, v24, v60\n\t"
  "v_mfma_f32_16x16x32_bf16 v[48:51], a[48:51], v[16:19], v[48:51]\n\t"
  "s_andn2_b32 s75, s75, s58\n\t"
  "s_cselect_b32 s62, s51, 0\n\t"
  "s_cselect_b32 s72, s71, 155648\n\t"
  "v_mul_f32 v25, v25, v61\n\t"
  "v_mfma_f32_16x16x32_bf16 v[52:55], a[52:55], v[16:19], v[52:55]\n\t"
  "s_lshl_b32 s73, s75, 13\n\t"
  "s_or_b32 s58, s58, s75\n\t"
  "v_cvt_pk_bf16_f32 v70, v24, v25\n\t"
  "v_mul_f32 v60, v22, v22\n\t"
  "v_mfma_f32_16x16x32_bf16 v[56:59], a[56:59], v[16:19], v[56:59]\n\t"
  "ds_read_b128 v[16:19], v1 offset:23552\n\t"
  "s_waitcnt lgkmcnt(2)\n\t"
  "v_mfma_f32_16x16x32_bf16 v[40:43], a[60:63], v[8:11], v[40:43]\n\t"
  "s_mov_b32 m0, s72\n\t"
  "s_add_u32 s76, s68, 0\n\t"
  "buffer_load_dwordx4 v0, s[60:63], s76 offen sc1 lds\n\t"
  "v_mul_f32 v61, v23, v23\n\t"
  "v_mfma_f32_16x16x32_bf16 v[44:47], a[64:67], v[8:11], v[44:47]\n\t"
  "v_fma_f32 v60, v60, v76, v78\n\t"
  "v_fma_f32 v61, v61, v76, v78\n\t"
  "v_mfma_f32_16x16x32_bf16 v[48:51], a[68:71], v[8:11], v[48:51]\n\t"
  "v_mul_f32 v60, v60, v22\n\t"
  "v_mul_f32 v61, v61, v23\n\t"
  "v_mfma_f32_16x16x32_bf16 v[52:55], a[72:75], v[8:11], v[52:55]\n\t"
  "v_exp_f32 v60, v60\n\t"
  "v_mfma_f32_16x16x32_bf16 v[56:59], a[76:79], v[8:11], v[56:59]\n\t"
  "ds_read_b128 v[8:11], v1 offset:24576\n\t"
  "s_waitcnt lgkmcnt(2)\n\t"
  "v_mfma_f32_16x16x32_bf16 v[40:43], a[80:83], v[12:15], v[40:43]\n\t"
  "s_add_u32 s72, s72, s73\n\t"
  "buffer_load_dwordx4 v0, s[60:63], s76 offen offset:1024 sc1 lds\n\t"
  "v_exp_f32 v61, v61\n\t"
  "v_mfma_f32_16x16x32_bf16 v[44:47], a[84:87], v[12:15], v[44:47]\n\t"
  "v_mul_f32 v26, v22, v26\n\t"
  "v_mul_f32 v27, v23, v27\n\t"
  "v_mfma_f32_16x16x32_bf16 v[48:51], a[88:91], v[12:15], v[48:51]\n\t"
  "v_add_f32 v60, 1.0, v60\n\t"
  "v_add_f32 v61, 1.0, v61\n\t"
  "v_mfma_f32_16x16x32_bf16 v[52:55], a[92:95], v[12:15], v[52:55]\n\t"
  "v_rcp_f32 v60, v60\n\t"
  "v_mfma_f32_16x16x32_bf16 v[56:59], a[96:99], v[12:15], v[56:59]\n\t"
  "ds_read_b128 v[12:15], v1 offset:25600\n\t"
  "s_waitcnt lgkmcnt(2)\n\t"
  "v_mfma_f32_16x16x32_bf16 v[40:43], a[100:103], v[16:19], v[40:43]\n\t"
  "s_mov_b32 m0, s72\n\t"
  "s_add_u32 s76, s68, 16384\n\t"
  "buffer_load_dwordx4 v0, s[60:63], s76 offen sc1 lds\n\t"
  "v_rcp_f32 v61, v61\n\t"
  "v_mfma_f32_16x16x32_bf16 v[44:47], a[104:107], v[16:19], v[44:47]\n\t"
  "v_mul_f32 v26, v26, v60\n\t"
  "v_mul_f32 v27, v27, v61\n\t"
  "v_mfma_f32_16x16x32_bf16 v[48:51], a[108:111], v[16:19], v[48:51]\n\t"
  "v_cvt_pk_bf16_f32 v71, v26, v27\n\t"
  "v_mul_f32 v60, v28, v28\n\t"
  "v_mul_f32 v61, v29, v29\n\t"
  "v_mfma_f32_16x16x32_bf16 v[52:55], a[112:115], v[16:19], v[52:55]\n\t"
  "v_fma_f32 v60, v60, v76, v78\n\t"
  "v_fma_f32 v61, v61, v76, v78\n\t"
  "v_mfma_f32_16x16x32_bf16 v[56:59], a[116:119], v[16:19], v[56:59]\n\t"
  "ds_read_b128 v[16:19], v1 offset:26624\n\t"
  "s_waitcnt lgkmcnt(2)\n\t"
  "v_mfma_f32_16x16x32_bf16 v[40:43], a[120:123], v[8:11], v[40:43]\n\t"
  "s_add_u32 s72, s72, s73\n\t"
  "buffer_load_dwordx4 v0, s[60:63], s76 offen offset:1024 sc1 lds\n\t"
  "v_mul_f32 v60, v60, v28\n\t"
  "v_mfma_f32_16x16x32_bf16 v[44:47], a[124:127], v[8:11], v[44:47]\n\t"
  "v_mul_f32 v61, v61, v29\n\t"
  "v_exp_f32 v60, v60\n\t"
  "v_mfma_f32_16x16x32_bf16 v[48:51], a[128:131], v[8:11], v[48:51]\n\t"
  "v_exp_f32 v61, v61\n\t"
  "v_mul_f32 v32, v28, v32\n\t"
  "v_mfma_f32_16x16x32_bf16 v[52:55], a[132:135], v[8:11], v[52:55]\n\t"
  "v_mul_f32 v33, v29, v33\n\t"
  "v_add_f32 v60, 1.0, v60\n\t"
  "v_mfma_f32_16x16x32_bf16 v[56:59], a[136:139], v[8:11], v[56:59]\n\t"
  "ds_read_b128 v[8:11], v1 offset:27648\n\t"
  "s_waitcnt lgkmcnt(2)\n\t"
  "v_mfma_f32_16x16x32_bf16 v[40:43], a[140:143], v[12:15], v[40:43]\n\t"
  "buffer_load_dword v3, v2, s[36:39], 0 offen sc1\n\t"
  "v_add_f32 v61, 1.0, v61\n\t"
  "v_mfma_f32_16x16x32_bf16 v[44:47], a[144:147], v[12:15], v[44:47]\n\t"
  "v_rcp_f32 v60, v60\n\t"
  "v_mfma_f32_16x16x32_bf16 v[48:51], a[148:151], v[12:15], v[48:51]\n\t"
  "v_rcp_f32 v61, v61\n\t"
  "v_mul_f32 v32, v32, v60\n\t"
  "v_mfma_f32_16x16x32_bf16 v[52:55], a[152:155], v[12:15], v[52:55]\n\t"
  "v_mul_f32 v33, v33, v61\n\t"
  "v_cvt_pk_bf16_f32 v72, v32, v33\n\t"
  "v_mfma_f32_16x16x32_bf16 v[56:59], a[156:159], v[12:15], v[56:59]\n\t"
  "ds_read_b128 v[12:15], v1 offset:28672\n\t"
  "s_waitcnt lgkmcnt(2)\n\t"
  "v_mfma_f32_16x16x32_bf16 v[40:43], a[160:163], v[16:19], v[40:43]\n\t"
  "v_mul_f32 v60, v30, v30\n\t"
  "v_mul_f32 v61, v31, v31\n\t"
  "v_mfma_f32_16x16x32_bf16 v[44:47], a[164:167], v[16:19], v[44:47]\n\t"
  "v_fma_f32 v60, v60, v76, v78\n\t"
  "v_fma_f32 v61, v61, v76, v78\n\t"
  "v_mfma_f32_16x16x32_bf16 v[48:51], a[168:171], v[16:19], v[48:51]\n\t"
  "v_mul_f32 v60, v60, v30\n\t"
  "v_mul_f32 v61, v61, v31\n\t"
  "v_mfma_f32_16x16x32_bf16 v[52:55], a[172:175], v[16:19], v[52:55]\n\t"
  "v_exp_f32 v60, v60\n\t"
  "v_mfma_f32_16x16x32_bf16 v[56:59], a[176:179], v[16:19], v[56:59]\n\t"
  "ds_read_b128 v[16:19], v1 offset:29696\n\t"
  "s_waitcnt lgkmcnt(2)\n\t"
  "v_mfma_f32_16x16x32_bf16 v[40:43], a[180:183], v[8:11], v[40:43]\n\t"
  "v_exp_f32 v61, v61\n\t"
  "v_mfma_f32_16x16x32_bf16 v[44:47], a[184:187], v[8:11], v[44:47]\n\t"
  "v_mul_f32 v34, v30, v34\n\t"
  "v_mul_f32 v35, v31, v35\n\t"
  "v_mfma_f32_16x16x32_bf16 v[48:51], a[188:191], v[8:11], v[48:51]\n\t"
  "v_add_f32 v60, 1.0, v60\n\t"
  "v_add_f32 v61, 1.0, v61\n\t"
  "v_mfma_f32_16x16x32_bf16 v[52:55], a[192:195], v[8:11], v[52:55]\n\t"
  "v_rcp_f32 v60, v60\n\t"
  "v_mfma_f32_16x16x32_bf16 v[56:59], a[196:199], v[8:11], v[56:59]\n\t"
  "ds_read_b128 v[8:11], v1 offset:30720\n\t"
  "s_waitcnt lgkmcnt(2)\n\t"
  "v_mfma_f32_16x16x32_bf16 v[40:43], a[200:203], v[12:15], v[40:43]\n\t"
  "v_rcp_f32 v61, v61\n\t"
  "v_mfma_f32_16x16x32_bf16 v[44:47], a[204:207], v[12:15], v[44:47]\n\t"
  "v_mul_f32 v34, v34, v60\n\t"
  "v_mul_f32 v35, v35, v61\n\t"
  "v_mfma_f32_16x16x32_bf16 v[48:51], a[208:211], v[12:15], v[48:51]\n\t"
  "v_cvt_pk_bf16_f32 v73, v34, v35\n\t"
  "s_add_u32 s77, s69, s44\n\t"
  "v_mfma_f32_16x16x32_bf16 v[52:55], a[212:215], v[12:15], v[52:55]\n\t"
  "buffer_store_dwordx4 v[70:73], v0, s[36:39], s77 offen sc1\n\t"
  "v_mfma_f32_16x16x32_bf16 v[56:59], a[216:219], v[12:15], v[56:59]\n\t"
  "ds_read_b128 v[12:15], v1 offset:31744\n\t"
  "s_waitcnt lgkmcnt(2)\n\t"
  "v_mfma_f32_16x16x32_bf16 v[40:43], a[220:223], v[16:19], v[40:43]\n\t"
  "v_cmp_le_i32 vcc, s57, v3\n\t"
  "v_mul_f32 v60, v36, v36\n\t"
  "v_mfma_f32_16x16x32_bf16 v[44:47], a[224:227], v[16:19], v[44:47]\n\t"
  "s_cmp_eq_u64 vcc, -1\n\t"
  "s_cselect_b32 s75, 1, 0\n\t"
  "v_mul_f32 v61, v37, v37\n\t"
  "v_fma_f32 v60, v60, v76, v78\n\t"
  "v_mfma_f32_16x16x32_bf16 v[48:51], a[228:231], v[16:19], v[48:51]\n\t"
  "s_andn2_b32 s75, s75, s58\n\t"
  "s_cselect_b32 s62, s51, 0\n\t"
  "s_cselect_b32 s72, s71, 155648\n\t"
  "v_fma_f32 v61, v61, v76, v78\n\t"
  "v_mfma_f32_16x16x32_bf16 v[52:55], a[232:235], v[16:19], v[52:55]\n\t"
  "s_lshl_b32 s73, s75, 13\n\t"
  "s_or_b32 s58, s58, s75\n\t"
  "v_mul_f32 v60, v60, v36\n\t"
  "v_mul_f32 v61, v61, v37\n\t"
  "v_mfma_f32_16x16x32_bf16 v[56:59], a[236:239], v[16:19], v[56:59]\n\t"
  "ds_read_b128 v[16:19], v1 offset:32768\n\t"
  "s_waitcnt lgkmcnt(2)\n\t"
  "v_mfma_f32_16x16x32_bf16 v[40:43], a[240:243], v[8:11], v[40:43]\n\t"
  "s_mov_b32 m0, s72\n\t"
  "s_add_u32 s76, s68, 0\n\t"
  "buffer_load_dwordx4 v0, s[60:63], s76 offen sc1 lds\n\t"
  "v_exp_f32 v60, v60\n\t"
  "v_mfma_f32_16x16x32_bf16 v[44:47], a[244:247], v[8:11], v[44:47]\n\t"
  "v_exp_f32 v61, v61\n\t"
  "v_mfma_f32_16x16x32_bf16 v[48:51], a[248:251], v[8:11], v[48:51]\n\t"
  "v_mul_f32 v38, v36, v38\n\t"
  "v_mul_f32 v39, v37, v39\n\t"
  "v_add_f32 v60, 1.0, v60\n\t"
  "v_mfma_f32_16x16x32_bf16 v[52:55], a[252:255], v[8:11], v[52:55]\n\t"
  "v_add_f32 v61, 1.0, v61\n\t"
  "v_rcp_f32 v60, v60\n\t"
  "v_mfma_f32_16x16x32_bf16 v[56:59], v[224:227], v[8:11], v[56:59]\n\t"
  "ds_read_b128 v[8:11], v1 offset:33792\n\t"
  "s_waitcnt lgkmcnt(2)\n\t"
  "v_mfma_f32_16x16x32_bf16 v[40:43], v[228:231], v[12:15], v[40:43]\n\t"
  "s_add_u32 s72, s72, s73\n\t"
  "buffer_load_dwordx4 v0, s[60:63], s76 offen offset:1024 sc1 lds\n\t"
  "v_rcp_f32 v61, v61\n\t"
  "v_mfma_f32_16x16x32_bf16 v[44:47], v[232:235], v[12:15], v[44:47]\n\t"
  "v_mul_f32 v38, v38, v60\n\t"
  "v_mul_f32 v39, v39, v61\n\t"
  "v_mfma_f32_16x16x32_bf16 v[48:51], v[236:239], v[12:15], v[48:51]\n\t"
  "v_cvt_pk_bf16_f32 v74, v38, v39\n\t"
  "ds_write_b32 v6, v74 offset:0\n\t"
  "v_mfma_f32_16x16x32_bf16 v[52:55], v[240:243], v[12:15], v[52:55]\n\t"
  "v_mfma_f32_16x16x32_bf16 v[56:59], v[244:247], v[12:15], v[56:59]\n\t"
  "ds_read_b128 v[12:15], v1 offset:34816\n\t"
  "s_waitcnt lgkmcnt(3)\n\t"
  "v_mfma_f32_16x16x32_bf16 v[40:43], v[248:251], v[16:19], v[40:43]\n\t"
  "buffer_load_dword v3, v2, s[36:39], 0 offen sc1\n\t"
  "v_mfma_f32_16x16x32_bf16 v[44:47], v[252:255], v[16:19], v[44:47]\n\t"
  "v_mfma_f32_16x16x32_bf16 v[48:51], v[192:195], v[16:19], v[48:51]\n\t"
  "v_mfma_f32_16x16x32_bf16 v[52:55], v[196:199], v[16:19], v[52:55]\n\t"
  "v_mfma_f32_16x16x32_bf16 v[56:59], v[200:203], v[16:19], v[56:59]\n\t"
  "ds_read_b128 v[16:19], v1 offset:35840\n\t"
  "s_waitcnt lgkmcnt(3)\n\t"
  "v_mfma_f32_16x16x32_bf16 v[40:43], v[204:207], v[8:11], v[40:43]\n\t"
  "v_add_u32 v1, s74, v1\n\t"
  "v_mfma_f32_16x16x32_bf16 v[44:47], v[208:211], v[8:11], v[44:47]\n\t"
  "s_mov_b32 m0, s72\n\t"
  "s_add_u32 s76, s68, 16384\n\t"
  "buffer_load_dwordx4 v0, s[60:63], s76 offen sc1 lds\n\t"
  "v_mfma_f32_16x16x32_bf16 v[48:51], v[212:215], v[8:11], v[48:51]\n\t"
  "v_mfma_f32_16x16x32_bf16 v[52:55], v[216:219], v[8:11], v[52:55]\n\t"
  "v_mfma_f32_16x16x32_bf16 v[56:59], v[220:223], v[8:11], v[56:59]\n\t"
  "s_waitcnt lgkmcnt(1)\n\t"
  "v_mfma_f32_16x16x32_bf16 v[40:43], v[160:163], v[12:15], v[40:43]\n\t"
  "v_mfma_f32_16x16x32_bf16 v[44:47], v[164:167], v[12:15], v[44:47]\n\t"
  "s_lshl_b32 s52, s58, 7\n\t"
  "s_add_u32 s52, s52, s57\n\t"
  "v_mfma_f32_16x16x32_bf16 v[48:51], v[168:171], v[12:15], v[48:51]\n\t"
  "s_add_u32 s72, s72, s73\n\t"
  "buffer_load_dwordx4 v0, s[60:63], s76 offen offset:1024 sc1 lds\n\t"
  "v_mfma_f32_16x16x32_bf16 v[52:55], v[172:175], v[12:15], v[52:55]\n\t"
  "v_mfma_f32_16x16x32_bf16 v[56:59], v[176:179], v[12:15], v[56:59]\n\t"
  "s_waitcnt lgkmcnt(0)\n\t"
  "v_mfma_f32_16x16x32_bf16 v[40:43], v[180:183], v[16:19], v[40:43]\n\t"
  "v_mfma_f32_16x16x32_bf16 v[44:47], v[184:187], v[16:19], v[44:47]\n\t"
  "v_mfma_f32_16x16x32_bf16 v[48:51], v[188:191], v[16:19], v[48:51]\n\t"
  "v_mfma_f32_16x16x32_bf16 v[52:55], v[128:131], v[16:19], v[52:55]\n\t"
  "v_mfma_f32_16x16x32_bf16 v[56:59], v[132:135], v[16:19], v[56:59]\n\t"
  "s_nop 0\n\t"
  "v_cmp_le_i32 vcc, s52, v3\n\t"
  "s_cmp_eq_u64 vcc, -1\n\t"
  "s_cbranch_scc1 80f\n\t"
  "70:\n\t"
  "s_nop 3\n\t"
  "v_pk_mul_f32 v[60:61], v[40:41], v[40:41]\n\t"
  "s_nop 1\n\t"
  "v_pk_mul_f32 v[68:69], v[56:57], v[56:57]\n\t"
  "v_pk_mul_f32 v[44:45], v[40:41], v[44:45]\n\t"
  "v_pk_mul_f32 v[58:59], v[56:57], v[58:59]\n\t"
  "v_pk_fma_f32 v[60:61], v[60:61], v[76:77], v[78:79]\n\t"
  "v_pk_fma_f32 v[68:69], v[68:69], v[76:77], v[78:79]\n\t"
  "v_pk_mul_f32 v[60:61], v[60:61], v[40:41]\n\t"
  "v_pk_mul_f32 v[68:69], v[68:69], v[56:57]\n\t"
  "v_exp_f32 v60, v60\n\t"
  "v_exp_f32 v61, v61\n\t"
  "v_exp_f32 v68, v68\n\t"
  "v_exp_f32 v69, v69\n\t"
  "v_pk_add_f32 v[60:61], v[60:61], v[80:81]\n\t"
  "v_pk_add_f32 v[68:69], v[68:69], v[80:81]\n\t"
  "v_rcp_f32 v60, v60\n\t"
  "v_rcp_f32 v61, v61\n\t"
  "v_rcp_f32 v68, v68\n\t"
  "v_rcp_f32 v69, v69\n\t"
  "v_pk_mul_f32 v[44:45], v[44:45], v[60:61]\n\t"
  "v_pk_mul_f32 v[58:59], v[58:59], v[68:69]\n\t"
  "v_cvt_pk_bf16_f32 v70, v44, v45\n\t"
  "v_cvt_pk_bf16_f32 v74, v58, v59\n\t"
  "ds_write_b32 v6, v74 offset:1024\n\t"
  "v_pk_mul_f32 v[62:63], v[42:43], v[42:43]\n\t"
  "v_pk_mul_f32 v[64:65], v[48:49], v[48:49]\n\t"
  "v_pk_mul_f32 v[66:67], v[50:51], v[50:51]\n\t"
  "v_pk_mul_f32 v[46:47], v[42:43], v[46:47]\n\t"
  "v_pk_mul_f32 v[52:53], v[48:49], v[52:53]\n\t"
  "v_pk_mul_f32 v[54:55], v[50:51], v[54:55]\n\t"
  "v_pk_fma_f32 v[62:63], v[62:63], v[76:77], v[78:79]\n\t"
  "v_pk_fma_f32 v[64:65], v[64:65], v[76:77], v[78:79]\n\t"
  "v_pk_fma_f32 v[66:67], v[66:67], v[76:77], v[78:79]\n\t"
  "v_pk_mul_f32 v[62:63], v[62:63], v[42:43]\n\t"
  "v_pk_mul_f32 v[64:65], v[64:65], v[48:49]\n\t"
  "v_pk_mul_f32 v[66:67], v[66:67], v[50:51]\n\t"
  "v_exp_f32 v62, v62\n\t"
  "v_exp_f32 v63, v63\n\t"
  "v_exp_f32 v64, v64\n\t"
  "v_exp_f32 v65, v65\n\t"
  "v_exp_f32 v66, v66\n\t"
  "v_exp_f32 v67, v67\n\t"
  "v_pk_add_f32 v[62:63], v[62:63], v[80:81]\n\t"
  "v_pk_add_f32 v[64:65], v[64:65], v[80:81]\n\t"
  "v_pk_add_f32 v[66:67], v[66:67], v[80:81]\n\t"
  "v_rcp_f32 v62, v62\n\t"
  "v_rcp_f32 v63, v63\n\t"
  "v_rcp_f32 v64, v64\n\t"
  "v_rcp_f32 v65, v65\n\t"
  "v_rcp_f32 v66, v66\n\t"
  "v_rcp_f32 v67, v67\n\t"
  "v_pk_mul_f32 v[46:47], v[46:47], v[62:63]\n\t"
  "v_pk_mul_f32 v[52:53], v[52:53], v[64:65]\n\t"
  "v_pk_mul_f32 v[54:55], v[54:55], v[66:67]\n\t"
  "v_cvt_pk_bf16_f32 v71, v46, v47\n\t"
  "v_cvt_pk_bf16_f32 v72, v52, v53\n\t"
  "v_cvt_pk_bf16_f32 v73, v54, v55\n\t"
  "s_waitcnt lgkmcnt(0)\n\t"
  "s_barrier\n\t"
  "ds_read_b128 v[60:63], v94\n\t"
  "s_add_u32 s77, s69, s45\n\t"
  "buffer_store_dwordx4 v[70:73], v0, s[36:39], s77 offen sc1\n\t"
  "s_add_u32 s77, s69, s46\n\t"
  "s_waitcnt lgkmcnt(0)\n\t"
  "buffer_store_dwordx4 v[60:63], v7, s[36:39], s77 offen sc1\n\t"
  "s_mov_b32 s86, s51\n\t"
  "s_mov_b32 s66, 0\n\t"
  "s_cmp_lg_u32 s58, 0\n\t"
  "s_cbranch_scc1 5f\n\t"
  "v_cmp_le_i32 vcc, s57, v3\n\t"
  "s_cmp_eq_u64 vcc, -1\n\t"
  "s_cbranch_scc1 41f\n\t"
  "buffer_load_dword v3, v2, s[36:39], 0 offen sc1\n\t"
  "s_sleep 2\n\t"
  "v_cmp_le_i32 vcc, s57, v3\n\t"
  "s_cmp_eq_u64 vcc, -1\n\t"
  "s_cbranch_scc1 41f\n\t"
  "buffer_load_dword v3, v2, s[36:39], 0 offen sc1\n\t"
  "s_sleep 2\n\t"
  "v_cmp_le_i32 vcc, s57, v3\n\t"
  "s_cmp_eq_u64 vcc, -1\n\t"
  "s_cbranch_scc1 41f\n\t"
  "buffer_load_dword v3, v2, s[36:39], 0 offen sc1\n\t"
  "s_sleep 2\n\t"
  "v_cmp_le_i32 vcc, s57, v3\n\t"
  "s_cmp_eq_u64 vcc, -1\n\t"
  "s_cbranch_scc1 41f\n\t"
  "buffer_load_dword v3, v2, s[36:39], 0 offen sc1\n\t"
  "s_sleep 2\n\t"
  "s_mov_b32 s86, 0\n\t"
  "v_mov_b32 v86, s56\n\t"
  "s_waitcnt vmcnt(4)\n\t"
  "buffer_store_dword v86, off, s[36:39], s40 sc1\n\t"
  "s_cmp_eq_u32 s56, 63\n\t"
  "s_cbranch_scc1 9f\n\t"
  "3:\n\t"
  "s_mov_b32 s79, 0\n\t"
  "s_mov_b32 s66, 0\n\t"
  "4:\n\t"
  "v_cmp_le_i32 vcc, s57, v3\n\t"
  "s_cmp_eq_u64 vcc, -1\n\t"
  "s_cbranch_scc1 41f\n\t"
  "s_waitcnt vmcnt(8)\n\t"
  "buffer_load_dword v3, v2, s[36:39], 0 offen sc1\n\t"
  "s_sleep 4\n\t"
  "s_add_u32 s79, s79, 1\n\t"
  "s_cmp_lt_u32 s79, s80\n\t"
  "s_cbranch_scc1 4b\n\t"
  "s_mov_b32 s80, 0\n\t"
  "41:\n\t"
  "s_add_u32 m0, s71, 0\n\t"
  "s_add_u32 s76, s68, 0\n\t"
  "buffer_load_dwordx4 v0, s[36:39], s76 offen sc1 lds\n\t"
  "buffer_load_dwordx4 v0, s[36:39], s76 offen offset:1024 sc1 lds\n\t"
  "s_add_u32 m0, s71, 8192\n\t"
  "s_add_u32 s76, s68, 16384\n\t"
  "buffer_load_dwordx4 v0, s[36:39], s76 offen sc1 lds\n\t"
  "buffer_load_dwordx4 v0, s[36:39], s76 offen offset:1024 sc1 lds\n\t"
  "v_mov_b32 v86, s56\n\t"
  "buffer_store_dword v86, off, s[64:67], s40 sc1\n\t"
  "s_waitcnt vmcnt(5)\n\t"
  "buffer_store_dword v86, off, s[84:87], s40 sc1\n\t"
  "s_mov_b32 s86, 0\n\t"
  "s_waitcnt vmcnt(2)\n\t"
  "s_branch 6f\n\t"
  "5:\n\t"
  "s_waitcnt vmcnt(2)\n\t"
  "6:\n\t"
  "s_waitcnt lgkmcnt(0)\n\t"
  "s_barrier\n\t"
  "ds_read_b128 v[8:11], v1 offset:0\n\t"
  "ds_read_b128 v[12:15], v1 offset:1024\n\t"
  "s_branch 1b\n\t"
  "80:\n\t"
  "s_add_u32 m0, s71, 0\n\t"
  "s_add_u32 s76, s68, 0\n\t"
  "buffer_load_dwordx4 v0, s[36:39], s76 offen sc1 lds\n\t"
  "buffer_load_dwordx4 v0, s[36:39], s76 offen offset:1024 sc1 lds\n\t"
  "s_add_u32 m0, s71, 8192\n\t"
  "s_add_u32 s76, s68, 16384\n\t"
  "buffer_load_dwordx4 v0, s[36:39], s76 offen sc1 lds\n\t"
  "buffer_load_dwordx4 v0, s[36:39], s76 offen offset:1024 sc1 lds\n\t"
  "s_mov_b32 s58, 1\n\t"
  "s_mov_b32 s52, 127\n\t"
  "s_branch 70b\n\t"
  "9:\n\t"
  ".Lws_end_C:\n\t"
  "s_waitcnt vmcnt(0)\n\t"
  "s_waitcnt vmcnt(0) lgkmcnt(0)\n\t"
  : "+{v[0:7]}"(lc), "+{v[8:23]}"(so) :: CLOB_ALL);
}

constexpr float SCL = 0.08838834764831845f * 1.4426950408889634f;
constexpr float MASKED = -1.0e9f / 0.08838834764831845f;

__device__ __forceinline__ int kv_flag(const Ctx& c, int h) {
  const int sel = c.lane & 3;
  const int wv = (sel & 1) ? (4 * h + 3) / 3 : (4 * h) / 3;
  return flag_at(c.layer, R_KV + (sel >> 1) * 11 + (wv >> 2), wv & 3);
}

__device__ __forceinline__ void role_qa(Ctx& c, const Params& p) {
  const int j = c.idx - R_QA, head = 2 * j + (c.w >> 1), mm = c.w & 1;
  v16u so = stage_offsets<4>(c, HD * 2, 0, j * 512);
  v8u lc = {(unsigned)(c.lane * 16), (unsigned)xn_off((c.lane >> 2) & 3, c.lane >> 4, c.lane & 3), (unsigned)((c.lane >> 2) * 32 + (c.lane & 1) * 16), 0u, (unsigned)base_a(c, c.lane),
            (unsigned)(c.lane < 4 ? kv_flag(c, head) : c.lane < 32 ? flag_at(c.layer, R_CH + head, 3) : flag_at(c.layer, F_XN + ((c.lane >> 2) & 7), 0)),
            (unsigned)(16 * mm + (c.lane & 15) - 4 * (c.lane >> 4)), (unsigned)((c.lane ^ (c.lane >> 4)) * 8)};
  lc[3] = stage_scalars(c, p.wq, DM * HD * 2, HD * 2, 0, 0, (mm ? A_VF : A_KF) + head * 16384, A_KM + spd(head * 4096), 0,
                        A_OF + spd((head >> 3) * 65536 + mm * 32768 + (head & 7) * 4096), A_XN + c.w * 1024, 0);
  asm volatile(
  "s_nop 4\n\t"
  "v_readlane_b32 s36, v3, 0\n\t"
  "v_readlane_b32 s37, v3, 1\n\t"
  "v_readlane_b32 s38, v3, 2\n\t"
  "v_readlane_b32 s39, v3, 3\n\t"
  "v_readlane_b32 s40, v3, 4\n\t"
  "v_readlane_b32 s41, v3, 5\n\t"
  "v_readlane_b32 s42, v3, 6\n\t"
  "v_readlane_b32 s43, v3, 7\n\t"
  "v_readlane_b32 s44, v3, 8\n\t"
  "v_readlane_b32 s45, v3, 9\n\t"
  "v_readlane_b32 s46, v3, 10\n\t"
  "v_readlane_b32 s47, v3, 11\n\t"
  "v_readlane_b32 s48, v3, 12\n\t"
  "v_readlane_b32 s49, v3, 13\n\t"
  "v_readlane_b32 s50, v3, 14\n\t"
  "v_readlane_b32 s51, v3, 15\n\t"
  "s_nop 4\n\t"
  "v_readlane_b32 s56, v3, 16\n\t"
  "v_readlane_b32 s57, v3, 17\n\t"
  "v_readlane_b32 s58, v3, 18\n\t"
  "v_readlane_b32 s60, v3, 19\n\t"
  "s_mov_b32 s59, s39\n\t"
  "s_mul_i32 s61, s41, 4\n\t"
  "s_lshr_b32 s61, s61, 1\n\t"
  "s_mov_b32 s62, 0\n\t"
  "s_nop 2\n\t"
  "s_add_u32 m0, s61, 0\n\t"
  "s_nop 0\n\t"
  "buffer_load_dwordx4 v8, s[56:59], s62 offen lds\n\t"
  "s_add_u32 m0, s61, 1024\n\t"
  "s_nop 0\n\t"
  "buffer_load_dwordx4 v9, s[56:59], s62 offen lds\n\t"
  "s_add_u32 m0, s61, 2048\n\t"
  "s_nop 0\n\t"
  "buffer_load_dwordx4 v10, s[56:59], s62 offen lds\n\t"
  "s_add_u32 m0, s61, 3072\n\t"
  "s_nop 0\n\t"
  "buffer_load_dwordx4 v11, s[56:59], s62 offen lds\n\t"
  "s_add_u32 s62, s62, s60\n\t"
  "s_add_u32 m0, s61, 16384\n\t"
  "s_nop 0\n\t"
  "buffer_load_dwordx4 v8, s[56:59], s62 offen lds\n\t"
  "s_add_u32 m0, s61, 17408\n\t"
  "s_nop 0\n\t"
  "buffer_load_dwordx4 v9, s[56:59], s62 offen lds\n\t"
  "s_add_u32 m0, s61, 18432\n\t"
  "s_nop 0\n\t"
  "buffer_load_dwordx4 v10, s[56:59], s62 offen lds\n\t"
  "s_add_u32 m0, s61, 19456\n\t"
  "s_nop 0\n\t"
  "buffer_load_dwordx4 v11, s[56:59], s62 offen lds\n\t"
  "s_add_u32 s62, s62, s60\n\t"
  "s_add_u32 m0, s61, 32768\n\t"
  "s_nop 0\n\t"
  "buffer_load_dwordx4 v8, s[56:59], s62 offen lds\n\t"
  "s_add_u32 m0, s61, 33792\n\t"
  "s_nop 0\n\t"
  "buffer_load_dwordx4 v9, s[56:59], s62 offen lds\n\t"
  "s_add_u32 m0, s61, 34816\n\t"
  "s_nop 0\n\t"
  "buffer_load_dwordx4 v10, s[56:59], s62 offen lds\n\t"
  "s_add_u32 m0, s61, 35840\n\t"
  "s_nop 0\n\t"
  "buffer_load_dwordx4 v11, s[56:59], s62 offen lds\n\t"
  "s_add_u32 s62, s62, s60\n\t"
  "s_add_u32 m0, s61, 49152\n\t"
  "s_nop 0\n\t"
  "buffer_load_dwordx4 v8, s[56:59], s62 offen lds\n\t"
  "s_add_u32 m0, s61, 50176\n\t"
  "s_nop 0\n\t"
  "buffer_load_dwordx4 v9, s[56:59], s62 offen lds\n\t"
  "s_add_u32 m0, s61, 51200\n\t"
  "s_nop 0\n\t"
  "buffer_load_dwordx4 v10, s[56:59], s62 offen lds\n\t"
  "s_add_u32 m0, s61, 52224\n\t"
  "s_nop 0\n\t"
  "buffer_load_dwordx4 v11, s[56:59], s62 offen lds\n\t"
  "s_add_u32 s62, s62, s60\n\t"
  "s_add_u32 m0, s61, 65536\n\t"
  "s_nop 0\n\t"
  "buffer_load_dwordx4 v8, s[56:59], s62 offen lds\n\t"
  "s_add_u32 m0, s61, 66560\n\t"
  "s_nop 0\n\t"
  "buffer_load_dwordx4 v9, s[56:59], s62 offen lds\n\t"
  "s_add_u32 m0, s61, 67584\n\t"
  "s_nop 0\n\t"
  "buffer_load_dwordx4 v10, s[56:59], s62 offen lds\n\t"
  "s_add_u32 m0, s61, 68608\n\t"
  "s_nop 0\n\t"
  "buffer_load_dwordx4 v11, s[56:59], s62 offen lds\n\t"
  "s_add_u32 s62, s62, s60\n\t"
  "s_add_u32 m0, s61, 81920\n\t"
  "s_nop 0\n\t"
  "buffer_load_dwordx4 v8, s[56:59], s62 offen lds\n\t"
  "s_add_u32 m0, s61, 82944\n\t"
  "s_nop 0\n\t"
  "buffer_load_dwordx4 v9, s[56:59], s62 offen lds\n\t"
  "s_add_u32 m0, s61, 83968\n\t"
  "s_nop 0\n\t"
  "buffer_load_dwordx4 v10, s[56:59], s62 offen lds\n\t"
  "s_add_u32 m0, s61, 84992\n\t"
  "s_nop 0\n\t"
  "buffer_load_dwordx4 v11, s[56:59], s62 offen lds\n\t"
  "s_add_u32 s62, s62, s60\n\t"
  "s_add_u32 m0, s61, 98304\n\t"
  "s_nop 0\n\t"
  "buffer_load_dwordx4 v8, s[56:59], s62 offen lds\n\t"
  "s_add_u32 m0, s61, 99328\n\t"
  "s_nop 0\n\t"
  "buffer_load_dwordx4 v9, s[56:59], s62 offen lds\n\t"
  "s_add_u32 m0, s61, 100352\n\t"
  "s_nop 0\n\t"
  "buffer_load_dwordx4 v10, s[56:59], s62 offen lds\n\t"
  "s_add_u32 m0, s61, 101376\n\t"
  "s_nop 0\n\t"
  "buffer_load_dwordx4 v11, s[56:59], s62 offen lds\n\t"
  "s_add_u32 s62, s62, s60\n\t"
  "s_waitcnt vmcnt(24) lgkmcnt(0)\n\t"
  "s_barrier\n\t"
  "s_add_u32 m0, s61, 114688\n\t"
  "s_nop 0\n\t"
  "buffer_load_dwordx4 v8, s[56:59], s62 offen lds\n\t"
  "s_add_u32 m0, s61, 115712\n\t"
  "s_nop 0\n\t"
  "buffer_load_dwordx4 v9, s[56:59], s62 offen lds\n\t"
  "s_add_u32 m0, s61, 116736\n\t"
  "s_nop 0\n\t"
  "buffer_load_dwordx4 v10, s[56:59], s62 offen lds\n\t"
  "s_add_u32 m0, s61, 117760\n\t"
  "s_nop 0\n\t"
  "buffer_load_dwordx4 v11, s[56:59], s62 offen lds\n\t"
  "s_add_u32 s62, s62, s60\n\t"
  "v_add_u32 v20, 0, v14\n\t"
  "ds_read_b64_tr_b16 a[0:1], v20\n\t"
  "ds_read_b64_tr_b16 a[2:3], v20 offset:8192\n\t"
  "v_add_u32 v20, 0, v15\n\t"
  "ds_read_b64_tr_b16 a[4:5], v20\n\t"
  "ds_read_b64_tr_b16 a[6:7], v20 offset:8192\n\t"
  "v_add_u32 v20, 0, v16\n\t"
  "ds_read_b64_tr_b16 a[8:9], v20\n\t"
  "ds_read_b64_tr_b16 a[10:11], v20 offset:8192\n\t"
  "v_add_u32 v20, 0, v17\n\t"
  "ds_read_b64_tr_b16 a[12:13], v20\n\t"
  "ds_read_b64_tr_b16 a[14:15], v20 offset:8192\n\t"
  "s_waitcnt vmcnt(24) lgkmcnt(0)\n\t"
  "s_barrier\n\t"
  "s_add_u32 m0, s61, 0\n\t"
  "s_nop 0\n\t"
  "buffer_load_dwordx4 v8, s[56:59], s62 offen lds\n\t"
  "s_add_u32 m0, s61, 1024\n\t"
  "s_nop 0\n\t"
  "buffer_load_dwordx4 v9, s[56:59], s62 offen lds\n\t"
  "s_add_u32 m0, s61, 2048\n\t"
  "s_nop 0\n\t"
  "buffer_load_dwordx4 v10, s[56:59], s62 offen lds\n\t"
  "s_add_u32 m0, s61, 3072\n\t"
  "s_nop 0\n\t"
  "buffer_load_dwordx4 v11, s[56:59], s62 offen lds\n\t"
  "s_add_u32 s62, s62, s60\n\t"
  "v_add_u32 v20, 16384, v14\n\t"
  "ds_read_b64_tr_b16 a[16:17], v20\n\t"
  "ds_read_b64_tr_b16 a[18:19], v20 offset:8192\n\t"
  "v_add_u32 v20, 16384, v15\n\t"
  "ds_read_b64_tr_b16 a[20:21], v20\n\t"
  "ds_read_b64_tr_b16 a[22:23], v20 offset:8192\n\t"
  "v_add_u32 v20, 16384, v16\n\t"
  "ds_read_b64_tr_b16 a[24:25], v20\n\t"
  "ds_read_b64_tr_b16 a[26:27], v20 offset:8192\n\t"
  "v_add_u32 v20, 16384, v17\n\t"
  "ds_read_b64_tr_b16 a[28:29], v20\n\t"
  "ds_read_b64_tr_b16 a[30:31], v20 offset:8192\n\t"
  "s_waitcnt vmcnt(24) lgkmcnt(0)\n\t"
  "s_barrier\n\t"
  "s_add_u32 m0, s61, 16384\n\t"
  "s_nop 0\n\t"
  "buffer_load_dwordx4 v8, s[56:59], s62 offen lds\n\t"
  "s_add_u32 m0, s61, 17408\n\t"
  "s_nop 0\n\t"
  "buffer_load_dwordx4 v9, s[56:59], s62 offen lds\n\t"
  "s_add_u32 m0, s61, 18432\n\t"
  "s_nop 0\n\t"
  "buffer_load_dwordx4 v10, s[56:59], s62 offen lds\n\t"
  "s_add_u32 m0, s61, 19456\n\t"
  "s_nop 0\n\t"
  "buffer_load_dwordx4 v11, s[56:59], s62 offen lds\n\t"
  "s_add_u32 s62, s62, s60\n\t"
  "v_add_u32 v20, 32768, v14\n\t"
  "ds_read_b64_tr_b16 a[32:33], v20\n\t"
  "ds_read_b64_tr_b16 a[34:35], v20 offset:8192\n\t"
  "v_add_u32 v20, 32768, v15\n\t"
  "ds_read_b64_tr_b16 a[36:37], v20\n\t"
  "ds_read_b64_tr_b16 a[38:39], v20 offset:8192\n\t"
  "v_add_u32 v20, 32768, v16\n\t"
  "ds_read_b64_tr_b16 a[40:41], v20\n\t"
  "ds_read_b64_tr_b16 a[42:43], v20 offset:8192\n\t"
  "v_add_u32 v20, 32768, v17\n\t"
  "ds_read_b64_tr_b16 a[44:45], v20\n\t"
  "ds_read_b64_tr_b16 a[46:47], v20 offset:8192\n\t"
  "s_waitcnt vmcnt(24) lgkmcnt(0)\n\t"
  "s_barrier\n\t"
  "s_add_u32 m0, s61, 32768\n\t"
  "s_nop 0\n\t"
  "buffer_load_dwordx4 v8, s[56:59], s62 offen lds\n\t"
  "s_add_u32 m0, s61, 33792\n\t"
  "s_nop 0\n\t"
  "buffer_load_dwordx4 v9, s[56:59], s62 offen lds\n\t"
  "s_add_u32 m0, s61, 34816\n\t"
  "s_nop 0\n\t"
  "buffer_load_dwordx4 v10, s[56:59], s62 offen lds\n\t"
  "s_add_u32 m0, s61, 35840\n\t"
  "s_nop 0\n\t"
  "buffer_load_dwordx4 v11, s[56:59], s62 offen lds\n\t"
  "s_add_u32 s62, s62, s60\n\t"
  "v_add_u32 v20, 49152, v14\n\t"
  "ds_read_b64_tr_b16 a[48:49], v20\n\t"
  "ds_read_b64_tr_b16 a[50:51], v20 offset:8192\n\t"
  "v_add_u32 v20, 49152, v15\n\t"
  "ds_read_b64_tr_b16 a[52:53], v20\n\t"
  "ds_read_b64_tr_b16 a[54:55], v20 offset:8192\n\t"
  "v_add_u32 v20, 49152, v16\n\t"
  "ds_read_b64_tr_b16 a[56:57], v20\n\t"
  "ds_read_b64_tr_b16 a[58:59], v20 offset:8192\n\t"
  "v_add_u32 v20, 49152, v17\n\t"
  "ds_read_b64_tr_b16 a[60:61], v20\n\t"
  "ds_read_b64_tr_b16 a[62:63], v20 offset:8192\n\t"
  "s_waitcnt vmcnt(24) lgkmcnt(0)\n\t"
  "s_barrier\n\t"
  "s_add_u32 m0, s61, 49152\n\t"
  "s_nop 0\n\t"
  "buffer_load_dwordx4 v8, s[56:59], s62 offen lds\n\t"
  "s_add_u32 m0, s61, 50176\n\t"
  "s_nop 0\n\t"
  "buffer_load_dwordx4 v9, s[56:59], s62 offen lds\n\t"
  "s_add_u32 m0, s61, 51200\n\t"
  "s_nop 0\n\t"
  "buffer_load_dwordx4 v10, s[56:59], s62 offen lds\n\t"
  "s_add_u32 m0, s61, 52224\n\t"
  "s_nop 0\n\t"
  "buffer_load_dwordx4 v11, s[56:59], s62 offen lds\n\t"
  "s_add_u32 s62, s62, s60\n\t"
  "v_add_u32 v20, 65536, v14\n\t"
  "ds_read_b64_tr_b16 a[64:65], v20\n\t"
  "ds_read_b64_tr_b16 a[66:67], v20 offset:8192\n\t"
  "v_add_u32 v20, 65536, v15\n\t"
  "ds_read_b64_tr_b16 a[68:69], v20\n\t"
  "ds_read_b64_tr_b16 a[70:71], v20 offset:8192\n\t"
  "v_add_u32 v20, 65536, v16\n\t"
  "ds_read_b64_tr_b16 a[72:73], v20\n\t"
  "ds_read_b64_tr_b16 a[74:75], v20 offset:8192\n\t"
  "v_add_u32 v20, 65536, v17\n\t"
  "ds_read_b64_tr_b16 a[76:77], v20\n\t"
  "ds_read_b64_tr_b16 a[78:79], v20 offset:8192\n\t"
  "s_waitcnt vmcnt(24) lgkmcnt(0)\n\t"
  "s_barrier\n\t"
  "s_add_u32 m0, s61, 65536\n\t"
  "s_nop 0\n\t"
  "buffer_load_dwordx4 v8, s[56:59], s62 offen lds\n\t"
  "s_add_u32 m0, s61, 66560\n\t"
  "s_nop 0\n\t"
  "buffer_load_dwordx4 v9, s[56:59], s62 offen lds\n\t"
  "s_add_u32 m0, s61, 67584\n\t"
  "s_nop 0\n\t"
  "buffer_load_dwordx4 v10, s[56:59], s62 offen lds\n\t"
  "s_add_u32 m0, s61, 68608\n\t"
  "s_nop 0\n\t"
  "buffer_load_dwordx4 v11, s[56:59], s62 offen lds\n\t"
  "s_add_u32 s62, s62, s60\n\t"
  "v_add_u32 v20, 81920, v14\n\t"
  "ds_read_b64_tr_b16 a[80:81], v20\n\t"
  "ds_read_b64_tr_b16 a[82:83], v20 offset:8192\n\t"
  "v_add_u32 v20, 81920, v15\n\t"
  "ds_read_b64_tr_b16 a[84:85], v20\n\t"
  "ds_read_b64_tr_b16 a[86:87], v20 offset:8192\n\t"
  "v_add_u32 v20, 81920, v16\n\t"
  "ds_read_b64_tr_b16 a[88:89], v20\n\t"
  "ds_read_b64_tr_b16 a[90:91], v20 offset:8192\n\t"
  "v_add_u32 v20, 81920, v17\n\t"
  "ds_read_b64_tr_b16 a[92:93], v20\n\t"
  "ds_read_b64_tr_b16 a[94:95], v20 offset:8192\n\t"
  "s_waitcnt vmcnt(24) lgkmcnt(0)\n\t"
  "s_barrier\n\t"
  "s_add_u32 m0, s61, 81920\n\t"
  "s_nop 0\n\t"
  "buffer_load_dwordx4 v8, s[56:59], s62 offen lds\n\t"
  "s_add_u32 m0, s61, 82944\n\t"
  "s_nop 0\n\t"
  "buffer_load_dwordx4 v9, s[56:59], s62 offen lds\n\t"
  "s_add_u32 m0, s61, 83968\n\t"
  "s_nop 0\n\t"
  "buffer_load_dwordx4 v10, s[56:59], s62 offen lds\n\t"
  "s_add_u32 m0, s61, 84992\n\t"
  "s_nop 0\n\t"
  "buffer_load_dwordx4 v11, s[56:59], s62 offen lds\n\t"
  "s_add_u32 s62, s62, s60\n\t"
  "v_add_u32 v20, 98304, v14\n\t"
  "ds_read_b64_tr_b16 a[96:97], v20\n\t"
  "ds_read_b64_tr_b16 a[98:99], v20 offset:8192\n\t"
  "v_add_u32 v20, 98304, v15\n\t"
  "ds_read_b64_tr_b16 a[100:101], v20\n\t"
  "ds_read_b64_tr_b16 a[102:103], v20 offset:8192\n\t"
  "v_add_u32 v20, 98304, v16\n\t"
  "ds_read_b64_tr_b16 a[104:105], v20\n\t"
  "ds_read_b64_tr_b16 a[106:107], v20 offset:8192\n\t"
  "v_add_u32 v20, 98304, v17\n\t"
  "ds_read_b64_tr_b16 a[108:109], v20\n\t"
  "ds_read_b64_tr_b16 a[110:111], v20 offset:8192\n\t"
  "s_waitcnt vmcnt(24) lgkmcnt(0)\n\t"
  "s_barrier\n\t"
  "s_add_u32 m0, s61, 98304\n\t"
  "s_nop 0\n\t"
  "buffer_load_dwordx4 v8, s[56:59], s62 offen lds\n\t"
  "s_add_u32 m0, s61, 99328\n\t"
  "s_nop 0\n\t"
  "buffer_load_dwordx4 v9, s[56:59], s62 offen lds\n\t"
  "s_add_u32 m0, s61, 100352\n\t"
  "s_nop 0\n\t"
  "buffer_load_dwordx4 v10, s[56:59], s62 offen lds\n\t"
  "s_add_u32 m0, s61, 101376\n\t"
  "s_nop 0\n\t"
  "buffer_load_dwordx4 v11, s[56:59], s62 offen lds\n\t"
  "s_add_u32 s62, s62, s60\n\t"
  "v_add_u32 v20, 114688, v14\n\t"
  "ds_read_b64_tr_b16 a[112:113], v20\n\t"
  "ds_read_b64_tr_b16 a[114:115], v20 offset:8192\n\t"
  "v_add_u32 v20, 114688, v15\n\t"
  "ds_read_b64_tr_b16 a[116:117], v20\n\t"
  "ds_read_b64_tr_b16 a[118:119], v20 offset:8192\n\t"
  "v_add_u32 v20, 114688, v16\n\t"
  "ds_read_b64_tr_b16 a[120:121], v20\n\t"
  "ds_read_b64_tr_b16 a[122:123], v20 offset:8192\n\t"
  "v_add_u32 v20, 114688, v17\n\t"
  "ds_read_b64_tr_b16 a[124:125], v20\n\t"
  "ds_read_b64_tr_b16 a[126:127], v20 offset:8192\n\t"
  "s_waitcnt vmcnt(24) lgkmcnt(0)\n\t"
  "s_barrier\n\t"
  "s_add_u32 m0, s61, 114688\n\t"
  "s_nop 0\n\t"
  "buffer_load_dwordx4 v8, s[56:59], s62 offen lds\n\t"
  "s_add_u32 m0, s61, 115712\n\t"
  "s_nop 0\n\t"
  "buffer_load_dwordx4 v9, s[56:59], s62 offen lds\n\t"
  "s_add_u32 m0, s61, 116736\n\t"
  "s_nop 0\n\t"
  "buffer_load_dwordx4 v10, s[56:59], s62 offen lds\n\t"
  "s_add_u32 m0, s61, 117760\n\t"
  "s_nop 0\n\t"
  "buffer_load_dwordx4 v11, s[56:59], s62 offen lds\n\t"
  "s_add_u32 s62, s62, s60\n\t"
  "v_add_u32 v20, 0, v14\n\t"
  "ds_read_b64_tr_b16 a[128:129], v20\n\t"
  "ds_read_b64_tr_b16 a[130:131], v20 offset:8192\n\t"
  "v_add_u32 v20, 0, v15\n\t"
  "ds_read_b64_tr_b16 a[132:133], v20\n\t"
  "ds_read_b64_tr_b16 a[134:135], v20 offset:8192\n\t"
  "v_add_u32 v20, 0, v16\n\t"
  "ds_read_b64_tr_b16 a[136:137], v20\n\t"
  "ds_read_b64_tr_b16 a[138:139], v20 offset:8192\n\t"
  "v_add_u32 v20, 0, v17\n\t"
  "ds_read_b64_tr_b16 a[140:141], v20\n\t"
  "ds_read_b64_tr_b16 a[142:143], v20 offset:8192\n\t"
  "s_waitcnt vmcnt(24) lgkmcnt(0)\n\t"
  "s_barrier\n\t"
  "s_add_u32 m0, s61, 0\n\t"
  "s_nop 0\n\t"
  "buffer_load_dwordx4 v8, s[56:59], s62 offen lds\n\t"
  "s_add_u32 m0, s61, 1024\n\t"
  "s_nop 0\n\t"
  "buffer_load_dwordx4 v9, s[56:59], s62 offen lds\n\t"
  "s_add_u32 m0, s61, 2048\n\t"
  "s_nop 0\n\t"
  "buffer_load_dwordx4 v10, s[56:59], s62 offen lds\n\t"
  "s_add_u32 m0, s61, 3072\n\t"
  "s_nop 0\n\t"
  "buffer_load_dwordx4 v11, s[56:59], s62 offen lds\n\t"
  "s_add_u32 s62, s62, s60\n\t"
  "v_add_u32 v20, 16384, v14\n\t"
  "ds_read_b64_tr_b16 a[144:145], v20\n\t"
  "ds_read_b64_tr_b16 a[146:147], v20 offset:8192\n\t"
  "v_add_u32 v20, 16384, v15\n\t"
  "ds_read_b64_tr_b16 a[148:149], v20\n\t"
  "ds_read_b64_tr_b16 a[150:151], v20 offset:8192\n\t"
  "v_add_u32 v20, 16384, v16\n\t"
  "ds_read_b64_tr_b16 a[152:153], v20\n\t"
  "ds_read_b64_tr_b16 a[154:155], v20 offset:8192\n\t"
  "v_add_u32 v20, 16384, v17\n\t"
  "ds_read_b64_tr_b16 a[156:157], v20\n\t"
  "ds_read_b64_tr_b16 a[158:159], v20 offset:8192\n\t"
  "s_waitcnt vmcnt(24) lgkmcnt(0)\n\t"
  "s_barrier\n\t"
  "s_add_u32 m0, s61, 16384\n\t"
  "s_nop 0\n\t"
  "buffer_load_dwordx4 v8, s[56:59], s62 offen lds\n\t"
  "s_add_u32 m0, s61, 17408\n\t"
  "s_nop 0\n\t"
  "buffer_load_dwordx4 v9, s[56:59], s62 offen lds\n\t"
  "s_add_u32 m0, s61, 18432\n\t"
  "s_nop 0\n\t"
  "buffer_load_dwordx4 v10, s[56:59], s62 offen lds\n\t"
  "s_add_u32 m0, s61, 19456\n\t"
  "s_nop 0\n\t"
  "buffer_load_dwordx4 v11, s[56:59], s62 offen lds\n\t"
  "s_add_u32 s62, s62, s60\n\t"
  "v_add_u32 v20, 32768, v14\n\t"
  "ds_read_b64_tr_b16 a[160:161], v20\n\t"
  "ds_read_b64_tr_b16 a[162:163], v20 offset:8192\n\t"
  "v_add_u32 v20, 32768, v15\n\t"
  "ds_read_b64_tr_b16 a[164:165], v20\n\t"
  "ds_read_b64_tr_b16 a[166:167], v20 offset:8192\n\t"
  "v_add_u32 v20, 32768, v16\n\t"
  "ds_read_b64_tr_b16 a[168:169], v20\n\t"
  "ds_read_b64_tr_b16 a[170:171], v20 offset:8192\n\t"
  "v_add_u32 v20, 32768, v17\n\t"
  "ds_read_b64_tr_b16 a[172:173], v20\n\t"
  "ds_read_b64_tr_b16 a[174:175], v20 offset:8192\n\t"
  "s_waitcnt vmcnt(24) lgkmcnt(0)\n\t"
  "s_barrier\n\t"
  "v_add_u32 v20, 49152, v14\n\t"
  "ds_read_b64_tr_b16 a[176:177], v20\n\t"
  "ds_read_b64_tr_b16 a[178:179], v20 offset:8192\n\t"
  "v_add_u32 v20, 49152, v15\n\t"
  "ds_read_b64_tr_b16 a[180:181], v20\n\t"
  "ds_read_b64_tr_b16 a[182:183], v20 offset:8192\n\t"
  "v_add_u32 v20, 49152, v16\n\t"
  "ds_read_b64_tr_b16 a[184:185], v20\n\t"
  "ds_read_b64_tr_b16 a[186:187], v20 offset:8192\n\t"
  "v_add_u32 v20, 49152, v17\n\t"
  "ds_read_b64_tr_b16 a[188:189], v20\n\t"
  "ds_read_b64_tr_b16 a[190:191], v20 offset:8192\n\t"
  "s_waitcnt vmcnt(20) lgkmcnt(0)\n\t"
  "s_barrier\n\t"
  "v_add_u32 v20, 65536, v14\n\t"
  "ds_read_b64_tr_b16 a[192:193], v20\n\t"
  "ds_read_b64_tr_b16 a[194:195], v20 offset:8192\n\t"
  "v_add_u32 v20, 65536, v15\n\t"
  "ds_read_b64_tr_b16 a[196:197], v20\n\t"
  "ds_read_b64_tr_b16 a[198:199], v20 offset:8192\n\t"
  "v_add_u32 v20, 65536, v16\n\t"
  "ds_read_b64_tr_b16 a[200:201], v20\n\t"
  "ds_read_b64_tr_b16 a[202:203], v20 offset:8192\n\t"
  "v_add_u32 v20, 65536, v17\n\t"
  "ds_read_b64_tr_b16 a[204:205], v20\n\t"
  "ds_read_b64_tr_b16 a[206:207], v20 offset:8192\n\t"
  "s_waitcnt vmcnt(16) lgkmcnt(0)\n\t"
  "s_barrier\n\t"
  "v_add_u32 v20, 81920, v14\n\t"
  "ds_read_b64_tr_b16 a[208:209], v20\n\t"
  "ds_read_b64_tr_b16 a[210:211], v20 offset:8192\n\t"
  "v_add_u32 v20, 81920, v15\n\t"
  "ds_read_b64_tr_b16 a[212:213], v20\n\t"
  "ds_read_b64_tr_b16 a[214:215], v20 offset:8192\n\t"
  "v_add_u32 v20, 81920, v16\n\t"
  "ds_read_b64_tr_b16 a[216:217], v20\n\t"
  "ds_read_b64_tr_b16 a[218:219], v20 offset:8192\n\t"
  "v_add_u32 v20, 81920, v17\n\t"
  "ds_read_b64_tr_b16 a[220:221], v20\n\t"
  "ds_read_b64_tr_b16 a[222:223], v20 offset:8192\n\t"
  "s_waitcnt vmcnt(12) lgkmcnt(0)\n\t"
  "s_barrier\n\t"
  "v_add_u32 v20, 98304, v14\n\t"
  "ds_read_b64_tr_b16 a[224:225], v20\n\t"
  "ds_read_b64_tr_b16 a[226:227], v20 offset:8192\n\t"
  "v_add_u32 v20, 98304, v15\n\t"
  "ds_read_b64_tr_b16 a[228:229], v20\n\t"
  "ds_read_b64_tr_b16 a[230:231], v20 offset:8192\n\t"
  "v_add_u32 v20, 98304, v16\n\t"
  "ds_read_b64_tr_b16 a[232:233], v20\n\t"
  "ds_read_b64_tr_b16 a[234:235], v20 offset:8192\n\t"
  "v_add_u32 v20, 98304, v17\n\t"
  "ds_read_b64_tr_b16 a[236:237], v20\n\t"
  "ds_read_b64_tr_b16 a[238:239], v20 offset:8192\n\t"
  "s_waitcnt vmcnt(8) lgkmcnt(0)\n\t"
  "s_barrier\n\t"
  "v_add_u32 v20, 114688, v14\n\t"
  "ds_read_b64_tr_b16 a[240:241], v20\n\t"
  "ds_read_b64_tr_b16 a[242:243], v20 offset:8192\n\t"
  "v_add_u32 v20, 114688, v15\n\t"
  "ds_read_b64_tr_b16 a[244:245], v20\n\t"
  "ds_read_b64_tr_b16 a[246:247], v20 offset:8192\n\t"
  "v_add_u32 v20, 114688, v16\n\t"
  "ds_read_b64_tr_b16 a[248:249], v20\n\t"
  "ds_read_b64_tr_b16 a[250:251], v20 offset:8192\n\t"
  "v_add_u32 v20, 114688, v17\n\t"
  "ds_read_b64_tr_b16 a[252:253], v20\n\t"
  "ds_read_b64_tr_b16 a[254:255], v20 offset:8192\n\t"
  "s_waitcnt vmcnt(4) lgkmcnt(0)\n\t"
  "s_barrier\n\t"
  "v_add_u32 v20, 0, v14\n\t"
  "ds_read_b64_tr_b16 v[224:225], v20\n\t"
  "ds_read_b64_tr_b16 v[226:227], v20 offset:8192\n\t"
  "v_add_u32 v20, 0, v15\n\t"
  "ds_read_b64_tr_b16 v[228:229], v20\n\t"
  "ds_read_b64_tr_b16 v[230:231], v20 offset:8192\n\t"
  "v_add_u32 v20, 0, v16\n\t"
  "ds_read_b64_tr_b16 v[232:233], v20\n\t"
  "ds_read_b64_tr_b16 v[234:235], v20 offset:8192\n\t"
  "v_add_u32 v20, 0, v17\n\t"
  "ds_read_b64_tr_b16 v[236:237], v20\n\t"
  "ds_read_b64_tr_b16 v[238:239], v20 offset:8192\n\t"
  "s_waitcnt vmcnt(0) lgkmcnt(0)\n\t"
  "s_barrier\n\t"
  "v_add_u32 v20, 16384, v14\n\t"
  "ds_read_b64_tr_b16 v[240:241], v20\n\t"
  "ds_read_b64_tr_b16 v[242:243], v20 offset:8192\n\t"
  "v_add_u32 v20, 16384, v15\n\t"
  "ds_read_b64_tr_b16 v[244:245], v20\n\t"
  "ds_read_b64_tr_b16 v[246:247], v20 offset:8192\n\t"
  "v_add_u32 v20, 16384, v16\n\t"
  "ds_read_b64_tr_b16 v[248:249], v20\n\t"
  "ds_read_b64_tr_b16 v[250:251], v20 offset:8192\n\t"
  "v_add_u32 v20, 16384, v17\n\t"
  "ds_read_b64_tr_b16 v[252:253], v20\n\t"
  "ds_read_b64_tr_b16 v[254:255], v20 offset:8192\n\t"
  "s_waitcnt lgkmcnt(0)\n\t"
  "s_barrier\n\t"
  "s_mov_b64 s[60:61], s[36:37]\n\t"
  "s_mov_b32 s63, s39\n\t"
  "s_mov_b64 s[84:85], s[36:37]\n\t"
  "s_mov_b32 s87, s39\n\t"
  "s_mov_b32 s86, 0\n\t"
  "s_lshl_b32 s64, s41, 2\n\t"
  "s_add_u32 s64, s64, 81920\n\t"
  "s_lshl_b32 s65, s41, 1\n\t"
  "s_add_u32 s65, s65, 114688\n\t"
  "s_and_b32 s76, s41, 4096\n\t"
  "s_lshl_b32 s76, s76, 2\n\t"
  "s_add_u32 s76, s76, 81920\n\t"
  "v_add_u32 v24, s76, v0\n\t"
  "s_and_b32 s76, s41, 2048\n\t"
  "s_xor_b32 s77, s76, 2048\n\t"
  "v_add_u32 v204, s76, v24\n\t"
  "v_add_u32 v205, s77, v24\n\t"
  "s_lshr_b32 s75, s76, 1\n\t"
  "s_add_u32 s75, s75, s65\n\t"
  "v_add_u32 v206, s75, v2\n\t"
  "v_xor_b32 v207, 1024, v206\n\t"
  "s_mul_i32 s76, s76, 9\n\t"
  "s_mul_i32 s77, s77, 9\n\t"
  "v_add_u32 v208, s76, v1\n\t"
  "v_add_u32 v1, s77, v1\n\t"
  "s_add_u32 s76, s41, 73728\n\t"
  "v_add_u32 v22, s76, v0\n\t"
  "v_xor_b32 v23, 2048, v22\n\t"
  "v_add_u32 v7, s65, v7\n\t"
  "v_mov_b32 v27, 0xd028965f\n\t"
  "v_mov_b32 v28, 0xff800000\n\t"
  "v_mov_b32 v29, 0x7f800000\n\t"
  "v_cmp_le_i32 vcc, 0, v6\n\t"
  "s_nop 1\n\t"
  "v_cndmask_b32 v32, v27, v28, vcc\n\t"
  "v_cndmask_b32 v40, v27, v29, vcc\n\t"
  "v_cmp_le_i32 vcc, 1, v6\n\t"
  "s_nop 1\n\t"
  "v_cndmask_b32 v33, v27, v28, vcc\n\t"
  "v_cndmask_b32 v41, v27, v29, vcc\n\t"
  "v_cmp_le_i32 vcc, 2, v6\n\t"
  "s_nop 1\n\t"
  "v_cndmask_b32 v34, v27, v28, vcc\n\t"
  "v_cndmask_b32 v42, v27, v29, vcc\n\t"
  "v_cmp_le_i32 vcc, 3, v6\n\t"
  "s_nop 1\n\t"
  "v_cndmask_b32 v35, v27, v28, vcc\n\t"
  "v_cndmask_b32 v43, v27, v29, vcc\n\t"
  "v_cmp_le_i32 vcc, 16, v6\n\t"
  "s_nop 1\n\t"
  "v_cndmask_b32 v36, v27, v28, vcc\n\t"
  "v_cndmask_b32 v44, v27, v29, vcc\n\t"
  "v_cmp_le_i32 vcc, 17, v6\n\t"
  "s_nop 1\n\t"
  "v_cndmask_b32 v37, v27, v28, vcc\n\t"
  "v_cndmask_b32 v45, v27, v29, vcc\n\t"
  "v_cmp_le_i32 vcc, 18, v6\n\t"
  "s_nop 1\n\t"
  "v_cndmask_b32 v38, v27, v28, vcc\n\t"
  "v_cndmask_b32 v46, v27, v29, vcc\n\t"
  "v_cmp_le_i32 vcc, 19, v6\n\t"
  "s_nop 1\n\t"
  "v_cndmask_b32 v39, v27, v28, vcc\n\t"
  "v_cndmask_b32 v47, v27, v29, vcc\n\t"
  "v_mov_b32 v180, 0x3f803f80\n\t"
  "v_mov_b32 v181, 0x3f803f80\n\t"
  "v_mov_b32 v182, 0x3f803f80\n\t"
  "v_mov_b32 v183, 0x3f803f80\n\t"
  "v_mov_b32 v177, 0\n\t"
  "v_mov_b32 v178, 0\n\t"
  "v_mov_b32 v20, -1\n\t"
  "s_mov_b32 s57, 0\n\t"
  "s_mov_b32 s56, -1\n\t"
  "s_mov_b32 s80, 0x20000\n\t"
  "s_mov_b32 s78, 0\n\t"
  "s_lshr_b32 s71, s41, 1\n\t"
  "s_mov_b32 s74, -36864\n\t"
  "v_readlane_b32 s68, v4, 0\n\t"
  "s_add_u32 s68, s68, s48\n\t"
  "s_branch 8f\n\t"
  "1:\n\t"
  "s_mov_b32 s56, s57\n\t"
  "v_readlane_b32 s69, v4, s56\n\t"
  "v_cmp_le_i32 vcc, s56, v20\n\t"
  "s_and_b32 s75, vcc_lo, 15\n\t"
  "s_cmp_eq_u32 s75, 15\n\t"
  "s_cselect_b32 s58, 1, 0\n\t"
  "s_cselect_b32 s62, s51, 0\n\t"
  "s_cselect_b32 s72, s64, 155648\n\t"
  "s_waitcnt lgkmcnt(0)\n\t"
  "s_barrier\n\t"
  "ds_read_b128 v[8:11], v1 offset:0\n\t"
  "ds_read_b128 v[12:15], v1 offset:1024\n\t"
  "ds_read_b128 v[16:19], v1 offset:2048\n\t"
  "s_waitcnt lgkmcnt(2)\n\t"
  "v_mfma_f32_16x16x32_bf16 v[48:51], a[0:3], v[8:11], 0\n\t"
  "s_add_u32 m0, s72, 0\n\t"
  "s_add_u32 s76, s69, s44\n\t"
  "buffer_load_dwordx4 v0, s[60:63], s76 offen offset:0 sc1 lds\n\t"
  "v_mfma_f32_16x16x32_bf16 v[52:55], a[4:7], v[8:11], 0\n\t"
  "s_add_u32 s57, s57, 1\n\t"
  "s_sub_u32 s71, s71, s74\n\t"
  "s_sub_u32 s74, 0, s74\n\t"
  "v_mfma_f32_16x16x32_bf16 v[56:59], a[8:11], v[8:11], 0\n\t"
  "v_readlane_b32 s68, v4, s57\n\t"
  "v_mfma_f32_16x16x32_bf16 v[60:63], a[12:15], v[8:11], 0\n\t"
  "ds_read_b128 v[8:11], v1 offset:3072\n\t"
  "s_waitcnt lgkmcnt(2)\n\t"
  "v_mfma_f32_16x16x32_bf16 v[48:51], a[16:19], v[12:15], v[48:51]\n\t"
  "s_add_u32 s68, s68, s48\n\t"
  "v_mfma_f32_16x16x32_bf16 v[52:55], a[20:23], v[12:15], v[52:55]\n\t"
  "buffer_load_dwordx4 v0, s[60:63], s76 offen offset:1024 sc1 lds\n\t"
  "v_mfma_f32_16x16x32_bf16 v[56:59], a[24:27], v[12:15], v[56:59]\n\t"
  "s_sub_u32 s75, s56, 1\n\t"
  "v_mov_b32 v21, s75\n\t"
  "v_mfma_f32_16x16x32_bf16 v[60:63], a[28:31], v[12:15], v[60:63]\n\t"
  "ds_read_b128 v[12:15], v1 offset:4096\n\t"
  "s_waitcnt lgkmcnt(2)\n\t"
  "v_mfma_f32_16x16x32_bf16 v[48:51], a[32:35], v[16:19], v[48:51]\n\t"
  "v_mfma_f32_16x16x32_bf16 v[52:55], a[36:39], v[16:19], v[52:55]\n\t"
  "v_mfma_f32_16x16x32_bf16 v[56:59], a[40:43], v[16:19], v[56:59]\n\t"
  "buffer_load_dwordx4 v0, s[60:63], s76 offen offset:2048 sc1 lds\n\t"
  "v_mfma_f32_16x16x32_bf16 v[60:63], a[44:47], v[16:19], v[60:63]\n\t"
  "ds_read_b128 v[16:19], v1 offset:5120\n\t"
  "s_waitcnt lgkmcnt(2)\n\t"
  "v_mfma_f32_16x16x32_bf16 v[48:51], a[48:51], v[8:11], v[48:51]\n\t"
  "v_mfma_f32_16x16x32_bf16 v[52:55], a[52:55], v[8:11], v[52:55]\n\t"
  "v_mfma_f32_16x16x32_bf16 v[56:59], a[56:59], v[8:11], v[56:59]\n\t"
  "v_mfma_f32_16x16x32_bf16 v[60:63], a[60:63], v[8:11], v[60:63]\n\t"
  "ds_read_b128 v[8:11], v1 offset:6144\n\t"
  "s_waitcnt lgkmcnt(2)\n\t"
  "v_mfma_f32_16x16x32_bf16 v[48:51], a[64:67], v[12:15], v[48:51]\n\t"
  "buffer_load_dword v20, v5, s[36:39], 0 offen sc1\n\t"
  "v_mfma_f32_16x16x32_bf16 v[52:55], a[68:71], v[12:15], v[52:55]\n\t"
  "v_mfma_f32_16x16x32_bf16 v[56:59], a[72:75], v[12:15], v[56:59]\n\t"
  "v_mfma_f32_16x16x32_bf16 v[60:63], a[76:79], v[12:15], v[60:63]\n\t"
  "ds_read_b128 v[12:15], v1 offset:7168\n\t"
  "s_waitcnt lgkmcnt(2)\n\t"
  "v_mfma_f32_16x16x32_bf16 v[48:51], a[80:83], v[16:19], v[48:51]\n\t"
  "buffer_load_dwordx4 v0, s[60:63], s76 offen offset:3072 sc1 lds\n\t"
  "v_mfma_f32_16x16x32_bf16 v[52:55], a[84:87], v[16:19], v[52:55]\n\t"
  "v_mfma_f32_16x16x32_bf16 v[56:59], a[88:91], v[16:19], v[56:59]\n\t"
  "v_mfma_f32_16x16x32_bf16 v[60:63], a[92:95], v[16:19], v[60:63]\n\t"
  "ds_read_b128 v[16:19], v1 offset:8192\n\t"
  "s_waitcnt lgkmcnt(2)\n\t"
  "v_mfma_f32_16x16x32_bf16 v[48:51], a[96:99], v[8:11], v[48:51]\n\t"
  "s_add_u32 m0, s72, 4096\n\t"
  "s_add_u32 s76, s76, 4096\n\t"
  "buffer_load_dwordx4 v0, s[60:63], s76 offen offset:0 sc1 lds\n\t"
  "v_mfma_f32_16x16x32_bf16 v[52:55], a[100:103], v[8:11], v[52:55]\n\t"
  "v_mfma_f32_16x16x32_bf16 v[56:59], a[104:107], v[8:11], v[56:59]\n\t"
  "v_mfma_f32_16x16x32_bf16 v[60:63], a[108:111], v[8:11], v[60:63]\n\t"
  "ds_read_b128 v[8:11], v1 offset:9216\n\t"
  "s_waitcnt lgkmcnt(2)\n\t"
  "v_mfma_f32_16x16x32_bf16 v[48:51], a[112:115], v[12:15], v[48:51]\n\t"
  "buffer_load_dwordx4 v0, s[60:63], s76 offen offset:1024 sc1 lds\n\t"
  "v_mfma_f32_16x16x32_bf16 v[52:55], a[116:119], v[12:15], v[52:55]\n\t"
  "v_mfma_f32_16x16x32_bf16 v[56:59], a[120:123], v[12:15], v[56:59]\n\t"
  "v_mfma_f32_16x16x32_bf16 v[60:63], a[124:127], v[12:15], v[60:63]\n\t"
  "ds_read_b128 v[12:15], v1 offset:10240\n\t"
  "s_waitcnt lgkmcnt(2)\n\t"
  "v_mfma_f32_16x16x32_bf16 v[48:51], a[128:131], v[16:19], v[48:51]\n\t"
  "buffer_load_dwordx4 v0, s[60:63], s76 offen offset:2048 sc1 lds\n\t"
  "v_mfma_f32_16x16x32_bf16 v[52:55], a[132:135], v[16:19], v[52:55]\n\t"
  "v_mfma_f32_16x16x32_bf16 v[56:59], a[136:139], v[16:19], v[56:59]\n\t"
  "v_mfma_f32_16x16x32_bf16 v[60:63], a[140:143], v[16:19], v[60:63]\n\t"
  "ds_read_b128 v[16:19], v1 offset:11264\n\t"
  "s_waitcnt lgkmcnt(2)\n\t"
  "v_mfma_f32_16x16x32_bf16 v[48:51], a[144:147], v[8:11], v[48:51]\n\t"
  "buffer_load_dwordx4 v0, s[60:63], s76 offen offset:3072 sc1 lds\n\t"
  "v_mfma_f32_16x16x32_bf16 v[52:55], a[148:151], v[8:11], v[52:55]\n\t"
  "v_mfma_f32_16x16x32_bf16 v[56:59], a[152:155], v[8:11], v[56:59]\n\t"
  "v_mfma_f32_16x16x32_bf16 v[60:63], a[156:159], v[8:11], v[60:63]\n\t"
  "ds_read_b128 v[8:11], v1 offset:12288\n\t"
  "s_waitcnt lgkmcnt(2)\n\t"
  "v_mfma_f32_16x16x32_bf16 v[48:51], a[160:163], v[12:15], v[48:51]\n\t"
  "v_mfma_f32_16x16x32_bf16 v[52:55], a[164:167], v[12:15], v[52:55]\n\t"
  "v_mfma_f32_16x16x32_bf16 v[56:59], a[168:171], v[12:15], v[56:59]\n\t"
  "v_mfma_f32_16x16x32_bf16 v[60:63], a[172:175], v[12:15], v[60:63]\n\t"
  "ds_read_b128 v[12:15], v1 offset:13312\n\t"
  "s_waitcnt lgkmcnt(2)\n\t"
  "v_mfma_f32_16x16x32_bf16 v[48:51], a[176:179], v[16:19], v[48:51]\n\t"
  "v_cmp_le_i32 vcc, s57, v20\n\t"
  "v_mfma_f32_16x16x32_bf16 v[52:55], a[180:183], v[16:19], v[52:55]\n\t"
  "s_cmp_eq_u32 vcc_hi, -1\n\t"
  "s_cselect_b32 s59, 1, 0\n\t"
  "s_cselect_b32 s62, s51, 0\n\t"
  "s_cselect_b32 s72, s71, 155648\n\t"
  "v_mfma_f32_16x16x32_bf16 v[56:59], a[184:187], v[16:19], v[56:59]\n\t"
  "s_lshl_b32 s73, s59, 12\n\t"
  "v_mfma_f32_16x16x32_bf16 v[60:63], a[188:191], v[16:19], v[60:63]\n\t"
  "ds_read_b128 v[16:19], v1 offset:14336\n\t"
  "s_waitcnt lgkmcnt(2)\n\t"
  "v_mfma_f32_16x16x32_bf16 v[48:51], a[192:195], v[8:11], v[48:51]\n\t"
  "s_mov_b32 m0, s72\n\t"
  "s_add_u32 s76, s68, 0\n\t"
  "buffer_load_dwordx4 v0, s[60:63], s76 offen offset:0 sc1 lds\n\t"
  "v_mfma_f32_16x16x32_bf16 v[52:55], a[196:199], v[8:11], v[52:55]\n\t"
  "s_add_u32 s72, s72, s73\n\t"
  "v_mfma_f32_16x16x32_bf16 v[56:59], a[200:203], v[8:11], v[56:59]\n\t"
  "v_mfma_f32_16x16x32_bf16 v[60:63], a[204:207], v[8:11], v[60:63]\n\t"
  "ds_read_b128 v[8:11], v1 offset:15360\n\t"
  "s_waitcnt lgkmcnt(2)\n\t"
  "v_mfma_f32_16x16x32_bf16 v[48:51], a[208:211], v[12:15], v[48:51]\n\t"
  "s_waitcnt vmcnt(10)\n\t"
  "buffer_store_dword v21, off, s[84:87], s40 sc1\n\t"
  "v_mfma_f32_16x16x32_bf16 v[52:55], a[212:215], v[12:15], v[52:55]\n\t"
  "v_mfma_f32_16x16x32_bf16 v[56:59], a[216:219], v[12:15], v[56:59]\n\t"
  "v_mfma_f32_16x16x32_bf16 v[60:63], a[220:223], v[12:15], v[60:63]\n\t"
  "ds_read_b128 v[12:15], v1 offset:16384\n\t"
  "s_waitcnt lgkmcnt(2)\n\t"
  "v_mfma_f32_16x16x32_bf16 v[48:51], a[224:227], v[16:19], v[48:51]\n\t"
  "s_mov_b32 m0, s72\n\t"
  "s_add_u32 s76, s68, 4096\n\t"
  "buffer_load_dwordx4 v0, s[60:63], s76 offen offset:0 sc1 lds\n\t"
  "v_mfma_f32_16x16x32_bf16 v[52:55], a[228:231], v[16:19], v[52:55]\n\t"
  "s_add_u32 s72, s72, s73\n\t"
  "v_mfma_f32_16x16x32_bf16 v[56:59], a[232:235], v[16:19], v[56:59]\n\t"
  "v_mfma_f32_16x16x32_bf16 v[60:63], a[236:239], v[16:19], v[60:63]\n\t"
  "ds_read_b128 v[16:19], v1 offset:17408\n\t"
  "s_waitcnt lgkmcnt(2)\n\t"
  "v_mfma_f32_16x16x32_bf16 v[48:51], a[240:243], v[8:11], v[48:51]\n\t"
  "s_mov_b32 m0, s72\n\t"
  "s_add_u32 s76, s68, 16384\n\t"
  "buffer_load_dwordx4 v0, s[60:63], s76 offen offset:0 sc1 lds\n\t"
  "v_mfma_f32_16x16x32_bf16 v[52:55], a[244:247], v[8:11], v[52:55]\n\t"
  "s_add_u32 s72, s72, s73\n\t"
  "v_mfma_f32_16x16x32_bf16 v[56:59], a[248:251], v[8:11], v[56:59]\n\t"
  "v_mfma_f32_16x16x32_bf16 v[60:63], a[252:255], v[8:11], v[60:63]\n\t"
  "ds_read_b128 v[8:11], v208 offset:0\n\t"
  "s_waitcnt lgkmcnt(2)\n\t"
  "v_mfma_f32_16x16x32_bf16 v[48:51], v[224:227], v[12:15], v[48:51]\n\t"
  "v_add_u32 v1, s74, v1\n\t"
  "v_mfma_f32_16x16x32_bf16 v[52:55], v[228:231], v[12:15], v[52:55]\n\t"
  "s_mov_b32 m0, s72\n\t"
  "s_add_u32 s76, s68, 20480\n\t"
  "buffer_load_dwordx4 v0, s[60:63], s76 offen offset:0 sc1 lds\n\t"
  "v_mfma_f32_16x16x32_bf16 v[56:59], v[232:235], v[12:15], v[56:59]\n\t"
  "s_add_u32 s72, s72, s73\n\t"
  "v_mfma_f32_16x16x32_bf16 v[60:63], v[236:239], v[12:15], v[60:63]\n\t"
  "ds_read_b128 v[12:15], v208 offset:1024\n\t"
  "s_waitcnt lgkmcnt(2)\n\t"
  "v_mfma_f32_16x16x32_bf16 v[48:51], v[240:243], v[16:19], v[48:51]\n\t"
  "v_mfma_f32_16x16x32_bf16 v[52:55], v[244:247], v[16:19], v[52:55]\n\t"
  "v_mfma_f32_16x16x32_bf16 v[56:59], v[248:251], v[16:19], v[56:59]\n\t"
  "s_mov_b32 m0, s72\n\t"
  "s_add_u32 s76, s68, 32768\n\t"
  "buffer_load_dwordx4 v0, s[60:63], s76 offen offset:0 sc1 lds\n\t"
  "v_mfma_f32_16x16x32_bf16 v[60:63], v[252:255], v[16:19], v[60:63]\n\t"
  "ds_read_b128 v[16:19], v208 offset:2048\n\t"
  "s_waitcnt lgkmcnt(2)\n\t"
  "v_mfma_f32_16x16x32_bf16 v[64:67], a[0:3], v[8:11], 0\n\t"
  "s_add_u32 s72, s72, s73\n\t"
  "v_mfma_f32_16x16x32_bf16 v[68:71], a[4:7], v[8:11], 0\n\t"
  "v_mfma_f32_16x16x32_bf16 v[72:75], a[8:11], v[8:11], 0\n\t"
  "v_cvt_pk_bf16_f32 v48, v48, v49\n\t"
  "v_cvt_pk_bf16_f32 v49, v50, v51\n\t"
  "v_mfma_f32_16x16x32_bf16 v[76:79], a[12:15], v[8:11], 0\n\t"
  "ds_read_b128 v[8:11], v208 offset:3072\n\t"
  "s_waitcnt lgkmcnt(2)\n\t"
  "v_mfma_f32_16x16x32_bf16 v[64:67], a[16:19], v[12:15], v[64:67]\n\t"
  "buffer_load_dword v20, v5, s[36:39], 0 offen sc1\n\t"
  "v_mfma_f32_16x16x32_bf16 v[68:71], a[20:23], v[12:15], v[68:71]\n\t"
  "v_cvt_pk_bf16_f32 v50, v52, v53\n\t"
  "v_cvt_pk_bf16_f32 v51, v54, v55\n\t"
  "v_mfma_f32_16x16x32_bf16 v[72:75], a[24:27], v[12:15], v[72:75]\n\t"
  "ds_write_b128 v22, v[48:51] offset:0\n\t"
  "v_mfma_f32_16x16x32_bf16 v[76:79], a[28:31], v[12:15], v[76:79]\n\t"
  "ds_read_b128 v[12:15], v208 offset:4096\n\t"
  "s_waitcnt lgkmcnt(3)\n\t"
  "v_mfma_f32_16x16x32_bf16 v[64:67], a[32:35], v[16:19], v[64:67]\n\t"
  "s_mov_b32 m0, s72\n\t"
  "s_add_u32 s76, s68, 36864\n\t"
  "buffer_load_dwordx4 v0, s[60:63], s76 offen offset:0 sc1 lds\n\t"
  "v_mfma_f32_16x16x32_bf16 v[68:71], a[36:39], v[16:19], v[68:71]\n\t"
  "s_add_u32 s72, s72, s73\n\t"
  "v_mfma_f32_16x16x32_bf16 v[72:75], a[40:43], v[16:19], v[72:75]\n\t"
  "v_cvt_pk_bf16_f32 v56, v56, v57\n\t"
  "v_cvt_pk_bf16_f32 v57, v58, v59\n\t"
  "v_mfma_f32_16x16x32_bf16 v[76:79], a[44:47], v[16:19], v[76:79]\n\t"
  "ds_read_b128 v[16:19], v208 offset:5120\n\t"
  "s_waitcnt lgkmcnt(3)\n\t"
  "v_mfma_f32_16x16x32_bf16 v[64:67], a[48:51], v[8:11], v[64:67]\n\t"
  "s_mov_b32 m0, s72\n\t"
  "s_add_u32 s76, s68, 49152\n\t"
  "buffer_load_dwordx4 v0, s[60:63], s76 offen offset:0 sc1 lds\n\t"
  "v_mfma_f32_16x16x32_bf16 v[68:71], a[52:55], v[8:11], v[68:71]\n\t"
  "s_add_u32 s72, s72, s73\n\t"
  "v_cvt_pk_bf16_f32 v58, v60, v61\n\t"
  "v_cvt_pk_bf16_f32 v59, v62, v63\n\t"
  "v_mfma_f32_16x16x32_bf16 v[72:75], a[56:59], v[8:11], v[72:75]\n\t"
  "ds_write_b128 v22, v[56:59] offset:1024\n\t"
  "v_mfma_f32_16x16x32_bf16 v[76:79], a[60:63], v[8:11], v[76:79]\n\t"
  "ds_read_b128 v[8:11], v208 offset:6144\n\t"
  "s_waitcnt lgkmcnt(3)\n\t"
  "v_mfma_f32_16x16x32_bf16 v[64:67], a[64:67], v[12:15], v[64:67]\n\t"
  "s_mov_b32 m0, s72\n\t"
  "s_add_u32 s76, s68, 53248\n\t"
  "buffer_load_dwordx4 v0, s[60:63], s76 offen offset:0 sc1 lds\n\t"
  "v_mfma_f32_16x16x32_bf16 v[68:71], a[68:71], v[12:15], v[68:71]\n\t"
  "s_add_u32 s72, s72, s73\n\t"
  "v_mfma_f32_16x16x32_bf16 v[72:75], a[72:75], v[12:15], v[72:75]\n\t"
  "v_mfma_f32_16x16x32_bf16 v[76:79], a[76:79], v[12:15], v[76:79]\n\t"
  "ds_read_b128 v[12:15], v208 offset:7168\n\t"
  "s_waitcnt lgkmcnt(3)\n\t"
  "v_mfma_f32_16x16x32_bf16 v[64:67], a[80:83], v[16:19], v[64:67]\n\t"
  "s_mov_b32 m0, s72\n\t"
  "s_add_u32 s76, s68, 65536\n\t"
  "buffer_load_dwordx4 v0, s[60:63], s76 offen offset:0 sc1 lds\n\t"
  "v_mfma_f32_16x16x32_bf16 v[68:71], a[84:87], v[16:19], v[68:71]\n\t"
  "s_add_u32 s72, s72, s73\n\t"
  "v_mfma_f32_16x16x32_bf16 v[72:75], a[88:91], v[16:19], v[72:75]\n\t"
  "v_mfma_f32_16x16x32_bf16 v[76:79], a[92:95], v[16:19], v[76:79]\n\t"
  "ds_read_b128 v[16:19], v208 offset:8192\n\t"
  "s_waitcnt lgkmcnt(2)\n\t"
  "v_mfma_f32_16x16x32_bf16 v[64:67], a[96:99], v[8:11], v[64:67]\n\t"
  "s_and_b32 s75, s58, s58\n\t"
  "s_cbranch_scc0 20f\n\t"
  "12:\n\t"
  "s_waitcnt vmcnt(11)\n\t"
  "14:\n\t"
  "s_barrier\n\t"
  "v_mfma_f32_16x16x32_bf16 v[68:71], a[100:103], v[8:11], v[68:71]\n\t"
  "ds_read_b128 v[212:215], v23 offset:0\n\t"
  "v_mfma_f32_16x16x32_bf16 v[72:75], a[104:107], v[8:11], v[72:75]\n\t"
  "v_mfma_f32_16x16x32_bf16 v[76:79], a[108:111], v[8:11], v[76:79]\n\t"
  "ds_read_b128 v[8:11], v208 offset:9216\n\t"
  "s_waitcnt lgkmcnt(3)\n\t"
  "v_mfma_f32_16x16x32_bf16 v[64:67], a[112:115], v[12:15], v[64:67]\n\t"
  "v_cmp_le_i32 vcc, s56, v20\n\t"
  "v_mfma_f32_16x16x32_bf16 v[68:71], a[116:119], v[12:15], v[68:71]\n\t"
  "s_bitcmp1_b32 vcc_lo, 4\n\t"
  "s_cselect_b32 s70, 1, 0\n\t"
  "s_cselect_b32 s62, s51, 0\n\t"
  "s_cselect_b32 s72, s65, 155648\n\t"
  "v_mfma_f32_16x16x32_bf16 v[72:75], a[120:123], v[12:15], v[72:75]\n\t"
  "s_mov_b32 m0, s72\n\t"
  "s_add_u32 s76, s69, s45\n\t"
  "buffer_load_dwordx4 v0, s[60:63], s76 offen offset:0 sc1 lds\n\t"
  "v_mfma_f32_16x16x32_bf16 v[76:79], a[124:127], v[12:15], v[76:79]\n\t"
  "ds_read_b128 v[12:15], v208 offset:10240\n\t"
  "s_waitcnt lgkmcnt(3)\n\t"
  "v_mfma_f32_16x16x32_bf16 v[64:67], a[128:131], v[16:19], v[64:67]\n\t"
  "ds_read_b128 v[216:219], v23 offset:1024\n\t"
  "v_mfma_f32_16x16x32_bf16 v[68:71], a[132:135], v[16:19], v[68:71]\n\t"
  "v_mfma_f32_16x16x32_bf16 v[72:75], a[136:139], v[16:19], v[72:75]\n\t"
  "v_mfma_f32_16x16x32_bf16 v[76:79], a[140:143], v[16:19], v[76:79]\n\t"
  "ds_read_b128 v[16:19], v208 offset:11264\n\t"
  "s_waitcnt lgkmcnt(3)\n\t"
  "v_mfma_f32_16x16x32_bf16 v[64:67], a[144:147], v[8:11], v[64:67]\n\t"
  "buffer_load_dwordx4 v0, s[60:63], s76 offen offset:1024 sc1 lds\n\t"
  "v_mfma_f32_16x16x32_bf16 v[68:71], a[148:151], v[8:11], v[68:71]\n\t"
  "ds_read_b128 v[96:99], v205 offset:0\n\t"
  "v_mfma_f32_16x16x32_bf16 v[72:75], a[152:155], v[8:11], v[72:75]\n\t"
  "v_mfma_f32_16x16x32_bf16 v[76:79], a[156:159], v[8:11], v[76:79]\n\t"
  "ds_read_b128 v[8:11], v208 offset:12288\n\t"
  "s_waitcnt lgkmcnt(4)\n\t"
  "v_mfma_f32_16x16x32_bf16 v[64:67], a[160:163], v[12:15], v[64:67]\n\t"
  "buffer_load_dwordx4 v0, s[60:63], s76 offen offset:2048 sc1 lds\n\t"
  "v_mfma_f32_16x16x32_bf16 v[68:71], a[164:167], v[12:15], v[68:71]\n\t"
  "ds_read_b128 v[100:103], v205 offset:4096\n\t"
  "v_mfma_f32_16x16x32_bf16 v[72:75], a[168:171], v[12:15], v[72:75]\n\t"
  "v_mfma_f32_16x16x32_bf16 v[76:79], a[172:175], v[12:15], v[76:79]\n\t"
  "ds_read_b128 v[12:15], v208 offset:13312\n\t"
  "s_waitcnt lgkmcnt(4)\n\t"
  "v_mfma_f32_16x16x32_bf16 v[64:67], a[176:179], v[16:19], v[64:67]\n\t"
  "buffer_load_dwordx4 v0, s[60:63], s76 offen offset:3072 sc1 lds\n\t"
  "v_mfma_f32_16x16x32_bf16 v[68:71], a[180:183], v[16:19], v[68:71]\n\t"
  "ds_read_b128 v[104:107], v205 offset:1024\n\t"
  "v_mfma_f32_16x16x32_bf16 v[72:75], a[184:187], v[16:19], v[72:75]\n\t"
  "v_mfma_f32_16x16x32_bf16 v[76:79], a[188:191], v[16:19], v[76:79]\n\t"
  "ds_read_b128 v[16:19], v208 offset:14336\n\t"
  "s_waitcnt lgkmcnt(4)\n\t"
  "v_mfma_f32_16x16x32_bf16 v[64:67], a[192:195], v[8:11], v[64:67]\n\t"
  "ds_read_b128 v[108:111], v205 offset:5120\n\t"
  "v_mfma_f32_16x16x32_bf16 v[68:71], a[196:199], v[8:11], v[68:71]\n\t"
  "v_mfma_f32_16x16x32_bf16 v[72:75], a[200:203], v[8:11], v[72:75]\n\t"
  "v_mfma_f32_16x16x32_bf16 v[76:79], a[204:207], v[8:11], v[76:79]\n\t"
  "ds_read_b128 v[8:11], v208 offset:15360\n\t"
  "s_waitcnt lgkmcnt(4)\n\t"
  "v_mfma_f32_16x16x32_bf16 v[64:67], a[208:211], v[12:15], v[64:67]\n\t"
  "buffer_load_dword v20, v5, s[36:39], 0 offen sc1\n\t"
  "v_mfma_f32_16x16x32_bf16 v[68:71], a[212:215], v[12:15], v[68:71]\n\t"
  "ds_read_b128 v[112:115], v204 offset:0\n\t"
  "v_mfma_f32_16x16x32_bf16 v[72:75], a[216:219], v[12:15], v[72:75]\n\t"
  "v_mfma_f32_16x16x32_bf16 v[76:79], a[220:223], v[12:15], v[76:79]\n\t"
  "ds_read_b128 v[12:15], v208 offset:16384\n\t"
  "s_waitcnt lgkmcnt(4)\n\t"
  "v_mfma_f32_16x16x32_bf16 v[64:67], a[224:227], v[16:19], v[64:67]\n\t"
  "ds_read_b128 v[116:119], v204 offset:4096\n\t"
  "v_mfma_f32_16x16x32_bf16 v[68:71], a[228:231], v[16:19], v[68:71]\n\t"
  "v_mfma_f32_16x16x32_bf16 v[72:75], a[232:235], v[16:19], v[72:75]\n\t"
  "v_mfma_f32_16x16x32_bf16 v[76:79], a[236:239], v[16:19], v[76:79]\n\t"
  "ds_read_b128 v[16:19], v208 offset:17408\n\t"
  "s_waitcnt lgkmcnt(4)\n\t"
  "v_mfma_f32_16x16x32_bf16 v[64:67], a[240:243], v[8:11], v[64:67]\n\t"
  "ds_read_b128 v[120:123], v204 offset:1024\n\t"
  "v_mfma_f32_16x16x32_bf16 v[68:71], a[244:247], v[8:11], v[68:71]\n\t"
  "v_mfma_f32_16x16x32_bf16 v[72:75], a[248:251], v[8:11], v[72:75]\n\t"
  "v_mfma_f32_16x16x32_bf16 v[76:79], a[252:255], v[8:11], v[76:79]\n\t"
  "s_waitcnt lgkmcnt(3)\n\t"
  "v_mfma_f32_16x16x32_bf16 v[64:67], v[224:227], v[12:15], v[64:67]\n\t"
  "v_add_u32 v208, s74, v208\n\t"
  "v_mfma_f32_16x16x32_bf16 v[68:71], v[228:231], v[12:15], v[68:71]\n\t"
  "ds_read_b128 v[124:127], v204 offset:5120\n\t"
  "v_mfma_f32_16x16x32_bf16 v[72:75], v[232:235], v[12:15], v[72:75]\n\t"
  "v_mfma_f32_16x16x32_bf16 v[76:79], v[236:239], v[12:15], v[76:79]\n\t"
  "s_waitcnt lgkmcnt(2)\n\t"
  "v_mfma_f32_16x16x32_bf16 v[64:67], v[240:243], v[16:19], v[64:67]\n\t"
  "v_mfma_f32_16x16x32_bf16 v[68:71], v[244:247], v[16:19], v[68:71]\n\t"
  "v_mfma_f32_16x16x32_bf16 v[72:75], v[248:251], v[16:19], v[72:75]\n\t"
  "v_mfma_f32_16x16x32_bf16 v[76:79], v[252:255], v[16:19], v[76:79]\n\t"
  "v_mfma_f32_16x16x32_bf16 v[160:163], v[96:99], v[212:215], 0\n\t"
  "v_cmp_le_i32 vcc, s56, v20\n\t"
  "v_mfma_f32_16x16x32_bf16 v[164:167], v[100:103], v[212:215], 0\n\t"
  "v_cvt_pk_bf16_f32 v64, v64, v65\n\t"
  "v_cvt_pk_bf16_f32 v65, v66, v67\n\t"
  "v_mfma_f32_16x16x32_bf16 v[160:163], v[104:107], v[216:219], v[160:163]\n\t"
  "v_cvt_pk_bf16_f32 v66, v68, v69\n\t"
  "v_cvt_pk_bf16_f32 v67, v70, v71\n\t"
  "v_mfma_f32_16x16x32_bf16 v[164:167], v[108:111], v[216:219], v[164:167]\n\t"
  "v_cvt_pk_bf16_f32 v72, v72, v73\n\t"
  "v_cvt_pk_bf16_f32 v73, v74, v75\n\t"
  "v_mfma_f32_16x16x32_bf16 v[160:163], v[112:115], v[64:67], v[160:163]\n\t"
  "v_cvt_pk_bf16_f32 v74, v76, v77\n\t"
  "v_cvt_pk_bf16_f32 v75, v78, v79\n\t"
  "v_mfma_f32_16x16x32_bf16 v[164:167], v[116:119], v[64:67], v[164:167]\n\t"
  "s_add_u32 s77, s69, s47\n\t"
  "s_waitcnt lgkmcnt(1)\n\t"
  "v_mfma_f32_16x16x32_bf16 v[160:163], v[120:123], v[72:75], v[160:163]\n\t"
  "s_waitcnt lgkmcnt(0)\n\t"
  "v_mfma_f32_16x16x32_bf16 v[164:167], v[124:127], v[72:75], v[164:167]\n\t"
  "s_cmp_eq_u32 s70, 1\n\t"
  "s_cbranch_scc0 30f\n\t"
  "s_waitcnt vmcnt(1)\n\t"
  "13:\n\t"
  "ds_read_b128 v[80:83], v207 offset:0\n\t"
  "ds_read_b128 v[84:87], v207 offset:512\n\t"
  "ds_read_b128 v[88:91], v206 offset:0\n\t"
  "ds_read_b128 v[92:95], v206 offset:512\n\t"
  "ds_read2st64_b64 v[188:191], v7 offset0:4 offset1:5\n\t"
  "ds_read2st64_b64 v[192:195], v7 offset0:6 offset1:7\n\t"
  "v_med3_f32 v160, v160, v32, v40\n\t"
  "v_med3_f32 v161, v161, v33, v41\n\t"
  "v_med3_f32 v162, v162, v34, v42\n\t"
  "v_med3_f32 v163, v163, v35, v43\n\t"
  "v_med3_f32 v164, v164, v36, v44\n\t"
  "v_med3_f32 v165, v165, v37, v45\n\t"
  "v_med3_f32 v166, v166, v38, v46\n\t"
  "v_med3_f32 v167, v167, v39, v47\n\t"
  "s_waitcnt lgkmcnt(5)\n\t"
  "v_mfma_f32_16x16x32_bf16 v[168:171], v[80:83], v[212:215], 0\n\t"
  "ds_read_b128 v[128:131], v24 offset:8192\n\t"
  "ds_read_b128 v[132:135], v24 offset:9216\n\t"
  "s_waitcnt lgkmcnt(6)\n\t"
  "v_mfma_f32_16x16x32_bf16 v[168:171], v[84:87], v[216:219], v[168:171]\n\t"
  "ds_read_b128 v[136:139], v24 offset:10240\n\t"
  "ds_read_b128 v[140:143], v24 offset:11264\n\t"
  "s_waitcnt lgkmcnt(7)\n\t"
  "v_mfma_f32_16x16x32_bf16 v[168:171], v[88:91], v[64:67], v[168:171]\n\t"
  "ds_read_b128 v[144:147], v24 offset:12288\n\t"
  "ds_read_b128 v[148:151], v24 offset:13312\n\t"
  "s_waitcnt lgkmcnt(8)\n\t"
  "v_mfma_f32_16x16x32_bf16 v[168:171], v[92:95], v[72:75], v[168:171]\n\t"
  "ds_read_b128 v[152:155], v24 offset:14336\n\t"
  "ds_read_b128 v[156:159], v24 offset:15360\n\t"
  "v_max3_f32 v26, v160, v161, v162\n\t"
  "v_max3_f32 v27, v163, v164, v165\n\t"
  "v_max3_f32 v26, v26, v166, v167\n\t"
  "v_max_f32 v26, v26, v27\n\t"
  "s_nop 1\n\t"
  "v_max3_f32 v27, v26, v170, v171\n\t"
  "v_max3_f32 v26, v26, v170, v171\n\t"
  "s_nop 1\n\t"
  "v_permlane32_swap_b32 v26, v27\n\t"
  "v_max_f32 v28, v26, v27\n\t"
  "v_max_f32 v26, v26, v27\n\t"
  "s_nop 1\n\t"
  "v_permlane16_swap_b32 v26, v28\n\t"
  "v_max_f32 v26, v26, v28\n\t"
  "v_mul_f32 v26, 0xbe0293ee, v26\n\t"
  "v_fmamk_f32 v160, v160, 0x3e0293ee, v26\n\t"
  "v_fmamk_f32 v161, v161, 0x3e0293ee, v26\n\t"
  "v_fmamk_f32 v162, v162, 0x3e0293ee, v26\n\t"
  "v_fmamk_f32 v163, v163, 0x3e0293ee, v26\n\t"
  "v_fmamk_f32 v164, v164, 0x3e0293ee, v26\n\t"
  "v_fmamk_f32 v165, v165, 0x3e0293ee, v26\n\t"
  "v_fmamk_f32 v166, v166, 0x3e0293ee, v26\n\t"
  "v_fmamk_f32 v167, v167, 0x3e0293ee, v26\n\t"
  "v_exp_f32 v160, v160\n\t"
  "v_exp_f32 v161, v161\n\t"
  "v_exp_f32 v162, v162\n\t"
  "v_exp_f32 v163, v163\n\t"
  "v_exp_f32 v164, v164\n\t"
  "v_exp_f32 v165, v165\n\t"
  "v_exp_f32 v166, v166\n\t"
  "v_exp_f32 v167, v167\n\t"
  "v_fmamk_f32 v170, v170, 0x3e0293ee, v26\n\t"
  "v_cvt_pk_bf16_f32 v172, v160, v161\n\t"
  "v_cvt_pk_bf16_f32 v173, v162, v163\n\t"
  "v_cvt_pk_bf16_f32 v174, v164, v165\n\t"
  "v_cvt_pk_bf16_f32 v175, v166, v167\n\t"
  "v_fmamk_f32 v171, v171, 0x3e0293ee, v26\n\t"
  "v_exp_f32 v170, v170\n\t"
  "v_mfma_f32_16x16x32_bf16 v[184:187], v[180:183], v[172:175], 0\n\t"
  "v_exp_f32 v171, v171\n\t"
  "s_waitcnt lgkmcnt(7)\n\t"
  "v_mfma_f32_16x16x32_bf16 v[96:99], v[128:131], v[172:175], 0\n\t"
  "s_waitcnt lgkmcnt(6)\n\t"
  "v_mfma_f32_16x16x32_bf16 v[100:103], v[132:135], v[172:175], 0\n\t"
  "v_cvt_pk_bf16_f32 v176, v170, v171\n\t"
  "v_cvt_pk_bf16_f32 v179, v170, v171\n\t"
  "s_waitcnt lgkmcnt(5)\n\t"
  "v_mfma_f32_16x16x32_bf16 v[104:107], v[136:139], v[172:175], 0\n\t"
  "s_waitcnt lgkmcnt(4)\n\t"
  "v_mfma_f32_16x16x32_bf16 v[108:111], v[140:143], v[172:175], 0\n\t"
  "v_mfma_f32_16x16x16_bf16 v[184:187], v[180:181], v[176:177], v[184:187]\n\t"
  "v_mfma_f32_16x16x16_bf16 v[96:99], v[188:189], v[176:177], v[96:99]\n\t"
  "v_mfma_f32_16x16x16_bf16 v[100:103], v[188:189], v[178:179], v[100:103]\n\t"
  "v_rcp_f32 v184, v184\n\t"
  "v_mfma_f32_16x16x16_bf16 v[104:107], v[190:191], v[176:177], v[104:107]\n\t"
  "v_mfma_f32_16x16x16_bf16 v[108:111], v[190:191], v[178:179], v[108:111]\n\t"
  "v_mul_f32 v96, v96, v184\n\t"
  "v_mul_f32 v97, v97, v184\n\t"
  "s_waitcnt lgkmcnt(3)\n\t"
  "v_mfma_f32_16x16x32_bf16 v[112:115], v[144:147], v[172:175], 0\n\t"
  "v_mul_f32 v98, v98, v184\n\t"
  "v_mul_f32 v99, v99, v184\n\t"
  "s_waitcnt lgkmcnt(2)\n\t"
  "v_mfma_f32_16x16x32_bf16 v[116:119], v[148:151], v[172:175], 0\n\t"
  "v_mul_f32 v100, v100, v184\n\t"
  "v_mul_f32 v101, v101, v184\n\t"
  "s_waitcnt lgkmcnt(1)\n\t"
  "v_mfma_f32_16x16x32_bf16 v[120:123], v[152:155], v[172:175], 0\n\t"
  "v_mul_f32 v102, v102, v184\n\t"
  "v_mul_f32 v103, v103, v184\n\t"
  "s_waitcnt lgkmcnt(0)\n\t"
  "v_mfma_f32_16x16x32_bf16 v[124:127], v[156:159], v[172:175], 0\n\t"
  "v_cvt_pk_bf16_f32 v96, v96, v97\n\t"
  "v_cvt_pk_bf16_f32 v97, v98, v99\n\t"
  "v_mfma_f32_16x16x16_bf16 v[112:115], v[192:193], v[176:177], v[112:115]\n\t"
  "v_cvt_pk_bf16_f32 v98, v100, v101\n\t"
  "v_cvt_pk_bf16_f32 v99, v102, v103\n\t"
  "v_mfma_f32_16x16x16_bf16 v[116:119], v[192:193], v[178:179], v[116:119]\n\t"
  "buffer_store_dwordx4 v[96:99], v0, s[36:39], s77 offen offset:0 sc1\n\t"
  "v_mul_f32 v104, v104, v184\n\t"
  "v_mfma_f32_16x16x16_bf16 v[120:123], v[194:195], v[176:177], v[120:123]\n\t"
  "v_mul_f32 v105, v105, v184\n\t"
  "v_mul_f32 v106, v106, v184\n\t"
  "v_mfma_f32_16x16x16_bf16 v[124:127], v[194:195], v[178:179], v[124:127]\n\t"
  "v_mul_f32 v107, v107, v184\n\t"
  "v_mul_f32 v108, v108, v184\n\t"
  "v_mul_f32 v109, v109, v184\n\t"
  "v_mul_f32 v110, v110, v184\n\t"
  "v_mul_f32 v111, v111, v184\n\t"
  "v_cvt_pk_bf16_f32 v104, v104, v105\n\t"
  "v_cvt_pk_bf16_f32 v105, v106, v107\n\t"
  "v_cvt_pk_bf16_f32 v106, v108, v109\n\t"
  "v_cvt_pk_bf16_f32 v107, v110, v111\n\t"
  "buffer_store_dwordx4 v[104:107], v0, s[36:39], s77 offen offset:1024 sc1\n\t"
  "v_pk_mul_f32 v[112:113], v[112:113], v[184:185] op_sel_hi:[1,0]\n\t"
  "v_pk_mul_f32 v[114:115], v[114:115], v[184:185] op_sel_hi:[1,0]\n\t"
  "v_pk_mul_f32 v[116:117], v[116:117], v[184:185] op_sel_hi:[1,0]\n\t"
  "v_pk_mul_f32 v[118:119], v[118:119], v[184:185] op_sel_hi:[1,0]\n\t"
  "v_cvt_pk_bf16_f32 v112, v112, v113\n\t"
  "v_cvt_pk_bf16_f32 v113, v114, v115\n\t"
  "v_cvt_pk_bf16_f32 v114, v116, v117\n\t"
  "v_cvt_pk_bf16_f32 v115, v118, v119\n\t"
  "buffer_store_dwordx4 v[112:115], v0, s[36:39], s77 offen offset:2048 sc1\n\t"
  "v_pk_mul_f32 v[120:121], v[120:121], v[184:185] op_sel_hi:[1,0]\n\t"
  "v_pk_mul_f32 v[122:123], v[122:123], v[184:185] op_sel_hi:[1,0]\n\t"
  "v_pk_mul_f32 v[124:125], v[124:125], v[184:185] op_sel_hi:[1,0]\n\t"
  "v_pk_mul_f32 v[126:127], v[126:127], v[184:185] op_sel_hi:[1,0]\n\t"
  "v_cvt_pk_bf16_f32 v120, v120, v121\n\t"
  "v_cvt_pk_bf16_f32 v121, v122, v123\n\t"
  "v_cvt_pk_bf16_f32 v122, v124, v125\n\t"
  "v_cvt_pk_bf16_f32 v123, v126, v127\n\t"
  "buffer_store_dwordx4 v[120:123], v0, s[36:39], s77 offen offset:3072 sc1\n\t"
  "s_mov_b32 s86, s51\n\t"
  "s_cmp_lg_u32 s59, 0\n\t"
  "s_waitcnt vmcnt(4)\n\t"
  "s_cbranch_scc1 1b\n\t"
  "s_mov_b32 s86, 0\n\t"
  "v_mov_b32 v21, s56\n\t"
  "s_mov_b32 s79, 0\n\t"
  "buffer_load_dword v20, v5, s[36:39], 0 offen sc1\n\t"
  "s_waitcnt vmcnt(0)\n\t"
  "buffer_store_dword v21, off, s[36:39], s40 sc1\n\t"
  "s_cmp_eq_u32 s56, 63\n\t"
  "s_cbranch_scc1 9f\n\t"
  "s_branch 83f\n\t"
  "8:\n\t"
  "s_mov_b32 s79, 0\n\t"
  "81:\n\t"
  "buffer_load_dword v20, v5, s[36:39], 0 offen sc1\n\t"
  "s_waitcnt vmcnt(0)\n\t"
  "83:\n\t"
  "v_cmp_le_i32 vcc, s57, v20\n\t"
  "s_cmp_eq_u32 vcc_hi, -1\n\t"
  "s_cbranch_scc1 82f\n\t"
  "s_sleep 1\n\t"
  "s_add_u32 s79, s79, 1\n\t"
  "s_cmp_lt_u32 s79, s80\n\t"
  "s_cbranch_scc1 81b\n\t"
  "s_mov_b32 s80, 0\n\t"
  "82:\n\t"
  "s_add_u32 m0, s71, 0\n\t"
  "s_add_u32 s76, s68, 0\n\t"
  "buffer_load_dwordx4 v0, s[36:39], s76 offen offset:0 sc1 lds\n\t"
  "s_add_u32 m0, s71, 4096\n\t"
  "s_add_u32 s76, s68, 4096\n\t"
  "buffer_load_dwordx4 v0, s[36:39], s76 offen offset:0 sc1 lds\n\t"
  "s_add_u32 m0, s71, 8192\n\t"
  "s_add_u32 s76, s68, 16384\n\t"
  "buffer_load_dwordx4 v0, s[36:39], s76 offen offset:0 sc1 lds\n\t"
  "s_add_u32 m0, s71, 12288\n\t"
  "s_add_u32 s76, s68, 20480\n\t"
  "buffer_load_dwordx4 v0, s[36:39], s76 offen offset:0 sc1 lds\n\t"
  "s_add_u32 m0, s71, 16384\n\t"
  "s_add_u32 s76, s68, 32768\n\t"
  "buffer_load_dwordx4 v0, s[36:39], s76 offen offset:0 sc1 lds\n\t"
  "s_add_u32 m0, s71, 20480\n\t"
  "s_add_u32 s76, s68, 36864\n\t"
  "buffer_load_dwordx4 v0, s[36:39], s76 offen offset:0 sc1 lds\n\t"
  "s_add_u32 m0, s71, 24576\n\t"
  "s_add_u32 s76, s68, 49152\n\t"
  "buffer_load_dwordx4 v0, s[36:39], s76 offen offset:0 sc1 lds\n\t"
  "s_add_u32 m0, s71, 28672\n\t"
  "s_add_u32 s76, s68, 53248\n\t"
  "buffer_load_dwordx4 v0, s[36:39], s76 offen offset:0 sc1 lds\n\t"
  "s_add_u32 m0, s71, 32768\n\t"
  "s_add_u32 s76, s68, 65536\n\t"
  "buffer_load_dwordx4 v0, s[36:39], s76 offen offset:0 sc1 lds\n\t"
  "buffer_load_dword v20, v5, s[36:39], 0 offen sc1\n\t"
  "s_waitcnt vmcnt(0)\n\t"
  "s_branch 1b\n\t"
  "20:\n\t"
  "s_mov_b32 s66, m0\n\t"
  "s_mov_b32 s67, s76\n\t"
  "s_cmp_lg_u32 s58, 0\n\t"
  "s_cbranch_scc1 23f\n\t"
  "s_mov_b32 s79, 0\n\t"
  "21:\n\t"
  "v_cmp_le_i32 vcc, s56, v20\n\t"
  "s_and_b32 s75, vcc_lo, 15\n\t"
  "s_cmp_eq_u32 s75, 15\n\t"
  "s_cbranch_scc1 22f\n\t"
  "buffer_load_dword v20, v5, s[36:39], 0 offen sc1\n\t"
  "s_waitcnt vmcnt(0)\n\t"
  "s_sleep 1\n\t"
  "s_add_u32 s79, s79, 1\n\t"
  "s_cmp_lt_u32 s79, s80\n\t"
  "s_cbranch_scc1 21b\n\t"
  "s_mov_b32 s80, 0\n\t"
  "22:\n\t"
  "s_mov_b32 m0, s64\n\t"
  "s_add_u32 s76, s69, s44\n\t"
  "buffer_load_dwordx4 v0, s[36:39], s76 offen offset:0 sc1 lds\n\t"
  "buffer_load_dwordx4 v0, s[36:39], s76 offen offset:1024 sc1 lds\n\t"
  "buffer_load_dwordx4 v0, s[36:39], s76 offen offset:2048 sc1 lds\n\t"
  "buffer_load_dwordx4 v0, s[36:39], s76 offen offset:3072 sc1 lds\n\t"
  "s_add_u32 m0, s64, 4096\n\t"
  "s_add_u32 s76, s76, 4096\n\t"
  "buffer_load_dwordx4 v0, s[36:39], s76 offen offset:0 sc1 lds\n\t"
  "buffer_load_dwordx4 v0, s[36:39], s76 offen offset:1024 sc1 lds\n\t"
  "buffer_load_dwordx4 v0, s[36:39], s76 offen offset:2048 sc1 lds\n\t"
  "buffer_load_dwordx4 v0, s[36:39], s76 offen offset:3072 sc1 lds\n\t"
  "s_waitcnt vmcnt(0)\n\t"
  "23:\n\t"
  "s_waitcnt vmcnt(11)\n\t"
  "s_branch 24f\n\t"
  "v_cmp_le_i32 vcc, s56, v20\n\t"
  "s_lshr_b32 s75, vcc_lo, 4\n\t"
  "s_andn2_b32 s75, s75, s70\n\t"
  "s_bitcmp1_b32 s75, 0\n\t"
  "s_cbranch_scc0 24f\n\t"
  "s_mov_b32 m0, s65\n\t"
  "s_add_u32 s76, s69, s45\n\t"
  "buffer_load_dwordx4 v0, s[36:39], s76 offen offset:0 sc1 lds\n\t"
  "buffer_load_dwordx4 v0, s[36:39], s76 offen offset:1024 sc1 lds\n\t"
  "buffer_load_dwordx4 v0, s[36:39], s76 offen offset:2048 sc1 lds\n\t"
  "buffer_load_dwordx4 v0, s[36:39], s76 offen offset:3072 sc1 lds\n\t"
  "s_mov_b32 s70, 2\n\t"
  "24:\n\t"
  "s_mov_b32 m0, s66\n\t"
  "s_mov_b32 s76, s67\n\t"
  "s_branch 14b\n\t"
  "30:\n\t"
  "s_cmp_eq_u32 s70, 2\n\t"
  "s_cbranch_scc0 35f\n\t"
  "s_waitcnt vmcnt(5)\n\t"
  "s_branch 13b\n\t"
  "35:\n\t"
  "s_bitcmp1_b32 vcc_lo, 4\n\t"
  "s_cbranch_scc1 34f\n\t"
  "s_mov_b32 s79, 0\n\t"
  "31:\n\t"
  "buffer_load_dword v20, v5, s[36:39], 0 offen sc1\n\t"
  "s_waitcnt vmcnt(0)\n\t"
  "v_cmp_le_i32 vcc, s56, v20\n\t"
  "s_bitcmp1_b32 vcc_lo, 4\n\t"
  "s_cbranch_scc1 34f\n\t"
  "s_cmp_lg_u32 s59, 0\n\t"
  "s_cbranch_scc1 33f\n\t"
  "v_cmp_le_i32 vcc, s57, v20\n\t"
  "s_cmp_eq_u32 vcc_hi, -1\n\t"
  "s_cbranch_scc0 33f\n\t"
  "s_add_u32 m0, s71, 0\n\t"
  "s_add_u32 s76, s68, 0\n\t"
  "buffer_load_dwordx4 v0, s[36:39], s76 offen offset:0 sc1 lds\n\t"
  "s_add_u32 m0, s71, 4096\n\t"
  "s_add_u32 s76, s68, 4096\n\t"
  "buffer_load_dwordx4 v0, s[36:39], s76 offen offset:0 sc1 lds\n\t"
  "s_add_u32 m0, s71, 8192\n\t"
  "s_add_u32 s76, s68, 16384\n\t"
  "buffer_load_dwordx4 v0, s[36:39], s76 offen offset:0 sc1 lds\n\t"
  "s_add_u32 m0, s71, 12288\n\t"
  "s_add_u32 s76, s68, 20480\n\t"
  "buffer_load_dwordx4 v0, s[36:39], s76 offen offset:0 sc1 lds\n\t"
  "s_add_u32 m0, s71, 16384\n\t"
  "s_add_u32 s76, s68, 32768\n\t"
  "buffer_load_dwordx4 v0, s[36:39], s76 offen offset:0 sc1 lds\n\t"
  "s_add_u32 m0, s71, 20480\n\t"
  "s_add_u32 s76, s68, 36864\n\t"
  "buffer_load_dwordx4 v0, s[36:39], s76 offen offset:0 sc1 lds\n\t"
  "s_add_u32 m0, s71, 24576\n\t"
  "s_add_u32 s76, s68, 49152\n\t"
  "buffer_load_dwordx4 v0, s[36:39], s76 offen offset:0 sc1 lds\n\t"
  "s_add_u32 m0, s71, 28672\n\t"
  "s_add_u32 s76, s68, 53248\n\t"
  "buffer_load_dwordx4 v0, s[36:39], s76 offen offset:0 sc1 lds\n\t"
  "s_add_u32 m0, s71, 32768\n\t"
  "s_add_u32 s76, s68, 65536\n\t"
  "buffer_load_dwordx4 v0, s[36:39], s76 offen offset:0 sc1 lds\n\t"
  "s_mov_b32 s59, 1\n\t"
  "33:\n\t"
  "s_sleep 1\n\t"
  "s_add_u32 s79, s79, 1\n\t"
  "s_cmp_lt_u32 s79, s80\n\t"
  "s_cbranch_scc1 31b\n\t"
  "s_mov_b32 s80, 0\n\t"
  "34:\n\t"
  "s_mov_b32 m0, s65\n\t"
  "s_add_u32 s76, s69, s45\n\t"
  "buffer_load_dwordx4 v0, s[36:39], s76 offen offset:0 sc1 lds\n\t"
  "buffer_load_dwordx4 v0, s[36:39], s76 offen offset:1024 sc1 lds\n\t"
  "buffer_load_dwordx4 v0, s[36:39], s76 offen offset:2048 sc1 lds\n\t"
  "buffer_load_dwordx4 v0, s[36:39], s76 offen offset:3072 sc1 lds\n\t"
  "s_waitcnt vmcnt(0)\n\t"
  "s_branch 13b\n\t"
  "9:\n\t"
  "s_waitcnt vmcnt(0) lgkmcnt(0)\n\t"
  : "+{v[0:7]}"(lc), "+{v[8:23]}"(so) :: CLOB_ALL);
}

struct NormOff { int v[5]; };
__device__ __forceinline__ NormOff norm_off(int lane, int tp) {
  const int m = (lane >> 4) & 1, hi = lane >> 5, tb = (lane >> 3) & 1, x = xn_off(tp >> 1, (lane >> 1) & 3, (tp & 1) * 2 + (lane & 1));
  NormOff r;
  #pragma unroll
  for (int k = 0; k < 4; ++k) r.v[k] = opaque(spd((m * 18 + 4 * k + 2 * hi + tb) * 1024) + x);
  r.v[4] = opaque(spd((m * 18 + 16 + hi) * 1024) + x + tb * 8);
  return r;
}
__device__ __forceinline__ int row_off(int lane, int tp) { return ((16 * ((lane >> 4) & 1) + 2 * tp + (lane & 1)) * DM + ((lane >> 1) & 3) * 4) * 2; }
__device__ __forceinline__ int col_off(int lane, int u) { return (u < 8 ? 8 * (u >> 1) + (u & 1) + 4 * (lane >> 5) + 2 * ((lane >> 3) & 1) : 32 + 2 * (lane >> 5) + ((lane >> 3) & 1)) * 32; }
#define N_AHEAD 8
template <int stage, int layer> __device__ __forceinline__ void role_e(Ctx& c, const Params& p) {
  const int tp = stage == 0 ? 4 * c.layer + c.w : 2 * (c.idx - R_E) + (c.w & 1), l = c.lane;
  if (c.w == 0 && l < 2) *(volatile LAS int*)(c.lds + 16 * 9216 + l * 4) = -1;
  bar();
  STAMP(65, 0);
  const NormOff no = norm_off(l, tp);
  v16u so = {};
  #pragma unroll
  for (int k = 0; k < 5; ++k) so[k] = no.v[k];
  so[5] = row_off(l, tp) + col_off(l, 0); so[6] = row_off(l, tp) + col_off(l, 8);
  #pragma unroll
  for (int e = 0; e < 8; ++e) so[8 + e] = ((l & 15) >> 1) == e ? ((l & 1) ? 0x3F800000u : 0x00003F80u) : 0u;
  const bool res = stage == 1 && l >= 48;
  const int rf = flag_at(c.layer, F_XN + tp, l >= 56 ? 4 : 0);
  c.myflag = stage == 1 ? flag_at(c.layer, F_XN1 + tp, 0) : layer == 1 ? flag_at(0, F_END + tp, 0) : flag_at(stage == 0 ? 0 : 1, F_XN + tp, 0);
  const bool gate = stage != 1 && layer == 0 && l >= 48;
  const int gf = stage == 0 ? flag_at(1, F_XN + ((l >> 1) & 7), 0) : flag_at(0, F_END + ((l >> 1) & 7), 0);
  const int ph = stage == 0 ? c.layer * 8192 : stage == 2 ? 8192 : 0;
  v8u lc = {(unsigned)(l * 16), (unsigned)((c.w & 1) * 9216 + l * 16),
            (unsigned)(gate ? gf : stage == 0 ? flag_at(0, R_CH + tp, 3) : res ? rf : flag_at(c.layer, (stage == 1 ? R_B : R_D) + min(l >> 2, 11), l & 3)), 0u,
            (unsigned)((stage == 1 ? base_c(c, l) + C_P1 : base_a(c, l) + A_OF) + tp * sp(24 * 1024)),
            (unsigned)(stage == 1 ? base_b(c, l) + B_XN1 : (base_a(c, l) ^ ph) + A_XN),
            (unsigned)((base_b(c, l) ^ ph) + B_XRES + tp * sp(8192)), gate ? (unsigned)-RINGN : stage == 0 ? (unsigned)-N_AHEAD : !res ? 0u : l >= 56 ? (unsigned)-62 : 2u};
  lc[3] = stage_scalars(c, stage == 0 ? p.x : p.out, 2048 * DM * 2, 0, 0, 0, 0, 0, 0, 0, 0, 0);
  if constexpr (stage == 0) asm volatile(
  "s_nop 4\n\t"
  "v_readlane_b32 s36, v3, 0\n\t"
  "v_readlane_b32 s37, v3, 1\n\t"
  "v_readlane_b32 s38, v3, 2\n\t"
  "v_readlane_b32 s39, v3, 3\n\t"
  "v_readlane_b32 s40, v3, 4\n\t"
  "v_readlane_b32 s41, v3, 5\n\t"
  "v_readlane_b32 s42, v3, 6\n\t"
  "v_readlane_b32 s43, v3, 7\n\t"
  "v_readlane_b32 s44, v3, 8\n\t"
  "v_readlane_b32 s45, v3, 9\n\t"
  "v_readlane_b32 s46, v3, 10\n\t"
  "v_readlane_b32 s47, v3, 11\n\t"
  "v_readlane_b32 s48, v3, 12\n\t"
  "v_readlane_b32 s49, v3, 13\n\t"
  "v_readlane_b32 s50, v3, 14\n\t"
  "v_readlane_b32 s51, v3, 15\n\t"
  "v_readlane_b32 s56, v3, 16\n\t"
  "v_readlane_b32 s57, v3, 17\n\t"
  "v_readlane_b32 s58, v3, 18\n\t"
  "s_mov_b32 s59, s39\n\t"
  "s_mov_b32 s72, 0\n\t"
  "s_mov_b32 s80, 0x40000\n\t"
  "s_mov_b32 s71, 0\n\t"
  "v_mov_b32 v3, -1\n\t"
  "s_nop 2\n\t"
  "s_lshr_b32 s73, s41, 9\n\t"
  "s_and_b32 s73, s73, 4\n\t"
  "s_add_u32 s73, s73, 147456\n\t"
  "v_mov_b32 v228, s73\n\t"
  "v_lshrrev_b32 v15, 1, v0\n\t"
  "buffer_load_dwordx2 v[204:205], v13, s[56:59], s71 offen offset:0\n\t"
  "buffer_load_dwordx2 v[206:207], v13, s[56:59], s71 offen offset:32\n\t"
  "buffer_load_dwordx2 v[208:209], v13, s[56:59], s71 offen offset:256\n\t"
  "buffer_load_dwordx2 v[210:211], v13, s[56:59], s71 offen offset:288\n\t"
  "buffer_load_dwordx2 v[212:213], v13, s[56:59], s71 offen offset:512\n\t"
  "buffer_load_dwordx2 v[214:215], v13, s[56:59], s71 offen offset:544\n\t"
  "buffer_load_dwordx2 v[216:217], v13, s[56:59], s71 offen offset:768\n\t"
  "buffer_load_dwordx2 v[218:219], v13, s[56:59], s71 offen offset:800\n\t"
  "buffer_load_dwordx2 v[220:221], v14, s[56:59], s71 offen offset:0\n\t"
  "s_mov_b32 s71, 36864\n\t"
  "buffer_load_dwordx2 v[24:25], v13, s[56:59], s71 offen offset:0\n\t"
  "buffer_load_dwordx2 v[26:27], v13, s[56:59], s71 offen offset:32\n\t"
  "buffer_load_dwordx2 v[28:29], v13, s[56:59], s71 offen offset:256\n\t"
  "buffer_load_dwordx2 v[30:31], v13, s[56:59], s71 offen offset:288\n\t"
  "buffer_load_dwordx2 v[32:33], v13, s[56:59], s71 offen offset:512\n\t"
  "buffer_load_dwordx2 v[34:35], v13, s[56:59], s71 offen offset:544\n\t"
  "buffer_load_dwordx2 v[36:37], v13, s[56:59], s71 offen offset:768\n\t"
  "buffer_load_dwordx2 v[38:39], v13, s[56:59], s71 offen offset:800\n\t"
  "buffer_load_dwordx2 v[40:41], v14, s[56:59], s71 offen offset:0\n\t"
  "1:\n\t"
  "v_add_u32 v230, s72, v7\n\t"
  "v_min_i32 v230, 63, v230\n\t"
  "v_readlane_b32 s68, v4, s72\n\t"
  "v_readlane_b32 s69, v5, s72\n\t"
  "v_readlane_b32 s70, v6, s72\n\t"
  "s_add_u32 s71, s72, 2\n\t"
  "s_min_u32 s71, s71, 63\n\t"
  "s_mul_i32 s71, s71, 36864\n\t"
  "s_sub_u32 s76, s72, 1\n\t"
  "v_mov_b32 v229, s76\n\t"
  "s_waitcnt vmcnt(9)\n\t"
  "buffer_store_dword v229, off, s[36:39], s40 sc1\n\t"
  "s_mov_b32 s79, 0\n\t"
  "21:\n\t"
  "v_cmp_le_i32 vcc, v230, v3\n\t"
  "s_cmp_eq_u64 vcc, -1\n\t"
  "s_cbranch_scc1 22f\n\t"
  "s_waitcnt vmcnt(24)\n\t"
  "buffer_load_dword v3, v2, s[36:39], 0 offen sc1\n\t"
  "s_sleep 10\n\t"
  "s_add_u32 s79, s79, 1\n\t"
  "s_cmp_lt_u32 s79, s80\n\t"
  "s_cbranch_scc1 21b\n\t"
  "s_mov_b32 s80, 0\n\t"
  "22:\n\t"
  "v_lshlrev_b32 v132, 16, v204\n\t"
  "v_and_b32 v133, 0xffff0000, v204\n\t"
  "v_lshlrev_b32 v134, 16, v205\n\t"
  "v_and_b32 v135, 0xffff0000, v205\n\t"
  "v_lshlrev_b32 v136, 16, v206\n\t"
  "v_and_b32 v137, 0xffff0000, v206\n\t"
  "v_lshlrev_b32 v138, 16, v207\n\t"
  "v_and_b32 v139, 0xffff0000, v207\n\t"
  "v_lshlrev_b32 v140, 16, v208\n\t"
  "v_and_b32 v141, 0xffff0000, v208\n\t"
  "v_lshlrev_b32 v142, 16, v209\n\t"
  "v_and_b32 v143, 0xffff0000, v209\n\t"
  "v_lshlrev_b32 v144, 16, v210\n\t"
  "v_and_b32 v145, 0xffff0000, v210\n\t"
  "v_lshlrev_b32 v146, 16, v211\n\t"
  "v_and_b32 v147, 0xffff0000, v211\n\t"
  "v_lshlrev_b32 v148, 16, v212\n\t"
  "v_and_b32 v149, 0xffff0000, v212\n\t"
  "v_lshlrev_b32 v150, 16, v213\n\t"
  "v_and_b32 v151, 0xffff0000, v213\n\t"
  "v_lshlrev_b32 v152, 16, v214\n\t"
  "v_and_b32 v153, 0xffff0000, v214\n\t"
  "v_lshlrev_b32 v154, 16, v215\n\t"
  "v_and_b32 v155, 0xffff0000, v215\n\t"
  "v_lshlrev_b32 v156, 16, v216\n\t"
  "v_and_b32 v157, 0xffff0000, v216\n\t"
  "v_lshlrev_b32 v158, 16, v217\n\t"
  "v_and_b32 v159, 0xffff0000, v217\n\t"
  "v_lshlrev_b32 v160, 16, v218\n\t"
  "v_and_b32 v161, 0xffff0000, v218\n\t"
  "v_lshlrev_b32 v162, 16, v219\n\t"
  "v_and_b32 v163, 0xffff0000, v219\n\t"
  "v_lshlrev_b32 v164, 16, v220\n\t"
  "v_and_b32 v165, 0xffff0000, v220\n\t"
  "v_lshlrev_b32 v166, 16, v221\n\t"
  "v_and_b32 v167, 0xffff0000, v221\n\t"
  "v_pk_add_f32 v[222:223], v[132:133], v[136:137]\n\t"
  "v_pk_add_f32 v[224:225], v[134:135], v[138:139]\n\t"
  "v_pk_add_f32 v[222:223], v[222:223], v[140:141]\n\t"
  "v_pk_add_f32 v[224:225], v[224:225], v[142:143]\n\t"
  "v_pk_add_f32 v[222:223], v[222:223], v[144:145]\n\t"
  "v_pk_add_f32 v[224:225], v[224:225], v[146:147]\n\t"
  "v_pk_add_f32 v[222:223], v[222:223], v[148:149]\n\t"
  "v_pk_add_f32 v[224:225], v[224:225], v[150:151]\n\t"
  "v_pk_add_f32 v[222:223], v[222:223], v[152:153]\n\t"
  "v_pk_add_f32 v[224:225], v[224:225], v[154:155]\n\t"
  "v_pk_add_f32 v[222:223], v[222:223], v[156:157]\n\t"
  "v_pk_add_f32 v[224:225], v[224:225], v[158:159]\n\t"
  "v_pk_add_f32 v[222:223], v[222:223], v[160:161]\n\t"
  "v_pk_add_f32 v[224:225], v[224:225], v[162:163]\n\t"
  "v_add_f32 v222, v222, v223\n\t"
  "v_add_f32 v224, v224, v225\n\t"
  "v_add_f32 v222, v222, v224\n\t"
  "v_add_f32 v223, v164, v165\n\t"
  "v_add_f32 v224, v166, v167\n\t"
  "v_add_f32 v222, v222, v223\n\t"
  "v_add_f32 v222, v222, v224\n\t"
  "s_nop 1\n\t"
  "v_add_f32_dpp v222, v222, v222 row_ror:2 row_mask:0xf bank_mask:0xf\n\t"
  "s_nop 1\n\t"
  "v_add_f32_dpp v222, v222, v222 row_ror:4 row_mask:0xf bank_mask:0xf\n\t"
  "s_nop 1\n\t"
  "v_add_f32_dpp v222, v222, v222 row_ror:8 row_mask:0xf bank_mask:0xf\n\t"
  "v_mov_b32 v223, v222\n\t"
  "s_nop 1\n\t"
  "v_permlane32_swap_b32 v222, v223\n\t"
  "v_add_f32 v222, v222, v223\n\t"
  "v_mul_f32 v222, 0xbae38e39, v222\n\t"
  "v_pk_add_f32 v[168:169], v[132:133], v[222:223] op_sel_hi:[1,0]\n\t"
  "v_pk_add_f32 v[170:171], v[134:135], v[222:223] op_sel_hi:[1,0]\n\t"
  "v_pk_add_f32 v[172:173], v[136:137], v[222:223] op_sel_hi:[1,0]\n\t"
  "v_pk_add_f32 v[174:175], v[138:139], v[222:223] op_sel_hi:[1,0]\n\t"
  "v_pk_add_f32 v[176:177], v[140:141], v[222:223] op_sel_hi:[1,0]\n\t"
  "v_pk_add_f32 v[178:179], v[142:143], v[222:223] op_sel_hi:[1,0]\n\t"
  "v_pk_add_f32 v[180:181], v[144:145], v[222:223] op_sel_hi:[1,0]\n\t"
  "v_pk_add_f32 v[182:183], v[146:147], v[222:223] op_sel_hi:[1,0]\n\t"
  "v_pk_add_f32 v[184:185], v[148:149], v[222:223] op_sel_hi:[1,0]\n\t"
  "v_pk_add_f32 v[186:187], v[150:151], v[222:223] op_sel_hi:[1,0]\n\t"
  "v_pk_add_f32 v[188:189], v[152:153], v[222:223] op_sel_hi:[1,0]\n\t"
  "v_pk_add_f32 v[190:191], v[154:155], v[222:223] op_sel_hi:[1,0]\n\t"
  "v_pk_add_f32 v[192:193], v[156:157], v[222:223] op_sel_hi:[1,0]\n\t"
  "v_pk_add_f32 v[194:195], v[158:159], v[222:223] op_sel_hi:[1,0]\n\t"
  "v_pk_add_f32 v[196:197], v[160:161], v[222:223] op_sel_hi:[1,0]\n\t"
  "v_pk_add_f32 v[198:199], v[162:163], v[222:223] op_sel_hi:[1,0]\n\t"
  "v_pk_add_f32 v[200:201], v[164:165], v[222:223] op_sel_hi:[1,0]\n\t"
  "v_pk_add_f32 v[202:203], v[166:167], v[222:223] op_sel_hi:[1,0]\n\t"
  "v_pk_mul_f32 v[224:225], v[168:169], v[168:169]\n\t"
  "v_pk_mul_f32 v[226:227], v[170:171], v[170:171]\n\t"
  "v_pk_fma_f32 v[224:225], v[172:173], v[172:173], v[224:225]\n\t"
  "v_pk_fma_f32 v[226:227], v[174:175], v[174:175], v[226:227]\n\t"
  "v_pk_fma_f32 v[224:225], v[176:177], v[176:177], v[224:225]\n\t"
  "v_pk_fma_f32 v[226:227], v[178:179], v[178:179], v[226:227]\n\t"
  "v_pk_fma_f32 v[224:225], v[180:181], v[180:181], v[224:225]\n\t"
  "v_pk_fma_f32 v[226:227], v[182:183], v[182:183], v[226:227]\n\t"
  "v_pk_fma_f32 v[224:225], v[184:185], v[184:185], v[224:225]\n\t"
  "v_pk_fma_f32 v[226:227], v[186:187], v[186:187], v[226:227]\n\t"
  "v_pk_fma_f32 v[224:225], v[188:189], v[188:189], v[224:225]\n\t"
  "v_pk_fma_f32 v[226:227], v[190:191], v[190:191], v[226:227]\n\t"
  "v_pk_fma_f32 v[224:225], v[192:193], v[192:193], v[224:225]\n\t"
  "v_pk_fma_f32 v[226:227], v[194:195], v[194:195], v[226:227]\n\t"
  "v_pk_fma_f32 v[224:225], v[196:197], v[196:197], v[224:225]\n\t"
  "v_pk_fma_f32 v[226:227], v[198:199], v[198:199], v[226:227]\n\t"
  "v_pk_fma_f32 v[224:225], v[200:201], v[200:201], v[224:225]\n\t"
  "v_pk_fma_f32 v[226:227], v[202:203], v[202:203], v[226:227]\n\t"
  "s_nop 0\n\t"
  "v_pk_add_f32 v[224:225], v[224:225], v[226:227]\n\t"
  "v_mov_b32 v226, 0x3727c5ac\n\t"
  "v_add_f32 v222, v224, v225\n\t"
  "s_nop 1\n\t"
  "v_add_f32_dpp v222, v222, v222 row_ror:2 row_mask:0xf bank_mask:0xf\n\t"
  "s_nop 1\n\t"
  "v_add_f32_dpp v222, v222, v222 row_ror:4 row_mask:0xf bank_mask:0xf\n\t"
  "s_nop 1\n\t"
  "v_add_f32_dpp v222, v222, v222 row_ror:8 row_mask:0xf bank_mask:0xf\n\t"
  "v_mov_b32 v223, v222\n\t"
  "s_nop 1\n\t"
  "v_permlane32_swap_b32 v222, v223\n\t"
  "v_add_f32 v222, v222, v223\n\t"
  "v_fmamk_f32 v222, v222, 0x3ae38e39, v226\n\t"
  "v_rsq_f32 v222, v222\n\t"
  "s_nop 0\n\t"
  "v_pk_mul_f32 v[168:169], v[168:169], v[222:223] op_sel_hi:[1,0]\n\t"
  "v_pk_mul_f32 v[170:171], v[170:171], v[222:223] op_sel_hi:[1,0]\n\t"
  "v_pk_mul_f32 v[172:173], v[172:173], v[222:223] op_sel_hi:[1,0]\n\t"
  "v_pk_mul_f32 v[174:175], v[174:175], v[222:223] op_sel_hi:[1,0]\n\t"
  "v_cvt_pk_bf16_f32 v168, v168, v169\n\t"
  "v_cvt_pk_bf16_f32 v169, v170, v171\n\t"
  "v_cvt_pk_bf16_f32 v170, v172, v173\n\t"
  "v_cvt_pk_bf16_f32 v171, v174, v175\n\t"
  "buffer_store_dwordx4 v[168:171], v8, s[36:39], s69 offen sc1\n\t"
  "v_pk_mul_f32 v[176:177], v[176:177], v[222:223] op_sel_hi:[1,0]\n\t"
  "v_pk_mul_f32 v[178:179], v[178:179], v[222:223] op_sel_hi:[1,0]\n\t"
  "v_pk_mul_f32 v[180:181], v[180:181], v[222:223] op_sel_hi:[1,0]\n\t"
  "v_pk_mul_f32 v[182:183], v[182:183], v[222:223] op_sel_hi:[1,0]\n\t"
  "v_cvt_pk_bf16_f32 v176, v176, v177\n\t"
  "v_cvt_pk_bf16_f32 v177, v178, v179\n\t"
  "v_cvt_pk_bf16_f32 v178, v180, v181\n\t"
  "v_cvt_pk_bf16_f32 v179, v182, v183\n\t"
  "buffer_store_dwordx4 v[176:179], v9, s[36:39], s69 offen sc1\n\t"
  "v_pk_mul_f32 v[184:185], v[184:185], v[222:223] op_sel_hi:[1,0]\n\t"
  "v_pk_mul_f32 v[186:187], v[186:187], v[222:223] op_sel_hi:[1,0]\n\t"
  "v_pk_mul_f32 v[188:189], v[188:189], v[222:223] op_sel_hi:[1,0]\n\t"
  "v_pk_mul_f32 v[190:191], v[190:191], v[222:223] op_sel_hi:[1,0]\n\t"
  "v_cvt_pk_bf16_f32 v184, v184, v185\n\t"
  "v_cvt_pk_bf16_f32 v185, v186, v187\n\t"
  "v_cvt_pk_bf16_f32 v186, v188, v189\n\t"
  "v_cvt_pk_bf16_f32 v187, v190, v191\n\t"
  "buffer_store_dwordx4 v[184:187], v10, s[36:39], s69 offen sc1\n\t"
  "v_pk_mul_f32 v[192:193], v[192:193], v[222:223] op_sel_hi:[1,0]\n\t"
  "v_pk_mul_f32 v[194:195], v[194:195], v[222:223] op_sel_hi:[1,0]\n\t"
  "v_pk_mul_f32 v[196:197], v[196:197], v[222:223] op_sel_hi:[1,0]\n\t"
  "v_pk_mul_f32 v[198:199], v[198:199], v[222:223] op_sel_hi:[1,0]\n\t"
  "v_cvt_pk_bf16_f32 v192, v192, v193\n\t"
  "v_cvt_pk_bf16_f32 v193, v194, v195\n\t"
  "v_cvt_pk_bf16_f32 v194, v196, v197\n\t"
  "v_cvt_pk_bf16_f32 v195, v198, v199\n\t"
  "buffer_store_dwordx4 v[192:195], v11, s[36:39], s69 offen sc1\n\t"
  "v_pk_mul_f32 v[200:201], v[200:201], v[222:223] op_sel_hi:[1,0]\n\t"
  "v_pk_mul_f32 v[202:203], v[202:203], v[222:223] op_sel_hi:[1,0]\n\t"
  "v_cvt_pk_bf16_f32 v200, v200, v201\n\t"
  "v_cvt_pk_bf16_f32 v201, v202, v203\n\t"
  "buffer_store_dwordx2 v[200:201], v12, s[36:39], s69 offen sc1\n\t"
  "buffer_store_dwordx4 v[204:207], v0, s[36:39], s70 offen offset:0 sc1\n\t"
  "buffer_store_dwordx4 v[208:211], v0, s[36:39], s70 offen offset:1024 sc1\n\t"
  "buffer_store_dwordx4 v[212:215], v0, s[36:39], s70 offen offset:2048 sc1\n\t"
  "buffer_store_dwordx4 v[216:219], v0, s[36:39], s70 offen offset:3072 sc1\n\t"
  "s_add_u32 s76, s70, 4096\n\t"
  "buffer_store_dwordx2 v[220:221], v15, s[36:39], s76 offen sc1\n\t"
  "buffer_load_dword v3, v2, s[36:39], 0 offen sc1\n\t"
  "buffer_load_dwordx2 v[204:205], v13, s[56:59], s71 offen offset:0\n\t"
  "buffer_load_dwordx2 v[206:207], v13, s[56:59], s71 offen offset:32\n\t"
  "buffer_load_dwordx2 v[208:209], v13, s[56:59], s71 offen offset:256\n\t"
  "buffer_load_dwordx2 v[210:211], v13, s[56:59], s71 offen offset:288\n\t"
  "buffer_load_dwordx2 v[212:213], v13, s[56:59], s71 offen offset:512\n\t"
  "buffer_load_dwordx2 v[214:215], v13, s[56:59], s71 offen offset:544\n\t"
  "buffer_load_dwordx2 v[216:217], v13, s[56:59], s71 offen offset:768\n\t"
  "buffer_load_dwordx2 v[218:219], v13, s[56:59], s71 offen offset:800\n\t"
  "buffer_load_dwordx2 v[220:221], v14, s[56:59], s71 offen offset:0\n\t"
  "s_add_u32 s72, s72, 1\n\t"
  "v_add_u32 v230, s72, v7\n\t"
  "v_min_i32 v230, 63, v230\n\t"
  "v_readlane_b32 s68, v4, s72\n\t"
  "v_readlane_b32 s69, v5, s72\n\t"
  "v_readlane_b32 s70, v6, s72\n\t"
  "s_add_u32 s71, s72, 2\n\t"
  "s_min_u32 s71, s71, 63\n\t"
  "s_mul_i32 s71, s71, 36864\n\t"
  "s_sub_u32 s76, s72, 1\n\t"
  "v_mov_b32 v229, s76\n\t"
  "s_waitcnt vmcnt(9)\n\t"
  "buffer_store_dword v229, off, s[36:39], s40 sc1\n\t"
  "s_mov_b32 s79, 0\n\t"
  "21:\n\t"
  "v_cmp_le_i32 vcc, v230, v3\n\t"
  "s_cmp_eq_u64 vcc, -1\n\t"
  "s_cbranch_scc1 22f\n\t"
  "s_waitcnt vmcnt(24)\n\t"
  "buffer_load_dword v3, v2, s[36:39], 0 offen sc1\n\t"
  "s_sleep 10\n\t"
  "s_add_u32 s79, s79, 1\n\t"
  "s_cmp_lt_u32 s79, s80\n\t"
  "s_cbranch_scc1 21b\n\t"
  "s_mov_b32 s80, 0\n\t"
  "22:\n\t"
  "v_lshlrev_b32 v132, 16, v24\n\t"
  "v_and_b32 v133, 0xffff0000, v24\n\t"
  "v_lshlrev_b32 v134, 16, v25\n\t"
  "v_and_b32 v135, 0xffff0000, v25\n\t"
  "v_lshlrev_b32 v136, 16, v26\n\t"
  "v_and_b32 v137, 0xffff0000, v26\n\t"
  "v_lshlrev_b32 v138, 16, v27\n\t"
  "v_and_b32 v139, 0xffff0000, v27\n\t"
  "v_lshlrev_b32 v140, 16, v28\n\t"
  "v_and_b32 v141, 0xffff0000, v28\n\t"
  "v_lshlrev_b32 v142, 16, v29\n\t"
  "v_and_b32 v143, 0xffff0000, v29\n\t"
  "v_lshlrev_b32 v144, 16, v30\n\t"
  "v_and_b32 v145, 0xffff0000, v30\n\t"
  "v_lshlrev_b32 v146, 16, v31\n\t"
  "v_and_b32 v147, 0xffff0000, v31\n\t"
  "v_lshlrev_b32 v148, 16, v32\n\t"
  "v_and_b32 v149, 0xffff0000, v32\n\t"
  "v_lshlrev_b32 v150, 16, v33\n\t"
  "v_and_b32 v151, 0xffff0000, v33\n\t"
  "v_lshlrev_b32 v152, 16, v34\n\t"
  "v_and_b32 v153, 0xffff0000, v34\n\t"
  "v_lshlrev_b32 v154, 16, v35\n\t"
  "v_and_b32 v155, 0xffff0000, v35\n\t"
  "v_lshlrev_b32 v156, 16, v36\n\t"
  "v_and_b32 v157, 0xffff0000, v36\n\t"
  "v_lshlrev_b32 v158, 16, v37\n\t"
  "v_and_b32 v159, 0xffff0000, v37\n\t"
  "v_lshlrev_b32 v160, 16, v38\n\t"
  "v_and_b32 v161, 0xffff0000, v38\n\t"
  "v_lshlrev_b32 v162, 16, v39\n\t"
  "v_and_b32 v163, 0xffff0000, v39\n\t"
  "v_lshlrev_b32 v164, 16, v40\n\t"
  "v_and_b32 v165, 0xffff0000, v40\n\t"
  "v_lshlrev_b32 v166, 16, v41\n\t"
  "v_and_b32 v167, 0xffff0000, v41\n\t"
  "v_pk_add_f32 v[222:223], v[132:133], v[136:137]\n\t"
  "v_pk_add_f32 v[224:225], v[134:135], v[138:139]\n\t"
  "v_pk_add_f32 v[222:223], v[222:223], v[140:141]\n\t"
  "v_pk_add_f32 v[224:225], v[224:225], v[142:143]\n\t"
  "v_pk_add_f32 v[222:223], v[222:223], v[144:145]\n\t"
  "v_pk_add_f32 v[224:225], v[224:225], v[146:147]\n\t"
  "v_pk_add_f32 v[222:223], v[222:223], v[148:149]\n\t"
  "v_pk_add_f32 v[224:225], v[224:225], v[150:151]\n\t"
  "v_pk_add_f32 v[222:223], v[222:223], v[152:153]\n\t"
  "v_pk_add_f32 v[224:225], v[224:225], v[154:155]\n\t"
  "v_pk_add_f32 v[222:223], v[222:223], v[156:157]\n\t"
  "v_pk_add_f32 v[224:225], v[224:225], v[158:159]\n\t"
  "v_pk_add_f32 v[222:223], v[222:223], v[160:161]\n\t"
  "v_pk_add_f32 v[224:225], v[224:225], v[162:163]\n\t"
  "v_add_f32 v222, v222, v223\n\t"
  "v_add_f32 v224, v224, v225\n\t"
  "v_add_f32 v222, v222, v224\n\t"
  "v_add_f32 v223, v164, v165\n\t"
  "v_add_f32 v224, v166, v167\n\t"
  "v_add_f32 v222, v222, v223\n\t"
  "v_add_f32 v222, v222, v224\n\t"
  "s_nop 1\n\t"
  "v_add_f32_dpp v222, v222, v222 row_ror:2 row_mask:0xf bank_mask:0xf\n\t"
  "s_nop 1\n\t"
  "v_add_f32_dpp v222, v222, v222 row_ror:4 row_mask:0xf bank_mask:0xf\n\t"
  "s_nop 1\n\t"
  "v_add_f32_dpp v222, v222, v222 row_ror:8 row_mask:0xf bank_mask:0xf\n\t"
  "v_mov_b32 v223, v222\n\t"
  "s_nop 1\n\t"
  "v_permlane32_swap_b32 v222, v223\n\t"
  "v_add_f32 v222, v222, v223\n\t"
  "v_mul_f32 v222, 0xbae38e39, v222\n\t"
  "v_pk_add_f32 v[168:169], v[132:133], v[222:223] op_sel_hi:[1,0]\n\t"
  "v_pk_add_f32 v[170:171], v[134:135], v[222:223] op_sel_hi:[1,0]\n\t"
  "v_pk_add_f32 v[172:173], v[136:137], v[222:223] op_sel_hi:[1,0]\n\t"
  "v_pk_add_f32 v[174:175], v[138:139], v[222:223] op_sel_hi:[1,0]\n\t"
  "v_pk_add_f32 v[176:177], v[140:141], v[222:223] op_sel_hi:[1,0]\n\t"
  "v_pk_add_f32 v[178:179], v[142:143], v[222:223] op_sel_hi:[1,0]\n\t"
  "v_pk_add_f32 v[180:181], v[144:145], v[222:223] op_sel_hi:[1,0]\n\t"
  "v_pk_add_f32 v[182:183], v[146:147], v[222:223] op_sel_hi:[1,0]\n\t"
  "v_pk_add_f32 v[184:185], v[148:149], v[222:223] op_sel_hi:[1,0]\n\t"
  "v_pk_add_f32 v[186:187], v[150:151], v[222:223] op_sel_hi:[1,0]\n\t"
  "v_pk_add_f32 v[188:189], v[152:153], v[222:223] op_sel_hi:[1,0]\n\t"
  "v_pk_add_f32 v[190:191], v[154:155], v[222:223] op_sel_hi:[1,0]\n\t"
  "v_pk_add_f32 v[192:193], v[156:157], v[222:223] op_sel_hi:[1,0]\n\t"
  "v_pk_add_f32 v[194:195], v[158:159], v[222:223] op_sel_hi:[1,0]\n\t"
  "v_pk_add_f32 v[196:197], v[160:161], v[222:223] op_sel_hi:[1,0]\n\t"
  "v_pk_add_f32 v[198:199], v[162:163], v[222:223] op_sel_hi:[1,0]\n\t"
  "v_pk_add_f32 v[200:201], v[164:165], v[222:223] op_sel_hi:[1,0]\n\t"
  "v_pk_add_f32 v[202:203], v[166:167], v[222:223] op_sel_hi:[1,0]\n\t"
  "v_pk_mul_f32 v[224:225], v[168:169], v[168:169]\n\t"
  "v_pk_mul_f32 v[226:227], v[170:171], v[170:171]\n\t"
  "v_pk_fma_f32 v[224:225], v[172:173], v[172:173], v[224:225]\n\t"
  "v_pk_fma_f32 v[226:227], v[174:175], v[174:175], v[226:227]\n\t"
  "v_pk_fma_f32 v[224:225], v[176:177], v[176:177], v[224:225]\n\t"
  "v_pk_fma_f32 v[226:227], v[178:179], v[178:179], v[226:227]\n\t"
  "v_pk_fma_f32 v[224:225], v[180:181], v[180:181], v[224:225]\n\t"
  "v_pk_fma_f32 v[226:227], v[182:183], v[182:183], v[226:227]\n\t"
  "v_pk_fma_f32 v[224:225], v[184:185], v[184:185], v[224:225]\n\t"
  "v_pk_fma_f32 v[226:227], v[186:187], v[186:187], v[226:227]\n\t"
  "v_pk_fma_f32 v[224:225], v[188:189], v[188:189], v[224:225]\n\t"
  "v_pk_fma_f32 v[226:227], v[190:191], v[190:191], v[226:227]\n\t"
  "v_pk_fma_f32 v[224:225], v[192:193], v[192:193], v[224:225]\n\t"
  "v_pk_fma_f32 v[226:227], v[194:195], v[194:195], v[226:227]\n\t"
  "v_pk_fma_f32 v[224:225], v[196:197], v[196:197], v[224:225]\n\t"
  "v_pk_fma_f32 v[226:227], v[198:199], v[198:199], v[226:227]\n\t"
  "v_pk_fma_f32 v[224:225], v[200:201], v[200:201], v[224:225]\n\t"
  "v_pk_fma_f32 v[226:227], v[202:203], v[202:203], v[226:227]\n\t"
  "s_nop 0\n\t"
  "v_pk_add_f32 v[224:225], v[224:225], v[226:227]\n\t"
  "v_mov_b32 v226, 0x3727c5ac\n\t"
  "v_add_f32 v222, v224, v225\n\t"
  "s_nop 1\n\t"
  "v_add_f32_dpp v222, v222, v222 row_ror:2 row_mask:0xf bank_mask:0xf\n\t"
  "s_nop 1\n\t"
  "v_add_f32_dpp v222, v222, v222 row_ror:4 row_mask:0xf bank_mask:0xf\n\t"
  "s_nop 1\n\t"
  "v_add_f32_dpp v222, v222, v222 row_ror:8 row_mask:0xf bank_mask:0xf\n\t"
  "v_mov_b32 v223, v222\n\t"
  "s_nop 1\n\t"
  "v_permlane32_swap_b32 v222, v223\n\t"
  "v_add_f32 v222, v222, v223\n\t"
  "v_fmamk_f32 v222, v222, 0x3ae38e39, v226\n\t"
  "v_rsq_f32 v222, v222\n\t"
  "s_nop 0\n\t"
  "v_pk_mul_f32 v[168:169], v[168:169], v[222:223] op_sel_hi:[1,0]\n\t"
  "v_pk_mul_f32 v[170:171], v[170:171], v[222:223] op_sel_hi:[1,0]\n\t"
  "v_pk_mul_f32 v[172:173], v[172:173], v[222:223] op_sel_hi:[1,0]\n\t"
  "v_pk_mul_f32 v[174:175], v[174:175], v[222:223] op_sel_hi:[1,0]\n\t"
  "v_cvt_pk_bf16_f32 v168, v168, v169\n\t"
  "v_cvt_pk_bf16_f32 v169, v170, v171\n\t"
  "v_cvt_pk_bf16_f32 v170, v172, v173\n\t"
  "v_cvt_pk_bf16_f32 v171, v174, v175\n\t"
  "buffer_store_dwordx4 v[168:171], v8, s[36:39], s69 offen sc1\n\t"
  "v_pk_mul_f32 v[176:177], v[176:177], v[222:223] op_sel_hi:[1,0]\n\t"
  "v_pk_mul_f32 v[178:179], v[178:179], v[222:223] op_sel_hi:[1,0]\n\t"
  "v_pk_mul_f32 v[180:181], v[180:181], v[222:223] op_sel_hi:[1,0]\n\t"
  "v_pk_mul_f32 v[182:183], v[182:183], v[222:223] op_sel_hi:[1,0]\n\t"
  "v_cvt_pk_bf16_f32 v176, v176, v177\n\t"
  "v_cvt_pk_bf16_f32 v177, v178, v179\n\t"
  "v_cvt_pk_bf16_f32 v178, v180, v181\n\t"
  "v_cvt_pk_bf16_f32 v179, v182, v183\n\t"
  "buffer_store_dwordx4 v[176:179], v9, s[36:39], s69 offen sc1\n\t"
  "v_pk_mul_f32 v[184:185], v[184:185], v[222:223] op_sel_hi:[1,0]\n\t"
  "v_pk_mul_f32 v[186:187], v[186:187], v[222:223] op_sel_hi:[1,0]\n\t"
  "v_pk_mul_f32 v[188:189], v[188:189], v[222:223] op_sel_hi:[1,0]\n\t"
  "v_pk_mul_f32 v[190:191], v[190:191], v[222:223] op_sel_hi:[1,0]\n\t"
  "v_cvt_pk_bf16_f32 v184, v184, v185\n\t"
  "v_cvt_pk_bf16_f32 v185, v186, v187\n\t"
  "v_cvt_pk_bf16_f32 v186, v188, v189\n\t"
  "v_cvt_pk_bf16_f32 v187, v190, v191\n\t"
  "buffer_store_dwordx4 v[184:187], v10, s[36:39], s69 offen sc1\n\t"
  "v_pk_mul_f32 v[192:193], v[192:193], v[222:223] op_sel_hi:[1,0]\n\t"
  "v_pk_mul_f32 v[194:195], v[194:195], v[222:223] op_sel_hi:[1,0]\n\t"
  "v_pk_mul_f32 v[196:197], v[196:197], v[222:223] op_sel_hi:[1,0]\n\t"
  "v_pk_mul_f32 v[198:199], v[198:199], v[222:223] op_sel_hi:[1,0]\n\t"
  "v_cvt_pk_bf16_f32 v192, v192, v193\n\t"
  "v_cvt_pk_bf16_f32 v193, v194, v195\n\t"
  "v_cvt_pk_bf16_f32 v194, v196, v197\n\t"
  "v_cvt_pk_bf16_f32 v195, v198, v199\n\t"
  "buffer_store_dwordx4 v[192:195], v11, s[36:39], s69 offen sc1\n\t"
  "v_pk_mul_f32 v[200:201], v[200:201], v[222:223] op_sel_hi:[1,0]\n\t"
  "v_pk_mul_f32 v[202:203], v[202:203], v[222:223] op_sel_hi:[1,0]\n\t"
  "v_cvt_pk_bf16_f32 v200, v200, v201\n\t"
  "v_cvt_pk_bf16_f32 v201, v202, v203\n\t"
  "buffer_store_dwordx2 v[200:201], v12, s[36:39], s69 offen sc1\n\t"
  "buffer_store_dwordx4 v[24:27], v0, s[36:39], s70 offen offset:0 sc1\n\t"
  "buffer_store_dwordx4 v[28:31], v0, s[36:39], s70 offen offset:1024 sc1\n\t"
  "buffer_store_dwordx4 v[32:35], v0, s[36:39], s70 offen offset:2048 sc1\n\t"
  "buffer_store_dwordx4 v[36:39], v0, s[36:39], s70 offen offset:3072 sc1\n\t"
  "s_add_u32 s76, s70, 4096\n\t"
  "buffer_store_dwordx2 v[40:41], v15, s[36:39], s76 offen sc1\n\t"
  "buffer_load_dword v3, v2, s[36:39], 0 offen sc1\n\t"
  "buffer_load_dwordx2 v[24:25], v13, s[56:59], s71 offen offset:0\n\t"
  "buffer_load_dwordx2 v[26:27], v13, s[56:59], s71 offen offset:32\n\t"
  "buffer_load_dwordx2 v[28:29], v13, s[56:59], s71 offen offset:256\n\t"
  "buffer_load_dwordx2 v[30:31], v13, s[56:59], s71 offen offset:288\n\t"
  "buffer_load_dwordx2 v[32:33], v13, s[56:59], s71 offen offset:512\n\t"
  "buffer_load_dwordx2 v[34:35], v13, s[56:59], s71 offen offset:544\n\t"
  "buffer_load_dwordx2 v[36:37], v13, s[56:59], s71 offen offset:768\n\t"
  "buffer_load_dwordx2 v[38:39], v13, s[56:59], s71 offen offset:800\n\t"
  "buffer_load_dwordx2 v[40:41], v14, s[56:59], s71 offen offset:0\n\t"
  "s_add_u32 s72, s72, 1\n\t"
  "s_cmp_lt_u32 s72, 64\n\t"
  "s_cbranch_scc1 1b\n\t"
  "v_mov_b32 v229, 63\n\t"
  "s_waitcnt vmcnt(0)\n\t"
  "buffer_store_dword v229, off, s[36:39], s40 sc1\n\t"
  "s_waitcnt vmcnt(0)\n\t"
  "buffer_store_dword v229, off, s[36:39], s40 offset:16 sc1\n\t"
  "s_waitcnt vmcnt(0) lgkmcnt(0)\n\t"
  : "+{v[0:7]}"(lc), "+{v[8:23]}"(so) :: CLOB_ALL);
  else if constexpr (stage == 1) asm volatile(
  "s_nop 4\n\t"
  "v_readlane_b32 s36, v3, 0\n\t"
  "v_readlane_b32 s37, v3, 1\n\t"
  "v_readlane_b32 s38, v3, 2\n\t"
  "v_readlane_b32 s39, v3, 3\n\t"
  "v_readlane_b32 s40, v3, 4\n\t"
  "v_readlane_b32 s41, v3, 5\n\t"
  "v_readlane_b32 s42, v3, 6\n\t"
  "v_readlane_b32 s43, v3, 7\n\t"
  "v_readlane_b32 s44, v3, 8\n\t"
  "v_readlane_b32 s45, v3, 9\n\t"
  "v_readlane_b32 s46, v3, 10\n\t"
  "v_readlane_b32 s47, v3, 11\n\t"
  "v_readlane_b32 s48, v3, 12\n\t"
  "v_readlane_b32 s49, v3, 13\n\t"
  "v_readlane_b32 s50, v3, 14\n\t"
  "v_readlane_b32 s51, v3, 15\n\t"
  "v_readlane_b32 s56, v3, 16\n\t"
  "v_readlane_b32 s57, v3, 17\n\t"
  "v_readlane_b32 s58, v3, 18\n\t"
  "s_mov_b32 s59, s39\n\t"
  "s_mov_b32 s72, 0\n\t"
  "s_mov_b32 s80, 0x40000\n\t"
  "s_mov_b32 s71, 0\n\t"
  "v_mov_b32 v3, -1\n\t"
  "s_nop 2\n\t"
  "s_lshr_b32 s73, s41, 9\n\t"
  "s_and_b32 s73, s73, 4\n\t"
  "s_add_u32 s73, s73, 147456\n\t"
  "v_mov_b32 v228, s73\n\t"
  "v_lshrrev_b32 v15, 1, v0\n\t"
  "v_add_u32 v230, -1, v7\n\t"
  "s_mov_b32 s79, 0\n\t"
  "71:\n\t"
  "v_cmp_le_i32 vcc, v230, v3\n\t"
  "s_cmp_eq_u64 vcc, -1\n\t"
  "s_cbranch_scc1 72f\n\t"
  "s_waitcnt vmcnt(24)\n\t"
  "buffer_load_dword v3, v2, s[36:39], 0 offen sc1\n\t"
  "s_sleep 4\n\t"
  "s_add_u32 s79, s79, 1\n\t"
  "s_cmp_lt_u32 s79, s80\n\t"
  "s_cbranch_scc1 71b\n\t"
  "s_mov_b32 s80, 0\n\t"
  "72:\n\t"
  "v_readlane_b32 s70, v6, 0\n\t"
  "s_nop 4\n\t"
  "buffer_load_dwordx4 v[204:207], v0, s[36:39], s70 offen offset:0 sc1\n\t"
  "buffer_load_dwordx4 v[208:211], v0, s[36:39], s70 offen offset:1024 sc1\n\t"
  "buffer_load_dwordx4 v[212:215], v0, s[36:39], s70 offen offset:2048 sc1\n\t"
  "buffer_load_dwordx4 v[216:219], v0, s[36:39], s70 offen offset:3072 sc1\n\t"
  "s_add_u32 s76, s70, 4096\n\t"
  "buffer_load_dwordx2 v[220:221], v15, s[36:39], s76 offen sc1\n\t"
  "v_add_u32 v230, s72, v7\n\t"
  "v_min_i32 v230, 63, v230\n\t"
  "v_readlane_b32 s68, v4, s72\n\t"
  "1:\n\t"
  "s_mov_b32 s79, 0\n\t"
  "21:\n\t"
  "v_cmp_le_i32 vcc, v230, v3\n\t"
  "s_cmp_eq_u64 vcc, -1\n\t"
  "s_cbranch_scc1 22f\n\t"
  "s_waitcnt vmcnt(24)\n\t"
  "buffer_load_dword v3, v2, s[36:39], 0 offen sc1\n\t"
  "s_sleep 4\n\t"
  "s_add_u32 s79, s79, 1\n\t"
  "s_cmp_lt_u32 s79, s80\n\t"
  "s_cbranch_scc1 21b\n\t"
  "s_mov_b32 s80, 0\n\t"
  "22:\n\t"
  "s_add_u32 s76, s68, 0\n\t"
  "buffer_load_dwordx4 v[24:27], v0, s[36:39], s76 offen offset:0 sc1\n\t"
  "buffer_load_dwordx4 v[28:31], v0, s[36:39], s76 offen offset:1024 sc1\n\t"
  "buffer_load_dwordx4 v[32:35], v0, s[36:39], s76 offen offset:2048 sc1\n\t"
  "buffer_load_dwordx4 v[36:39], v0, s[36:39], s76 offen offset:3072 sc1\n\t"
  "s_add_u32 s76, s68, 4096\n\t"
  "buffer_load_dwordx4 v[40:43], v0, s[36:39], s76 offen offset:0 sc1\n\t"
  "buffer_load_dwordx4 v[44:47], v0, s[36:39], s76 offen offset:1024 sc1\n\t"
  "buffer_load_dwordx4 v[48:51], v0, s[36:39], s76 offen offset:2048 sc1\n\t"
  "buffer_load_dwordx4 v[52:55], v0, s[36:39], s76 offen offset:3072 sc1\n\t"
  "s_add_u32 s76, s68, 16384\n\t"
  "buffer_load_dwordx4 v[56:59], v0, s[36:39], s76 offen offset:0 sc1\n\t"
  "buffer_load_dwordx4 v[60:63], v0, s[36:39], s76 offen offset:1024 sc1\n\t"
  "buffer_load_dwordx4 v[64:67], v0, s[36:39], s76 offen offset:2048 sc1\n\t"
  "buffer_load_dwordx4 v[68:71], v0, s[36:39], s76 offen offset:3072 sc1\n\t"
  "s_add_u32 s76, s68, 20480\n\t"
  "buffer_load_dwordx4 v[72:75], v0, s[36:39], s76 offen offset:0 sc1\n\t"
  "buffer_load_dwordx4 v[76:79], v0, s[36:39], s76 offen offset:1024 sc1\n\t"
  "buffer_load_dwordx4 v[80:83], v0, s[36:39], s76 offen offset:2048 sc1\n\t"
  "buffer_load_dwordx4 v[84:87], v0, s[36:39], s76 offen offset:3072 sc1\n\t"
  "s_add_u32 s76, s68, 32768\n\t"
  "buffer_load_dwordx4 v[88:91], v0, s[36:39], s76 offen offset:0 sc1\n\t"
  "buffer_load_dwordx4 v[92:95], v0, s[36:39], s76 offen offset:1024 sc1\n\t"
  "buffer_load_dword v3, v2, s[36:39], 0 offen sc1\n\t"
  "5:\n\t"
  "s_add_u32 s75, s72, 1\n\t"
  "v_readlane_b32 s69, v5, s72\n\t"
  "v_readlane_b32 s70, v6, s75\n\t"
  "s_and_b32 s76, s72, 7\n\t"
  "s_mul_i32 s76, s76, 18432\n\t"
  "v_add_u32 v231, s76, v1\n\t"
  "s_mul_i32 s71, s72, 36864\n\t"
  "v_mov_b32 v229, s72\n\t"
  "v_add_u32 v230, s75, v7\n\t"
  "v_min_i32 v230, 63, v230\n\t"
  "v_readlane_b32 s68, v4, s75\n\t"
  "s_waitcnt vmcnt(19)\n\t"
  "v_lshlrev_b32 v96, 16, v204\n\t"
  "v_and_b32 v97, 0xffff0000, v204\n\t"
  "v_lshlrev_b32 v98, 16, v205\n\t"
  "v_and_b32 v99, 0xffff0000, v205\n\t"
  "v_lshlrev_b32 v100, 16, v206\n\t"
  "v_and_b32 v101, 0xffff0000, v206\n\t"
  "v_lshlrev_b32 v102, 16, v207\n\t"
  "v_and_b32 v103, 0xffff0000, v207\n\t"
  "v_lshlrev_b32 v104, 16, v208\n\t"
  "v_and_b32 v105, 0xffff0000, v208\n\t"
  "v_lshlrev_b32 v106, 16, v209\n\t"
  "v_and_b32 v107, 0xffff0000, v209\n\t"
  "v_lshlrev_b32 v108, 16, v210\n\t"
  "v_and_b32 v109, 0xffff0000, v210\n\t"
  "v_lshlrev_b32 v110, 16, v211\n\t"
  "v_and_b32 v111, 0xffff0000, v211\n\t"
  "v_lshlrev_b32 v112, 16, v212\n\t"
  "v_and_b32 v113, 0xffff0000, v212\n\t"
  "v_lshlrev_b32 v114, 16, v213\n\t"
  "v_and_b32 v115, 0xffff0000, v213\n\t"
  "v_lshlrev_b32 v116, 16, v214\n\t"
  "v_and_b32 v117, 0xffff0000, v214\n\t"
  "v_lshlrev_b32 v118, 16, v215\n\t"
  "v_and_b32 v119, 0xffff0000, v215\n\t"
  "v_lshlrev_b32 v120, 16, v216\n\t"
  "v_and_b32 v121, 0xffff0000, v216\n\t"
  "v_lshlrev_b32 v122, 16, v217\n\t"
  "v_and_b32 v123, 0xffff0000, v217\n\t"
  "v_lshlrev_b32 v124, 16, v218\n\t"
  "v_and_b32 v125, 0xffff0000, v218\n\t"
  "v_lshlrev_b32 v126, 16, v219\n\t"
  "v_and_b32 v127, 0xffff0000, v219\n\t"
  "v_lshlrev_b32 v128, 16, v220\n\t"
  "v_and_b32 v129, 0xffff0000, v220\n\t"
  "v_lshlrev_b32 v130, 16, v221\n\t"
  "v_and_b32 v131, 0xffff0000, v221\n\t"
  "s_waitcnt vmcnt(18)\n\t"
  "v_mfma_f32_16x16x32_bf16 v[132:135], v[16:19], v[24:27], v[96:99]\n\t"
  "s_waitcnt vmcnt(17)\n\t"
  "v_mfma_f32_16x16x32_bf16 v[132:135], v[20:23], v[28:31], v[132:135]\n\t"
  "s_waitcnt vmcnt(16)\n\t"
  "v_mfma_f32_16x16x32_bf16 v[136:139], v[16:19], v[32:35], v[100:103]\n\t"
  "s_waitcnt vmcnt(15)\n\t"
  "v_mfma_f32_16x16x32_bf16 v[136:139], v[20:23], v[36:39], v[136:139]\n\t"
  "s_waitcnt vmcnt(14)\n\t"
  "v_mfma_f32_16x16x32_bf16 v[140:143], v[16:19], v[40:43], v[104:107]\n\t"
  "s_waitcnt vmcnt(13)\n\t"
  "v_mfma_f32_16x16x32_bf16 v[140:143], v[20:23], v[44:47], v[140:143]\n\t"
  "v_add_f32 v222, v132, v136\n\t"
  "v_add_f32 v223, v133, v137\n\t"
  "v_add_f32 v224, v134, v138\n\t"
  "v_add_f32 v225, v135, v139\n\t"
  "s_waitcnt vmcnt(12)\n\t"
  "v_mfma_f32_16x16x32_bf16 v[144:147], v[16:19], v[48:51], v[108:111]\n\t"
  "s_waitcnt vmcnt(11)\n\t"
  "v_mfma_f32_16x16x32_bf16 v[144:147], v[20:23], v[52:55], v[144:147]\n\t"
  "v_add_f32 v222, v222, v140\n\t"
  "v_add_f32 v223, v223, v141\n\t"
  "v_add_f32 v224, v224, v142\n\t"
  "v_add_f32 v225, v225, v143\n\t"
  "s_waitcnt vmcnt(10)\n\t"
  "v_mfma_f32_16x16x32_bf16 v[148:151], v[16:19], v[56:59], v[112:115]\n\t"
  "s_waitcnt vmcnt(9)\n\t"
  "v_mfma_f32_16x16x32_bf16 v[148:151], v[20:23], v[60:63], v[148:151]\n\t"
  "v_add_f32 v222, v222, v144\n\t"
  "v_add_f32 v223, v223, v145\n\t"
  "v_add_f32 v224, v224, v146\n\t"
  "v_add_f32 v225, v225, v147\n\t"
  "s_waitcnt vmcnt(8)\n\t"
  "v_mfma_f32_16x16x32_bf16 v[152:155], v[16:19], v[64:67], v[116:119]\n\t"
  "s_waitcnt vmcnt(7)\n\t"
  "v_mfma_f32_16x16x32_bf16 v[152:155], v[20:23], v[68:71], v[152:155]\n\t"
  "v_add_f32 v222, v222, v148\n\t"
  "v_add_f32 v223, v223, v149\n\t"
  "v_add_f32 v224, v224, v150\n\t"
  "v_add_f32 v225, v225, v151\n\t"
  "s_waitcnt vmcnt(6)\n\t"
  "v_mfma_f32_16x16x32_bf16 v[156:159], v[16:19], v[72:75], v[120:123]\n\t"
  "s_waitcnt vmcnt(5)\n\t"
  "v_mfma_f32_16x16x32_bf16 v[156:159], v[20:23], v[76:79], v[156:159]\n\t"
  "v_add_f32 v222, v222, v152\n\t"
  "v_add_f32 v223, v223, v153\n\t"
  "v_add_f32 v224, v224, v154\n\t"
  "v_add_f32 v225, v225, v155\n\t"
  "s_waitcnt vmcnt(4)\n\t"
  "v_mfma_f32_16x16x32_bf16 v[160:163], v[16:19], v[80:83], v[124:127]\n\t"
  "s_waitcnt vmcnt(3)\n\t"
  "v_mfma_f32_16x16x32_bf16 v[160:163], v[20:23], v[84:87], v[160:163]\n\t"
  "v_add_f32 v222, v222, v156\n\t"
  "v_add_f32 v223, v223, v157\n\t"
  "v_add_f32 v224, v224, v158\n\t"
  "v_add_f32 v225, v225, v159\n\t"
  "s_waitcnt vmcnt(2)\n\t"
  "v_mfma_f32_16x16x32_bf16 v[164:167], v[16:19], v[88:91], v[128:131]\n\t"
  "s_waitcnt vmcnt(1)\n\t"
  "v_mfma_f32_16x16x32_bf16 v[164:167], v[20:23], v[92:95], v[164:167]\n\t"
  "v_add_f32 v222, v222, v160\n\t"
  "v_add_f32 v223, v223, v161\n\t"
  "v_add_f32 v224, v224, v162\n\t"
  "v_add_f32 v225, v225, v163\n\t"
  "buffer_load_dword v3, v2, s[36:39], 0 offen sc1\n\t"
  "v_add_f32 v222, v222, v223\n\t"
  "v_add_f32 v224, v224, v225\n\t"
  "v_add_f32 v222, v222, v224\n\t"
  "v_add_f32 v223, v164, v165\n\t"
  "v_add_f32 v224, v166, v167\n\t"
  "v_add_f32 v222, v222, v223\n\t"
  "v_add_f32 v222, v222, v224\n\t"
  "s_nop 1\n\t"
  "v_add_f32_dpp v222, v222, v222 row_ror:2 row_mask:0xf bank_mask:0xf\n\t"
  "s_nop 1\n\t"
  "v_add_f32_dpp v222, v222, v222 row_ror:4 row_mask:0xf bank_mask:0xf\n\t"
  "s_nop 1\n\t"
  "v_add_f32_dpp v222, v222, v222 row_ror:8 row_mask:0xf bank_mask:0xf\n\t"
  "v_mov_b32 v223, v222\n\t"
  "s_nop 1\n\t"
  "v_permlane32_swap_b32 v222, v223\n\t"
  "v_add_f32 v222, v222, v223\n\t"
  "v_mul_f32 v222, 0xbae38e39, v222\n\t"
  "v_pk_add_f32 v[168:169], v[132:133], v[222:223] op_sel_hi:[1,0]\n\t"
  "v_pk_add_f32 v[170:171], v[134:135], v[222:223] op_sel_hi:[1,0]\n\t"
  "v_pk_add_f32 v[172:173], v[136:137], v[222:223] op_sel_hi:[1,0]\n\t"
  "v_pk_add_f32 v[174:175], v[138:139], v[222:223] op_sel_hi:[1,0]\n\t"
  "v_pk_add_f32 v[176:177], v[140:141], v[222:223] op_sel_hi:[1,0]\n\t"
  "v_pk_add_f32 v[178:179], v[142:143], v[222:223] op_sel_hi:[1,0]\n\t"
  "v_pk_add_f32 v[180:181], v[144:145], v[222:223] op_sel_hi:[1,0]\n\t"
  "v_pk_add_f32 v[182:183], v[146:147], v[222:223] op_sel_hi:[1,0]\n\t"
  "v_pk_add_f32 v[184:185], v[148:149], v[222:223] op_sel_hi:[1,0]\n\t"
  "v_pk_add_f32 v[186:187], v[150:151], v[222:223] op_sel_hi:[1,0]\n\t"
  "v_pk_add_f32 v[188:189], v[152:153], v[222:223] op_sel_hi:[1,0]\n\t"
  "v_pk_add_f32 v[190:191], v[154:155], v[222:223] op_sel_hi:[1,0]\n\t"
  "v_pk_add_f32 v[192:193], v[156:157], v[222:223] op_sel_hi:[1,0]\n\t"
  "v_pk_add_f32 v[194:195], v[158:159], v[222:223] op_sel_hi:[1,0]\n\t"
  "v_pk_add_f32 v[196:197], v[160:161], v[222:223] op_sel_hi:[1,0]\n\t"
  "v_pk_add_f32 v[198:199], v[162:163], v[222:223] op_sel_hi:[1,0]\n\t"
  "v_pk_add_f32 v[200:201], v[164:165], v[222:223] op_sel_hi:[1,0]\n\t"
  "v_pk_add_f32 v[202:203], v[166:167], v[222:223] op_sel_hi:[1,0]\n\t"
  "v_pk_mul_f32 v[224:225], v[168:169], v[168:169]\n\t"
  "v_pk_mul_f32 v[226:227], v[170:171], v[170:171]\n\t"
  "v_pk_fma_f32 v[224:225], v[172:173], v[172:173], v[224:225]\n\t"
  "v_pk_fma_f32 v[226:227], v[174:175], v[174:175], v[226:227]\n\t"
  "v_pk_fma_f32 v[224:225], v[176:177], v[176:177], v[224:225]\n\t"
  "v_pk_fma_f32 v[226:227], v[178:179], v[178:179], v[226:227]\n\t"
  "v_pk_fma_f32 v[224:225], v[180:181], v[180:181], v[224:225]\n\t"
  "v_pk_fma_f32 v[226:227], v[182:183], v[182:183], v[226:227]\n\t"
  "v_pk_fma_f32 v[224:225], v[184:185], v[184:185], v[224:225]\n\t"
  "v_pk_fma_f32 v[226:227], v[186:187], v[186:187], v[226:227]\n\t"
  "v_pk_fma_f32 v[224:225], v[188:189], v[188:189], v[224:225]\n\t"
  "v_pk_fma_f32 v[226:227], v[190:191], v[190:191], v[226:227]\n\t"
  "v_pk_fma_f32 v[224:225], v[192:193], v[192:193], v[224:225]\n\t"
  "v_pk_fma_f32 v[226:227], v[194:195], v[194:195], v[226:227]\n\t"
  "v_pk_fma_f32 v[224:225], v[196:197], v[196:197], v[224:225]\n\t"
  "v_pk_fma_f32 v[226:227], v[198:199], v[198:199], v[226:227]\n\t"
  "v_pk_fma_f32 v[224:225], v[200:201], v[200:201], v[224:225]\n\t"
  "v_pk_fma_f32 v[226:227], v[202:203], v[202:203], v[226:227]\n\t"
  "s_nop 0\n\t"
  "v_pk_add_f32 v[224:225], v[224:225], v[226:227]\n\t"
  "v_mov_b32 v226, 0x3727c5ac\n\t"
  "v_add_f32 v222, v224, v225\n\t"
  "s_nop 1\n\t"
  "v_add_f32_dpp v222, v222, v222 row_ror:2 row_mask:0xf bank_mask:0xf\n\t"
  "s_nop 1\n\t"
  "v_add_f32_dpp v222, v222, v222 row_ror:4 row_mask:0xf bank_mask:0xf\n\t"
  "s_nop 1\n\t"
  "v_add_f32_dpp v222, v222, v222 row_ror:8 row_mask:0xf bank_mask:0xf\n\t"
  "v_mov_b32 v223, v222\n\t"
  "s_nop 1\n\t"
  "v_permlane32_swap_b32 v222, v223\n\t"
  "v_add_f32 v222, v222, v223\n\t"
  "v_fmamk_f32 v222, v222, 0x3ae38e39, v226\n\t"
  "v_rsq_f32 v222, v222\n\t"
  "s_nop 0\n\t"
  "v_pk_mul_f32 v[168:169], v[168:169], v[222:223] op_sel_hi:[1,0]\n\t"
  "v_pk_mul_f32 v[170:171], v[170:171], v[222:223] op_sel_hi:[1,0]\n\t"
  "v_pk_mul_f32 v[172:173], v[172:173], v[222:223] op_sel_hi:[1,0]\n\t"
  "v_pk_mul_f32 v[174:175], v[174:175], v[222:223] op_sel_hi:[1,0]\n\t"
  "v_cvt_pk_bf16_f32 v168, v168, v169\n\t"
  "v_cvt_pk_bf16_f32 v169, v170, v171\n\t"
  "v_cvt_pk_bf16_f32 v170, v172, v173\n\t"
  "v_cvt_pk_bf16_f32 v171, v174, v175\n\t"
  "buffer_store_dwordx4 v[168:171], v8, s[36:39], s69 offen sc1\n\t"
  "v_pk_mul_f32 v[176:177], v[176:177], v[222:223] op_sel_hi:[1,0]\n\t"
  "v_pk_mul_f32 v[178:179], v[178:179], v[222:223] op_sel_hi:[1,0]\n\t"
  "v_pk_mul_f32 v[180:181], v[180:181], v[222:223] op_sel_hi:[1,0]\n\t"
  "v_pk_mul_f32 v[182:183], v[182:183], v[222:223] op_sel_hi:[1,0]\n\t"
  "v_cvt_pk_bf16_f32 v176, v176, v177\n\t"
  "v_cvt_pk_bf16_f32 v177, v178, v179\n\t"
  "v_cvt_pk_bf16_f32 v178, v180, v181\n\t"
  "v_cvt_pk_bf16_f32 v179, v182, v183\n\t"
  "buffer_store_dwordx4 v[176:179], v9, s[36:39], s69 offen sc1\n\t"
  "v_pk_mul_f32 v[184:185], v[184:185], v[222:223] op_sel_hi:[1,0]\n\t"
  "v_pk_mul_f32 v[186:187], v[186:187], v[222:223] op_sel_hi:[1,0]\n\t"
  "v_pk_mul_f32 v[188:189], v[188:189], v[222:223] op_sel_hi:[1,0]\n\t"
  "v_pk_mul_f32 v[190:191], v[190:191], v[222:223] op_sel_hi:[1,0]\n\t"
  "v_cvt_pk_bf16_f32 v184, v184, v185\n\t"
  "v_cvt_pk_bf16_f32 v185, v186, v187\n\t"
  "v_cvt_pk_bf16_f32 v186, v188, v189\n\t"
  "v_cvt_pk_bf16_f32 v187, v190, v191\n\t"
  "buffer_store_dwordx4 v[184:187], v10, s[36:39], s69 offen sc1\n\t"
  "v_pk_mul_f32 v[192:193], v[192:193], v[222:223] op_sel_hi:[1,0]\n\t"
  "v_pk_mul_f32 v[194:195], v[194:195], v[222:223] op_sel_hi:[1,0]\n\t"
  "v_pk_mul_f32 v[196:197], v[196:197], v[222:223] op_sel_hi:[1,0]\n\t"
  "v_pk_mul_f32 v[198:199], v[198:199], v[222:223] op_sel_hi:[1,0]\n\t"
  "v_cvt_pk_bf16_f32 v192, v192, v193\n\t"
  "v_cvt_pk_bf16_f32 v193, v194, v195\n\t"
  "v_cvt_pk_bf16_f32 v194, v196, v197\n\t"
  "v_cvt_pk_bf16_f32 v195, v198, v199\n\t"
  "buffer_store_dwordx4 v[192:195], v11, s[36:39], s69 offen sc1\n\t"
  "v_pk_mul_f32 v[200:201], v[200:201], v[222:223] op_sel_hi:[1,0]\n\t"
  "v_pk_mul_f32 v[202:203], v[202:203], v[222:223] op_sel_hi:[1,0]\n\t"
  "v_cvt_pk_bf16_f32 v200, v200, v201\n\t"
  "v_cvt_pk_bf16_f32 v201, v202, v203\n\t"
  "buffer_store_dwordx2 v[200:201], v12, s[36:39], s69 offen sc1\n\t"
  "buffer_load_dwordx4 v[204:207], v0, s[36:39], s70 offen offset:0 sc1\n\t"
  "buffer_load_dwordx4 v[208:211], v0, s[36:39], s70 offen offset:1024 sc1\n\t"
  "buffer_load_dwordx4 v[212:215], v0, s[36:39], s70 offen offset:2048 sc1\n\t"
  "buffer_load_dwordx4 v[216:219], v0, s[36:39], s70 offen offset:3072 sc1\n\t"
  "s_add_u32 s76, s70, 4096\n\t"
  "buffer_load_dwordx2 v[220:221], v15, s[36:39], s76 offen sc1\n\t"
  "s_sub_u32 s77, s72, 8\n\t"
  "s_mov_b32 s79, 0\n\t"
  "3:\n\t"
  "ds_read_b32 v223, v228\n\t"
  "s_waitcnt lgkmcnt(0)\n\t"
  "v_cmp_le_i32 vcc, s77, v223\n\t"
  "s_cbranch_vccnz 4f\n\t"
  "s_sleep 2\n\t"
  "s_add_u32 s79, s79, 1\n\t"
  "s_cmp_lt_u32 s79, s80\n\t"
  "s_cbranch_scc1 3b\n\t"
  "s_mov_b32 s80, 0\n\t"
  "4:\n\t"
  "ds_write_b128 v231, v[132:135] offset:0\n\t"
  "ds_write_b128 v231, v[136:139] offset:1024\n\t"
  "ds_write_b128 v231, v[140:143] offset:2048\n\t"
  "ds_write_b128 v231, v[144:147] offset:3072\n\t"
  "ds_write_b128 v231, v[148:151] offset:4096\n\t"
  "ds_write_b128 v231, v[152:155] offset:5120\n\t"
  "ds_write_b128 v231, v[156:159] offset:6144\n\t"
  "ds_write_b128 v231, v[160:163] offset:7168\n\t"
  "ds_write_b128 v231, v[164:167] offset:8192\n\t"
  "s_cmp_eq_u32 s75, 64\n\t"
  "s_cbranch_scc1 6f\n\t"
  "v_cmp_le_i32 vcc, v230, v3\n\t"
  "s_cmp_eq_u64 vcc, -1\n\t"
  "s_cbranch_scc0 6f\n\t"
  "s_add_u32 s76, s68, 0\n\t"
  "buffer_load_dwordx4 v[24:27], v0, s[36:39], s76 offen offset:0 sc1\n\t"
  "buffer_load_dwordx4 v[28:31], v0, s[36:39], s76 offen offset:1024 sc1\n\t"
  "buffer_load_dwordx4 v[32:35], v0, s[36:39], s76 offen offset:2048 sc1\n\t"
  "buffer_load_dwordx4 v[36:39], v0, s[36:39], s76 offen offset:3072 sc1\n\t"
  "s_add_u32 s76, s68, 4096\n\t"
  "buffer_load_dwordx4 v[40:43], v0, s[36:39], s76 offen offset:0 sc1\n\t"
  "buffer_load_dwordx4 v[44:47], v0, s[36:39], s76 offen offset:1024 sc1\n\t"
  "buffer_load_dwordx4 v[48:51], v0, s[36:39], s76 offen offset:2048 sc1\n\t"
  "buffer_load_dwordx4 v[52:55], v0, s[36:39], s76 offen offset:3072 sc1\n\t"
  "s_add_u32 s76, s68, 16384\n\t"
  "buffer_load_dwordx4 v[56:59], v0, s[36:39], s76 offen offset:0 sc1\n\t"
  "buffer_load_dwordx4 v[60:63], v0, s[36:39], s76 offen offset:1024 sc1\n\t"
  "buffer_load_dwordx4 v[64:67], v0, s[36:39], s76 offen offset:2048 sc1\n\t"
  "buffer_load_dwordx4 v[68:71], v0, s[36:39], s76 offen offset:3072 sc1\n\t"
  "s_add_u32 s76, s68, 20480\n\t"
  "buffer_load_dwordx4 v[72:75], v0, s[36:39], s76 offen offset:0 sc1\n\t"
  "buffer_load_dwordx4 v[76:79], v0, s[36:39], s76 offen offset:1024 sc1\n\t"
  "buffer_load_dwordx4 v[80:83], v0, s[36:39], s76 offen offset:2048 sc1\n\t"
  "buffer_load_dwordx4 v[84:87], v0, s[36:39], s76 offen offset:3072 sc1\n\t"
  "s_add_u32 s76, s68, 32768\n\t"
  "buffer_load_dwordx4 v[88:91], v0, s[36:39], s76 offen offset:0 sc1\n\t"
  "buffer_load_dwordx4 v[92:95], v0, s[36:39], s76 offen offset:1024 sc1\n\t"
  "s_waitcnt vmcnt(23) lgkmcnt(0)\n\t"
  "buffer_store_dword v229, off, s[36:39], s40 sc1\n\t"
  "s_mov_b32 s72, s75\n\t"
  "s_branch 5b\n\t"
  "6:\n\t"
  "s_waitcnt vmcnt(5) lgkmcnt(0)\n\t"
  "buffer_store_dword v229, off, s[36:39], s40 sc1\n\t"
  "s_mov_b32 s72, s75\n\t"
  "s_cmp_lt_u32 s72, 64\n\t"
  "s_cbranch_scc1 1b\n\t"
  "s_waitcnt vmcnt(0) lgkmcnt(0)\n\t"
  : "+{v[0:7]}"(lc), "+{v[8:23]}"(so) :: CLOB_ALL);
  else if constexpr (layer == 0) asm volatile(
  "s_nop 4\n\t"
  "v_readlane_b32 s36, v3, 0\n\t"
  "v_readlane_b32 s37, v3, 1\n\t"
  "v_readlane_b32 s38, v3, 2\n\t"
  "v_readlane_b32 s39, v3, 3\n\t"
  "v_readlane_b32 s40, v3, 4\n\t"
  "v_readlane_b32 s41, v3, 5\n\t"
  "v_readlane_b32 s42, v3, 6\n\t"
  "v_readlane_b32 s43, v3, 7\n\t"
  "v_readlane_b32 s44, v3, 8\n\t"
  "v_readlane_b32 s45, v3, 9\n\t"
  "v_readlane_b32 s46, v3, 10\n\t"
  "v_readlane_b32 s47, v3, 11\n\t"
  "v_readlane_b32 s48, v3, 12\n\t"
  "v_readlane_b32 s49, v3, 13\n\t"
  "v_readlane_b32 s50, v3, 14\n\t"
  "v_readlane_b32 s51, v3, 15\n\t"
  "v_readlane_b32 s56, v3, 16\n\t"
  "v_readlane_b32 s57, v3, 17\n\t"
  "v_readlane_b32 s58, v3, 18\n\t"
  "s_mov_b32 s59, s39\n\t"
  "s_mov_b32 s72, 0\n\t"
  "s_mov_b32 s80, 0x40000\n\t"
  "s_mov_b32 s71, 0\n\t"
  "v_mov_b32 v3, -1\n\t"
  "s_nop 2\n\t"
  "s_lshr_b32 s73, s41, 9\n\t"
  "s_and_b32 s73, s73, 4\n\t"
  "s_add_u32 s73, s73, 147456\n\t"
  "v_mov_b32 v228, s73\n\t"
  "v_lshrrev_b32 v15, 1, v0\n\t"
  "v_add_u32 v230, s72, v7\n\t"
  "v_min_i32 v230, 63, v230\n\t"
  "v_readlane_b32 s68, v4, s72\n\t"
  "1:\n\t"
  "s_mov_b32 s79, 0\n\t"
  "21:\n\t"
  "v_cmp_le_i32 vcc, v230, v3\n\t"
  "s_cmp_eq_u64 vcc, -1\n\t"
  "s_cbranch_scc1 22f\n\t"
  "s_waitcnt vmcnt(24)\n\t"
  "buffer_load_dword v3, v2, s[36:39], 0 offen sc1\n\t"
  "s_sleep 4\n\t"
  "s_add_u32 s79, s79, 1\n\t"
  "s_cmp_lt_u32 s79, s80\n\t"
  "s_cbranch_scc1 21b\n\t"
  "s_mov_b32 s80, 0\n\t"
  "22:\n\t"
  "s_add_u32 s76, s68, 0\n\t"
  "buffer_load_dwordx4 v[24:27], v0, s[36:39], s76 offen offset:0 sc1\n\t"
  "buffer_load_dwordx4 v[28:31], v0, s[36:39], s76 offen offset:1024 sc1\n\t"
  "buffer_load_dwordx4 v[32:35], v0, s[36:39], s76 offen offset:2048 sc1\n\t"
  "buffer_load_dwordx4 v[36:39], v0, s[36:39], s76 offen offset:3072 sc1\n\t"
  "s_add_u32 s76, s68, 4096\n\t"
  "buffer_load_dwordx4 v[40:43], v0, s[36:39], s76 offen offset:0 sc1\n\t"
  "buffer_load_dwordx4 v[44:47], v0, s[36:39], s76 offen offset:1024 sc1\n\t"
  "buffer_load_dwordx4 v[48:51], v0, s[36:39], s76 offen offset:2048 sc1\n\t"
  "buffer_load_dwordx4 v[52:55], v0, s[36:39], s76 offen offset:3072 sc1\n\t"
  "s_add_u32 s76, s68, 16384\n\t"
  "buffer_load_dwordx4 v[56:59], v0, s[36:39], s76 offen offset:0 sc1\n\t"
  "buffer_load_dwordx4 v[60:63], v0, s[36:39], s76 offen offset:1024 sc1\n\t"
  "buffer_load_dwordx4 v[64:67], v0, s[36:39], s76 offen offset:2048 sc1\n\t"
  "buffer_load_dwordx4 v[68:71], v0, s[36:39], s76 offen offset:3072 sc1\n\t"
  "s_add_u32 s76, s68, 20480\n\t"
  "buffer_load_dwordx4 v[72:75], v0, s[36:39], s76 offen offset:0 sc1\n\t"
  "buffer_load_dwordx4 v[76:79], v0, s[36:39], s76 offen offset:1024 sc1\n\t"
  "buffer_load_dwordx4 v[80:83], v0, s[36:39], s76 offen offset:2048 sc1\n\t"
  "buffer_load_dwordx4 v[84:87], v0, s[36:39], s76 offen offset:3072 sc1\n\t"
  "s_add_u32 s76, s68, 32768\n\t"
  "buffer_load_dwordx4 v[88:91], v0, s[36:39], s76 offen offset:0 sc1\n\t"
  "buffer_load_dwordx4 v[92:95], v0, s[36:39], s76 offen offset:1024 sc1\n\t"
  "buffer_load_dword v3, v2, s[36:39], 0 offen sc1\n\t"
  "5:\n\t"
  "s_add_u32 s75, s72, 1\n\t"
  "v_readlane_b32 s69, v5, s72\n\t"
  "v_readlane_b32 s70, v6, s72\n\t"
  "s_and_b32 s76, s72, 7\n\t"
  "s_mul_i32 s76, s76, 18432\n\t"
  "v_add_u32 v231, s76, v1\n\t"
  "s_mul_i32 s71, s72, 36864\n\t"
  "v_mov_b32 v229, s72\n\t"
  "v_add_u32 v230, s75, v7\n\t"
  "v_min_i32 v230, 63, v230\n\t"
  "v_readlane_b32 s68, v4, s75\n\t"
  "ds_read_b128 v[96:99], v231 offset:0\n\t"
  "ds_read_b128 v[100:103], v231 offset:1024\n\t"
  "ds_read_b128 v[104:107], v231 offset:2048\n\t"
  "ds_read_b128 v[108:111], v231 offset:3072\n\t"
  "ds_read_b128 v[112:115], v231 offset:4096\n\t"
  "ds_read_b128 v[116:119], v231 offset:5120\n\t"
  "ds_read_b128 v[120:123], v231 offset:6144\n\t"
  "ds_read_b128 v[124:127], v231 offset:7168\n\t"
  "ds_read_b128 v[128:131], v231 offset:8192\n\t"
  "ds_write_b32 v228, v229\n\t"
  "s_waitcnt vmcnt(18)\n\t"
  "s_waitcnt lgkmcnt(9)\n\t"
  "v_mfma_f32_16x16x32_bf16 v[132:135], v[16:19], v[24:27], v[96:99]\n\t"
  "s_waitcnt vmcnt(17)\n\t"
  "v_mfma_f32_16x16x32_bf16 v[132:135], v[20:23], v[28:31], v[132:135]\n\t"
  "s_waitcnt vmcnt(16)\n\t"
  "s_waitcnt lgkmcnt(8)\n\t"
  "v_mfma_f32_16x16x32_bf16 v[136:139], v[16:19], v[32:35], v[100:103]\n\t"
  "s_waitcnt vmcnt(15)\n\t"
  "v_mfma_f32_16x16x32_bf16 v[136:139], v[20:23], v[36:39], v[136:139]\n\t"
  "s_waitcnt vmcnt(14)\n\t"
  "s_waitcnt lgkmcnt(7)\n\t"
  "v_mfma_f32_16x16x32_bf16 v[140:143], v[16:19], v[40:43], v[104:107]\n\t"
  "s_waitcnt vmcnt(13)\n\t"
  "v_mfma_f32_16x16x32_bf16 v[140:143], v[20:23], v[44:47], v[140:143]\n\t"
  "v_add_f32 v222, v132, v136\n\t"
  "v_add_f32 v223, v133, v137\n\t"
  "v_add_f32 v224, v134, v138\n\t"
  "v_add_f32 v225, v135, v139\n\t"
  "s_waitcnt vmcnt(12)\n\t"
  "s_waitcnt lgkmcnt(6)\n\t"
  "v_mfma_f32_16x16x32_bf16 v[144:147], v[16:19], v[48:51], v[108:111]\n\t"
  "s_waitcnt vmcnt(11)\n\t"
  "v_mfma_f32_16x16x32_bf16 v[144:147], v[20:23], v[52:55], v[144:147]\n\t"
  "v_add_f32 v222, v222, v140\n\t"
  "v_add_f32 v223, v223, v141\n\t"
  "v_add_f32 v224, v224, v142\n\t"
  "v_add_f32 v225, v225, v143\n\t"
  "s_waitcnt vmcnt(10)\n\t"
  "s_waitcnt lgkmcnt(5)\n\t"
  "v_mfma_f32_16x16x32_bf16 v[148:151], v[16:19], v[56:59], v[112:115]\n\t"
  "s_waitcnt vmcnt(9)\n\t"
  "v_mfma_f32_16x16x32_bf16 v[148:151], v[20:23], v[60:63], v[148:151]\n\t"
  "v_add_f32 v222, v222, v144\n\t"
  "v_add_f32 v223, v223, v145\n\t"
  "v_add_f32 v224, v224, v146\n\t"
  "v_add_f32 v225, v225, v147\n\t"
  "s_waitcnt vmcnt(8)\n\t"
  "s_waitcnt lgkmcnt(4)\n\t"
  "v_mfma_f32_16x16x32_bf16 v[152:155], v[16:19], v[64:67], v[116:119]\n\t"
  "s_waitcnt vmcnt(7)\n\t"
  "v_mfma_f32_16x16x32_bf16 v[152:155], v[20:23], v[68:71], v[152:155]\n\t"
  "v_add_f32 v222, v222, v148\n\t"
  "v_add_f32 v223, v223, v149\n\t"
  "v_add_f32 v224, v224, v150\n\t"
  "v_add_f32 v225, v225, v151\n\t"
  "s_waitcnt vmcnt(6)\n\t"
  "s_waitcnt lgkmcnt(3)\n\t"
  "v_mfma_f32_16x16x32_bf16 v[156:159], v[16:19], v[72:75], v[120:123]\n\t"
  "s_waitcnt vmcnt(5)\n\t"
  "v_mfma_f32_16x16x32_bf16 v[156:159], v[20:23], v[76:79], v[156:159]\n\t"
  "v_add_f32 v222, v222, v152\n\t"
  "v_add_f32 v223, v223, v153\n\t"
  "v_add_f32 v224, v224, v154\n\t"
  "v_add_f32 v225, v225, v155\n\t"
  "s_waitcnt vmcnt(4)\n\t"
  "s_waitcnt lgkmcnt(2)\n\t"
  "v_mfma_f32_16x16x32_bf16 v[160:163], v[16:19], v[80:83], v[124:127]\n\t"
  "s_waitcnt vmcnt(3)\n\t"
  "v_mfma_f32_16x16x32_bf16 v[160:163], v[20:23], v[84:87], v[160:163]\n\t"
  "v_add_f32 v222, v222, v156\n\t"
  "v_add_f32 v223, v223, v157\n\t"
  "v_add_f32 v224, v224, v158\n\t"
  "v_add_f32 v225, v225, v159\n\t"
  "s_waitcnt vmcnt(2)\n\t"
  "s_waitcnt lgkmcnt(1)\n\t"
  "v_mfma_f32_16x16x32_bf16 v[164:167], v[16:19], v[88:91], v[128:131]\n\t"
  "s_waitcnt vmcnt(1)\n\t"
  "v_mfma_f32_16x16x32_bf16 v[164:167], v[20:23], v[92:95], v[164:167]\n\t"
  "v_add_f32 v222, v222, v160\n\t"
  "v_add_f32 v223, v223, v161\n\t"
  "v_add_f32 v224, v224, v162\n\t"
  "v_add_f32 v225, v225, v163\n\t"
  "buffer_load_dword v3, v2, s[36:39], 0 offen sc1\n\t"
  "v_add_f32 v222, v222, v223\n\t"
  "v_add_f32 v224, v224, v225\n\t"
  "v_add_f32 v222, v222, v224\n\t"
  "v_add_f32 v223, v164, v165\n\t"
  "v_add_f32 v224, v166, v167\n\t"
  "v_add_f32 v222, v222, v223\n\t"
  "v_add_f32 v222, v222, v224\n\t"
  "s_nop 1\n\t"
  "v_add_f32_dpp v222, v222, v222 row_ror:2 row_mask:0xf bank_mask:0xf\n\t"
  "s_nop 1\n\t"
  "v_add_f32_dpp v222, v222, v222 row_ror:4 row_mask:0xf bank_mask:0xf\n\t"
  "s_nop 1\n\t"
  "v_add_f32_dpp v222, v222, v222 row_ror:8 row_mask:0xf bank_mask:0xf\n\t"
  "v_mov_b32 v223, v222\n\t"
  "s_nop 1\n\t"
  "v_permlane32_swap_b32 v222, v223\n\t"
  "v_add_f32 v222, v222, v223\n\t"
  "v_mul_f32 v222, 0xbae38e39, v222\n\t"
  "v_pk_add_f32 v[168:169], v[132:133], v[222:223] op_sel_hi:[1,0]\n\t"
  "v_pk_add_f32 v[170:171], v[134:135], v[222:223] op_sel_hi:[1,0]\n\t"
  "v_pk_add_f32 v[172:173], v[136:137], v[222:223] op_sel_hi:[1,0]\n\t"
  "v_pk_add_f32 v[174:175], v[138:139], v[222:223] op_sel_hi:[1,0]\n\t"
  "v_pk_add_f32 v[176:177], v[140:141], v[222:223] op_sel_hi:[1,0]\n\t"
  "v_pk_add_f32 v[178:179], v[142:143], v[222:223] op_sel_hi:[1,0]\n\t"
  "v_pk_add_f32 v[180:181], v[144:145], v[222:223] op_sel_hi:[1,0]\n\t"
  "v_pk_add_f32 v[182:183], v[146:147], v[222:223] op_sel_hi:[1,0]\n\t"
  "v_pk_add_f32 v[184:185], v[148:149], v[222:223] op_sel_hi:[1,0]\n\t"
  "v_pk_add_f32 v[186:187], v[150:151], v[222:223] op_sel_hi:[1,0]\n\t"
  "v_pk_add_f32 v[188:189], v[152:153], v[222:223] op_sel_hi:[1,0]\n\t"
  "v_pk_add_f32 v[190:191], v[154:155], v[222:223] op_sel_hi:[1,0]\n\t"
  "v_pk_add_f32 v[192:193], v[156:157], v[222:223] op_sel_hi:[1,0]\n\t"
  "v_pk_add_f32 v[194:195], v[158:159], v[222:223] op_sel_hi:[1,0]\n\t"
  "v_pk_add_f32 v[196:197], v[160:161], v[222:223] op_sel_hi:[1,0]\n\t"
  "v_pk_add_f32 v[198:199], v[162:163], v[222:223] op_sel_hi:[1,0]\n\t"
  "v_pk_add_f32 v[200:201], v[164:165], v[222:223] op_sel_hi:[1,0]\n\t"
  "v_pk_add_f32 v[202:203], v[166:167], v[222:223] op_sel_hi:[1,0]\n\t"
  "v_pk_mul_f32 v[224:225], v[168:169], v[168:169]\n\t"
  "v_pk_mul_f32 v[226:227], v[170:171], v[170:171]\n\t"
  "v_pk_fma_f32 v[224:225], v[172:173], v[172:173], v[224:225]\n\t"
  "v_pk_fma_f32 v[226:227], v[174:175], v[174:175], v[226:227]\n\t"
  "v_pk_fma_f32 v[224:225], v[176:177], v[176:177], v[224:225]\n\t"
  "v_pk_fma_f32 v[226:227], v[178:179], v[178:179], v[226:227]\n\t"
  "v_pk_fma_f32 v[224:225], v[180:181], v[180:181], v[224:225]\n\t"
  "v_pk_fma_f32 v[226:227], v[182:183], v[182:183], v[226:227]\n\t"
  "v_pk_fma_f32 v[224:225], v[184:185], v[184:185], v[224:225]\n\t"
  "v_pk_fma_f32 v[226:227], v[186:187], v[186:187], v[226:227]\n\t"
  "v_pk_fma_f32 v[224:225], v[188:189], v[188:189], v[224:225]\n\t"
  "v_pk_fma_f32 v[226:227], v[190:191], v[190:191], v[226:227]\n\t"
  "v_pk_fma_f32 v[224:225], v[192:193], v[192:193], v[224:225]\n\t"
  "v_pk_fma_f32 v[226:227], v[194:195], v[194:195], v[226:227]\n\t"
  "v_pk_fma_f32 v[224:225], v[196:197], v[196:197], v[224:225]\n\t"
  "v_pk_fma_f32 v[226:227], v[198:199], v[198:199], v[226:227]\n\t"
  "v_pk_fma_f32 v[224:225], v[200:201], v[200:201], v[224:225]\n\t"
  "v_pk_fma_f32 v[226:227], v[202:203], v[202:203], v[226:227]\n\t"
  "s_nop 0\n\t"
  "v_pk_add_f32 v[224:225], v[224:225], v[226:227]\n\t"
  "v_mov_b32 v226, 0x3727c5ac\n\t"
  "v_add_f32 v222, v224, v225\n\t"
  "s_nop 1\n\t"
  "v_add_f32_dpp v222, v222, v222 row_ror:2 row_mask:0xf bank_mask:0xf\n\t"
  "s_nop 1\n\t"
  "v_add_f32_dpp v222, v222, v222 row_ror:4 row_mask:0xf bank_mask:0xf\n\t"
  "s_nop 1\n\t"
  "v_add_f32_dpp v222, v222, v222 row_ror:8 row_mask:0xf bank_mask:0xf\n\t"
  "v_mov_b32 v223, v222\n\t"
  "s_nop 1\n\t"
  "v_permlane32_swap_b32 v222, v223\n\t"
  "v_add_f32 v222, v222, v223\n\t"
  "v_fmamk_f32 v222, v222, 0x3ae38e39, v226\n\t"
  "v_rsq_f32 v222, v222\n\t"
  "s_nop 0\n\t"
  "v_pk_mul_f32 v[168:169], v[168:169], v[222:223] op_sel_hi:[1,0]\n\t"
  "v_pk_mul_f32 v[170:171], v[170:171], v[222:223] op_sel_hi:[1,0]\n\t"
  "v_pk_mul_f32 v[172:173], v[172:173], v[222:223] op_sel_hi:[1,0]\n\t"
  "v_pk_mul_f32 v[174:175], v[174:175], v[222:223] op_sel_hi:[1,0]\n\t"
  "v_cvt_pk_bf16_f32 v168, v168, v169\n\t"
  "v_cvt_pk_bf16_f32 v169, v170, v171\n\t"
  "v_cvt_pk_bf16_f32 v170, v172, v173\n\t"
  "v_cvt_pk_bf16_f32 v171, v174, v175\n\t"
  "buffer_store_dwordx4 v[168:171], v8, s[36:39], s69 offen sc1\n\t"
  "v_pk_mul_f32 v[176:177], v[176:177], v[222:223] op_sel_hi:[1,0]\n\t"
  "v_pk_mul_f32 v[178:179], v[178:179], v[222:223] op_sel_hi:[1,0]\n\t"
  "v_pk_mul_f32 v[180:181], v[180:181], v[222:223] op_sel_hi:[1,0]\n\t"
  "v_pk_mul_f32 v[182:183], v[182:183], v[222:223] op_sel_hi:[1,0]\n\t"
  "v_cvt_pk_bf16_f32 v176, v176, v177\n\t"
  "v_cvt_pk_bf16_f32 v177, v178, v179\n\t"
  "v_cvt_pk_bf16_f32 v178, v180, v181\n\t"
  "v_cvt_pk_bf16_f32 v179, v182, v183\n\t"
  "buffer_store_dwordx4 v[176:179], v9, s[36:39], s69 offen sc1\n\t"
  "v_pk_mul_f32 v[184:185], v[184:185], v[222:223] op_sel_hi:[1,0]\n\t"
  "v_pk_mul_f32 v[186:187], v[186:187], v[222:223] op_sel_hi:[1,0]\n\t"
  "v_pk_mul_f32 v[188:189], v[188:189], v[222:223] op_sel_hi:[1,0]\n\t"
  "v_pk_mul_f32 v[190:191], v[190:191], v[222:223] op_sel_hi:[1,0]\n\t"
  "v_cvt_pk_bf16_f32 v184, v184, v185\n\t"
  "v_cvt_pk_bf16_f32 v185, v186, v187\n\t"
  "v_cvt_pk_bf16_f32 v186, v188, v189\n\t"
  "v_cvt_pk_bf16_f32 v187, v190, v191\n\t"
  "buffer_store_dwordx4 v[184:187], v10, s[36:39], s69 offen sc1\n\t"
  "v_pk_mul_f32 v[192:193], v[192:193], v[222:223] op_sel_hi:[1,0]\n\t"
  "v_pk_mul_f32 v[194:195], v[194:195], v[222:223] op_sel_hi:[1,0]\n\t"
  "v_pk_mul_f32 v[196:197], v[196:197], v[222:223] op_sel_hi:[1,0]\n\t"
  "v_pk_mul_f32 v[198:199], v[198:199], v[222:223] op_sel_hi:[1,0]\n\t"
  "v_cvt_pk_bf16_f32 v192, v192, v193\n\t"
  "v_cvt_pk_bf16_f32 v193, v194, v195\n\t"
  "v_cvt_pk_bf16_f32 v194, v196, v197\n\t"
  "v_cvt_pk_bf16_f32 v195, v198, v199\n\t"
  "buffer_store_dwordx4 v[192:195], v11, s[36:39], s69 offen sc1\n\t"
  "v_pk_mul_f32 v[200:201], v[200:201], v[222:223] op_sel_hi:[1,0]\n\t"
  "v_pk_mul_f32 v[202:203], v[202:203], v[222:223] op_sel_hi:[1,0]\n\t"
  "v_cvt_pk_bf16_f32 v200, v200, v201\n\t"
  "v_cvt_pk_bf16_f32 v201, v202, v203\n\t"
  "buffer_store_dwordx2 v[200:201], v12, s[36:39], s69 offen sc1\n\t"
  "v_cvt_pk_bf16_f32 v204, v132, v133\n\t"
  "v_cvt_pk_bf16_f32 v205, v134, v135\n\t"
  "v_cvt_pk_bf16_f32 v206, v136, v137\n\t"
  "v_cvt_pk_bf16_f32 v207, v138, v139\n\t"
  "v_cvt_pk_bf16_f32 v208, v140, v141\n\t"
  "v_cvt_pk_bf16_f32 v209, v142, v143\n\t"
  "v_cvt_pk_bf16_f32 v210, v144, v145\n\t"
  "v_cvt_pk_bf16_f32 v211, v146, v147\n\t"
  "v_cvt_pk_bf16_f32 v212, v148, v149\n\t"
  "v_cvt_pk_bf16_f32 v213, v150, v151\n\t"
  "v_cvt_pk_bf16_f32 v214, v152, v153\n\t"
  "v_cvt_pk_bf16_f32 v215, v154, v155\n\t"
  "v_cvt_pk_bf16_f32 v216, v156, v157\n\t"
  "v_cvt_pk_bf16_f32 v217, v158, v159\n\t"
  "v_cvt_pk_bf16_f32 v218, v160, v161\n\t"
  "v_cvt_pk_bf16_f32 v219, v162, v163\n\t"
  "v_cvt_pk_bf16_f32 v220, v164, v165\n\t"
  "v_cvt_pk_bf16_f32 v221, v166, v167\n\t"
  "buffer_store_dwordx4 v[204:207], v0, s[36:39], s70 offen offset:0 sc1\n\t"
  "buffer_store_dwordx4 v[208:211], v0, s[36:39], s70 offen offset:1024 sc1\n\t"
  "buffer_store_dwordx4 v[212:215], v0, s[36:39], s70 offen offset:2048 sc1\n\t"
  "buffer_store_dwordx4 v[216:219], v0, s[36:39], s70 offen offset:3072 sc1\n\t"
  "s_add_u32 s76, s70, 4096\n\t"
  "buffer_store_dwordx2 v[220:221], v15, s[36:39], s76 offen sc1\n\t"
  "s_cmp_eq_u32 s75, 64\n\t"
  "s_cbranch_scc1 6f\n\t"
  "v_cmp_le_i32 vcc, v230, v3\n\t"
  "s_cmp_eq_u64 vcc, -1\n\t"
  "s_cbranch_scc0 6f\n\t"
  "s_add_u32 s76, s68, 0\n\t"
  "buffer_load_dwordx4 v[24:27], v0, s[36:39], s76 offen offset:0 sc1\n\t"
  "buffer_load_dwordx4 v[28:31], v0, s[36:39], s76 offen offset:1024 sc1\n\t"
  "buffer_load_dwordx4 v[32:35], v0, s[36:39], s76 offen offset:2048 sc1\n\t"
  "buffer_load_dwordx4 v[36:39], v0, s[36:39], s76 offen offset:3072 sc1\n\t"
  "s_add_u32 s76, s68, 4096\n\t"
  "buffer_load_dwordx4 v[40:43], v0, s[36:39], s76 offen offset:0 sc1\n\t"
  "buffer_load_dwordx4 v[44:47], v0, s[36:39], s76 offen offset:1024 sc1\n\t"
  "buffer_load_dwordx4 v[48:51], v0, s[36:39], s76 offen offset:2048 sc1\n\t"
  "buffer_load_dwordx4 v[52:55], v0, s[36:39], s76 offen offset:3072 sc1\n\t"
  "s_add_u32 s76, s68, 16384\n\t"
  "buffer_load_dwordx4 v[56:59], v0, s[36:39], s76 offen offset:0 sc1\n\t"
  "buffer_load_dwordx4 v[60:63], v0, s[36:39], s76 offen offset:1024 sc1\n\t"
  "buffer_load_dwordx4 v[64:67], v0, s[36:39], s76 offen offset:2048 sc1\n\t"
  "buffer_load_dwordx4 v[68:71], v0, s[36:39], s76 offen offset:3072 sc1\n\t"
  "s_add_u32 s76, s68, 20480\n\t"
  "buffer_load_dwordx4 v[72:75], v0, s[36:39], s76 offen offset:0 sc1\n\t"
  "buffer_load_dwordx4 v[76:79], v0, s[36:39], s76 offen offset:1024 sc1\n\t"
  "buffer_load_dwordx4 v[80:83], v0, s[36:39], s76 offen offset:2048 sc1\n\t"
  "buffer_load_dwordx4 v[84:87], v0, s[36:39], s76 offen offset:3072 sc1\n\t"
  "s_add_u32 s76, s68, 32768\n\t"
  "buffer_load_dwordx4 v[88:91], v0, s[36:39], s76 offen offset:0 sc1\n\t"
  "buffer_load_dwordx4 v[92:95], v0, s[36:39], s76 offen offset:1024 sc1\n\t"
  "s_waitcnt vmcnt(23) lgkmcnt(0)\n\t"
  "buffer_store_dword v229, off, s[36:39], s40 sc1\n\t"
  "s_mov_b32 s72, s75\n\t"
  "s_branch 5b\n\t"
  "6:\n\t"
  "s_waitcnt vmcnt(5) lgkmcnt(0)\n\t"
  "buffer_store_dword v229, off, s[36:39], s40 sc1\n\t"
  "s_mov_b32 s72, s75\n\t"
  "s_cmp_lt_u32 s72, 64\n\t"
  "s_cbranch_scc1 1b\n\t"
  "s_waitcnt vmcnt(0)\n\t"
  "buffer_store_dword v229, off, s[36:39], s40 offset:16 sc1\n\t"
  "s_waitcnt vmcnt(0) lgkmcnt(0)\n\t"
  : "+{v[0:7]}"(lc), "+{v[8:23]}"(so) :: CLOB_ALL);
  else asm volatile(
  "s_nop 4\n\t"
  "v_readlane_b32 s36, v3, 0\n\t"
  "v_readlane_b32 s37, v3, 1\n\t"
  "v_readlane_b32 s38, v3, 2\n\t"
  "v_readlane_b32 s39, v3, 3\n\t"
  "v_readlane_b32 s40, v3, 4\n\t"
  "v_readlane_b32 s41, v3, 5\n\t"
  "v_readlane_b32 s42, v3, 6\n\t"
  "v_readlane_b32 s43, v3, 7\n\t"
  "v_readlane_b32 s44, v3, 8\n\t"
  "v_readlane_b32 s45, v3, 9\n\t"
  "v_readlane_b32 s46, v3, 10\n\t"
  "v_readlane_b32 s47, v3, 11\n\t"
  "v_readlane_b32 s48, v3, 12\n\t"
  "v_readlane_b32 s49, v3, 13\n\t"
  "v_readlane_b32 s50, v3, 14\n\t"
  "v_readlane_b32 s51, v3, 15\n\t"
  "v_readlane_b32 s56, v3, 16\n\t"
  "v_readlane_b32 s57, v3, 17\n\t"
  "v_readlane_b32 s58, v3, 18\n\t"
  "s_mov_b32 s59, s39\n\t"
  "s_mov_b32 s72, 0\n\t"
  "s_mov_b32 s80, 0x40000\n\t"
  "s_mov_b32 s71, 0\n\t"
  "v_mov_b32 v3, -1\n\t"
  "s_nop 2\n\t"
  "s_lshr_b32 s73, s41, 9\n\t"
  "s_and_b32 s73, s73, 4\n\t"
  "s_add_u32 s73, s73, 147456\n\t"
  "v_mov_b32 v228, s73\n\t"
  "v_lshrrev_b32 v15, 1, v0\n\t"
  "v_add_u32 v230, s72, v7\n\t"
  "v_min_i32 v230, 63, v230\n\t"
  "v_readlane_b32 s68, v4, s72\n\t"
  "1:\n\t"
  "s_mov_b32 s79, 0\n\t"
  "21:\n\t"
  "v_cmp_le_i32 vcc, v230, v3\n\t"
  "s_cmp_eq_u64 vcc, -1\n\t"
  "s_cbranch_scc1 22f\n\t"
  "s_waitcnt vmcnt(24)\n\t"
  "buffer_load_dword v3, v2, s[36:39], 0 offen sc1\n\t"
  "s_sleep 4\n\t"
  "s_add_u32 s79, s79, 1\n\t"
  "s_cmp_lt_u32 s79, s80\n\t"
  "s_cbranch_scc1 21b\n\t"
  "s_mov_b32 s80, 0\n\t"
  "22:\n\t"
  "s_add_u32 s76, s68, 0\n\t"
  "buffer_load_dwordx4 v[24:27], v0, s[36:39], s76 offen offset:0 sc1\n\t"
  "buffer_load_dwordx4 v[28:31], v0, s[36:39], s76 offen offset:1024 sc1\n\t"
  "buffer_load_dwordx4 v[32:35], v0, s[36:39], s76 offen offset:2048 sc1\n\t"
  "buffer_load_dwordx4 v[36:39], v0, s[36:39], s76 offen offset:3072 sc1\n\t"
  "s_add_u32 s76, s68, 4096\n\t"
  "buffer_load_dwordx4 v[40:43], v0, s[36:39], s76 offen offset:0 sc1\n\t"
  "buffer_load_dwordx4 v[44:47], v0, s[36:39], s76 offen offset:1024 sc1\n\t"
  "buffer_load_dwordx4 v[48:51], v0, s[36:39], s76 offen offset:2048 sc1\n\t"
  "buffer_load_dwordx4 v[52:55], v0, s[36:39], s76 offen offset:3072 sc1\n\t"
  "s_add_u32 s76, s68, 16384\n\t"
  "buffer_load_dwordx4 v[56:59], v0, s[36:39], s76 offen offset:0 sc1\n\t"
  "buffer_load_dwordx4 v[60:63], v0, s[36:39], s76 offen offset:1024 sc1\n\t"
  "buffer_load_dwordx4 v[64:67], v0, s[36:39], s76 offen offset:2048 sc1\n\t"
  "buffer_load_dwordx4 v[68:71], v0, s[36:39], s76 offen offset:3072 sc1\n\t"
  "s_add_u32 s76, s68, 20480\n\t"
  "buffer_load_dwordx4 v[72:75], v0, s[36:39], s76 offen offset:0 sc1\n\t"
  "buffer_load_dwordx4 v[76:79], v0, s[36:39], s76 offen offset:1024 sc1\n\t"
  "buffer_load_dwordx4 v[80:83], v0, s[36:39], s76 offen offset:2048 sc1\n\t"
  "buffer_load_dwordx4 v[84:87], v0, s[36:39], s76 offen offset:3072 sc1\n\t"
  "s_add_u32 s76, s68, 32768\n\t"
  "buffer_load_dwordx4 v[88:91], v0, s[36:39], s76 offen offset:0 sc1\n\t"
  "buffer_load_dwordx4 v[92:95], v0, s[36:39], s76 offen offset:1024 sc1\n\t"
  "5:\n\t"
  "s_add_u32 s75, s72, 1\n\t"
  "v_readlane_b32 s69, v5, s72\n\t"
  "v_readlane_b32 s70, v6, s72\n\t"
  "s_and_b32 s76, s72, 7\n\t"
  "s_mul_i32 s76, s76, 18432\n\t"
  "v_add_u32 v231, s76, v1\n\t"
  "s_mul_i32 s71, s72, 36864\n\t"
  "v_mov_b32 v229, s72\n\t"
  "v_add_u32 v230, s75, v7\n\t"
  "v_min_i32 v230, 63, v230\n\t"
  "v_readlane_b32 s68, v4, s75\n\t"
  "ds_read_b128 v[96:99], v231 offset:0\n\t"
  "ds_read_b128 v[100:103], v231 offset:1024\n\t"
  "ds_read_b128 v[104:107], v231 offset:2048\n\t"
  "ds_read_b128 v[108:111], v231 offset:3072\n\t"
  "ds_read_b128 v[112:115], v231 offset:4096\n\t"
  "ds_read_b128 v[116:119], v231 offset:5120\n\t"
  "ds_read_b128 v[120:123], v231 offset:6144\n\t"
  "ds_read_b128 v[124:127], v231 offset:7168\n\t"
  "ds_read_b128 v[128:131], v231 offset:8192\n\t"
  "ds_write_b32 v228, v229\n\t"
  "s_waitcnt vmcnt(17)\n\t"
  "s_waitcnt lgkmcnt(9)\n\t"
  "v_mfma_f32_16x16x32_bf16 v[132:135], v[16:19], v[24:27], v[96:99]\n\t"
  "s_waitcnt vmcnt(16)\n\t"
  "v_mfma_f32_16x16x32_bf16 v[132:135], v[20:23], v[28:31], v[132:135]\n\t"
  "s_waitcnt vmcnt(15)\n\t"
  "s_waitcnt lgkmcnt(8)\n\t"
  "v_mfma_f32_16x16x32_bf16 v[136:139], v[16:19], v[32:35], v[100:103]\n\t"
  "s_waitcnt vmcnt(14)\n\t"
  "v_mfma_f32_16x16x32_bf16 v[136:139], v[20:23], v[36:39], v[136:139]\n\t"
  "s_waitcnt vmcnt(13)\n\t"
  "s_waitcnt lgkmcnt(7)\n\t"
  "v_mfma_f32_16x16x32_bf16 v[140:143], v[16:19], v[40:43], v[104:107]\n\t"
  "s_waitcnt vmcnt(12)\n\t"
  "v_mfma_f32_16x16x32_bf16 v[140:143], v[20:23], v[44:47], v[140:143]\n\t"
  "s_waitcnt vmcnt(11)\n\t"
  "s_waitcnt lgkmcnt(6)\n\t"
  "v_mfma_f32_16x16x32_bf16 v[144:147], v[16:19], v[48:51], v[108:111]\n\t"
  "s_waitcnt vmcnt(10)\n\t"
  "v_mfma_f32_16x16x32_bf16 v[144:147], v[20:23], v[52:55], v[144:147]\n\t"
  "s_waitcnt vmcnt(9)\n\t"
  "s_waitcnt lgkmcnt(5)\n\t"
  "v_mfma_f32_16x16x32_bf16 v[148:151], v[16:19], v[56:59], v[112:115]\n\t"
  "s_waitcnt vmcnt(8)\n\t"
  "v_mfma_f32_16x16x32_bf16 v[148:151], v[20:23], v[60:63], v[148:151]\n\t"
  "s_waitcnt vmcnt(7)\n\t"
  "s_waitcnt lgkmcnt(4)\n\t"
  "v_mfma_f32_16x16x32_bf16 v[152:155], v[16:19], v[64:67], v[116:119]\n\t"
  "s_waitcnt vmcnt(6)\n\t"
  "v_mfma_f32_16x16x32_bf16 v[152:155], v[20:23], v[68:71], v[152:155]\n\t"
  "s_waitcnt vmcnt(5)\n\t"
  "s_waitcnt lgkmcnt(3)\n\t"
  "v_mfma_f32_16x16x32_bf16 v[156:159], v[16:19], v[72:75], v[120:123]\n\t"
  "s_waitcnt vmcnt(4)\n\t"
  "v_mfma_f32_16x16x32_bf16 v[156:159], v[20:23], v[76:79], v[156:159]\n\t"
  "s_waitcnt vmcnt(3)\n\t"
  "s_waitcnt lgkmcnt(2)\n\t"
  "v_mfma_f32_16x16x32_bf16 v[160:163], v[16:19], v[80:83], v[124:127]\n\t"
  "s_waitcnt vmcnt(2)\n\t"
  "v_mfma_f32_16x16x32_bf16 v[160:163], v[20:23], v[84:87], v[160:163]\n\t"
  "s_waitcnt vmcnt(1)\n\t"
  "s_waitcnt lgkmcnt(1)\n\t"
  "v_mfma_f32_16x16x32_bf16 v[164:167], v[16:19], v[88:91], v[128:131]\n\t"
  "s_waitcnt vmcnt(0)\n\t"
  "v_mfma_f32_16x16x32_bf16 v[164:167], v[20:23], v[92:95], v[164:167]\n\t"
  "buffer_load_dword v3, v2, s[36:39], 0 offen sc1\n\t"
  "buffer_store_dword v229, off, s[36:39], s40 sc1\n\t"
  "v_cvt_pk_bf16_f32 v168, v132, v133\n\t"
  "v_cvt_pk_bf16_f32 v169, v134, v135\n\t"
  "buffer_store_dwordx2 v[168:169], v13, s[56:59], s71 offen offset:0\n\t"
  "v_cvt_pk_bf16_f32 v170, v136, v137\n\t"
  "v_cvt_pk_bf16_f32 v171, v138, v139\n\t"
  "buffer_store_dwordx2 v[170:171], v13, s[56:59], s71 offen offset:32\n\t"
  "v_cvt_pk_bf16_f32 v172, v140, v141\n\t"
  "v_cvt_pk_bf16_f32 v173, v142, v143\n\t"
  "buffer_store_dwordx2 v[172:173], v13, s[56:59], s71 offen offset:256\n\t"
  "v_cvt_pk_bf16_f32 v174, v144, v145\n\t"
  "v_cvt_pk_bf16_f32 v175, v146, v147\n\t"
  "buffer_store_dwordx2 v[174:175], v13, s[56:59], s71 offen offset:288\n\t"
  "v_cvt_pk_bf16_f32 v176, v148, v149\n\t"
  "v_cvt_pk_bf16_f32 v177, v150, v151\n\t"
  "buffer_store_dwordx2 v[176:177], v13, s[56:59], s71 offen offset:512\n\t"
  "v_cvt_pk_bf16_f32 v178, v152, v153\n\t"
  "v_cvt_pk_bf16_f32 v179, v154, v155\n\t"
  "buffer_store_dwordx2 v[178:179], v13, s[56:59], s71 offen offset:544\n\t"
  "v_cvt_pk_bf16_f32 v180, v156, v157\n\t"
  "v_cvt_pk_bf16_f32 v181, v158, v159\n\t"
  "buffer_store_dwordx2 v[180:181], v13, s[56:59], s71 offen offset:768\n\t"
  "v_cvt_pk_bf16_f32 v182, v160, v161\n\t"
  "v_cvt_pk_bf16_f32 v183, v162, v163\n\t"
  "buffer_store_dwordx2 v[182:183], v13, s[56:59], s71 offen offset:800\n\t"
  "v_cvt_pk_bf16_f32 v184, v164, v165\n\t"
  "v_cvt_pk_bf16_f32 v185, v166, v167\n\t"
  "buffer_store_dwordx2 v[184:185], v14, s[56:59], s71 offen offset:0\n\t"
  "s_mov_b32 s72, s75\n\t"
  "s_cmp_lt_u32 s72, 64\n\t"
  "s_cbranch_scc1 1b\n\t"
  "s_waitcnt vmcnt(0) lgkmcnt(0)\n\t"
  : "+{v[0:7]}"(lc), "+{v[8:23]}"(so) :: CLOB_ALL);
}

__device__ __forceinline__ int mem_col(int set, int e, int s) {
  if (set < 16) return 144 * (set >> 2) + 16 * (4 * ((set >> 1) & 1) + e) + 8 * (set & 1) + s;
  return 144 * (2 * (set - 16) + (s >> 2)) + 128 + 4 * (s & 3) + e;
}
__device__ __forceinline__ void sp_slot(int ks, int rho, int& ga, int& hb, int& s) {
  const int g = (rho >> 2) & 3, jj = (rho >> 4) * 4 + (rho & 3);
  ga = 2 * (ks & 1) + (g >> 1); hb = (jj >> 1) & 1; s = 2 * (2 * (g & 1) + (jj >> 2)) + (jj & 1);
}
__device__ __forceinline__ int wo_row(int ks, int rho) {
  int ga, hb, s; sp_slot(ks, rho, ga, hb, s);
  return 32 * (2 * (ks >> 1) + (ga >> 1)) + 16 * (s >> 2) + 4 * (2 * (ga & 1) + hb) + (s & 3);
}
#define CH_D1 4
#define CH_D0 1
#define CH_D3 0
#define CH_D2 6

__device__ __forceinline__ void role_chain(Ctx& c, const Params& p) {
  constexpr int LK = 9216, LI = LK + 8192, LO = LI + 4096;
  const int h = c.idx, w = c.w, lane = c.lane, t = lane * 16;
  v32u a = {};
  v16u b = {};
  #pragma unroll
  for (int q = 0; q < 2; ++q) {
    int row, byte, ga, hb, sl; slab_src<8>(w * 2 + q, lane, row, byte); sp_slot(0, row, ga, hb, sl);
    a[20 + q] = mem_col(0, 2 * ga + hb, sl) * (HD * 2) + h * 256 + byte;
    a[22 + q] = (mem_col(16, 2 * ga + hb, sl) - 128) * (HD * 2) + h * 256 + byte;
    a[24 + q] = slab_rd<8>(lane, 2 * w + q);
  }
  const v4u sq = make_srd(p.wq, DM * HD * 2), sk = make_srd(p.wk, DM * HD * 2), sv = make_srd(p.wv, DM * HD * 2), so = make_srd(p.wo, 0), sm = make_srd(p.mem0, 0);
  sfor<0, 6>([&](auto k) {
    const int d = 90112 + (k >> 1) * 24576 + (w * 2 + (k & 1)) * 1024, r = (k < 4 ? (k >> 1) * 8 : 64) * HD * 2;
    dma<0, false>(sq, a[20 + (k & 1)], r, d); dma<0, false>(sk, a[20 + (k & 1)], r, d + 8192); dma<0, false>(sv, a[20 + (k & 1)], r, d + 16384);
  });
  __builtin_amdgcn_sched_barrier(0);
  #pragma unroll
  for (int k = 0; k < 9; ++k) { int row, byte; slab_src<36>(k, lane, row, byte); a[2 + k] = (h * 128 + wo_row(0, row) + wo_row(0, 8 * w)) * (DM * 2) + byte; }
  #pragma unroll
  for (int k = 0; k < 9; ++k) {
    const unsigned r = slab_rd<36>(lane, w * 9 + k);
    if (k < 6) a[26 + k] = r; else b[k - 6] = r;
    const int i = ((w * 9 + k) * 64 + lane) * 2, un = i >> 6, j = (i >> 3) & 7;
    b[4 + k] = ((2 * (un & 3) + (j & 1)) * DM + mem_col(un >> 2, j >> 1, i & 7)) * 2;
  }
  c.pb = 0;
  const int fa = flag_at(c.layer, R_KV + (((4 * h) / 3) >> 2), ((4 * h) / 3) & 3), fb = flag_at(c.layer, R_KV + (((4 * h + 3) / 3) >> 2), ((4 * h + 3) / 3) & 3);
  const int ue = ((h >> 3) << 4) + (h & 7), unit = w < 2 ? ue + 8 * w : 64 + h;
  const v8u lanec = {(unsigned)t, (lane & 2) ? 0xEEEEu : 0x4444u,
                     (unsigned)((lane >> 5) * 512 + ((lane >> 2) & 3) * 128 + ((lane >> 4) & 1) * 64 + (lane & 3) * 16), (unsigned)((lane ^ (lane >> 4)) * 8),
                     (unsigned)(LI + w * 1024 + (lane & 15) * 32 + ((lane >> 4) ^ ((lane >> 2) & 3)) * 8),
                     (unsigned)((lane & 2) ? ((t >> 1) & 0x1E0) + (t & 0x10) : LO + w * 512 + (lane >> 5) * 256 + (((lane & 15) ^ ((lane >> 3) & 2)) * 16)),
                     (unsigned)(((t & 0x80) << 8) + ((t & 0x200) << 5) + ((t & 0x100) << 4) + (t & 0x70)),
                     (unsigned)(((t & 0x200) << 5) + ((t & 0x100) << 4) + ((t & 0xF0) >> 2))};
  const int dbg = DBG_OFF + ((((c.layer * 128 + c.idx) * 4 + c.w) * 5 + 4) * 64 + 4) * 4;
  const int xs[33] = {c.layer ^ POLX, 0, (int)c.rs.x, (int)c.rs.y, (int)c.rs.z, (int)c.rs.w,
                      A_VF + h * 16384 + w * 2048, A_KM + spd(h * 4096) + w * 512, A_KM + spd(h * 4096) + 2048 + w * 512,
                      RING_OFF, RING_OFF, MEM_OFF + 32768, flag_at(c.layer, h, w), w, h, fa, fb - fa, h * 128 + 8 * w * 16384,
                      h * 128 + (32 + 2 * (w >> 1)) * 16384 + (w & 1) * 64, (unit >> 1) * 16384 + (unit & 1) * 4096,
                      unit * 128 + (unit >= 64 ? 8192 : 0), (ue >> 1) * 16384 + (ue & 1) * 4096 + w * 1024, dbg,
                      (int)sq.x, (int)sq.y, (int)sk.x, (int)sk.y, (int)sv.x, (int)sv.y, (int)so.x, (int)so.y, (int)sm.x, (int)sm.y};
  a[0] = lane_table(lane, xs);
#define K_FIRST 0
  asm volatile(
  "s_nop 4\n\t"
  "v_readlane_b32 s34, v8, 0\n\t"
  "v_readlane_b32 s35, v8, 1\n\t"
  "v_readlane_b32 s36, v8, 2\n\t"
  "v_readlane_b32 s37, v8, 3\n\t"
  "v_readlane_b32 s38, v8, 4\n\t"
  "v_readlane_b32 s39, v8, 5\n\t"
  "v_readlane_b32 s40, v8, 6\n\t"
  "v_readlane_b32 s41, v8, 7\n\t"
  "v_readlane_b32 s42, v8, 8\n\t"
  "v_readlane_b32 s43, v8, 9\n\t"
  "v_readlane_b32 s44, v8, 10\n\t"
  "v_readlane_b32 s45, v8, 11\n\t"
  "v_readlane_b32 s46, v8, 12\n\t"
  "v_readlane_b32 s47, v8, 13\n\t"
  "v_readlane_b32 s48, v8, 14\n\t"
  "v_readlane_b32 s49, v8, 15\n\t"
  "v_readlane_b32 s50, v8, 16\n\t"
  "v_readlane_b32 s51, v8, 17\n\t"
  "v_readlane_b32 s52, v8, 18\n\t"
  "v_readlane_b32 s53, v8, 19\n\t"
  "v_readlane_b32 s54, v8, 20\n\t"
  "v_readlane_b32 s55, v8, 21\n\t"
  "v_readlane_b32 s79, v8, 22\n\t"
  "v_readlane_b32 s56, v8, 23\n\t"
  "v_readlane_b32 s57, v8, 24\n\t"
  "s_mov_b32 s58, 4718592\n\t"
  "s_mov_b32 s59, s39\n\t"
  "v_readlane_b32 s60, v8, 25\n\t"
  "v_readlane_b32 s61, v8, 26\n\t"
  "s_mov_b32 s62, 4718592\n\t"
  "s_mov_b32 s63, s39\n\t"
  "v_readlane_b32 s64, v8, 27\n\t"
  "v_readlane_b32 s65, v8, 28\n\t"
  "s_mov_b32 s66, 4718592\n\t"
  "s_mov_b32 s67, s39\n\t"
  "v_readlane_b32 s68, v8, 29\n\t"
  "v_readlane_b32 s69, v8, 30\n\t"
  "s_mov_b32 s70, 4718592\n\t"
  "s_mov_b32 s71, s39\n\t"
  "s_lshl_b32 s72, s47, 11\n\t"
  "s_mul_i32 s73, s47, 9216\n\t"
  "s_lshl_b32 s75, s47, 14\n\t"
  "s_add_u32 s75, s75, 24576\n\t"
  "v_add_u32 v54, s75, v0\n\t"
  "s_mov_b32 s74, 0\n\t"
  "s_add_u32 m0, s73, 0\n\t"
  "s_nop 0\n\t"
  "buffer_load_dwordx4 v10, s[68:71], s74 offen lds\n\t"
  "s_add_u32 m0, s73, 1024\n\t"
  "s_nop 0\n\t"
  "buffer_load_dwordx4 v11, s[68:71], s74 offen lds\n\t"
  "s_add_u32 m0, s73, 2048\n\t"
  "s_nop 0\n\t"
  "buffer_load_dwordx4 v12, s[68:71], s74 offen lds\n\t"
  "s_add_u32 m0, s73, 3072\n\t"
  "s_nop 0\n\t"
  "buffer_load_dwordx4 v13, s[68:71], s74 offen lds\n\t"
  "s_add_u32 m0, s73, 4096\n\t"
  "s_nop 0\n\t"
  "buffer_load_dwordx4 v14, s[68:71], s74 offen lds\n\t"
  "s_add_u32 m0, s73, 5120\n\t"
  "s_nop 0\n\t"
  "buffer_load_dwordx4 v15, s[68:71], s74 offen lds\n\t"
  "s_add_u32 m0, s73, 6144\n\t"
  "s_nop 0\n\t"
  "buffer_load_dwordx4 v16, s[68:71], s74 offen lds\n\t"
  "s_add_u32 m0, s73, 7168\n\t"
  "s_nop 0\n\t"
  "buffer_load_dwordx4 v17, s[68:71], s74 offen lds\n\t"
  "s_add_u32 m0, s73, 8192\n\t"
  "s_nop 0\n\t"
  "buffer_load_dwordx4 v18, s[68:71], s74 offen lds\n\t"
  "s_mov_b32 s74, 36864\n\t"
  "s_add_u32 m0, s73, 36864\n\t"
  "s_nop 0\n\t"
  "buffer_load_dwordx4 v10, s[68:71], s74 offen lds\n\t"
  "s_add_u32 m0, s73, 37888\n\t"
  "s_nop 0\n\t"
  "buffer_load_dwordx4 v11, s[68:71], s74 offen lds\n\t"
  "s_add_u32 m0, s73, 38912\n\t"
  "s_nop 0\n\t"
  "buffer_load_dwordx4 v12, s[68:71], s74 offen lds\n\t"
  "s_add_u32 m0, s73, 39936\n\t"
  "s_nop 0\n\t"
  "buffer_load_dwordx4 v13, s[68:71], s74 offen lds\n\t"
  "s_add_u32 m0, s73, 40960\n\t"
  "s_nop 0\n\t"
  "buffer_load_dwordx4 v14, s[68:71], s74 offen lds\n\t"
  "s_add_u32 m0, s73, 41984\n\t"
  "s_nop 0\n\t"
  "buffer_load_dwordx4 v15, s[68:71], s74 offen lds\n\t"
  "s_add_u32 m0, s73, 43008\n\t"
  "s_nop 0\n\t"
  "buffer_load_dwordx4 v16, s[68:71], s74 offen lds\n\t"
  "s_add_u32 m0, s73, 44032\n\t"
  "s_nop 0\n\t"
  "buffer_load_dwordx4 v17, s[68:71], s74 offen lds\n\t"
  "s_add_u32 m0, s73, 45056\n\t"
  "s_nop 0\n\t"
  "buffer_load_dwordx4 v18, s[68:71], s74 offen lds\n\t"
  "v_lshrrev_b32 v53, 2, v0\n\t"
  "s_mov_b32 s75, 239075328\n\t"
  "buffer_load_dword v62, v53, s[36:39], s75 offen offset:8\n\t"
  "buffer_load_dword v63, v53, s[36:39], s75 offen\n\t"
  "v_readlane_b32 s84, v8, 31\n\t"
  "v_readlane_b32 s85, v8, 32\n\t"
  "s_mov_b32 s86, 9216\n\t"
  "s_mov_b32 s87, s39\n\t"
  "s_cmp_eq_u32 s47, 3\n\t"
  "s_cselect_b32 s75, 8, 2\n\t"
  "s_nop 1\n\t"
  "buffer_load_ushort v43, v44, s[84:87], 2 offen\n\t"
  "buffer_load_ushort v44, v44, s[84:87], 0 offen\n\t"
  "buffer_load_ushort v64, v45, s[84:87], 2 offen\n\t"
  "buffer_load_ushort v45, v45, s[84:87], 0 offen\n\t"
  "buffer_load_ushort v65, v46, s[84:87], 2 offen\n\t"
  "buffer_load_ushort v46, v46, s[84:87], 0 offen\n\t"
  "buffer_load_ushort v66, v47, s[84:87], 2 offen\n\t"
  "buffer_load_ushort v47, v47, s[84:87], 0 offen\n\t"
  "buffer_load_ushort v67, v48, s[84:87], 2 offen\n\t"
  "buffer_load_ushort v48, v48, s[84:87], 0 offen\n\t"
  "buffer_load_ushort v68, v49, s[84:87], s75 offen\n\t"
  "buffer_load_ushort v49, v49, s[84:87], 0 offen\n\t"
  "buffer_load_ushort v69, v50, s[84:87], s75 offen\n\t"
  "buffer_load_ushort v50, v50, s[84:87], 0 offen\n\t"
  "buffer_load_ushort v70, v51, s[84:87], s75 offen\n\t"
  "buffer_load_ushort v51, v51, s[84:87], 0 offen\n\t"
  "buffer_load_ushort v71, v52, s[84:87], s75 offen\n\t"
  "buffer_load_ushort v52, v52, s[84:87], 0 offen\n\t"
  "s_waitcnt vmcnt(50) lgkmcnt(0)\n\t"
  "s_barrier\n\t"
  "v_add_u32 v55, 90112, v32\n\t"
  "v_add_u32 v53, 90112, v33\n\t"
  "ds_read_b64_tr_b16 a[0:1], v55 offset:0\n\t"
  "ds_read_b64_tr_b16 a[2:3], v55 offset:4096\n\t"
  "ds_read_b64_tr_b16 a[8:9], v53 offset:0\n\t"
  "ds_read_b64_tr_b16 a[10:11], v53 offset:4096\n\t"
  "ds_read_b64_tr_b16 a[16:17], v55 offset:8192\n\t"
  "ds_read_b64_tr_b16 a[18:19], v55 offset:12288\n\t"
  "ds_read_b64_tr_b16 a[24:25], v53 offset:8192\n\t"
  "ds_read_b64_tr_b16 a[26:27], v53 offset:12288\n\t"
  "ds_read_b64_tr_b16 a[32:33], v55 offset:16384\n\t"
  "ds_read_b64_tr_b16 a[34:35], v55 offset:20480\n\t"
  "ds_read_b64_tr_b16 a[40:41], v53 offset:16384\n\t"
  "ds_read_b64_tr_b16 a[42:43], v53 offset:20480\n\t"
  "s_waitcnt vmcnt(44) lgkmcnt(0)\n\t"
  "s_barrier\n\t"
  "s_mov_b32 s74, 589824\n\t"
  "s_add_u32 m0, s72, 90112\n\t"
  "s_nop 0\n\t"
  "buffer_load_dwordx4 v28, s[56:59], s74 offen lds\n\t"
  "s_add_u32 m0, s72, 91136\n\t"
  "s_nop 0\n\t"
  "buffer_load_dwordx4 v29, s[56:59], s74 offen lds\n\t"
  "s_add_u32 m0, s72, 98304\n\t"
  "s_nop 0\n\t"
  "buffer_load_dwordx4 v28, s[60:63], s74 offen lds\n\t"
  "s_add_u32 m0, s72, 99328\n\t"
  "s_nop 0\n\t"
  "buffer_load_dwordx4 v29, s[60:63], s74 offen lds\n\t"
  "s_add_u32 m0, s72, 106496\n\t"
  "s_nop 0\n\t"
  "buffer_load_dwordx4 v28, s[64:67], s74 offen lds\n\t"
  "s_add_u32 m0, s72, 107520\n\t"
  "s_nop 0\n\t"
  "buffer_load_dwordx4 v29, s[64:67], s74 offen lds\n\t"
  "v_add_u32 v55, 114688, v32\n\t"
  "v_add_u32 v53, 114688, v33\n\t"
  "ds_read_b64_tr_b16 a[4:5], v55 offset:0\n\t"
  "ds_read_b64_tr_b16 a[6:7], v55 offset:4096\n\t"
  "ds_read_b64_tr_b16 a[12:13], v53 offset:0\n\t"
  "ds_read_b64_tr_b16 a[14:15], v53 offset:4096\n\t"
  "ds_read_b64_tr_b16 a[20:21], v55 offset:8192\n\t"
  "ds_read_b64_tr_b16 a[22:23], v55 offset:12288\n\t"
  "ds_read_b64_tr_b16 a[28:29], v53 offset:8192\n\t"
  "ds_read_b64_tr_b16 a[30:31], v53 offset:12288\n\t"
  "ds_read_b64_tr_b16 a[36:37], v55 offset:16384\n\t"
  "ds_read_b64_tr_b16 a[38:39], v55 offset:20480\n\t"
  "ds_read_b64_tr_b16 a[44:45], v53 offset:16384\n\t"
  "ds_read_b64_tr_b16 a[46:47], v53 offset:20480\n\t"
  "s_waitcnt vmcnt(26) lgkmcnt(0)\n\t"
  "s_barrier\n\t"
  "s_mov_b32 s74, 1179648\n\t"
  "s_add_u32 m0, s72, 114688\n\t"
  "s_nop 0\n\t"
  "buffer_load_dwordx4 v28, s[56:59], s74 offen lds\n\t"
  "s_add_u32 m0, s72, 115712\n\t"
  "s_nop 0\n\t"
  "buffer_load_dwordx4 v29, s[56:59], s74 offen lds\n\t"
  "s_add_u32 m0, s72, 122880\n\t"
  "s_nop 0\n\t"
  "buffer_load_dwordx4 v28, s[60:63], s74 offen lds\n\t"
  "s_add_u32 m0, s72, 123904\n\t"
  "s_nop 0\n\t"
  "buffer_load_dwordx4 v29, s[60:63], s74 offen lds\n\t"
  "s_add_u32 m0, s72, 131072\n\t"
  "s_nop 0\n\t"
  "buffer_load_dwordx4 v28, s[64:67], s74 offen lds\n\t"
  "s_add_u32 m0, s72, 132096\n\t"
  "s_nop 0\n\t"
  "buffer_load_dwordx4 v29, s[64:67], s74 offen lds\n\t"
  "ds_read_b64_tr_b16 v[184:185], v34 offset:0\n\t"
  "ds_read_b64_tr_b16 v[186:187], v34 offset:18432\n\t"
  "ds_read_b64_tr_b16 v[188:189], v34 offset:36864\n\t"
  "ds_read_b64_tr_b16 v[190:191], v34 offset:55296\n\t"
  "ds_read_b64_tr_b16 v[192:193], v35 offset:0\n\t"
  "ds_read_b64_tr_b16 v[194:195], v35 offset:18432\n\t"
  "ds_read_b64_tr_b16 v[196:197], v35 offset:36864\n\t"
  "ds_read_b64_tr_b16 v[198:199], v35 offset:55296\n\t"
  "ds_read_b64_tr_b16 v[200:201], v36 offset:0\n\t"
  "ds_read_b64_tr_b16 v[202:203], v36 offset:18432\n\t"
  "ds_read_b64_tr_b16 v[204:205], v36 offset:36864\n\t"
  "ds_read_b64_tr_b16 v[206:207], v36 offset:55296\n\t"
  "ds_read_b64_tr_b16 v[208:209], v37 offset:0\n\t"
  "ds_read_b64_tr_b16 v[210:211], v37 offset:18432\n\t"
  "ds_read_b64_tr_b16 v[212:213], v37 offset:36864\n\t"
  "ds_read_b64_tr_b16 v[214:215], v37 offset:55296\n\t"
  "ds_read_b64_tr_b16 v[216:217], v38 offset:0\n\t"
  "ds_read_b64_tr_b16 v[218:219], v38 offset:18432\n\t"
  "ds_read_b64_tr_b16 v[220:221], v38 offset:36864\n\t"
  "ds_read_b64_tr_b16 v[222:223], v38 offset:55296\n\t"
  "ds_read_b64_tr_b16 v[224:225], v39 offset:0\n\t"
  "ds_read_b64_tr_b16 v[226:227], v39 offset:18432\n\t"
  "ds_read_b64_tr_b16 v[228:229], v39 offset:36864\n\t"
  "ds_read_b64_tr_b16 v[230:231], v39 offset:55296\n\t"
  "ds_read_b64_tr_b16 v[232:233], v40 offset:0\n\t"
  "ds_read_b64_tr_b16 v[234:235], v40 offset:18432\n\t"
  "ds_read_b64_tr_b16 v[236:237], v40 offset:36864\n\t"
  "ds_read_b64_tr_b16 v[238:239], v40 offset:55296\n\t"
  "ds_read_b64_tr_b16 v[240:241], v41 offset:0\n\t"
  "ds_read_b64_tr_b16 v[242:243], v41 offset:18432\n\t"
  "ds_read_b64_tr_b16 v[244:245], v41 offset:36864\n\t"
  "ds_read_b64_tr_b16 v[246:247], v41 offset:55296\n\t"
  "ds_read_b64_tr_b16 v[248:249], v42 offset:0\n\t"
  "ds_read_b64_tr_b16 v[250:251], v42 offset:18432\n\t"
  "ds_read_b64_tr_b16 v[252:253], v42 offset:36864\n\t"
  "ds_read_b64_tr_b16 v[254:255], v42 offset:55296\n\t"
  "s_waitcnt vmcnt(50) lgkmcnt(0)\n\t"
  "s_barrier\n\t"
  "v_add_u32 v55, 139264, v32\n\t"
  "v_add_u32 v53, 139264, v33\n\t"
  "ds_read_b64_tr_b16 v[56:57], v53 offset:8192\n\t"
  "ds_read_b64_tr_b16 v[58:59], v53 offset:12288\n\t"
  "ds_read_b64_tr_b16 a[48:49], v55 offset:0\n\t"
  "ds_read_b64_tr_b16 a[50:51], v55 offset:4096\n\t"
  "ds_read_b64_tr_b16 a[56:57], v53 offset:0\n\t"
  "ds_read_b64_tr_b16 a[58:59], v53 offset:4096\n\t"
  "ds_read_b64_tr_b16 a[64:65], v55 offset:8192\n\t"
  "ds_read_b64_tr_b16 a[66:67], v55 offset:12288\n\t"
  "ds_read_b64_tr_b16 a[72:73], v55 offset:16384\n\t"
  "ds_read_b64_tr_b16 a[74:75], v55 offset:20480\n\t"
  "ds_read_b64_tr_b16 a[80:81], v53 offset:16384\n\t"
  "ds_read_b64_tr_b16 a[82:83], v53 offset:20480\n\t"
  "s_waitcnt lgkmcnt(10)\n\t"
  "ds_write_b128 v54, v[56:59] offset:0\n\t"
  "s_waitcnt vmcnt(6) lgkmcnt(0)\n\t"
  "s_barrier\n\t"
  "s_mov_b32 s74, 1245184\n\t"
  "s_add_u32 m0, s72, 139264\n\t"
  "s_nop 0\n\t"
  "buffer_load_dwordx4 v28, s[56:59], s74 offen lds\n\t"
  "s_add_u32 m0, s72, 140288\n\t"
  "s_nop 0\n\t"
  "buffer_load_dwordx4 v29, s[56:59], s74 offen lds\n\t"
  "s_add_u32 m0, s72, 147456\n\t"
  "s_nop 0\n\t"
  "buffer_load_dwordx4 v28, s[60:63], s74 offen lds\n\t"
  "s_add_u32 m0, s72, 148480\n\t"
  "s_nop 0\n\t"
  "buffer_load_dwordx4 v29, s[60:63], s74 offen lds\n\t"
  "s_add_u32 m0, s72, 155648\n\t"
  "s_nop 0\n\t"
  "buffer_load_dwordx4 v28, s[64:67], s74 offen lds\n\t"
  "s_add_u32 m0, s72, 156672\n\t"
  "s_nop 0\n\t"
  "buffer_load_dwordx4 v29, s[64:67], s74 offen lds\n\t"
  "v_add_u32 v55, 90112, v32\n\t"
  "v_add_u32 v53, 90112, v33\n\t"
  "ds_read_b64_tr_b16 v[56:57], v53 offset:8192\n\t"
  "ds_read_b64_tr_b16 v[58:59], v53 offset:12288\n\t"
  "ds_read_b64_tr_b16 a[52:53], v55 offset:0\n\t"
  "ds_read_b64_tr_b16 a[54:55], v55 offset:4096\n\t"
  "ds_read_b64_tr_b16 a[60:61], v53 offset:0\n\t"
  "ds_read_b64_tr_b16 a[62:63], v53 offset:4096\n\t"
  "ds_read_b64_tr_b16 a[68:69], v55 offset:8192\n\t"
  "ds_read_b64_tr_b16 a[70:71], v55 offset:12288\n\t"
  "ds_read_b64_tr_b16 a[76:77], v55 offset:16384\n\t"
  "ds_read_b64_tr_b16 a[78:79], v55 offset:20480\n\t"
  "ds_read_b64_tr_b16 a[84:85], v53 offset:16384\n\t"
  "ds_read_b64_tr_b16 a[86:87], v53 offset:20480\n\t"
  "s_waitcnt lgkmcnt(10)\n\t"
  "ds_write_b128 v54, v[56:59] offset:1024\n\t"
  "s_waitcnt vmcnt(6) lgkmcnt(0)\n\t"
  "s_barrier\n\t"
  "s_mov_b32 s74, 1703936\n\t"
  "s_add_u32 m0, s72, 90112\n\t"
  "s_nop 0\n\t"
  "buffer_load_dwordx4 v28, s[56:59], s74 offen lds\n\t"
  "s_add_u32 m0, s72, 91136\n\t"
  "s_nop 0\n\t"
  "buffer_load_dwordx4 v29, s[56:59], s74 offen lds\n\t"
  "s_add_u32 m0, s72, 98304\n\t"
  "s_nop 0\n\t"
  "buffer_load_dwordx4 v28, s[60:63], s74 offen lds\n\t"
  "s_add_u32 m0, s72, 99328\n\t"
  "s_nop 0\n\t"
  "buffer_load_dwordx4 v29, s[60:63], s74 offen lds\n\t"
  "s_add_u32 m0, s72, 106496\n\t"
  "s_nop 0\n\t"
  "buffer_load_dwordx4 v28, s[64:67], s74 offen lds\n\t"
  "s_add_u32 m0, s72, 107520\n\t"
  "s_nop 0\n\t"
  "buffer_load_dwordx4 v29, s[64:67], s74 offen lds\n\t"
  "v_add_u32 v55, 114688, v32\n\t"
  "v_add_u32 v53, 114688, v33\n\t"
  "ds_read_b64_tr_b16 v[56:57], v53 offset:8192\n\t"
  "ds_read_b64_tr_b16 v[58:59], v53 offset:12288\n\t"
  "ds_read_b64_tr_b16 a[88:89], v55 offset:0\n\t"
  "ds_read_b64_tr_b16 a[90:91], v55 offset:4096\n\t"
  "ds_read_b64_tr_b16 a[96:97], v53 offset:0\n\t"
  "ds_read_b64_tr_b16 a[98:99], v53 offset:4096\n\t"
  "ds_read_b64_tr_b16 a[104:105], v55 offset:8192\n\t"
  "ds_read_b64_tr_b16 a[106:107], v55 offset:12288\n\t"
  "ds_read_b64_tr_b16 a[112:113], v55 offset:16384\n\t"
  "ds_read_b64_tr_b16 a[114:115], v55 offset:20480\n\t"
  "ds_read_b64_tr_b16 a[120:121], v53 offset:16384\n\t"
  "ds_read_b64_tr_b16 a[122:123], v53 offset:20480\n\t"
  "s_waitcnt lgkmcnt(10)\n\t"
  "ds_write_b128 v54, v[56:59] offset:2048\n\t"
  "s_waitcnt vmcnt(6) lgkmcnt(0)\n\t"
  "s_barrier\n\t"
  "s_mov_b32 s74, 1769472\n\t"
  "s_add_u32 m0, s72, 114688\n\t"
  "s_nop 0\n\t"
  "buffer_load_dwordx4 v28, s[56:59], s74 offen lds\n\t"
  "s_add_u32 m0, s72, 115712\n\t"
  "s_nop 0\n\t"
  "buffer_load_dwordx4 v29, s[56:59], s74 offen lds\n\t"
  "s_add_u32 m0, s72, 122880\n\t"
  "s_nop 0\n\t"
  "buffer_load_dwordx4 v28, s[60:63], s74 offen lds\n\t"
  "s_add_u32 m0, s72, 123904\n\t"
  "s_nop 0\n\t"
  "buffer_load_dwordx4 v29, s[60:63], s74 offen lds\n\t"
  "s_add_u32 m0, s72, 131072\n\t"
  "s_nop 0\n\t"
  "buffer_load_dwordx4 v28, s[64:67], s74 offen lds\n\t"
  "s_add_u32 m0, s72, 132096\n\t"
  "s_nop 0\n\t"
  "buffer_load_dwordx4 v29, s[64:67], s74 offen lds\n\t"
  "v_cmp_ne_u32 vcc, s34, v63\n\t"
  "v_cmp_ne_u32 s[76:77], s34, v62\n\t"
  "s_and_b32 s75, vcc_lo, 1\n\t"
  "s_lshl_b32 s75, s75, 13\n\t"
  "s_add_u32 s43, s43, s75\n\t"
  "s_add_u32 s44, s44, s75\n\t"
  "s_add_u32 s46, s46, s75\n\t"
  "s_add_u32 s49, s49, s75\n\t"
  "s_and_b32 s75, vcc_lo, 2\n\t"
  "s_lshl_b32 s75, s75, 12\n\t"
  "s_add_u32 s45, s45, s75\n\t"
  "s_mov_b64 s[34:35], s[76:77]\n\t"
  "v_lshl_or_b32 v44, v43, 16, v44\n\t"
  "v_lshl_or_b32 v45, v64, 16, v45\n\t"
  "v_lshl_or_b32 v46, v65, 16, v46\n\t"
  "v_lshl_or_b32 v47, v66, 16, v47\n\t"
  "v_lshl_or_b32 v48, v67, 16, v48\n\t"
  "v_lshl_or_b32 v49, v68, 16, v49\n\t"
  "v_lshl_or_b32 v50, v69, 16, v50\n\t"
  "v_lshl_or_b32 v51, v70, 16, v51\n\t"
  "v_lshl_or_b32 v52, v71, 16, v52\n\t"
  "v_add_u32 v55, 139264, v32\n\t"
  "v_add_u32 v53, 139264, v33\n\t"
  "ds_read_b64_tr_b16 v[56:57], v53 offset:8192\n\t"
  "ds_read_b64_tr_b16 v[58:59], v53 offset:12288\n\t"
  "ds_read_b64_tr_b16 a[92:93], v55 offset:0\n\t"
  "ds_read_b64_tr_b16 a[94:95], v55 offset:4096\n\t"
  "ds_read_b64_tr_b16 a[100:101], v53 offset:0\n\t"
  "ds_read_b64_tr_b16 a[102:103], v53 offset:4096\n\t"
  "ds_read_b64_tr_b16 a[108:109], v55 offset:8192\n\t"
  "ds_read_b64_tr_b16 a[110:111], v55 offset:12288\n\t"
  "ds_read_b64_tr_b16 a[116:117], v55 offset:16384\n\t"
  "ds_read_b64_tr_b16 a[118:119], v55 offset:20480\n\t"
  "ds_read_b64_tr_b16 a[124:125], v53 offset:16384\n\t"
  "ds_read_b64_tr_b16 a[126:127], v53 offset:20480\n\t"
  "s_waitcnt lgkmcnt(10)\n\t"
  "ds_write_b128 v54, v[56:59] offset:3072\n\t"
  "s_waitcnt vmcnt(6) lgkmcnt(0)\n\t"
  "s_barrier\n\t"
  "s_mov_b32 s74, 2359296\n\t"
  "s_add_u32 m0, s72, 139264\n\t"
  "s_nop 0\n\t"
  "buffer_load_dwordx4 v28, s[56:59], s74 offen lds\n\t"
  "s_add_u32 m0, s72, 140288\n\t"
  "s_nop 0\n\t"
  "buffer_load_dwordx4 v29, s[56:59], s74 offen lds\n\t"
  "s_add_u32 m0, s72, 147456\n\t"
  "s_nop 0\n\t"
  "buffer_load_dwordx4 v28, s[60:63], s74 offen lds\n\t"
  "s_add_u32 m0, s72, 148480\n\t"
  "s_nop 0\n\t"
  "buffer_load_dwordx4 v29, s[60:63], s74 offen lds\n\t"
  "s_add_u32 m0, s72, 155648\n\t"
  "s_nop 0\n\t"
  "buffer_load_dwordx4 v28, s[64:67], s74 offen lds\n\t"
  "s_add_u32 m0, s72, 156672\n\t"
  "s_nop 0\n\t"
  "buffer_load_dwordx4 v29, s[64:67], s74 offen lds\n\t"
  "s_mov_b32 s74, 2424832\n\t"
  "s_add_u32 m0, s72, 0\n\t"
  "s_nop 0\n\t"
  "buffer_load_dwordx4 v28, s[56:59], s74 offen lds\n\t"
  "s_add_u32 m0, s72, 1024\n\t"
  "s_nop 0\n\t"
  "buffer_load_dwordx4 v29, s[56:59], s74 offen lds\n\t"
  "s_add_u32 m0, s72, 8192\n\t"
  "s_nop 0\n\t"
  "buffer_load_dwordx4 v28, s[60:63], s74 offen lds\n\t"
  "s_add_u32 m0, s72, 9216\n\t"
  "s_nop 0\n\t"
  "buffer_load_dwordx4 v29, s[60:63], s74 offen lds\n\t"
  "s_add_u32 m0, s72, 16384\n\t"
  "s_nop 0\n\t"
  "buffer_load_dwordx4 v28, s[64:67], s74 offen lds\n\t"
  "s_add_u32 m0, s72, 17408\n\t"
  "s_nop 0\n\t"
  "buffer_load_dwordx4 v29, s[64:67], s74 offen lds\n\t"
  "v_add_u32 v55, 90112, v32\n\t"
  "v_add_u32 v53, 90112, v33\n\t"
  "ds_read_b64_tr_b16 v[56:57], v53 offset:8192\n\t"
  "ds_read_b64_tr_b16 v[58:59], v53 offset:12288\n\t"
  "ds_read_b64_tr_b16 a[128:129], v55 offset:0\n\t"
  "ds_read_b64_tr_b16 a[130:131], v55 offset:4096\n\t"
  "ds_read_b64_tr_b16 a[136:137], v53 offset:0\n\t"
  "ds_read_b64_tr_b16 a[138:139], v53 offset:4096\n\t"
  "ds_read_b64_tr_b16 a[144:145], v55 offset:8192\n\t"
  "ds_read_b64_tr_b16 a[146:147], v55 offset:12288\n\t"
  "ds_read_b64_tr_b16 a[152:153], v55 offset:16384\n\t"
  "ds_read_b64_tr_b16 a[154:155], v55 offset:20480\n\t"
  "ds_read_b64_tr_b16 a[160:161], v53 offset:16384\n\t"
  "ds_read_b64_tr_b16 a[162:163], v53 offset:20480\n\t"
  "s_waitcnt lgkmcnt(10)\n\t"
  "ds_write_b128 v54, v[56:59] offset:4096\n\t"
  "s_waitcnt vmcnt(12) lgkmcnt(0)\n\t"
  "s_barrier\n\t"
  "s_mov_b32 s74, 2883584\n\t"
  "s_add_u32 m0, s72, 90112\n\t"
  "s_nop 0\n\t"
  "buffer_load_dwordx4 v28, s[56:59], s74 offen lds\n\t"
  "s_add_u32 m0, s72, 91136\n\t"
  "s_nop 0\n\t"
  "buffer_load_dwordx4 v29, s[56:59], s74 offen lds\n\t"
  "s_add_u32 m0, s72, 98304\n\t"
  "s_nop 0\n\t"
  "buffer_load_dwordx4 v28, s[60:63], s74 offen lds\n\t"
  "s_add_u32 m0, s72, 99328\n\t"
  "s_nop 0\n\t"
  "buffer_load_dwordx4 v29, s[60:63], s74 offen lds\n\t"
  "s_add_u32 m0, s72, 106496\n\t"
  "s_nop 0\n\t"
  "buffer_load_dwordx4 v28, s[64:67], s74 offen lds\n\t"
  "s_add_u32 m0, s72, 107520\n\t"
  "s_nop 0\n\t"
  "buffer_load_dwordx4 v29, s[64:67], s74 offen lds\n\t"
  "v_add_u32 v55, 114688, v32\n\t"
  "v_add_u32 v53, 114688, v33\n\t"
  "ds_read_b64_tr_b16 v[56:57], v53 offset:8192\n\t"
  "ds_read_b64_tr_b16 v[58:59], v53 offset:12288\n\t"
  "ds_read_b64_tr_b16 a[132:133], v55 offset:0\n\t"
  "ds_read_b64_tr_b16 a[134:135], v55 offset:4096\n\t"
  "ds_read_b64_tr_b16 a[140:141], v53 offset:0\n\t"
  "ds_read_b64_tr_b16 a[142:143], v53 offset:4096\n\t"
  "ds_read_b64_tr_b16 a[148:149], v55 offset:8192\n\t"
  "ds_read_b64_tr_b16 a[150:151], v55 offset:12288\n\t"
  "ds_read_b64_tr_b16 a[156:157], v55 offset:16384\n\t"
  "ds_read_b64_tr_b16 a[158:159], v55 offset:20480\n\t"
  "ds_read_b64_tr_b16 a[164:165], v53 offset:16384\n\t"
  "ds_read_b64_tr_b16 a[166:167], v53 offset:20480\n\t"
  "s_waitcnt lgkmcnt(10)\n\t"
  "ds_write_b128 v54, v[56:59] offset:5120\n\t"
  "s_waitcnt vmcnt(12) lgkmcnt(0)\n\t"
  "s_barrier\n\t"
  "s_mov_b32 s74, 2949120\n\t"
  "s_add_u32 m0, s72, 114688\n\t"
  "s_nop 0\n\t"
  "buffer_load_dwordx4 v28, s[56:59], s74 offen lds\n\t"
  "s_add_u32 m0, s72, 115712\n\t"
  "s_nop 0\n\t"
  "buffer_load_dwordx4 v29, s[56:59], s74 offen lds\n\t"
  "s_add_u32 m0, s72, 122880\n\t"
  "s_nop 0\n\t"
  "buffer_load_dwordx4 v28, s[60:63], s74 offen lds\n\t"
  "s_add_u32 m0, s72, 123904\n\t"
  "s_nop 0\n\t"
  "buffer_load_dwordx4 v29, s[60:63], s74 offen lds\n\t"
  "s_add_u32 m0, s72, 131072\n\t"
  "s_nop 0\n\t"
  "buffer_load_dwordx4 v28, s[64:67], s74 offen lds\n\t"
  "s_add_u32 m0, s72, 132096\n\t"
  "s_nop 0\n\t"
  "buffer_load_dwordx4 v29, s[64:67], s74 offen lds\n\t"
  "v_add_u32 v55, 139264, v32\n\t"
  "v_add_u32 v53, 139264, v33\n\t"
  "ds_read_b64_tr_b16 v[56:57], v53 offset:8192\n\t"
  "ds_read_b64_tr_b16 v[58:59], v53 offset:12288\n\t"
  "ds_read_b64_tr_b16 a[168:169], v55 offset:0\n\t"
  "ds_read_b64_tr_b16 a[170:171], v55 offset:4096\n\t"
  "ds_read_b64_tr_b16 a[176:177], v53 offset:0\n\t"
  "ds_read_b64_tr_b16 a[178:179], v53 offset:4096\n\t"
  "ds_read_b64_tr_b16 a[184:185], v55 offset:8192\n\t"
  "ds_read_b64_tr_b16 a[186:187], v55 offset:12288\n\t"
  "ds_read_b64_tr_b16 a[192:193], v55 offset:16384\n\t"
  "ds_read_b64_tr_b16 a[194:195], v55 offset:20480\n\t"
  "ds_read_b64_tr_b16 a[200:201], v53 offset:16384\n\t"
  "ds_read_b64_tr_b16 a[202:203], v53 offset:20480\n\t"
  "s_waitcnt lgkmcnt(10)\n\t"
  "ds_write_b128 v54, v[56:59] offset:6144\n\t"
  "s_waitcnt vmcnt(12) lgkmcnt(0)\n\t"
  "s_barrier\n\t"
  "s_mov_b32 s74, 3538944\n\t"
  "s_add_u32 m0, s72, 139264\n\t"
  "s_nop 0\n\t"
  "buffer_load_dwordx4 v28, s[56:59], s74 offen lds\n\t"
  "s_add_u32 m0, s72, 140288\n\t"
  "s_nop 0\n\t"
  "buffer_load_dwordx4 v29, s[56:59], s74 offen lds\n\t"
  "s_add_u32 m0, s72, 147456\n\t"
  "s_nop 0\n\t"
  "buffer_load_dwordx4 v28, s[60:63], s74 offen lds\n\t"
  "s_add_u32 m0, s72, 148480\n\t"
  "s_nop 0\n\t"
  "buffer_load_dwordx4 v29, s[60:63], s74 offen lds\n\t"
  "s_add_u32 m0, s72, 155648\n\t"
  "s_nop 0\n\t"
  "buffer_load_dwordx4 v28, s[64:67], s74 offen lds\n\t"
  "s_add_u32 m0, s72, 156672\n\t"
  "s_nop 0\n\t"
  "buffer_load_dwordx4 v29, s[64:67], s74 offen lds\n\t"
  "v_add_u32 v55, 0, v32\n\t"
  "v_add_u32 v53, 0, v33\n\t"
  "ds_read_b64_tr_b16 v[56:57], v53 offset:8192\n\t"
  "ds_read_b64_tr_b16 v[58:59], v53 offset:12288\n\t"
  "ds_read_b64_tr_b16 a[172:173], v55 offset:0\n\t"
  "ds_read_b64_tr_b16 a[174:175], v55 offset:4096\n\t"
  "ds_read_b64_tr_b16 a[180:181], v53 offset:0\n\t"
  "ds_read_b64_tr_b16 a[182:183], v53 offset:4096\n\t"
  "ds_read_b64_tr_b16 a[188:189], v55 offset:8192\n\t"
  "ds_read_b64_tr_b16 a[190:191], v55 offset:12288\n\t"
  "ds_read_b64_tr_b16 a[196:197], v55 offset:16384\n\t"
  "ds_read_b64_tr_b16 a[198:199], v55 offset:20480\n\t"
  "ds_read_b64_tr_b16 a[204:205], v53 offset:16384\n\t"
  "ds_read_b64_tr_b16 a[206:207], v53 offset:20480\n\t"
  "s_waitcnt lgkmcnt(10)\n\t"
  "ds_write_b128 v54, v[56:59] offset:7168\n\t"
  "s_waitcnt vmcnt(12) lgkmcnt(0)\n\t"
  "s_barrier\n\t"
  "s_mov_b32 s74, 3604480\n\t"
  "s_add_u32 m0, s72, 0\n\t"
  "s_nop 0\n\t"
  "buffer_load_dwordx4 v28, s[56:59], s74 offen lds\n\t"
  "s_add_u32 m0, s72, 1024\n\t"
  "s_nop 0\n\t"
  "buffer_load_dwordx4 v29, s[56:59], s74 offen lds\n\t"
  "s_add_u32 m0, s72, 8192\n\t"
  "s_nop 0\n\t"
  "buffer_load_dwordx4 v28, s[60:63], s74 offen lds\n\t"
  "s_add_u32 m0, s72, 9216\n\t"
  "s_nop 0\n\t"
  "buffer_load_dwordx4 v29, s[60:63], s74 offen lds\n\t"
  "s_add_u32 m0, s72, 16384\n\t"
  "s_nop 0\n\t"
  "buffer_load_dwordx4 v28, s[64:67], s74 offen lds\n\t"
  "s_add_u32 m0, s72, 17408\n\t"
  "s_nop 0\n\t"
  "buffer_load_dwordx4 v29, s[64:67], s74 offen lds\n\t"
  "v_add_u32 v55, 90112, v32\n\t"
  "v_add_u32 v53, 90112, v33\n\t"
  "ds_read_b64_tr_b16 v[56:57], v53 offset:8192\n\t"
  "ds_read_b64_tr_b16 v[58:59], v53 offset:12288\n\t"
  "ds_read_b64_tr_b16 a[208:209], v55 offset:0\n\t"
  "ds_read_b64_tr_b16 a[210:211], v55 offset:4096\n\t"
  "ds_read_b64_tr_b16 a[216:217], v53 offset:0\n\t"
  "ds_read_b64_tr_b16 a[218:219], v53 offset:4096\n\t"
  "ds_read_b64_tr_b16 a[224:225], v55 offset:8192\n\t"
  "ds_read_b64_tr_b16 a[226:227], v55 offset:12288\n\t"
  "ds_read_b64_tr_b16 a[232:233], v55 offset:16384\n\t"
  "ds_read_b64_tr_b16 a[234:235], v55 offset:20480\n\t"
  "ds_read_b64_tr_b16 a[240:241], v53 offset:16384\n\t"
  "ds_read_b64_tr_b16 a[242:243], v53 offset:20480\n\t"
  "s_waitcnt lgkmcnt(10)\n\t"
  "ds_write_b128 v54, v[56:59] offset:8192\n\t"
  "s_waitcnt vmcnt(12) lgkmcnt(0)\n\t"
  "s_barrier\n\t"
  "s_mov_b32 s74, 4063232\n\t"
  "s_add_u32 m0, s72, 90112\n\t"
  "s_nop 0\n\t"
  "buffer_load_dwordx4 v28, s[56:59], s74 offen lds\n\t"
  "s_add_u32 m0, s72, 91136\n\t"
  "s_nop 0\n\t"
  "buffer_load_dwordx4 v29, s[56:59], s74 offen lds\n\t"
  "s_add_u32 m0, s72, 98304\n\t"
  "s_nop 0\n\t"
  "buffer_load_dwordx4 v28, s[60:63], s74 offen lds\n\t"
  "s_add_u32 m0, s72, 99328\n\t"
  "s_nop 0\n\t"
  "buffer_load_dwordx4 v29, s[60:63], s74 offen lds\n\t"
  "s_add_u32 m0, s72, 106496\n\t"
  "s_nop 0\n\t"
  "buffer_load_dwordx4 v28, s[64:67], s74 offen lds\n\t"
  "s_add_u32 m0, s72, 107520\n\t"
  "s_nop 0\n\t"
  "buffer_load_dwordx4 v29, s[64:67], s74 offen lds\n\t"
  "v_add_u32 v55, 114688, v32\n\t"
  "v_add_u32 v53, 114688, v33\n\t"
  "ds_read_b64_tr_b16 v[56:57], v53 offset:8192\n\t"
  "ds_read_b64_tr_b16 v[58:59], v53 offset:12288\n\t"
  "ds_read_b64_tr_b16 a[212:213], v55 offset:0\n\t"
  "ds_read_b64_tr_b16 a[214:215], v55 offset:4096\n\t"
  "ds_read_b64_tr_b16 a[220:221], v53 offset:0\n\t"
  "ds_read_b64_tr_b16 a[222:223], v53 offset:4096\n\t"
  "ds_read_b64_tr_b16 a[228:229], v55 offset:8192\n\t"
  "ds_read_b64_tr_b16 a[230:231], v55 offset:12288\n\t"
  "ds_read_b64_tr_b16 a[236:237], v55 offset:16384\n\t"
  "ds_read_b64_tr_b16 a[238:239], v55 offset:20480\n\t"
  "ds_read_b64_tr_b16 a[244:245], v53 offset:16384\n\t"
  "ds_read_b64_tr_b16 a[246:247], v53 offset:20480\n\t"
  "s_waitcnt lgkmcnt(10)\n\t"
  "ds_write_b128 v54, v[56:59] offset:9216\n\t"
  "s_waitcnt vmcnt(12) lgkmcnt(0)\n\t"
  "s_barrier\n\t"
  "s_mov_b32 s74, 4128768\n\t"
  "s_add_u32 m0, s72, 114688\n\t"
  "s_nop 0\n\t"
  "buffer_load_dwordx4 v28, s[56:59], s74 offen lds\n\t"
  "s_add_u32 m0, s72, 115712\n\t"
  "s_nop 0\n\t"
  "buffer_load_dwordx4 v29, s[56:59], s74 offen lds\n\t"
  "s_add_u32 m0, s72, 122880\n\t"
  "s_nop 0\n\t"
  "buffer_load_dwordx4 v28, s[60:63], s74 offen lds\n\t"
  "s_add_u32 m0, s72, 123904\n\t"
  "s_nop 0\n\t"
  "buffer_load_dwordx4 v29, s[60:63], s74 offen lds\n\t"
  "s_add_u32 m0, s72, 131072\n\t"
  "s_nop 0\n\t"
  "buffer_load_dwordx4 v28, s[64:67], s74 offen lds\n\t"
  "s_add_u32 m0, s72, 132096\n\t"
  "s_nop 0\n\t"
  "buffer_load_dwordx4 v29, s[64:67], s74 offen lds\n\t"
  "v_add_u32 v55, 139264, v32\n\t"
  "v_add_u32 v53, 139264, v33\n\t"
  "ds_read_b64_tr_b16 v[56:57], v53 offset:8192\n\t"
  "ds_read_b64_tr_b16 v[58:59], v53 offset:12288\n\t"
  "ds_read_b64_tr_b16 a[248:249], v55 offset:0\n\t"
  "ds_read_b64_tr_b16 a[250:251], v55 offset:4096\n\t"
  "ds_read_b64_tr_b16 v[72:73], v53 offset:0\n\t"
  "ds_read_b64_tr_b16 v[74:75], v53 offset:4096\n\t"
  "ds_read_b64_tr_b16 v[80:81], v55 offset:8192\n\t"
  "ds_read_b64_tr_b16 v[82:83], v55 offset:12288\n\t"
  "ds_read_b64_tr_b16 v[88:89], v55 offset:16384\n\t"
  "ds_read_b64_tr_b16 v[90:91], v55 offset:20480\n\t"
  "ds_read_b64_tr_b16 v[96:97], v53 offset:16384\n\t"
  "ds_read_b64_tr_b16 v[98:99], v53 offset:20480\n\t"
  "s_waitcnt lgkmcnt(10)\n\t"
  "ds_write_b128 v54, v[56:59] offset:10240\n\t"
  "s_waitcnt vmcnt(12) lgkmcnt(0)\n\t"
  "s_barrier\n\t"
  "s_mov_b32 s74, 1048576\n\t"
  "s_add_u32 m0, s72, 139264\n\t"
  "s_nop 0\n\t"
  "buffer_load_dwordx4 v30, s[56:59], s74 offen lds\n\t"
  "s_add_u32 m0, s72, 140288\n\t"
  "s_nop 0\n\t"
  "buffer_load_dwordx4 v31, s[56:59], s74 offen lds\n\t"
  "s_add_u32 m0, s72, 147456\n\t"
  "s_nop 0\n\t"
  "buffer_load_dwordx4 v30, s[60:63], s74 offen lds\n\t"
  "s_add_u32 m0, s72, 148480\n\t"
  "s_nop 0\n\t"
  "buffer_load_dwordx4 v31, s[60:63], s74 offen lds\n\t"
  "s_add_u32 m0, s72, 155648\n\t"
  "s_nop 0\n\t"
  "buffer_load_dwordx4 v30, s[64:67], s74 offen lds\n\t"
  "s_add_u32 m0, s72, 156672\n\t"
  "s_nop 0\n\t"
  "buffer_load_dwordx4 v31, s[64:67], s74 offen lds\n\t"
  "v_add_u32 v55, 0, v32\n\t"
  "v_add_u32 v53, 0, v33\n\t"
  "ds_read_b64_tr_b16 v[56:57], v53 offset:8192\n\t"
  "ds_read_b64_tr_b16 v[58:59], v53 offset:12288\n\t"
  "ds_read_b64_tr_b16 a[252:253], v55 offset:0\n\t"
  "ds_read_b64_tr_b16 a[254:255], v55 offset:4096\n\t"
  "ds_read_b64_tr_b16 v[76:77], v53 offset:0\n\t"
  "ds_read_b64_tr_b16 v[78:79], v53 offset:4096\n\t"
  "ds_read_b64_tr_b16 v[84:85], v55 offset:8192\n\t"
  "ds_read_b64_tr_b16 v[86:87], v55 offset:12288\n\t"
  "ds_read_b64_tr_b16 v[92:93], v55 offset:16384\n\t"
  "ds_read_b64_tr_b16 v[94:95], v55 offset:20480\n\t"
  "ds_read_b64_tr_b16 v[100:101], v53 offset:16384\n\t"
  "ds_read_b64_tr_b16 v[102:103], v53 offset:20480\n\t"
  "s_waitcnt lgkmcnt(10)\n\t"
  "ds_write_b128 v54, v[56:59] offset:11264\n\t"
  "s_waitcnt vmcnt(12) lgkmcnt(0)\n\t"
  "s_barrier\n\t"
  "s_mov_b32 s74, 3407872\n\t"
  "s_add_u32 m0, s72, 0\n\t"
  "s_nop 0\n\t"
  "buffer_load_dwordx4 v30, s[56:59], s74 offen lds\n\t"
  "s_add_u32 m0, s72, 1024\n\t"
  "s_nop 0\n\t"
  "buffer_load_dwordx4 v31, s[56:59], s74 offen lds\n\t"
  "s_add_u32 m0, s72, 8192\n\t"
  "s_nop 0\n\t"
  "buffer_load_dwordx4 v30, s[60:63], s74 offen lds\n\t"
  "s_add_u32 m0, s72, 9216\n\t"
  "s_nop 0\n\t"
  "buffer_load_dwordx4 v31, s[60:63], s74 offen lds\n\t"
  "s_add_u32 m0, s72, 16384\n\t"
  "s_nop 0\n\t"
  "buffer_load_dwordx4 v30, s[64:67], s74 offen lds\n\t"
  "s_add_u32 m0, s72, 17408\n\t"
  "s_nop 0\n\t"
  "buffer_load_dwordx4 v31, s[64:67], s74 offen lds\n\t"
  "v_add_u32 v55, 90112, v32\n\t"
  "v_add_u32 v53, 90112, v33\n\t"
  "ds_read_b64_tr_b16 v[56:57], v53 offset:8192\n\t"
  "ds_read_b64_tr_b16 v[58:59], v53 offset:12288\n\t"
  "ds_read_b64_tr_b16 v[104:105], v55 offset:0\n\t"
  "ds_read_b64_tr_b16 v[106:107], v55 offset:4096\n\t"
  "ds_read_b64_tr_b16 v[112:113], v53 offset:0\n\t"
  "ds_read_b64_tr_b16 v[114:115], v53 offset:4096\n\t"
  "ds_read_b64_tr_b16 v[120:121], v55 offset:8192\n\t"
  "ds_read_b64_tr_b16 v[122:123], v55 offset:12288\n\t"
  "ds_read_b64_tr_b16 v[128:129], v55 offset:16384\n\t"
  "ds_read_b64_tr_b16 v[130:131], v55 offset:20480\n\t"
  "ds_read_b64_tr_b16 v[136:137], v53 offset:16384\n\t"
  "ds_read_b64_tr_b16 v[138:139], v53 offset:20480\n\t"
  "s_waitcnt lgkmcnt(10)\n\t"
  "ds_write_b128 v54, v[56:59] offset:12288\n\t"
  "s_waitcnt vmcnt(12) lgkmcnt(0)\n\t"
  "s_barrier\n\t"
  "v_add_u32 v55, 114688, v32\n\t"
  "v_add_u32 v53, 114688, v33\n\t"
  "ds_read_b64_tr_b16 v[56:57], v53 offset:8192\n\t"
  "ds_read_b64_tr_b16 v[58:59], v53 offset:12288\n\t"
  "ds_read_b64_tr_b16 v[108:109], v55 offset:0\n\t"
  "ds_read_b64_tr_b16 v[110:111], v55 offset:4096\n\t"
  "ds_read_b64_tr_b16 v[116:117], v53 offset:0\n\t"
  "ds_read_b64_tr_b16 v[118:119], v53 offset:4096\n\t"
  "ds_read_b64_tr_b16 v[124:125], v55 offset:8192\n\t"
  "ds_read_b64_tr_b16 v[126:127], v55 offset:12288\n\t"
  "ds_read_b64_tr_b16 v[132:133], v55 offset:16384\n\t"
  "ds_read_b64_tr_b16 v[134:135], v55 offset:20480\n\t"
  "ds_read_b64_tr_b16 v[140:141], v53 offset:16384\n\t"
  "ds_read_b64_tr_b16 v[142:143], v53 offset:20480\n\t"
  "s_waitcnt lgkmcnt(10)\n\t"
  "ds_write_b128 v54, v[56:59] offset:13312\n\t"
  "s_waitcnt vmcnt(6) lgkmcnt(0)\n\t"
  "s_barrier\n\t"
  "s_mov_b32 s74, 73728\n\t"
  "s_add_u32 m0, s73, 90112\n\t"
  "s_nop 0\n\t"
  "buffer_load_dwordx4 v10, s[68:71], s74 offen lds\n\t"
  "s_add_u32 m0, s73, 91136\n\t"
  "s_nop 0\n\t"
  "buffer_load_dwordx4 v11, s[68:71], s74 offen lds\n\t"
  "s_add_u32 m0, s73, 92160\n\t"
  "s_nop 0\n\t"
  "buffer_load_dwordx4 v12, s[68:71], s74 offen lds\n\t"
  "s_add_u32 m0, s73, 93184\n\t"
  "s_nop 0\n\t"
  "buffer_load_dwordx4 v13, s[68:71], s74 offen lds\n\t"
  "s_add_u32 m0, s73, 94208\n\t"
  "s_nop 0\n\t"
  "buffer_load_dwordx4 v14, s[68:71], s74 offen lds\n\t"
  "s_add_u32 m0, s73, 95232\n\t"
  "s_nop 0\n\t"
  "buffer_load_dwordx4 v15, s[68:71], s74 offen lds\n\t"
  "s_add_u32 m0, s73, 96256\n\t"
  "s_nop 0\n\t"
  "buffer_load_dwordx4 v16, s[68:71], s74 offen lds\n\t"
  "s_add_u32 m0, s73, 97280\n\t"
  "s_nop 0\n\t"
  "buffer_load_dwordx4 v17, s[68:71], s74 offen lds\n\t"
  "s_add_u32 m0, s73, 98304\n\t"
  "s_nop 0\n\t"
  "buffer_load_dwordx4 v18, s[68:71], s74 offen lds\n\t"
  "v_add_u32 v55, 139264, v32\n\t"
  "v_add_u32 v53, 139264, v33\n\t"
  "ds_read_b64_tr_b16 v[56:57], v53 offset:8192\n\t"
  "ds_read_b64_tr_b16 v[58:59], v53 offset:12288\n\t"
  "ds_read_b64_tr_b16 v[144:145], v55 offset:0\n\t"
  "ds_read_b64_tr_b16 v[146:147], v55 offset:4096\n\t"
  "ds_read_b64_tr_b16 v[152:153], v53 offset:0\n\t"
  "ds_read_b64_tr_b16 v[154:155], v53 offset:4096\n\t"
  "ds_read_b64_tr_b16 v[160:161], v55 offset:8192\n\t"
  "ds_read_b64_tr_b16 v[162:163], v55 offset:12288\n\t"
  "ds_read_b64_tr_b16 v[168:169], v55 offset:16384\n\t"
  "ds_read_b64_tr_b16 v[170:171], v55 offset:20480\n\t"
  "ds_read_b64_tr_b16 v[176:177], v53 offset:16384\n\t"
  "ds_read_b64_tr_b16 v[178:179], v53 offset:20480\n\t"
  "s_waitcnt lgkmcnt(10)\n\t"
  "ds_write_b128 v54, v[56:59] offset:14336\n\t"
  "s_waitcnt vmcnt(9) lgkmcnt(0)\n\t"
  "s_barrier\n\t"
  "s_mov_b32 s74, 110592\n\t"
  "s_add_u32 m0, s73, 126976\n\t"
  "s_nop 0\n\t"
  "buffer_load_dwordx4 v10, s[68:71], s74 offen lds\n\t"
  "s_add_u32 m0, s73, 128000\n\t"
  "s_nop 0\n\t"
  "buffer_load_dwordx4 v11, s[68:71], s74 offen lds\n\t"
  "s_add_u32 m0, s73, 129024\n\t"
  "s_nop 0\n\t"
  "buffer_load_dwordx4 v12, s[68:71], s74 offen lds\n\t"
  "s_add_u32 m0, s73, 130048\n\t"
  "s_nop 0\n\t"
  "buffer_load_dwordx4 v13, s[68:71], s74 offen lds\n\t"
  "s_add_u32 m0, s73, 131072\n\t"
  "s_nop 0\n\t"
  "buffer_load_dwordx4 v14, s[68:71], s74 offen lds\n\t"
  "s_add_u32 m0, s73, 132096\n\t"
  "s_nop 0\n\t"
  "buffer_load_dwordx4 v15, s[68:71], s74 offen lds\n\t"
  "s_add_u32 m0, s73, 133120\n\t"
  "s_nop 0\n\t"
  "buffer_load_dwordx4 v16, s[68:71], s74 offen lds\n\t"
  "s_add_u32 m0, s73, 134144\n\t"
  "s_nop 0\n\t"
  "buffer_load_dwordx4 v17, s[68:71], s74 offen lds\n\t"
  "s_add_u32 m0, s73, 135168\n\t"
  "s_nop 0\n\t"
  "buffer_load_dwordx4 v18, s[68:71], s74 offen lds\n\t"
  "v_add_u32 v55, 0, v32\n\t"
  "v_add_u32 v53, 0, v33\n\t"
  "ds_read_b64_tr_b16 v[56:57], v53 offset:8192\n\t"
  "ds_read_b64_tr_b16 v[58:59], v53 offset:12288\n\t"
  "ds_read_b64_tr_b16 v[148:149], v55 offset:0\n\t"
  "ds_read_b64_tr_b16 v[150:151], v55 offset:4096\n\t"
  "ds_read_b64_tr_b16 v[156:157], v53 offset:0\n\t"
  "ds_read_b64_tr_b16 v[158:159], v53 offset:4096\n\t"
  "ds_read_b64_tr_b16 v[164:165], v55 offset:8192\n\t"
  "ds_read_b64_tr_b16 v[166:167], v55 offset:12288\n\t"
  "ds_read_b64_tr_b16 v[172:173], v55 offset:16384\n\t"
  "ds_read_b64_tr_b16 v[174:175], v55 offset:20480\n\t"
  "ds_read_b64_tr_b16 v[180:181], v53 offset:16384\n\t"
  "ds_read_b64_tr_b16 v[182:183], v53 offset:20480\n\t"
  "s_waitcnt lgkmcnt(10)\n\t"
  "ds_write_b128 v54, v[56:59] offset:15360\n\t"
  "s_mul_i32 s75, s47, 9216\n\t"
  "s_add_u32 s75, s75, 90112\n\t"
  "v_add_u32 v9, s75, v0\n\t"
  "v_add_u32 v34, 90112, v34\n\t"
  "v_add_u32 v35, 90112, v35\n\t"
  "v_add_u32 v36, 90112, v36\n\t"
  "v_add_u32 v37, 90112, v37\n\t"
  "v_add_u32 v38, 90112, v38\n\t"
  "v_add_u32 v39, 90112, v39\n\t"
  "v_add_u32 v40, 90112, v40\n\t"
  "v_add_u32 v41, 90112, v41\n\t"
  "v_add_u32 v42, 90112, v42\n\t"
  "s_mul_i32 s75, s47, 2304\n\t"
  "v_lshrrev_b32 v53, 2, v0\n\t"
  "v_add_u32 v53, s75, v53\n\t"
  "s_waitcnt vmcnt(9) lgkmcnt(0)\n\t"
  "s_barrier\n\t"
  "ds_read_b64_tr_b16 v[10:11], v34 offset:0\n\t"
  "ds_read_b64_tr_b16 v[12:13], v34 offset:18432\n\t"
  "ds_read_b64_tr_b16 v[14:15], v35 offset:0\n\t"
  "ds_read_b64_tr_b16 v[16:17], v35 offset:18432\n\t"
  "ds_read_b64_tr_b16 v[18:19], v36 offset:0\n\t"
  "ds_read_b64_tr_b16 v[20:21], v36 offset:18432\n\t"
  "ds_read_b64_tr_b16 v[22:23], v37 offset:0\n\t"
  "ds_read_b64_tr_b16 v[24:25], v37 offset:18432\n\t"
  "ds_read_b64_tr_b16 v[26:27], v38 offset:0\n\t"
  "ds_read_b64_tr_b16 v[28:29], v38 offset:18432\n\t"
  "ds_read_b64_tr_b16 v[30:31], v39 offset:0\n\t"
  "ds_read_b64_tr_b16 v[32:33], v39 offset:18432\n\t"
  "ds_read_b64_tr_b16 v[56:57], v40 offset:0\n\t"
  "ds_read_b64_tr_b16 v[58:59], v40 offset:18432\n\t"
  "ds_read_b64_tr_b16 v[64:65], v41 offset:0\n\t"
  "ds_read_b64_tr_b16 v[66:67], v41 offset:18432\n\t"
  "ds_read_b64_tr_b16 v[68:69], v42 offset:0\n\t"
  "ds_read_b64_tr_b16 v[70:71], v42 offset:18432\n\t"
  "ds_write_b32 v53, v44 offset:0\n\t"
  "ds_write_b32 v53, v45 offset:256\n\t"
  "ds_write_b32 v53, v46 offset:512\n\t"
  "ds_write_b32 v53, v47 offset:768\n\t"
  "ds_write_b32 v53, v48 offset:1024\n\t"
  "ds_write_b32 v53, v49 offset:1280\n\t"
  "ds_write_b32 v53, v50 offset:1536\n\t"
  "ds_write_b32 v53, v51 offset:1792\n\t"
  "ds_write_b32 v53, v52 offset:2048\n\t"
  "s_waitcnt lgkmcnt(0)\n\t"
  "s_barrier\n\t"
  "ds_write_b128 v9, v[10:13] offset:0\n\t"
  "ds_write_b128 v9, v[14:17] offset:1024\n\t"
  "ds_write_b128 v9, v[18:21] offset:2048\n\t"
  "ds_write_b128 v9, v[22:25] offset:3072\n\t"
  "ds_write_b128 v9, v[26:29] offset:4096\n\t"
  "ds_write_b128 v9, v[30:33] offset:5120\n\t"
  "ds_write_b128 v9, v[56:59] offset:6144\n\t"
  "ds_write_b128 v9, v[64:67] offset:7168\n\t"
  "ds_write_b128 v9, v[68:71] offset:8192\n\t"
  "s_waitcnt vmcnt(0) lgkmcnt(0)\n\t"
  "s_barrier\n\t"
  "ds_read_b64_tr_b16 v[10:11], v34 offset:36864\n\t"
  "ds_read_b64_tr_b16 v[12:13], v34 offset:55296\n\t"
  "ds_read_b64_tr_b16 v[14:15], v35 offset:36864\n\t"
  "ds_read_b64_tr_b16 v[16:17], v35 offset:55296\n\t"
  "ds_read_b64_tr_b16 v[18:19], v36 offset:36864\n\t"
  "ds_read_b64_tr_b16 v[20:21], v36 offset:55296\n\t"
  "ds_read_b64_tr_b16 v[22:23], v37 offset:36864\n\t"
  "ds_read_b64_tr_b16 v[24:25], v37 offset:55296\n\t"
  "ds_read_b64_tr_b16 v[26:27], v38 offset:36864\n\t"
  "ds_read_b64_tr_b16 v[28:29], v38 offset:55296\n\t"
  "ds_read_b64_tr_b16 v[30:31], v39 offset:36864\n\t"
  "ds_read_b64_tr_b16 v[32:33], v39 offset:55296\n\t"
  "ds_read_b64_tr_b16 v[56:57], v40 offset:36864\n\t"
  "ds_read_b64_tr_b16 v[58:59], v40 offset:55296\n\t"
  "ds_read_b64_tr_b16 v[64:65], v41 offset:36864\n\t"
  "ds_read_b64_tr_b16 v[66:67], v41 offset:55296\n\t"
  "ds_read_b64_tr_b16 v[68:69], v42 offset:36864\n\t"
  "ds_read_b64_tr_b16 v[70:71], v42 offset:55296\n\t"
  "s_waitcnt lgkmcnt(0)\n\t"
  "s_barrier\n\t"
  "ds_write_b128 v9, v[10:13] offset:36864\n\t"
  "ds_write_b128 v9, v[14:17] offset:37888\n\t"
  "ds_write_b128 v9, v[18:21] offset:38912\n\t"
  "ds_write_b128 v9, v[22:25] offset:39936\n\t"
  "ds_write_b128 v9, v[26:29] offset:40960\n\t"
  "ds_write_b128 v9, v[30:33] offset:41984\n\t"
  "ds_write_b128 v9, v[56:59] offset:43008\n\t"
  "ds_write_b128 v9, v[64:67] offset:44032\n\t"
  "ds_write_b128 v9, v[68:71] offset:45056\n\t"
  "s_waitcnt lgkmcnt(0)\n\t"
  "s_mov_b32 s56, 0\n\t"
  "s_mov_b32 s65, 0x20000\n\t"
  "s_lshl_b32 s63, s47, 10\n\t"
  "s_lshl_b32 s64, s47, 11\n\t"
  "s_lshl_b32 s61, s47, 14\n\t"
  "s_add_u32 s61, s61, 24576\n\t"
  "s_mul_i32 s62, s47, 9216\n\t"
  "s_add_u32 s62, s62, 90112\n\t"
  "s_lshl_b32 s68, s47, 5\n\t"
  "s_add_u32 s68, s68, s48\n\t"
  "s_lshl_b32 s69, s47, 8\n\t"
  "s_add_u32 s67, s44, 1179648\n\t"
  "v_add_u32 v68, s61, v0\n\t"
  "ds_read_b128 v[44:47], v68 offset:0\n\t"
  "ds_read_b128 v[48:51], v68 offset:1024\n\t"
  "ds_read_b128 v[52:55], v68 offset:2048\n\t"
  "ds_read_b128 v[56:59], v68 offset:3072\n\t"
  "ds_read_b128 v[36:39], v68 offset:4096\n\t"
  "ds_read_b128 v[40:43], v68 offset:5120\n\t"
  "v_mov_b64 v[8:9], 0\n\t"
  "v_mov_b64 v[10:11], 0\n\t"
  "v_mov_b64 v[12:13], 0\n\t"
  "v_mov_b64 v[14:15], 0\n\t"
  "v_mov_b64 v[16:17], 0\n\t"
  "v_mov_b64 v[18:19], 0\n\t"
  "v_mov_b64 v[20:21], 0\n\t"
  "v_mov_b64 v[22:23], 0\n\t"
  "v_mov_b32 v64, -1\n\t"
  "v_mov_b32 v65, -1\n\t"
  "v_mov_b32 v66, -1\n\t"
  "v_mov_b32 v67, -1\n\t"
  "s_lshr_b32 s76, s48, 3\n\t"
  "s_lshl_b32 s76, s76, 14\n\t"
  "s_and_b32 s57, s48, 7\n\t"
  "s_lshl_b32 s57, s57, 10\n\t"
  "s_add_u32 s76, s76, s57\n\t"
  "s_add_u32 s76, s76, s69\n\t"
  "s_add_u32 s76, s76, 524288\n\t"
  "s_mov_b64 s[28:29], s[36:37]\n\t"
  "s_mov_b32 s31, s39\n\t"
  "s_mov_b32 s80, 0x33333333\n\t"
  "s_mov_b32 s81, 0x33333333\n\t"
  "s_mov_b32 s78, -1\n\t"
  "v_lshrrev_b32 v70, 2, v0\n\t"
  "s_add_u32 s70, s73, s40\n\t"
  "s_add_u32 s71, s73, s41\n\t"
  "s_add_u32 s60, s73, s42\n\t"
  "s_add_u32 s78, s78, 1\n\t"
  "s_cmp_eq_u32 s78, %[rn]\n\t"
  "s_cselect_b32 s78, 0, s78\n\t"
  "s_lshr_b64 s[74:75], s[34:35], s78\n\t"
  "s_and_b32 s74, s74, 1\n\t"
  "s_lshl_b32 s74, s74, 13\n\t"
  "s_add_u32 s73, s78, 2\n\t"
  "s_lshl_b32 s73, s73, 21\n\t"
  "s_or_b32 s73, s73, s74\n\t"
  "s_add_u32 s72, s73, s40\n\t"
  "s_sub_u32 s72, s72, 524288\n\t"
  "v_bfe_u32 v30, v0, 8, 1\n\t"
  "v_mul_lo_u32 v30, v30, s50\n\t"
  "v_bfe_u32 v31, v0, 9, 1\n\t"
  "v_mul_u32_u24 v31, 1408, v31\n\t"
  "v_add3_u32 v30, v30, v31, s49\n\t"
  "s_mov_b32 s66, 0\n\t"
  "8:\n\t"
  "buffer_load_dword v29, v30, s[36:39], 0 offen sc1\n\t"
  "s_waitcnt vmcnt(0)\n\t"
  "v_cmp_le_i32 vcc, %[k0], v29\n\t"
  "s_cmp_eq_u64 vcc, -1\n\t"
  "s_cbranch_scc1 81f\n\t"
  "s_sleep 2\n\t"
  "s_add_u32 s66, s66, 1\n\t"
  "s_cmp_lt_u32 s66, s65\n\t"
  "s_cbranch_scc1 8b\n\t"
  "s_mov_b32 s65, 0\n\t"
  "81:\n\t"
  "s_add_u32 m0, s64, 9216\n\t"
  "s_nop 0\n\t"
  "buffer_load_dwordx4 v0, s[36:39], s72 offen sc1 lds\n\t"
  "buffer_load_dwordx4 v0, s[36:39], s72 offen offset:1024 sc1 lds\n\t"
  "s_waitcnt vmcnt(0)\n\t"
  "s_add_u32 s70, s73, s40\n\t"
  "s_add_u32 s71, s73, s41\n\t"
  "s_add_u32 s60, s73, s42\n\t"
  "s_add_u32 s78, s78, 1\n\t"
  "s_cmp_eq_u32 s78, %[rn]\n\t"
  "s_cselect_b32 s78, 0, s78\n\t"
  "s_lshr_b64 s[74:75], s[34:35], s78\n\t"
  "s_and_b32 s74, s74, 1\n\t"
  "s_lshl_b32 s74, s74, 13\n\t"
  "s_add_u32 s73, s78, 2\n\t"
  "s_lshl_b32 s73, s73, 21\n\t"
  "s_or_b32 s73, s73, s74\n\t"
  "s_add_u32 s72, s73, s40\n\t"
  "s_sub_u32 s72, s72, 524288\n\t"
  "v_mov_b32 v60, v29\n\t"
  "s_add_u32 s59, s67, s55\n\t"
  "1:\n\t"
  "s_waitcnt lgkmcnt(0)\n\t"
  "s_barrier\n\t"
  "ds_read_b128 v[24:27], v2 offset:0\n\t"
  "ds_read_b128 v[28:31], v2 offset:1024\n\t"
  "ds_read_b128 v[32:35], v2 offset:2048\n\t"
  "s_add_u32 s58, s59, 65536\n\t"
  "buffer_store_dwordx4 v[64:67], v0, s[36:39], s58 offen sc1\n\t"
  "s_mov_b32 s57, s59\n\t"
  "s_add_u32 s59, s67, s76\n\t"
  "s_waitcnt lgkmcnt(2)\n\t"
  "v_smfmac_f32_16x16x64_bf16 v[8:11], v[24:27], a[0:7], v1\n\t"
  "v_smfmac_f32_16x16x64_bf16 v[12:15], v[24:27], a[8:15], v1\n\t"
  "v_smfmac_f32_16x16x64_bf16 v[16:19], v[24:27], a[16:23], v1\n\t"
  "v_smfmac_f32_16x16x64_bf16 v[20:23], v[24:27], a[24:31], v1\n\t"
  "ds_read_b128 v[24:27], v2 offset:3072\n\t"
  "s_waitcnt lgkmcnt(2)\n\t"
  "v_smfmac_f32_16x16x64_bf16 v[20:23], v[28:31], v[44:51], v1\n\t"
  "ds_read_b128 v[44:47], v68 offset:6144\n\t"
  "v_smfmac_f32_16x16x64_bf16 v[8:11], v[28:31], a[48:55], v1\n\t"
  "ds_read_b128 v[48:51], v68 offset:7168\n\t"
  "v_smfmac_f32_16x16x64_bf16 v[12:15], v[28:31], a[56:63], v1\n\t"
  "buffer_store_dwordx4 v[64:67], v0, s[36:39], s57 offen sc1\n\t"
  "v_cmp_lt_i32 vcc, s56, v60\n\t"
  "v_smfmac_f32_16x16x64_bf16 v[16:19], v[28:31], a[64:71], v1\n\t"
  "ds_read_b128 v[28:31], v2 offset:4096\n\t"
  "s_waitcnt lgkmcnt(4)\n\t"
  "v_smfmac_f32_16x16x64_bf16 v[20:23], v[32:35], v[52:59], v1\n\t"
  "ds_read_b128 v[52:55], v68 offset:8192\n\t"
  "v_smfmac_f32_16x16x64_bf16 v[8:11], v[32:35], a[88:95], v1\n\t"
  "ds_read_b128 v[56:59], v68 offset:9216\n\t"
  "v_smfmac_f32_16x16x64_bf16 v[12:15], v[32:35], a[96:103], v1\n\t"
  "buffer_store_dword v64, v70, s[36:39], s59 offen sc1\n\t"
  "v_smfmac_f32_16x16x64_bf16 v[16:19], v[32:35], a[104:111], v1\n\t"
  "ds_read_b128 v[32:35], v2 offset:5120\n\t"
  "s_waitcnt lgkmcnt(6)\n\t"
  "v_smfmac_f32_16x16x64_bf16 v[20:23], v[24:27], v[36:43], v1\n\t"
  "ds_read_b128 v[36:39], v68 offset:10240\n\t"
  "v_smfmac_f32_16x16x64_bf16 v[8:11], v[24:27], a[128:135], v1\n\t"
  "ds_read_b128 v[40:43], v68 offset:11264\n\t"
  "v_smfmac_f32_16x16x64_bf16 v[12:15], v[24:27], a[136:143], v1\n\t"
  "s_cmp_eq_u64 vcc, -1\n\t"
  "s_cselect_b32 s30, s38, 0\n\t"
  "buffer_load_dwordx4 v[60:63], v0, s[36:39], s70 offen sc1\n\t"
  "v_smfmac_f32_16x16x64_bf16 v[16:19], v[24:27], a[144:151], v1\n\t"
  "ds_read_b128 v[24:27], v2 offset:6144\n\t"
  "s_waitcnt lgkmcnt(6)\n\t"
  "v_smfmac_f32_16x16x64_bf16 v[20:23], v[28:31], v[44:51], v1\n\t"
  "ds_read_b128 v[44:47], v68 offset:12288\n\t"
  "v_smfmac_f32_16x16x64_bf16 v[8:11], v[28:31], a[168:175], v1\n\t"
  "ds_read_b128 v[48:51], v68 offset:13312\n\t"
  "v_smfmac_f32_16x16x64_bf16 v[12:15], v[28:31], a[176:183], v1\n\t"
  "buffer_load_dwordx4 v[64:67], v0, s[36:39], s70 offen offset:1024 sc1\n\t"
  "v_smfmac_f32_16x16x64_bf16 v[16:19], v[28:31], a[184:191], v1\n\t"
  "ds_read_b128 v[28:31], v2 offset:7168\n\t"
  "s_waitcnt lgkmcnt(6)\n\t"
  "v_smfmac_f32_16x16x64_bf16 v[20:23], v[32:35], v[52:59], v1\n\t"
  "ds_read_b128 v[52:55], v68 offset:14336\n\t"
  "v_smfmac_f32_16x16x64_bf16 v[8:11], v[32:35], a[208:215], v1\n\t"
  "ds_read_b128 v[56:59], v68 offset:15360\n\t"
  "v_smfmac_f32_16x16x64_bf16 v[12:15], v[32:35], a[216:223], v1\n\t"
  "v_smfmac_f32_16x16x64_bf16 v[16:19], v[32:35], a[224:231], v1\n\t"
  "ds_read_b128 v[32:35], v2 offset:8192\n\t"
  "s_waitcnt lgkmcnt(6)\n\t"
  "v_smfmac_f32_16x16x64_bf16 v[20:23], v[24:27], v[36:43], v1\n\t"
  "v_mov_b64 v[36:37], 0\n\t"
  "v_smfmac_f32_16x16x64_bf16 v[8:11], v[24:27], a[248:255], v1\n\t"
  "v_mov_b64 v[38:39], 0\n\t"
  "v_smfmac_f32_16x16x64_bf16 v[12:15], v[24:27], v[72:79], v1\n\t"
  "v_mov_b64 v[40:41], 0\n\t"
  "v_smfmac_f32_16x16x64_bf16 v[16:19], v[24:27], v[80:87], v1\n\t"
  "ds_read_b128 v[24:27], v2 offset:0\n\t"
  "s_waitcnt lgkmcnt(4)\n\t"
  "v_smfmac_f32_16x16x64_bf16 v[20:23], v[28:31], v[44:51], v1\n\t"
  "ds_read_b128 v[44:47], v0 offset:9216\n\t"
  "v_mov_b64 v[42:43], 0\n\t"
  "s_waitcnt lgkmcnt(2)\n\t"
  "v_smfmac_f32_16x16x64_bf16 v[20:23], v[32:35], v[52:59], v1\n\t"
  "ds_read_b128 v[48:51], v0 offset:13312\n\t"
  "v_smfmac_f32_16x16x64_bf16 v[8:11], v[28:31], v[104:111], v1\n\t"
  "ds_read_b128 v[52:55], v0 offset:10240\n\t"
  "v_smfmac_f32_16x16x64_bf16 v[8:11], v[32:35], v[144:151], v1\n\t"
  "ds_read_b128 v[56:59], v0 offset:14336\n\t"
  "v_add_f32 v20, v20, v22\n\t"
  "v_smfmac_f32_16x16x64_bf16 v[12:15], v[28:31], v[112:119], v1\n\t"
  "v_add_f32 v21, v21, v23\n\t"
  "v_smfmac_f32_16x16x64_bf16 v[12:15], v[32:35], v[152:159], v1\n\t"
  "v_cvt_pk_bf16_f32 v71, v20, v21\n\t"
  "v_smfmac_f32_16x16x64_bf16 v[16:19], v[28:31], v[120:127], v1\n\t"
  "ds_read_b128 v[28:31], v2 offset:1024\n\t"
  "v_add_f32 v8, v8, v10\n\t"
  "v_smfmac_f32_16x16x64_bf16 v[16:19], v[32:35], v[160:167], v1\n\t"
  "ds_read_b128 v[32:35], v2 offset:2048\n\t"
  "v_add_f32 v9, v9, v11\n\t"
  "v_cvt_pk_bf16_f32 v68, v8, v9\n\t"
  "v_add_f32 v12, v12, v14\n\t"
  "v_add_f32 v13, v13, v15\n\t"
  "v_cvt_pk_bf16_f32 v70, v12, v13\n\t"
  "s_nop 1\n\t"
  "v_add_f32 v16, v16, v18\n\t"
  "v_add_f32 v17, v17, v19\n\t"
  "v_cvt_pk_bf16_f32 v69, v16, v17\n\t"
  "ds_write2st64_b64 v4, v[68:69], v[70:71] offset1:1\n\t"
  "s_waitcnt lgkmcnt(7)\n\t"
  "v_smfmac_f32_16x16x64_bf16 v[36:39], v[24:27], a[32:39], v1\n\t"
  "v_smfmac_f32_16x16x64_bf16 v[40:43], v[24:27], a[40:47], v1\n\t"
  "ds_read_b128 v[24:27], v2 offset:3072\n\t"
  "s_waitcnt lgkmcnt(3)\n\t"
  "v_smfmac_f32_16x16x64_bf16 v[36:39], v[28:31], a[72:79], v1\n\t"
  "v_smfmac_f32_16x16x64_bf16 v[40:43], v[28:31], a[80:87], v1\n\t"
  "ds_read_b128 v[28:31], v2 offset:4096\n\t"
  "s_waitcnt lgkmcnt(2)\n\t"
  "s_barrier\n\t"
  "ds_read_b64_tr_b16 v[8:9], v3 offset:17408\n\t"
  "ds_read_b64_tr_b16 v[10:11], v3 offset:17920\n\t"
  "v_smfmac_f32_16x16x64_bf16 v[36:39], v[32:35], a[112:119], v1\n\t"
  "ds_read_b64_tr_b16 v[12:13], v3 offset:18432\n\t"
  "ds_read_b64_tr_b16 v[14:15], v3 offset:18944\n\t"
  "v_smfmac_f32_16x16x64_bf16 v[40:43], v[32:35], a[120:127], v1\n\t"
  "ds_read_b64_tr_b16 v[16:17], v3 offset:19456\n\t"
  "ds_read_b64_tr_b16 v[18:19], v3 offset:19968\n\t"
  "s_waitcnt lgkmcnt(7)\n\t"
  "v_smfmac_f32_16x16x64_bf16 v[36:39], v[24:27], a[152:159], v1\n\t"
  "ds_read_b64_tr_b16 v[20:21], v3 offset:20480\n\t"
  "ds_read_b64_tr_b16 v[22:23], v3 offset:20992\n\t"
  "v_smfmac_f32_16x16x64_bf16 v[40:43], v[24:27], a[160:167], v1\n\t"
  "s_waitcnt lgkmcnt(6)\n\t"
  "v_mfma_f32_16x16x32_bf16 v[68:71], v[44:47], v[8:11], 0\n\t"
  "v_mfma_f32_16x16x32_bf16 v[24:27], v[48:51], v[8:11], 0\n\t"
  "ds_read_b128 v[44:47], v0 offset:11264\n\t"
  "ds_read_b128 v[48:51], v0 offset:15360\n\t"
  "s_waitcnt lgkmcnt(6)\n\t"
  "v_mfma_f32_16x16x32_bf16 v[68:71], v[52:55], v[12:15], v[68:71]\n\t"
  "v_mfma_f32_16x16x32_bf16 v[24:27], v[56:59], v[12:15], v[24:27]\n\t"
  "ds_read_b128 v[52:55], v0 offset:12288\n\t"
  "ds_read_b128 v[56:59], v0 offset:16384\n\t"
  "v_mfma_f32_16x16x32_bf16 v[32:35], v[8:11], v[8:11], 0\n\t"
  "v_mfma_f32_16x16x32_bf16 v[32:35], v[12:15], v[12:15], v[32:35]\n\t"
  "ds_read_b128 v[8:11], v2 offset:5120\n\t"
  "ds_read_b128 v[12:15], v2 offset:6144\n\t"
  "s_waitcnt lgkmcnt(8)\n\t"
  "v_mfma_f32_16x16x32_bf16 v[32:35], v[16:19], v[16:19], v[32:35]\n\t"
  "s_waitcnt lgkmcnt(6)\n\t"
  "v_mfma_f32_16x16x32_bf16 v[32:35], v[20:23], v[20:23], v[32:35]\n\t"
  "s_waitcnt lgkmcnt(5)\n\t"
  "v_mfma_f32_16x16x32_bf16 v[68:71], v[44:47], v[16:19], v[68:71]\n\t"
  "s_waitcnt lgkmcnt(4)\n\t"
  "v_mfma_f32_16x16x32_bf16 v[24:27], v[48:51], v[16:19], v[24:27]\n\t"
  "ds_read_b128 v[16:19], v2 offset:7168\n\t"
  "s_waitcnt lgkmcnt(4)\n\t"
  "v_mfma_f32_16x16x32_bf16 v[68:71], v[52:55], v[20:23], v[68:71]\n\t"
  "s_waitcnt lgkmcnt(3)\n\t"
  "v_mfma_f32_16x16x32_bf16 v[24:27], v[56:59], v[20:23], v[24:27]\n\t"
  "ds_read_b128 v[20:23], v2 offset:8192\n\t"
  "v_smfmac_f32_16x16x64_bf16 v[36:39], v[28:31], a[192:199], v1\n\t"
  "v_add_u32 v55, s63, v3\n\t"
  "v_smfmac_f32_16x16x64_bf16 v[40:43], v[28:31], a[200:207], v1\n\t"
  "ds_read_b64_tr_b16 v[56:57], v55 offset:17408\n\t"
  "ds_read_b64_tr_b16 v[58:59], v55 offset:17920\n\t"
  "v_max3_f32 v28, v34, v35, v68\n\t"
  "v_max3_f32 v29, v69, v70, v71\n\t"
  "v_max3_f32 v30, v24, v25, v26\n\t"
  "v_max3_f32 v28, v28, v29, v27\n\t"
  "v_max_f32 v29, v28, v30\n\t"
  "v_max_f32 v28, v28, v30\n\t"
  "s_waitcnt lgkmcnt(5)\n\t"
  "v_smfmac_f32_16x16x64_bf16 v[36:39], v[8:11], a[232:239], v1\n\t"
  "s_add_u32 s57, s43, s51\n\t"
  "s_barrier\n\t"
  "v_permlane32_swap_b32 v28, v29\n\t"
  "v_max_f32 v30, v28, v29\n\t"
  "v_max_f32 v28, v28, v29\n\t"
  "v_smfmac_f32_16x16x64_bf16 v[40:43], v[8:11], a[240:247], v1\n\t"
  "s_not_b64 exec, s[80:81]\n\t"
  "s_waitcnt lgkmcnt(0)\n\t"
  "buffer_store_dwordx4 v[56:59], v5, s[36:39], s71 offen sc1\n\t"
  "s_mov_b64 exec, -1\n\t"
  "v_permlane16_swap_b32 v28, v30\n\t"
  "v_max_f32 v28, v28, v30\n\t"
  "v_mul_f32 v28, 0xbe0293ee, v28\n\t"
  "v_fmamk_f32 v68, v68, 0x3e0293ee, v28\n\t"
  "v_fmamk_f32 v69, v69, 0x3e0293ee, v28\n\t"
  "v_exp_f32 v68, v68\n\t"
  "v_exp_f32 v69, v69\n\t"
  "v_smfmac_f32_16x16x64_bf16 v[36:39], v[12:15], v[88:95], v1\n\t"
  "s_add_u32 s58, s57, 65536\n\t"
  "buffer_load_dwordx4 v0, s[28:31], s72 offen offset:0 sc1 lds\n\t"
  "v_fmamk_f32 v70, v70, 0x3e0293ee, v28\n\t"
  "v_fmamk_f32 v71, v71, 0x3e0293ee, v28\n\t"
  "v_cvt_pk_bf16_f32 v48, v68, v69\n\t"
  "v_exp_f32 v70, v70\n\t"
  "v_exp_f32 v71, v71\n\t"
  "v_smfmac_f32_16x16x64_bf16 v[40:43], v[12:15], v[96:103], v1\n\t"
  "s_add_u32 s59, s43, s52\n\t"
  "v_fmamk_f32 v24, v24, 0x3e0293ee, v28\n\t"
  "v_fmamk_f32 v25, v25, 0x3e0293ee, v28\n\t"
  "v_cvt_pk_bf16_f32 v49, v70, v71\n\t"
  "v_exp_f32 v24, v24\n\t"
  "v_exp_f32 v25, v25\n\t"
  "v_smfmac_f32_16x16x64_bf16 v[36:39], v[16:19], v[128:135], v1\n\t"
  "buffer_load_dwordx4 v0, s[28:31], s72 offen offset:1024 sc1 lds\n\t"
  "v_fmamk_f32 v26, v26, 0x3e0293ee, v28\n\t"
  "v_fmamk_f32 v27, v27, 0x3e0293ee, v28\n\t"
  "v_cvt_pk_bf16_f32 v50, v24, v25\n\t"
  "v_exp_f32 v26, v26\n\t"
  "v_exp_f32 v27, v27\n\t"
  "v_smfmac_f32_16x16x64_bf16 v[40:43], v[16:19], v[136:143], v1\n\t"
  "v_fmamk_f32 v34, v34, 0x3e0293ee, v28\n\t"
  "v_cvt_pk_bf16_f32 v51, v26, v27\n\t"
  "v_smfmac_f32_16x16x64_bf16 v[36:39], v[20:23], v[168:175], v1\n\t"
  "v_fmamk_f32 v35, v35, 0x3e0293ee, v28\n\t"
  "v_smfmac_f32_16x16x64_bf16 v[40:43], v[20:23], v[176:183], v1\n\t"
  "s_waitcnt vmcnt(3)\n\t"
  "v_add_u32 v29, s62, v0\n\t"
  "v_mfma_f32_16x16x32_bf16 v[8:11], v[60:63], v[48:51], 0\n\t"
  "v_exp_f32 v34, v34\n\t"
  "ds_read_b128 v[56:59], v29 offset:8192\n\t"
  "ds_read_b128 v[60:63], v29 offset:45056\n\t"
  "v_mfma_f32_16x16x32_bf16 v[12:15], v[64:67], v[48:51], 0\n\t"
  "v_exp_f32 v35, v35\n\t"
  "ds_read_b128 v[64:67], v29 offset:0\n\t"
  "ds_read_b128 v[68:71], v29 offset:36864\n\t"
  "v_mov_b32 v44, 0x3f803f80\n\t"
  "v_mov_b32 v45, 0x3f803f80\n\t"
  "v_mov_b64 v[46:47], v[44:45]\n\t"
  "v_cvt_pk_bf16_f32 v52, v34, v35\n\t"
  "v_mov_b32 v53, 0\n\t"
  "v_mfma_f32_16x16x32_bf16 v[16:19], v[44:47], v[48:51], 0\n\t"
  "v_add_f32 v36, v36, v38\n\t"
  "v_add_f32 v37, v37, v39\n\t"
  "v_add_f32 v40, v40, v42\n\t"
  "v_add_f32 v41, v41, v43\n\t"
  "v_cvt_pk_bf16_f32 v30, v36, v37\n\t"
  "v_cvt_pk_bf16_f32 v31, v40, v41\n\t"
  "v_mov_b32 v54, 0\n\t"
  "v_cvt_pk_bf16_f32 v55, v34, v35\n\t"
  "v_mfma_f32_16x16x16_bf16 v[16:19], v[44:45], v[52:53], v[16:19]\n\t"
  "ds_read_b128 v[36:39], v29 offset:1024\n\t"
  "ds_read_b128 v[40:43], v29 offset:37888\n\t"
  "v_mfma_f32_16x16x16_bf16 v[8:11], v[30:31], v[52:53], v[8:11]\n\t"
  "v_mov_b64 v[24:25], 0\n\t"
  "v_mov_b64 v[26:27], 0\n\t"
  "v_mfma_f32_16x16x16_bf16 v[12:15], v[30:31], v[54:55], v[12:15]\n\t"
  "v_rcp_f32 v16, v16\n\t"
  "s_mov_b64 exec, s[80:81]\n\t"
  "s_nop 1\n\t"
  "v_mul_f32 v8, v8, v16\n\t"
  "v_mul_f32 v9, v9, v16\n\t"
  "v_mul_f32 v10, v10, v16\n\t"
  "v_mul_f32 v11, v11, v16\n\t"
  "v_cvt_pk_bf16_f32 v8, v8, v9\n\t"
  "v_cvt_pk_bf16_f32 v9, v10, v11\n\t"
  "v_pk_mul_f32 v[12:13], v[12:13], v[16:17] op_sel_hi:[1,0]\n\t"
  "v_pk_mul_f32 v[14:15], v[14:15], v[16:17] op_sel_hi:[1,0]\n\t"
  "v_cvt_pk_bf16_f32 v10, v12, v13\n\t"
  "v_cvt_pk_bf16_f32 v11, v14, v15\n\t"
  "ds_write_b128 v5, v[8:11]\n\t"
  "s_mov_b64 exec, -1\n\t"
  "v_mov_b64 v[32:33], 0\n\t"
  "v_mov_b64 v[34:35], 0\n\t"
  "v_mov_b64 v[20:21], 0\n\t"
  "v_mov_b64 v[22:23], 0\n\t"
  "v_mov_b64 v[48:49], 0\n\t"
  "v_mov_b64 v[50:51], 0\n\t"
  "v_mov_b64 v[44:45], 0\n\t"
  "v_mov_b64 v[46:47], 0\n\t"
  "v_mov_b64 v[52:53], 0\n\t"
  "v_mov_b64 v[54:55], 0\n\t"
  "v_mov_b64 v[16:17], 0\n\t"
  "v_mov_b64 v[18:19], 0\n\t"
  "s_cmp_eq_u32 s56, 63\n\t"
  "s_cbranch_scc1 9f\n\t"
  "s_waitcnt lgkmcnt(0)\n\t"
  "s_barrier\n\t"
  "ds_read_b128 v[8:11], v0 offset:21504\n\t"
  "ds_read_b128 v[12:15], v0 offset:22528\n\t"
  "buffer_store_dwordx2 v[30:31], v3, s[36:39], s60 offen sc1\n\t"
  "2:\n\t"
  "s_waitcnt lgkmcnt(1)\n\t"
  "v_smfmac_f32_16x16x64_bf16 v[24:27], v[8:11], v[248:255], v1\n\t"
  "s_waitcnt lgkmcnt(0)\n\t"
  "v_smfmac_f32_16x16x64_bf16 v[24:27], v[12:15], v[56:63], v1\n\t"
  "ds_read_b128 v[56:59], v29 offset:2048\n\t"
  "v_smfmac_f32_16x16x64_bf16 v[32:35], v[8:11], v[184:191], v1\n\t"
  "ds_read_b128 v[60:63], v29 offset:38912\n\t"
  "v_smfmac_f32_16x16x64_bf16 v[32:35], v[12:15], v[64:71], v1\n\t"
  "ds_read_b128 v[64:67], v29 offset:3072\n\t"
  "v_add_f32 v24, v24, v26\n\t"
  "v_add_f32 v25, v25, v27\n\t"
  "v_smfmac_f32_16x16x64_bf16 v[20:23], v[8:11], v[192:199], v1\n\t"
  "ds_read_b128 v[68:71], v29 offset:39936\n\t"
  "v_cvt_pk_bf16_f32 v28, v24, v25\n\t"
  "buffer_store_dword v28, v7, s[36:39], s59 offen sc1\n\t"
  "v_smfmac_f32_16x16x64_bf16 v[20:23], v[12:15], v[36:43], v1\n\t"
  "ds_read_b128 v[36:39], v29 offset:4096\n\t"
  "v_mov_b64 v[24:25], 0\n\t"
  "v_mov_b64 v[26:27], 0\n\t"
  "v_smfmac_f32_16x16x64_bf16 v[48:51], v[8:11], v[200:207], v1\n\t"
  "ds_read_b128 v[40:43], v29 offset:40960\n\t"
  "v_add_f32 v32, v32, v34\n\t"
  "v_add_f32 v33, v33, v35\n\t"
  "s_waitcnt lgkmcnt(4)\n\t"
  "v_smfmac_f32_16x16x64_bf16 v[48:51], v[12:15], v[56:63], v1\n\t"
  "ds_read_b128 v[56:59], v29 offset:5120\n\t"
  "v_cvt_pk_bf16_f32 v32, v32, v33\n\t"
  "v_add_f32 v20, v20, v22\n\t"
  "v_smfmac_f32_16x16x64_bf16 v[44:47], v[8:11], v[208:215], v1\n\t"
  "ds_read_b128 v[60:63], v29 offset:41984\n\t"
  "v_add_f32 v21, v21, v23\n\t"
  "v_cvt_pk_bf16_f32 v33, v20, v21\n\t"
  "s_waitcnt lgkmcnt(4)\n\t"
  "v_smfmac_f32_16x16x64_bf16 v[44:47], v[12:15], v[64:71], v1\n\t"
  "ds_read_b128 v[64:67], v29 offset:6144\n\t"
  "v_mov_b64 v[20:21], 0\n\t"
  "v_mov_b64 v[22:23], 0\n\t"
  "v_smfmac_f32_16x16x64_bf16 v[52:55], v[8:11], v[216:223], v1\n\t"
  "ds_read_b128 v[68:71], v29 offset:43008\n\t"
  "v_add_f32 v48, v48, v50\n\t"
  "v_add_f32 v49, v49, v51\n\t"
  "s_waitcnt lgkmcnt(4)\n\t"
  "v_smfmac_f32_16x16x64_bf16 v[52:55], v[12:15], v[36:43], v1\n\t"
  "ds_read_b128 v[36:39], v29 offset:7168\n\t"
  "v_cvt_pk_bf16_f32 v34, v48, v49\n\t"
  "v_add_f32 v44, v44, v46\n\t"
  "v_smfmac_f32_16x16x64_bf16 v[16:19], v[8:11], v[224:231], v1\n\t"
  "ds_read_b128 v[40:43], v29 offset:44032\n\t"
  "v_add_f32 v45, v45, v47\n\t"
  "v_cvt_pk_bf16_f32 v35, v44, v45\n\t"
  "s_waitcnt lgkmcnt(4)\n\t"
  "v_smfmac_f32_16x16x64_bf16 v[16:19], v[12:15], v[56:63], v1\n\t"
  "buffer_store_dwordx4 v[32:35], v6, s[36:39], s57 offen sc1\n\t"
  "v_add_f32 v52, v52, v54\n\t"
  "v_smfmac_f32_16x16x64_bf16 v[24:27], v[8:11], v[232:239], v1\n\t"
  "v_add_f32 v53, v53, v55\n\t"
  "v_cvt_pk_bf16_f32 v52, v52, v53\n\t"
  "s_waitcnt lgkmcnt(2)\n\t"
  "v_smfmac_f32_16x16x64_bf16 v[24:27], v[12:15], v[64:71], v1\n\t"
  "v_add_f32 v16, v16, v18\n\t"
  "v_add_f32 v17, v17, v19\n\t"
  "v_smfmac_f32_16x16x64_bf16 v[20:23], v[8:11], v[240:247], v1\n\t"
  "v_cvt_pk_bf16_f32 v53, v16, v17\n\t"
  "s_waitcnt lgkmcnt(0)\n\t"
  "v_smfmac_f32_16x16x64_bf16 v[20:23], v[12:15], v[36:43], v1\n\t"
  "v_add_f32 v24, v24, v26\n\t"
  "v_add_f32 v25, v25, v27\n\t"
  "v_cvt_pk_bf16_f32 v54, v24, v25\n\t"
  "s_nop 4\n\t"
  "v_pk_add_f32 v[20:21], v[20:21], v[22:23]\n\t"
  "s_nop 0\n\t"
  "v_cvt_pk_bf16_f32 v55, v20, v21\n\t"
  "buffer_store_dwordx4 v[52:55], v6, s[36:39], s58 offen sc1\n\t"
  "v_add_u32 v68, s61, v0\n\t"
  "ds_read_b128 v[44:47], v68 offset:0\n\t"
  "ds_read_b128 v[48:51], v68 offset:1024\n\t"
  "ds_read_b128 v[36:39], v68 offset:4096\n\t"
  "ds_read_b128 v[40:43], v68 offset:5120\n\t"
  "v_mov_b64 v[8:9], 0\n\t"
  "v_mov_b64 v[10:11], 0\n\t"
  "v_mov_b64 v[12:13], 0\n\t"
  "v_mov_b64 v[14:15], 0\n\t"
  "v_mov_b64 v[16:17], 0\n\t"
  "v_mov_b64 v[18:19], 0\n\t"
  "v_mov_b64 v[20:21], 0\n\t"
  "v_mov_b64 v[22:23], 0\n\t"
  "v_add_u32 v69, s64, v0\n\t"
  "v_lshrrev_b32 v70, 2, v0\n\t"
  "v_add_u32 v71, s69, v70\n\t"
  "s_add_u32 s56, s56, 1\n\t"
  "s_cmp_ge_u32 s68, 72\n\t"
  "s_cbranch_scc1 3f\n\t"
  "v_and_b32 v58, 16, v0\n\t"
  "v_mov_b32 v56, 0x3f80\n\t"
  "v_lshlrev_b32 v58, v58, v56\n\t"
  "v_bfe_u32 v56, v0, 5, 3\n\t"
  "v_cmp_eq_u32 vcc, 0, v56\n\t"
  "s_nop 1\n\t"
  "v_cndmask_b32 v64, 0, v58, vcc\n\t"
  "v_cmp_eq_u32 vcc, 1, v56\n\t"
  "s_nop 1\n\t"
  "v_cndmask_b32 v65, 0, v58, vcc\n\t"
  "v_cmp_eq_u32 vcc, 2, v56\n\t"
  "s_nop 1\n\t"
  "v_cndmask_b32 v66, 0, v58, vcc\n\t"
  "v_cmp_eq_u32 vcc, 3, v56\n\t"
  "s_nop 1\n\t"
  "v_cndmask_b32 v67, 0, v58, vcc\n\t"
  "v_lshrrev_b32 v57, 6, v0\n\t"
  "v_and_b32 v57, 12, v57\n\t"
  "v_and_b32 v58, 0x70, v0\n\t"
  "v_or_b32 v57, v57, v58\n\t"
  "v_lshlrev_b32 v58, 24, v0\n\t"
  "v_and_b32 v58, 0x80000000, v58\n\t"
  "v_or_b32 v57, v57, v58\n\t"
  "v_lshlrev_b32 v56, 2, v0\n\t"
  "v_and_b32 v56, 0x80, v56\n\t"
  "v_lshrrev_b32 v58, 1, v0\n\t"
  "v_and_b32 v58, 0x60, v58\n\t"
  "v_or_b32 v56, v56, v58\n\t"
  "v_and_b32 v58, 0x310, v0\n\t"
  "v_or_b32 v56, v56, v58\n\t"
  "s_add_u32 s57, s43, s53\n\t"
  "s_add_u32 s58, s45, s54\n\t"
  "s_mov_b32 s66, 0\n\t"
  "s_sleep %[d0]\n\t"
  "s_cmp_lg_u32 s47, 1\n\t"
  "s_cbranch_scc1 5f\n\t"
  "s_sleep %[d1]\n\t"
  "5:\n\t"
  "buffer_load_dwordx4 v[24:27], v56, s[36:39], s57 offen offset:0 sc1\n\t"
  "buffer_load_dwordx4 v[28:31], v56, s[36:39], s57 offen offset:1024 sc1\n\t"
  "buffer_load_dwordx4 v[32:35], v56, s[36:39], s57 offen offset:2048 sc1\n\t"
  "buffer_load_dwordx4 v[60:63], v56, s[36:39], s57 offen offset:3072 sc1\n\t"
  "s_waitcnt vmcnt(7)\n\t"
  "s_barrier\n\t"
  "50:\n\t"
  "s_waitcnt vmcnt(3)\n\t"
  "v_mfma_f32_16x16x32_bf16 v[52:55], v[24:27], v[64:67], 0\n\t"
  "v_max3_u32 v58, v24, v25, v26\n\t"
  "v_max_u32 v58, v58, v27\n\t"
  "s_waitcnt vmcnt(2)\n\t"
  "v_mfma_f32_16x16x32_bf16 v[52:55], v[28:31], v[64:67], v[52:55]\n\t"
  "v_max3_u32 v58, v58, v28, v29\n\t"
  "v_max3_u32 v58, v58, v30, v31\n\t"
  "s_waitcnt vmcnt(1)\n\t"
  "v_mfma_f32_16x16x32_bf16 v[52:55], v[32:35], v[64:67], v[52:55]\n\t"
  "v_max3_u32 v58, v58, v32, v33\n\t"
  "v_max3_u32 v58, v58, v34, v35\n\t"
  "s_waitcnt vmcnt(0)\n\t"
  "v_mfma_f32_16x16x32_bf16 v[52:55], v[60:63], v[64:67], v[52:55]\n\t"
  "v_max3_u32 v58, v58, v60, v61\n\t"
  "v_max3_u32 v58, v58, v62, v63\n\t"
  "v_cmp_eq_u32 vcc, -1, v58\n\t"
  "s_cbranch_vccnz 51f\n\t"
  "52:\n\t"
  "s_nop 3\n\t"
  "v_add_f32 v52, v52, v54\n\t"
  "v_add_f32 v53, v53, v55\n\t"
  "v_cvt_pk_bf16_f32 v58, v52, v53\n\t"
  "buffer_store_dword v58, v57, s[36:39], s58 offen sc1\n\t"
  "v_mov_b32 v56, -1\n\t"
  "s_xor_b32 s58, s58, 0x10000\n\t"
  "buffer_store_dword v56, v57, s[36:39], s58 offen sc1\n\t"
  "6:\n\t"
  "s_barrier\n\t"
  "s_cmp_lg_u32 s30, 0\n\t"
  "s_cbranch_scc1 61f\n\t"
  "buffer_load_dwordx4 v0, s[36:39], s72 offen sc1 lds\n\t"
  "buffer_load_dwordx4 v0, s[36:39], s72 offen offset:1024 sc1 lds\n\t"
  "61:\n\t"
  "ds_read_b128 v[52:55], v68 offset:2048\n\t"
  "ds_read_b128 v[56:59], v68 offset:3072\n\t"
  "s_add_u32 s57, s45, s64\n\t"
  "s_add_u32 s58, s45, s69\n\t"
  "s_add_u32 s58, s58, 16384\n\t"
  "s_mov_b32 s66, 0\n\t"
  "s_sleep %[d2]\n\t"
  "buffer_load_dwordx4 v[24:27], v0, s[36:39], s57 offen sc1\n\t"
  "buffer_load_dwordx4 v[28:31], v0, s[36:39], s57 offen offset:1024 sc1\n\t"
  "buffer_load_dword v32, v70, s[36:39], s58 offen sc1\n\t"
  "s_add_u32 s70, s73, s40\n\t"
  "s_add_u32 s71, s73, s41\n\t"
  "s_add_u32 s60, s73, s42\n\t"
  "s_add_u32 s78, s78, 1\n\t"
  "s_cmp_eq_u32 s78, %[rn]\n\t"
  "s_cselect_b32 s78, 0, s78\n\t"
  "s_lshr_b64 s[74:75], s[34:35], s78\n\t"
  "s_and_b32 s74, s74, 1\n\t"
  "s_lshl_b32 s74, s74, 13\n\t"
  "s_add_u32 s73, s78, 2\n\t"
  "s_lshl_b32 s73, s73, 21\n\t"
  "s_or_b32 s73, s73, s74\n\t"
  "s_add_u32 s72, s73, s40\n\t"
  "s_sub_u32 s72, s72, 524288\n\t"
  "v_bfe_u32 v34, v0, 8, 1\n\t"
  "v_mul_lo_u32 v34, v34, s50\n\t"
  "v_bfe_u32 v35, v0, 9, 1\n\t"
  "v_mul_u32_u24 v35, 1408, v35\n\t"
  "v_add3_u32 v34, v34, v35, s49\n\t"
  "v_mov_b32 v64, -1\n\t"
  "v_mov_b32 v65, -1\n\t"
  "v_mov_b32 v66, -1\n\t"
  "v_mov_b32 v67, -1\n\t"
  "v_mov_b32 v60, -1\n\t"
  "buffer_load_dword v60, v34, s[36:39], 0 offen sc1\n\t"
  "s_mov_b32 s67, s43\n\t"
  "s_add_u32 s43, s43, 589824\n\t"
  "s_add_u32 s59, s44, 1769472\n\t"
  "s_cmp_eq_u32 s43, s59\n\t"
  "s_cselect_b32 s43, s44, s43\n\t"
  "s_add_u32 s45, s45, 32768\n\t"
  "s_bitset0_b32 s45, 17\n\t"
  "s_add_u32 s59, s67, s55\n\t"
  "s_waitcnt vmcnt(3)\n\t"
  "ds_write_b128 v69, v[24:27]\n\t"
  "v_max3_u32 v33, v24, v25, v26\n\t"
  "s_waitcnt vmcnt(2)\n\t"
  "ds_write_b128 v69, v[28:31] offset:1024\n\t"
  "v_max3_u32 v33, v33, v27, v28\n\t"
  "v_max3_u32 v33, v33, v29, v30\n\t"
  "s_waitcnt vmcnt(1)\n\t"
  "ds_write_b32 v71, v32 offset:8192\n\t"
  "v_max3_u32 v33, v33, v31, v32\n\t"
  "v_cmp_eq_u32 vcc, -1, v33\n\t"
  "s_cbranch_vccz 1b\n\t"
  "7:\n\t"
  "s_waitcnt lgkmcnt(0)\n\t"
  "buffer_load_dwordx4 v[24:27], v0, s[36:39], s57 offen sc1\n\t"
  "buffer_load_dwordx4 v[28:31], v0, s[36:39], s57 offen offset:1024 sc1\n\t"
  "buffer_load_dword v32, v70, s[36:39], s58 offen sc1\n\t"
  "s_waitcnt vmcnt(2)\n\t"
  "ds_write_b128 v69, v[24:27]\n\t"
  "v_max3_u32 v33, v24, v25, v26\n\t"
  "s_waitcnt vmcnt(1)\n\t"
  "ds_write_b128 v69, v[28:31] offset:1024\n\t"
  "v_max3_u32 v33, v33, v27, v28\n\t"
  "v_max3_u32 v33, v33, v29, v30\n\t"
  "s_waitcnt vmcnt(0)\n\t"
  "ds_write_b32 v71, v32 offset:8192\n\t"
  "v_max3_u32 v33, v33, v31, v32\n\t"
  "v_cmp_eq_u32 vcc, -1, v33\n\t"
  "s_cbranch_vccz 1b\n\t"
  "s_add_u32 s66, s66, 1\n\t"
  "s_cmp_lt_u32 s66, s65\n\t"
  "s_cbranch_scc1 7b\n\t"
  "s_mov_b32 s65, 0\n\t"
  "s_branch 1b\n\t"
  "51:\n\t"
  "buffer_load_dwordx4 v[24:27], v56, s[36:39], s57 offen offset:0 sc1\n\t"
  "buffer_load_dwordx4 v[28:31], v56, s[36:39], s57 offen offset:1024 sc1\n\t"
  "buffer_load_dwordx4 v[32:35], v56, s[36:39], s57 offen offset:2048 sc1\n\t"
  "buffer_load_dwordx4 v[60:63], v56, s[36:39], s57 offen offset:3072 sc1\n\t"
  "s_add_u32 s66, s66, 1\n\t"
  "s_cmp_lt_u32 s66, s65\n\t"
  "s_cbranch_scc1 50b\n\t"
  "s_mov_b32 s65, 0\n\t"
  "s_waitcnt vmcnt(0)\n\t"
  "s_branch 52b\n\t"
  "3:\n\t"
  "s_cmp_lg_u32 s47, 3\n\t"
  "s_cbranch_scc0 31f\n\t"
  "s_waitcnt vmcnt(3)\n\t"
  "s_barrier\n\t"
  "s_branch 6b\n\t"
  "31:\n\t"
  "v_bfe_u32 v30, v0, 8, 1\n\t"
  "v_mul_lo_u32 v30, v30, s50\n\t"
  "v_bfe_u32 v31, v0, 9, 1\n\t"
  "v_mul_u32_u24 v31, 1408, v31\n\t"
  "v_add3_u32 v30, v30, v31, s49\n\t"
  "buffer_load_dword v29, v30, s[36:39], 0 offen sc1\n\t"
  "s_sub_u32 s57, s56, 1\n\t"
  "v_mov_b32 v28, s57\n\t"
  "s_mov_b32 s66, 0\n\t"
  "s_waitcnt vmcnt(4)\n\t"
  "s_barrier\n\t"
  "buffer_store_dword v28, off, s[36:39], s46 sc1\n\t"
  "s_sleep %[d3]\n\t"
  "s_waitcnt vmcnt(1)\n\t"
  "32:\n\t"
  "v_cmp_le_i32 vcc, s56, v29\n\t"
  "s_cmp_eq_u64 vcc, -1\n\t"
  "s_cbranch_scc1 6b\n\t"
  "s_sleep 1\n\t"
  "buffer_load_dword v29, v30, s[36:39], 0 offen sc1\n\t"
  "s_waitcnt vmcnt(0)\n\t"
  "s_add_u32 s66, s66, 1\n\t"
  "s_cmp_lt_u32 s66, s65\n\t"
  "s_cbranch_scc1 32b\n\t"
  "s_mov_b32 s65, 0\n\t"
  "s_branch 6b\n\t"
  "9:\n\t"
  ".Lws_code_end:\n\t"
  "buffer_store_dwordx2 v[30:31], v3, s[36:39], s60 offen sc1\n\t"
  "s_waitcnt vmcnt(0) lgkmcnt(0)\n\t"
  "s_barrier\n\t"
  "v_mov_b32 v8, 63\n\t"
  "buffer_store_dword v8, off, s[36:39], s46 sc1\n\t"
  : "+{v[8:39]}"(a), "+{v[40:55]}"(b)
  : "{v[0:7]}"(lanec), [d1] "i"(CH_D1), [d2] "i"(CH_D2), [d3] "i"(CH_D3), [d0] "i"(CH_D0), [rn] "i"(RINGN), [k0] "i"(K_FIRST)
  :
    "s28", "s29", "s30", "s31", CLOB_HI);
}

#define NPROBE 6
__global__ void __launch_bounds__(512) init_kernel(unsigned char* ws) {
  const int b = blockIdx.x, idx = (b >> 3) * 4 + (b & 3);
  if (b & 4) {
    const rsrc_t r = __builtin_amdgcn_make_buffer_rsrc(ws, 0, INIT_BYTES, 0x00020000);
    #pragma unroll
    for (int k = 0; k < 4; ++k) {
      const int o = idx * (INIT_BYTES / 128) + k * 8192 + threadIdx.x * 16;
      const bool used = o < BLK ? o <= ABORT_OFF : o < BLK + 4 * 32768;
      if (used) __builtin_amdgcn_raw_buffer_store_b128(v4u{SENT, SENT, SENT, SENT}, r, o, 0, AUX_SC1);
    }
  } else if (threadIdx.x < 64 && idx < BLK_END) {
    const v4u rs = make_srd(ws, WS_USED);
    unsigned m[2] = {~0u, ~0u};
    #pragma unroll
    for (int r = 0; r < NPROBE; ++r) {
      unsigned long long t0, t1;
      asm volatile("s_memtime %0\n\ts_waitcnt lgkmcnt(0)\n\tbuffer_store_dword %2, off, %3, %4 sc1\n\ts_waitcnt vmcnt(0)\n\ts_memtime %1\n\ts_waitcnt lgkmcnt(0)"
                   : "=&s"(t0), "=&s"(t1) : "v"(SENT), "s"(rs), "s"(idx * BLK + 0x1F4000 + (idx & 31) * 256 + (r & 1) * 8192) : "memory");
      m[r & 1] = min(m[r & 1], (unsigned)(t1 - t0));
    }
    if (threadIdx.x == 0) ((unsigned*)(ws + POL_OFF))[idx] = m[1] < m[0] ? 1u : 0u;
  }
}

typedef __attribute__((address_space(4))) const Params* kparg_t;
template <class F> __device__ __forceinline__ void run_role(int who, int w, F f) {
  unsigned long long ka = (unsigned long long)__builtin_amdgcn_kernarg_segment_ptr();
  asm volatile("" : "+s"(ka), "+s"(who), "+s"(w));
  const kparg_t kp = (kparg_t)ka;
  Params p;
  p.x = kp->x; p.mem0 = kp->mem0; p.wq = kp->wq; p.wk = kp->wk; p.wv = kp->wv; p.wo = kp->wo; p.wg = kp->wg; p.wu = kp->wu; p.wd = kp->wd;
  p.out = kp->out; p.ws = kp->ws;
  Ctx c;
  c.r = __builtin_amdgcn_make_buffer_rsrc(p.ws, 0, WS_USED, 0x00020000);
  c.rs = make_srd(p.ws, WS_USED);
  { unsigned zero = 0; c.lds = (LAS unsigned char*)zero; }
  c.lane = fresh_lane();
  c.w = w;
  c.layer = who >> 7;
  c.idx = who & 127;
  c.dead = 0;
  c.pa = __ballot(__builtin_amdgcn_raw_buffer_load_b32(c.r, (BLK_A + c.lane) * 4, POL_OFF, 0) != (unsigned)(c.layer ^ POLX));
  c.pb = __ballot(__builtin_amdgcn_raw_buffer_load_b32(c.r, (c.lane < 48 ? BLK_B + c.lane : (c.lane & 1)) * 4, POL_OFF, 0) != (unsigned)(c.layer ^ POLX));
  c.myflag = flag_at(c.layer, c.idx, c.w);
  f(c, p);
}

__global__ void __launch_bounds__(256, 1) mega_kernel(Params) {
  const int w = __builtin_amdgcn_readfirstlane(threadIdx.x >> 6), b = blockIdx.x;
  {
    int o = (((b >> 3) & 3) * 256 + threadIdx.x) * 128;
    asm volatile("s_getpc_b64 s[34:35]\n0:\n\tv_mov_b32 v255, .Lws_code_end-0b\n\tv_max_u32 v255, .Lws_end_B-0b, v255\n\tv_max_u32 v255, .Lws_end_D-0b, v255\n\t"
                 "v_max_u32 v255, .Lws_end_C-0b, v255\n\tv_min_u32 %0, v255, %0\n\tglobal_load_dword v255, %0, s[34:35]" : "+v"(o) :: "s34", "s35", "v255");
  }
  const int idx = (b & 3) == 0 ? (b >> 3) : 32 + (b >> 3) * 3 + (b & 3) - 1;
  const int who = ((b >> 2) & 1) * 128 + idx;
#define STAG 0
#define WARM 0
  const bool urgent = who < R_B || (who >= R_KV && who < R_C) || idx == R_END;
  if (WARM && !urgent) run_role(who, w, [](Ctx& c, const Params& p) {
    const int k = (c.layer ? 73 + c.idx : c.idx < R_KV ? c.idx - R_B : c.idx - R_C + 44) * 4 + c.w;
    const void* m[4] = {p.wk, p.wv, p.wq, p.wo};
    unsigned acc = 0;
    #pragma unroll
    for (int j = 0; j < 4; ++j) {
      const rsrc_t r = __builtin_amdgcn_make_buffer_rsrc((void*)m[j], 0, DM * HD * 2, 0x00020000);
      acc |= __builtin_amdgcn_raw_buffer_load_b32(r, k < 576 ? k * 8192 + c.lane * 128 : (int)OOB, 0, 0);
    }
    asm volatile("" :: "v"(acc));
  });
  if (STAG && !urgent) for (int k = 0; k < (who < 128 ? STAG : 2 * STAG); ++k) __builtin_amdgcn_s_sleep(32);
  if (idx < R_B) run_role(who, w, [](Ctx& c, const Params& p) { role_chain(c, p); });
  else if (idx < R_D) run_role(who, w, [](Ctx& c, const Params& p) { role_proj<32>(c, p.wo); });
  else if (idx < R_E) run_role(who, w, [](Ctx& c, const Params& p) { role_proj<36>(c, p.wd); });
  else if (idx < R_QA && w < 2) run_role(who, w, [](Ctx& c, const Params& p) { role_e<1, 0>(c, p); });
  else if (who < R_QA) run_role(who, w, [](Ctx& c, const Params& p) { role_e<2, 0>(c, p); });
  else if (idx < R_QA) run_role(who, w, [](Ctx& c, const Params& p) { role_e<2, 1>(c, p); });
  else if (idx < R_KV) run_role(who, w, [](Ctx& c, const Params& p) { role_qa(c, p); });
  else if (idx < R_KV + 11) run_role(who, w, [](Ctx& c, const Params& p) { role_kv<false>(c, p); });
  else if (idx < R_C) run_role(who, w, [](Ctx& c, const Params& p) { role_kv<true>(c, p); });
  else if (idx < R_END) run_role(who, w, [](Ctx& c, const Params& p) { role_c(c, p); });
  else run_role(who, w, [](Ctx& c, const Params& p) { role_e<0, 0>(c, p); });
}

}

extern "C" void kernel_launch(void* const* d_in, const int* in_sizes, int n_in, void* d_out, int out_size, void* d_ws, size_t ws_size,
                              hipStream_t stream) {
  Params p;
  p.x = d_in[0]; p.mem0 = d_in[1]; p.wq = d_in[2]; p.wk = d_in[3]; p.wv = d_in[4]; p.wo = d_in[5];
  p.wg = d_in[8]; p.wu = d_in[9]; p.wd = d_in[10];
  p.out = d_out;
  p.ws = (unsigned char*)(((uintptr_t)d_ws + (2u << 20) - 1) & ~(uintptr_t)((2u << 20) - 1));
  (void)hipFuncSetAttribute((const void*)mega_kernel, hipFuncAttributeMaxDynamicSharedMemorySize, LDS_BYTES);
  init_kernel<<<256, 512, 0, stream>>>(p.ws);
  mega_kernel<<<256, 256, LDS_BYTES, stream>>>(p);
}
```
